```python
import torch

def _softmax(x, axis=-1):
    return torch.softmax(x, dim=axis)
def _gelu(x):
    return torch.nn.functional.gelu(x, approximate="tanh")
def _silu(x):
    return torch.nn.functional.silu(x)
def _sigmoid(x):
    return torch.sigmoid(x)
def _layernorm(x, axis=-1, eps=1e-5):
    m = x.mean(dim=axis, keepdim=True)
    v = x.var(dim=axis, keepdim=True, unbiased=False)
    return (x - m) / torch.sqrt(v + eps)
def _scatter_add(tgt, idx, src, axis=0):
    return tgt.clone().index_add_(axis, idx, src)
def _segment_sum(src, seg, n):
    out = torch.zeros((n,) + src.shape[1:], dtype=src.dtype)
    return out.index_add_(0, seg, src)
def _pad(x, pw):
    flat = tuple(v for pair in reversed(pw) for v in pair)
    return torch.nn.functional.pad(x, flat)

kernel_name = 'gated_mix__softmax_full_mha__mixer_channel_mlp__prompt'

def setup_inputs():

    _w = (lambda a: (lambda t: t.to(torch.bfloat16) if t.is_floating_point() else t)(torch.as_tensor(a)))
    def _A_setup():
        B, S, D = 1, 2048, 576
        return {
            "x": _w((lambda *s: torch.randn(*s, dtype=torch.bfloat16) * 0.05)(B, S, D)),
            "wqkv": _w((lambda *s: torch.randn(*s, dtype=torch.bfloat16) * 0.8)(D, 3 * D)),
            "wo": _w((lambda *s: torch.randn(*s, dtype=torch.bfloat16) * 0.1)(D, D)),
        }
    _A_in = _A_setup()

    def _B_setup():
        B, S, D, F = 1, 2048, 576, 4608
        return {
            "x": _w((lambda *s: torch.randn(*s, dtype=torch.bfloat16) * 0.05)(B, S, D)),
            "w1": _w((lambda *s: torch.randn(*s, dtype=torch.bfloat16) * 1.6)(D, F) * (D ** -0.5)),
            "b1": _w((lambda *s: torch.randn(*s, dtype=torch.bfloat16) * 0.8)(F)),
            "w2": _w((lambda *s: torch.randn(*s, dtype=torch.bfloat16) * 0.05)(F, D) * (F ** -0.5)),
            "b2": _w((lambda *s: torch.randn(*s, dtype=torch.bfloat16) * 0.05)(D)),
        }
    _B_in = _B_setup()

    return {
        "A_x": _A_in["x"],
        "A_wqkv": _A_in["wqkv"],
        "A_wo": _A_in["wo"],
        "B_x": _B_in["x"],
        "B_w1": _B_in["w1"],
        "B_b1": _B_in["b1"],
        "B_w2": _B_in["w2"],
        "B_b2": _B_in["b2"],
        "gate_w": _w((lambda *s: torch.randn(*s, dtype=torch.bfloat16) * 0.05)(576) * 16.0),
    }

def reference(A_x, A_wqkv, A_wo, B_x, B_w1, B_b1, B_w2, B_b2, gate_w):
    def _A_ref(x, wqkv, wo):
        B, S, D = x.shape
        H = 32; Dh = D // H
        qkv = (lambda x, *d: x.permute(*d))((x @ wqkv).reshape(B, S, 3, H, Dh), 2, 0, 3, 1, 4)
        q, k, v = qkv[0], qkv[1], qkv[2]
        scale = Dh ** -0.5
        scores = torch.einsum("bhqd,bhkd->bhqk", q, k) * scale
        p = _softmax(scores, axis=-1)
        o = torch.einsum("bhqk,bhkd->bhqd", p, v)
        return (lambda x, *d: x.permute(*d))(o, 0, 2, 1, 3).reshape(B, S, D) @ wo

    def _B_ref(x, w1, b1, w2, b2):
        y = _layernorm(x, axis=-1)
        h = _gelu(torch.einsum("bsd,df->bsf", y, w1) + b1)
        return torch.einsum("bsf,fd->bsd", h, w2) + b2

    x = A_x
    g = _sigmoid((x * gate_w).sum(-1, keepdims=True))
    a = _A_ref(x, A_wqkv, A_wo)
    b = _B_ref(x, B_w1, B_b1, B_w2, B_b2)
    return g * a + (1 - g) * b
```

```cpp
#include <hip/hip_runtime.h>
#include <stdint.h>

typedef __attribute__((ext_vector_type(8))) short bf16x8;
typedef __attribute__((ext_vector_type(4))) short s16x4;
typedef __attribute__((ext_vector_type(2))) __bf16 bf16x2_t;
typedef __attribute__((ext_vector_type(2))) float f32x2;
typedef __attribute__((ext_vector_type(4))) float f32x4;
typedef __attribute__((ext_vector_type(16))) float f32x16;
typedef __attribute__((ext_vector_type(4))) unsigned u32x4;
typedef __attribute__((ext_vector_type(2))) unsigned u32x2;
typedef __attribute__((address_space(3))) s16x4* lds_s16x4_p;
typedef __attribute__((address_space(3))) char* lds_p;

#define DI __device__ __forceinline__
#define MFMA32(a, b, c) __builtin_amdgcn_mfma_f32_32x32x16_bf16((a), (b), (c), 0, 0, 0)
#define MFMA32K8(a, b, c) __builtin_amdgcn_mfma_f32_32x32x8bf16_1k((a), (b), (c), 0, 0, 0)
#define WAIT_VM(n) asm volatile("s_waitcnt vmcnt(" #n ")" ::: "memory")
#define BARRIER() __builtin_amdgcn_s_barrier()

constexpr int S_ = 2048, D_ = 576, F_ = 4608, NQKV = 1728;
constexpr int TSTEP = 640;
constexpr int THEAD = 128 * TSTEP;
constexpr int TPART = 32 * THEAD;
constexpr size_t WS_T = 0;
constexpr size_t WS_H = 8u << 20;
constexpr size_t WS_O = 28u << 20;
constexpr size_t WS_P = 32u << 20;

DI unsigned pk2(float a, float b) {
  f32x2 v = {a, b};
  bf16x2_t r = __builtin_convertvector(v, bf16x2_t);
  return __builtin_bit_cast(unsigned, r);
}
DI float dot2bf(unsigned a, unsigned b, float acc) {
  return __builtin_amdgcn_fdot2_f32_bf16(__builtin_bit_cast(bf16x2_t, a), __builtin_bit_cast(bf16x2_t, b), acc, false);
}
DI float bf2f(unsigned short u) { return __uint_as_float(((unsigned)u) << 16); }
DI s16x4 tr_read(const char* p) {
  return __builtin_amdgcn_ds_read_tr16_b64_v4i16((lds_s16x4_p)(p));
}
DI void glds16(const char* g, char* l) {
  __builtin_amdgcn_global_load_lds((const unsigned*)g, (unsigned*)l, 16, 0, 0);
}
DI float xor32(float v) { return __shfl_xor(v, 32, 64); }

template <int XPW, int NB>
struct Stage {
  static constexpr int XBYTES = XPW * 8 * 1024;
  static constexpr int WBYTES = NB * 4 * 1024;
  static constexpr int BYTES = XBYTES + WBYTES;
  static constexpr int NWP = (4 * NB + 7) / 8;
};

template <int XPW, int NB, int MB, int NBW, bool TRANS, bool STATS>
DI void gemm_main(char* lds, const char* __restrict__ Xb, int ldx, int row0, const char* __restrict__ Wb, int ldw,
                  int col0, int ncols_total, int NK, int wave, int lane, int wm, int nb0, int statmb,
                  f32x16 (&acc)[MB][NBW], float& sx, float& sxx, float (&cs)[NBW]) {
  using ST = Stage<XPW, NB>;
  unsigned xoff[XPW];
#pragma unroll
  for (int i = 0; i < XPW; ++i) {
    int pi = wave * XPW + i;
    int row = pi * 8 + (lane >> 3);
    int c = (lane & 7) ^ ((row >> 1) & 7);
    xoff[i] = (unsigned)((row0 + row) * ldx + c * 16);
  }
  unsigned woff[ST::NWP];
#pragma unroll
  for (int i = 0; i < ST::NWP; ++i) {
    int pj = wave + 8 * i;
    int lin = pj * 1024 + lane * 16;
    int sub = lin >> 9;
    int kb = sub / NB, nb = sub - kb * NB;
    int kk = (lin >> 6) & 7, nc = (lin >> 4) & 3;
    int n = col0 + nb * 32 + nc * 8;
    n = n > ncols_total - 8 ? ncols_total - 8 : n;
    woff[i] = (unsigned)((kb * 8 + kk) * ldw + n * 2);
  }
  auto issue = [&](int kt, int buf) {
    const char* xs = Xb + kt * 128;
    const char* wsrc = Wb + (size_t)kt * 64 * ldw;
    char* dst = lds + buf * ST::BYTES;
#pragma unroll
    for (int i = 0; i < XPW; ++i) glds16(xs + xoff[i], dst + (wave * XPW + i) * 1024);
#pragma unroll
    for (int i = 0; i < ST::NWP; ++i) {
      if (wave + 8 * i < 4 * NB) glds16(wsrc + woff[i], dst + ST::XBYTES + (wave + 8 * i) * 1024);
    }
  };
  const int r = lane & 31, h = lane >> 5;
  const int q = (lane >> 2) & 3, p = lane & 3, g = (lane >> 4) & 1;
  const int xaddr0 = (wm * MB * 32 + r) * 128 + ((h ^ ((r >> 1) & 7)) << 4);
  const int waddr0 = ST::XBYTES + (h * NB + nb0) * 512 + q * 64 + g * 32 + p * 8;
  const unsigned ONES = 0x3F803F80u;

  issue(0, 0);
  for (int kt = 0; kt < NK; ++kt) {
    WAIT_VM(0);
    BARRIER();
    if (kt + 1 < NK) issue(kt + 1, (kt + 1) & 1);
    const char* buf = lds + (kt & 1) * ST::BYTES;
#pragma unroll
    for (int s = 0; s < 4; ++s) {
      bf16x8 xf[MB];
#pragma unroll
      for (int mb = 0; mb < MB; ++mb) xf[mb] = *(const bf16x8*)(buf + (xaddr0 ^ (s << 5)) + mb * 4096);
      bf16x8 wf[NBW];
#pragma unroll
      for (int nb = 0; nb < NBW; ++nb) {
        s16x4 lo = tr_read(buf + waddr0 + (2 * s * NB + nb) * 512);
        s16x4 hi = tr_read(buf + waddr0 + (2 * s * NB + nb) * 512 + 256);
        wf[nb] = __builtin_shufflevector(lo, hi, 0, 1, 2, 3, 4, 5, 6, 7);
      }
#pragma unroll
      for (int mb = 0; mb < MB; ++mb)
#pragma unroll
        for (int nb = 0; nb < NBW; ++nb) {
          if (TRANS) acc[mb][nb] = MFMA32(wf[nb], xf[mb], acc[mb][nb]);
          else acc[mb][nb] = MFMA32(xf[mb], wf[nb], acc[mb][nb]);
        }
      if (STATS) {
        u32x4 xs = __builtin_bit_cast(u32x4, statmb ? xf[MB - 1] : xf[0]);
#pragma unroll
        for (int i = 0; i < 4; ++i) { sx = dot2bf(xs[i], ONES, sx); sxx = dot2bf(xs[i], xs[i], sxx); }
        if (s == wm) {
#pragma unroll
          for (int nb = 0; nb < NBW; ++nb) {
            u32x4 wv = __builtin_bit_cast(u32x4, wf[nb]);
#pragma unroll
            for (int i = 0; i < 4; ++i) cs[nb] = dot2bf(wv[i], ONES, cs[nb]);
          }
        }
      }
    }
  }
}

template <int NBW>
DI void qkv_role(char* lds, const char* X, const char* Wqkv, char* T, int rt, int ct, int wave, int lane, int wm,
                 int nb0) {
  f32x16 acc[2][NBW];
#pragma unroll
  for (int a = 0; a < 2; ++a)
#pragma unroll
    for (int b = 0; b < NBW; ++b)
#pragma unroll
      for (int i = 0; i < 16; ++i) acc[a][b][i] = 0.f;
  float sx = 0.f, sxx = 0.f, cs[NBW];
  gemm_main<4, 7, 2, NBW, false, false>(lds, X, D_ * 2, rt * 256, Wqkv, NQKV * 2, ct * 216, NQKV, 9, wave, lane, wm,
                                        nb0, 0, acc, sx, sxx, cs);
  const int r = lane & 31, h = lane >> 5;
  const float QSCALE = 0.23570226039551584f * 1.4426950408889634f;
#pragma unroll
  for (int nb = 0; nb < NBW; ++nb) {
    int lc = (nb0 + nb) * 32 + r;
    if (lc < 216) {
      int n = ct * 216 + lc;
      int part = n / 576;
      int rem = n - part * 576;
      int head = rem / 18;
      int d = rem - head * 18;
      float sc = part == 0 ? QSCALE : 1.0f;
      char* base = T + (size_t)part * TPART + head * THEAD + h * (TSTEP / 2) + d * 16;
#pragma unroll
      for (int mb = 0; mb < 2; ++mb)
#pragma unroll
        for (int s = 0; s < 2; ++s) {
          int step = (rt * 256 + wm * 64 + mb * 32) / 16 + s;
          u32x4 v;
#pragma unroll
          for (int i = 0; i < 4; ++i) v[i] = pk2(acc[mb][nb][8 * s + 2 * i] * sc, acc[mb][nb][8 * s + 2 * i + 1] * sc);
          *(u32x4*)(base + step * TSTEP) = v;
          if (part == 2 && d == 17) {
            u32x4 o = {0x3F803F80u, 0x3F803F80u, 0x3F803F80u, 0x3F803F80u};
            *(u32x4*)(base + step * TSTEP + 16) = o;
          }
        }
    }
  }
}

DI float gelu_t(float v) {
  const float C0 = -2.0f * 1.4426950408889634f * 0.7978845608028654f;
  const float C1 = C0 * 0.044715f;
  float t = v * __builtin_fmaf(v * v, C1, C0);
  float e = __builtin_amdgcn_exp2f(t);
  return v * __builtin_amdgcn_rcpf(1.0f + e);
}

DI void mlp1_role(char* lds, const char* X, const char* W1, const unsigned short* b1, char* H, int rt, int ct,
                  int wave, int lane, int wm, int wn) {
  constexpr int NBW = 3;
  f32x16 acc[2][NBW];
#pragma unroll
  for (int a = 0; a < 2; ++a)
#pragma unroll
    for (int b = 0; b < NBW; ++b)
#pragma unroll
      for (int i = 0; i < 16; ++i) acc[a][b][i] = 0.f;
  float sx = 0.f, sxx = 0.f, cs[NBW] = {0.f, 0.f, 0.f};
  const int nb0 = wn * 3;
  gemm_main<4, 6, 2, NBW, true, true>(lds, X, D_ * 2, rt * 256, W1, F_ * 2, ct * 192, F_, 9, wave, lane, wm, nb0, wn,
                                      acc, sx, sxx, cs);
  const int r = lane & 31, h = lane >> 5;
  float* rowst = (float*)(lds + 122880);
  float* colp = (float*)(lds + 122880 + 2048);
  float* cc = (float*)(lds + 122880 + 2048 + 3072);
  sx += xor32(sx);
  sxx += xor32(sxx);
  {
    float mean = sx * (1.0f / 576.0f);
    float var = __builtin_fmaxf(sxx * (1.0f / 576.0f) - mean * mean, 0.f);
    float rstd = __builtin_amdgcn_rsqf(var + 1e-5f);
    if (h == 0) {
      f32x2 v = {rstd, rstd * mean};
      *(f32x2*)(rowst + (wm * 64 + wn * 32 + r) * 2) = v;
    }
  }
#pragma unroll
  for (int nb = 0; nb < NBW; ++nb) {
    float c = cs[nb] + xor32(cs[nb]);
    if (h == 0) colp[wm * 192 + (nb0 + nb) * 32 + r] = c;
  }
  __syncthreads();
  {
    int t = wave * 64 + lane;
    if (t < 192) {
      cc[t] = colp[t] + colp[192 + t] + colp[384 + t] + colp[576 + t];
      cc[192 + t] = bf2f(b1[ct * 192 + t]);
    }
  }
  __syncthreads();
#pragma unroll
  for (int mb = 0; mb < 2; ++mb) {
    int rowl = wm * 64 + mb * 32 + r;
    f32x2 st = *(const f32x2*)(rowst + rowl * 2);
    char* hrow = H + (size_t)(rt * 256 + rowl) * (F_ * 2) + ct * 384 + h * 16;
#pragma unroll
    for (int nb = 0; nb < NBW; ++nb) {
      u32x2 o2[4];
#pragma unroll
      for (int g = 0; g < 4; ++g) {
        int n = (nb0 + nb) * 32 + 8 * g + 4 * h;
        f32x4 c4 = *(const f32x4*)(cc + n);
        f32x4 b4 = *(const f32x4*)(cc + 192 + n);
        float v[4];
#pragma unroll
        for (int j = 0; j < 4; ++j) {
          float t = __builtin_fmaf(st[0], acc[mb][nb][4 * g + j], __builtin_fmaf(-st[1], c4[j], b4[j]));
          v[j] = gelu_t(t);
        }
        o2[g][0] = pk2(v[0], v[1]);
        o2[g][1] = pk2(v[2], v[3]);
      }
#pragma unroll
      for (int g = 0; g < 4; g += 2) {
        auto r0 = __builtin_amdgcn_permlane32_swap(o2[g][0], o2[g + 1][0], false, false);
        auto r1 = __builtin_amdgcn_permlane32_swap(o2[g][1], o2[g + 1][1], false, false);
        u32x4 w = {r0[0], r1[0], r0[1], r1[1]};
        *(u32x4*)(hrow + (nb0 + nb) * 64 + g * 16) = w;
      }
    }
  }
}

__global__ __launch_bounds__(512, 2) void kernelA(const char* __restrict__ X, const char* __restrict__ Wqkv,
                                                   const char* __restrict__ W1, const unsigned short* __restrict__ b1,
                                                   char* __restrict__ T, char* __restrict__ H) {
  __shared__ __attribute__((aligned(16))) char lds[122880 + 2048 + 3072 + 1536];
  const int wg = blockIdx.x;
  const int xcd = wg & 7, idx = wg >> 3;
  const int rt = (xcd & 1) * 4 + (idx & 3);
  const int ct = (xcd >> 1) + 4 * (idx >> 2);
  const int wave = __builtin_amdgcn_readfirstlane(threadIdx.x >> 6);
  const int lane = threadIdx.x & 63;
  const int wm = wave & 3, wn = wave >> 2;
  if (ct < 8) {
    if (wn == 0) qkv_role<4>(lds, X, Wqkv, T, rt, ct, wave, lane, wm, 0);
    else qkv_role<3>(lds, X, Wqkv, T, rt, ct, wave, lane, wm, 4);
  } else {
    mlp1_role(lds, X, W1, b1, H, rt, ct - 8, wave, lane, wm, wn);
  }
}

DI f32x16 qk_tile(const char* kb, const char* ke, bf16x8 qf, s16x4 qe, f32x16 c) {
  s16x4 lo = tr_read(kb);
  s16x4 hi = tr_read(kb + 64);
  s16x4 ex = tr_read(ke);
  bf16x8 kf = __builtin_shufflevector(lo, hi, 0, 1, 2, 3, 4, 5, 6, 7);
  f32x16 s = MFMA32(kf, qf, c);
  s = MFMA32K8(ex, qe, s);
  return s;
}
DI f32x16 pv_tile(const char* vb, const f32x16& pe, f32x16 o) {
  bf16x8 v0 = *(const bf16x8*)(vb);
  bf16x8 v1 = *(const bf16x8*)(vb + TSTEP);
  u32x4 p0, p1;
#pragma unroll
  for (int i = 0; i < 4; ++i) {
    p0[i] = pk2(pe[2 * i], pe[2 * i + 1]);
    p1[i] = pk2(pe[8 + 2 * i], pe[8 + 2 * i + 1]);
  }
  o = MFMA32(v0, __builtin_bit_cast(bf16x8, p0), o);
  o = MFMA32(v1, __builtin_bit_cast(bf16x8, p1), o);
  return o;
}
DI float rowmax16(const f32x16& s) {
  float m = __builtin_fmaxf(s[0], s[1]);
#pragma unroll
  for (int i = 2; i < 16; ++i) m = __builtin_fmaxf(m, s[i]);
  return __builtin_fmaxf(m, xor32(m));
}

__global__ __launch_bounds__(512, 2) void kernelAttn(const char* __restrict__ T, char* __restrict__ O) {
  __shared__ __attribute__((aligned(16))) char lds[2 * THEAD];
  const int wg = blockIdx.x;
  const int head = (wg & 7) + 8 * ((wg >> 3) & 3);
  const int qt = wg >> 5;
  const int wave = __builtin_amdgcn_readfirstlane(threadIdx.x >> 6);
  const int lane = threadIdx.x & 63;
  const int r = lane & 31, h = lane >> 5;
  const char* TK = T + (size_t)TPART + head * THEAD;
  const char* TV = T + (size_t)2 * TPART + head * THEAD;
  const int qi = qt * 256 + wave * 32 + r;
  const unsigned short* qp =
      (const unsigned short*)(T + head * THEAD + (qi >> 4) * TSTEP + ((qi >> 2) & 1) * (TSTEP / 2)) + (qi & 3) +
      4 * ((qi >> 3) & 1);
  unsigned short qv[8], qx[2];
#pragma unroll
  for (int j = 0; j < 8; ++j) qv[j] = qp[(8 * h + j) * 8];
  qx[0] = qp[16 * 8];
  qx[1] = qp[17 * 8];
  auto issue = [&](int c) {
#pragma unroll
    for (int i = 0; i < 5; ++i) {
      int pc = wave * 5 + i;
      int off = c * 20480 + (pc < 20 ? pc : pc - 20) * 1024;
      const char* src = (pc < 20 ? TK : TV) + off + lane * 16;
      glds16(src, lds + (pc < 20 ? 0 : THEAD) + off);
    }
  };
  issue(0);
  bf16x8 qf;
#pragma unroll
  for (int j = 0; j < 8; ++j) qf[j] = (short)qv[j];
  s16x4 qe;
  qe[0] = h ? (short)0 : (short)qx[0];
  qe[1] = h ? (short)0 : (short)qx[1];
  qe[2] = 0;
  qe[3] = 0;
  asm volatile("" : "+v"(qf), "+v"(qe));
  WAIT_VM(0);
  BARRIER();
  issue(1);
  issue(2);
  issue(3);

  const int q = (lane >> 2) & 3, p = lane & 3, g = (lane >> 4) & 1;
  const char* kb = lds + g * TSTEP + (p & 1) * (TSTEP / 2) + (8 * h + q) * 16 + (p >> 1) * 8;
  const char* ke = lds + g * TSTEP + (p & 1) * (TSTEP / 2) + (16 + (q & 1)) * 16 + (p >> 1) * 8;
  const char* vb = lds + THEAD + h * (TSTEP / 2) + (r < 18 ? r : 18) * 16;

  f32x16 zero;
#pragma unroll
  for (int i = 0; i < 16; ++i) zero[i] = 0.f;
  f32x16 o = zero;
  f32x16 sc = qk_tile(kb, ke, qf, qe, zero);
  float m0 = rowmax16(sc);
  f32x16 negm;
#pragma unroll
  for (int i = 0; i < 16; ++i) { negm[i] = -m0; sc[i] -= m0; }

  for (int c = 0; c < 4; ++c) {
    if (c == 1) { WAIT_VM(10); BARRIER(); }
    if (c == 2) { WAIT_VM(5); BARRIER(); }
    if (c == 3) { WAIT_VM(0); BARRIER(); }
#pragma unroll 4
    for (int tt = 0; tt < 16; ++tt) {
      int t = c * 16 + tt;
      if (tt == 0 && c > 0) sc = qk_tile(kb + t * 1280, ke + t * 1280, qf, qe, negm);
      f32x16 sn = sc;
      if (tt < 15) sn = qk_tile(kb + (t + 1) * 1280, ke + (t + 1) * 1280, qf, qe, negm);
      f32x16 pe;
#pragma unroll
      for (int i = 0; i < 16; ++i) pe[i] = __builtin_amdgcn_exp2f(sc[i]);
      o = pv_tile(vb + t * 1280, pe, o);
      sc = sn;
    }
  }
  float l = h ? o[8] : o[10];
  bool bad = !(l < 1.0e30f);
#pragma unroll
  for (int i = 0; i < 10; ++i) bad = bad || !(__builtin_fabsf(o[i]) < 1.0e37f);
  if (__any(bad)) {
    float m = -3.0e38f;
    o = zero;
    for (int t = 0; t < 64; ++t) {
      f32x16 s = qk_tile(kb + t * 1280, ke + t * 1280, qf, qe, zero);
      float mn = __builtin_fmaxf(m, rowmax16(s));
      float alpha = __builtin_amdgcn_exp2f(m - mn);
      m = mn;
      f32x16 pe;
#pragma unroll
      for (int i = 0; i < 16; ++i) { pe[i] = __builtin_amdgcn_exp2f(s[i] - mn); o[i] *= alpha; }
      o = pv_tile(vb + t * 1280, pe, o);
    }
    l = h ? o[8] : o[10];
  }
  float inv = 1.0f / l;
  unsigned* op = (unsigned*)(O + (size_t)qi * (D_ * 2) + head * 36 + h * 8);
  op[0] = pk2(o[0] * inv, o[1] * inv);
  op[1] = pk2(o[2] * inv, o[3] * inv);
  op[4] = pk2(o[4] * inv, o[5] * inv);
  op[5] = pk2(o[6] * inv, o[7] * inv);
  if (h == 0) op[8] = pk2(o[8] * inv, o[9] * inv);
}

template <int NBW>
DI void mlp2_role(char* lds, const char* H, const char* W2, float* P, int rt, int ct, int sp, int wave, int lane,
                  int wm, int nb0) {
  f32x16 acc[1][NBW];
#pragma unroll
  for (int b = 0; b < NBW; ++b)
#pragma unroll
    for (int i = 0; i < 16; ++i) acc[0][b][i] = 0.f;
  float sx = 0.f, sxx = 0.f, cs[NBW];
  gemm_main<2, 9, 1, NBW, true, false>(lds, H + sp * 1152, F_ * 2, rt * 128, W2 + (size_t)sp * 576 * (D_ * 2), D_ * 2,
                                       ct * 288, D_, 9, wave, lane, wm, nb0, 0, acc, sx, sxx, cs);
#pragma unroll
  for (int nb = 0; nb < NBW; ++nb) {
    float* pb = P + ((size_t)(sp * 64 + rt * 4 + wm) * 18 + ct * 9 + nb0 + nb) * 1024 + lane * 4;
#pragma unroll
    for (int g = 0; g < 4; ++g) {
      f32x4 v = {acc[0][nb][4 * g], acc[0][nb][4 * g + 1], acc[0][nb][4 * g + 2], acc[0][nb][4 * g + 3]};
      *(f32x4*)(pb + g * 256) = v;
    }
  }
}

__global__ __launch_bounds__(512, 2) void kernelMLP2(const char* __restrict__ H, const char* __restrict__ W2,
                                                      float* __restrict__ P) {
  __shared__ __attribute__((aligned(16))) char lds[2 * (16384 + 36864)];
  const int wg = blockIdx.x;
  const int sp = wg & 7, idx = wg >> 3;
  const int ct = idx & 1, rt = idx >> 1;
  const int wave = __builtin_amdgcn_readfirstlane(threadIdx.x >> 6);
  const int lane = threadIdx.x & 63;
  const int wm = wave & 3, wn = wave >> 2;
  if (wn == 0) mlp2_role<5>(lds, H, W2, P, rt, ct, sp, wave, lane, wm, 0);
  else mlp2_role<4>(lds, H, W2, P, rt, ct, sp, wave, lane, wm, 5);
}

__global__ __launch_bounds__(256, 2) void kernelC(const char* __restrict__ O, const char* __restrict__ Wo,
                                                   const float* __restrict__ P, const char* __restrict__ X,
                                                   const char* __restrict__ gw, const unsigned short* __restrict__ b2,
                                                   char* __restrict__ out) {
  __shared__ __attribute__((aligned(16))) char lds[4 * 16384 + 256];
  const int wg = blockIdx.x;
  const int ct = wg % 9, rt = wg / 9;
  const int wave = __builtin_amdgcn_readfirstlane(threadIdx.x >> 6);
  const int lane = threadIdx.x & 63;
  const int wm = wave & 1, wn = wave >> 1;
  const int r = lane & 31, h = lane >> 5;
  {
    int t = threadIdx.x;
    int row = rt * 64 + (t >> 2);
    const u32x4* xr = (const u32x4*)(X + (size_t)row * 1152) + (t & 3);
    const u32x4* gr = (const u32x4*)gw + (t & 3);
    float a = 0.f;
#pragma unroll
    for (int i = 0; i < 18; ++i) {
      u32x4 xv = xr[i * 4], gv = gr[i * 4];
#pragma unroll
      for (int j = 0; j < 4; ++j) a = dot2bf(xv[j], gv[j], a);
    }
    a += __shfl_xor(a, 1, 64);
    a += __shfl_xor(a, 2, 64);
    float gg = 1.0f / (1.0f + __builtin_amdgcn_exp2f(-1.4426950408889634f * a));
    if ((t & 3) == 0) ((float*)(lds + 65536))[t >> 2] = gg;
  }
  f32x16 ps;
#pragma unroll
  for (int i = 0; i < 16; ++i) ps[i] = 0.f;
  {
    const float* pb = P + ((size_t)(rt * 2 + wm) * 18 + ct * 2 + wn) * 1024 + lane * 4;
#pragma unroll
    for (int sp = 0; sp < 8; ++sp) {
#pragma unroll
      for (int g = 0; g < 4; ++g) {
        f32x4 v = *(const f32x4*)(pb + (size_t)sp * 64 * 18 * 1024 + g * 256);
#pragma unroll
        for (int j = 0; j < 4; ++j) ps[4 * g + j] += v[j];
      }
    }
  }
  unsigned xoff[2], woff[2];
#pragma unroll
  for (int i = 0; i < 2; ++i) {
    int pi = wave * 2 + i;
    int row = pi * 8 + (lane >> 3);
    int c = (lane & 7) ^ ((row >> 1) & 7);
    xoff[i] = (unsigned)((rt * 64 + row) * 1152 + c * 16);
    int lin = pi * 1024 + lane * 16;
    int sub = lin >> 9;
    int kb = sub >> 1, nb = sub & 1;
    int kk = (lin >> 6) & 7, nc = (lin >> 4) & 3;
    woff[i] = (unsigned)((kb * 8 + kk) * 1152 + (ct * 64 + nb * 32 + nc * 8) * 2);
  }
  auto issue = [&](int kt) {
    char* dst = lds + (kt & 3) * 16384;
#pragma unroll
    for (int i = 0; i < 2; ++i) glds16(O + kt * 128 + xoff[i], dst + (wave * 2 + i) * 1024);
#pragma unroll
    for (int i = 0; i < 2; ++i) glds16(Wo + (size_t)kt * 64 * 1152 + woff[i], dst + 8192 + (wave * 2 + i) * 1024);
  };
  asm volatile("" : "+v"(ps));
  issue(0);
  issue(1);
  issue(2);
  const int q = (lane >> 2) & 3, p = lane & 3, g = (lane >> 4) & 1;
  const int xaddr0 = (wm * 32 + r) * 128 + ((h ^ ((r >> 1) & 7)) << 4);
  const int waddr0 = 8192 + (h * 2 + wn) * 512 + q * 64 + g * 32 + p * 8;
  f32x16 acc;
#pragma unroll
  for (int i = 0; i < 16; ++i) acc[i] = 0.f;
#pragma unroll
  for (int kt = 0; kt < 9; ++kt) {
    if (kt < 7) WAIT_VM(8);
    else if (kt == 7) WAIT_VM(4);
    else WAIT_VM(0);
    BARRIER();
    if (kt + 3 < 9) issue(kt + 3);
    const char* buf = lds + (kt & 3) * 16384;
#pragma unroll
    for (int s = 0; s < 4; ++s) {
      bf16x8 xf = *(const bf16x8*)(buf + (xaddr0 ^ (s << 5)));
      s16x4 lo = tr_read(buf + waddr0 + (4 * s) * 512);
      s16x4 hi = tr_read(buf + waddr0 + (4 * s) * 512 + 256);
      bf16x8 wf = __builtin_shufflevector(lo, hi, 0, 1, 2, 3, 4, 5, 6, 7);
      acc = MFMA32(wf, xf, acc);
    }
  }
  float gg = ((const float*)(lds + 65536))[wm * 32 + r];
  float og = 1.0f - gg;
  int row = rt * 64 + wm * 32 + r;
  char* orow = out + (size_t)row * 1152 + (ct * 64 + wn * 32) * 2 + h * 16;
  u32x2 o2[4];
#pragma unroll
  for (int gq = 0; gq < 4; ++gq) {
    int n = ct * 64 + wn * 32 + 8 * gq + 4 * h;
    float v[4];
#pragma unroll
    for (int j = 0; j < 4; ++j) v[j] = gg * acc[4 * gq + j] + og * (ps[4 * gq + j] + bf2f(b2[n + j]));
    o2[gq][0] = pk2(v[0], v[1]);
    o2[gq][1] = pk2(v[2], v[3]);
  }
#pragma unroll
  for (int gq = 0; gq < 4; gq += 2) {
    auto r0 = __builtin_amdgcn_permlane32_swap(o2[gq][0], o2[gq + 1][0], false, false);
    auto r1 = __builtin_amdgcn_permlane32_swap(o2[gq][1], o2[gq + 1][1], false, false);
    u32x4 w = {r0[0], r1[0], r0[1], r1[1]};
    *(u32x4*)(orow + gq * 16) = w;
  }
}

extern "C" void kernel_launch(void* const* d_in, const int* in_sizes, int n_in, void* d_out, int out_size, void* d_ws,
                              size_t ws_size, hipStream_t stream) {
  const char* X = (const char*)d_in[0];
  const char* Wqkv = (const char*)d_in[1];
  const char* Wo = (const char*)d_in[2];
  const char* W1 = (const char*)d_in[4];
  const unsigned short* b1 = (const unsigned short*)d_in[5];
  const char* W2 = (const char*)d_in[6];
  const unsigned short* b2 = (const unsigned short*)d_in[7];
  const char* gw = (const char*)d_in[8];
  char* ws = (char*)d_ws;
  char* T = ws + WS_T;
  char* H = ws + WS_H;
  char* O = ws + WS_O;
  float* P = (float*)(ws + WS_P);
  kernelA<<<256, 512, 0, stream>>>(X, Wqkv, W1, b1, T, H);
  kernelAttn<<<256, 512, 0, stream>>>(T, O);
  kernelMLP2<<<256, 512, 0, stream>>>(H, W2, P);
  kernelC<<<288, 256, 0, stream>>>(O, Wo, P, X, gw, b2, (char*)d_out);
}
```

```cpp
#include <hip/hip_runtime.h>
#include <stdint.h>

typedef __attribute__((ext_vector_type(8))) short bf16x8;
typedef __attribute__((ext_vector_type(4))) short s16x4;
typedef __attribute__((ext_vector_type(2))) __bf16 bf16x2_t;
typedef __attribute__((ext_vector_type(2))) float f32x2;
typedef __attribute__((ext_vector_type(4))) float f32x4;
typedef __attribute__((ext_vector_type(16))) float f32x16;
typedef __attribute__((ext_vector_type(4))) unsigned u32x4;
typedef __attribute__((ext_vector_type(2))) unsigned u32x2;
typedef __attribute__((address_space(3))) s16x4* lds_s16x4_p;
typedef __attribute__((address_space(3))) char* lds_p;

#define STAMP(k, i)
#define DI __device__ __forceinline__
#define MFMA32(a, b, c) __builtin_amdgcn_mfma_f32_32x32x16_bf16((a), (b), (c), 0, 0, 0)
#define MFMA32K8(a, b, c) __builtin_amdgcn_mfma_f32_32x32x8bf16_1k((a), (b), (c), 0, 0, 0)
#define WAIT_VM(n) asm volatile("s_waitcnt vmcnt(" #n ")" ::: "memory")
#define BARRIER() __builtin_amdgcn_s_barrier()

constexpr int S_ = 2048, D_ = 576, F_ = 4608, NQKV = 1728;
constexpr int TSTEP = 640;
constexpr int THEAD = 128 * TSTEP;
constexpr int TPART = 32 * THEAD;
constexpr size_t WS_T = 0;
constexpr size_t WS_H = 8u << 20;
constexpr size_t WS_O = 28u << 20;
constexpr size_t WS_P = 32u << 20;
constexpr size_t WS_G = 31u << 20;

DI unsigned pk2(float a, float b) {
  f32x2 v = {a, b};
  bf16x2_t r = __builtin_convertvector(v, bf16x2_t);
  return __builtin_bit_cast(unsigned, r);
}
DI float dot2bf(unsigned a, unsigned b, float acc) {
  return __builtin_amdgcn_fdot2_f32_bf16(__builtin_bit_cast(bf16x2_t, a), __builtin_bit_cast(bf16x2_t, b), acc, false);
}
DI float bf2f(unsigned short u) { return __uint_as_float(((unsigned)u) << 16); }
DI s16x4 tr_read(const char* p) {
  return __builtin_amdgcn_ds_read_tr16_b64_v4i16((lds_s16x4_p)(p));
}
DI void glds16(const char* g, char* l) {
  __builtin_amdgcn_global_load_lds((const unsigned*)g, (unsigned*)l, 16, 0, 0);
}
DI float xor32(float v) { return __shfl_xor(v, 32, 64); }

DI void glds16s(const char* sbase, unsigned voff, unsigned lds_dst) {
  unsigned keep;
  asm volatile("s_mov_b32 %0, m0\n\ts_mov_b32 m0, %3\n\ts_nop 0\n\tglobal_load_lds_dwordx4 %1, %2\n\ts_mov_b32 m0, %0"
               : "=&s"(keep)
               : "v"(voff), "s"(sbase), "s"(lds_dst)
               : "memory");
}
DI unsigned lds_addr(const void* p) { return (unsigned)(uintptr_t)(lds_p)p; }

template <int I>
struct IC { static constexpr int v = I; };
template <int I, int N, class F>
DI void static_for(F&& f) {
  if constexpr (I < N) {
    f(IC<I>{});
    static_for<I + 1, N>(f);
  }
}
template <int N>
DI void wait_vm() { asm volatile("s_waitcnt vmcnt(%0)" ::"i"(N) : "memory"); }
#define SB() __builtin_amdgcn_sched_barrier(0)

template <int MB, int NBW>
struct Frags {
  bf16x8 x[MB];
  bf16x8 w[NBW];
  bf16x8 g;
};
#define MFMA4(a, b, c) __builtin_amdgcn_mfma_f32_4x4x4bf16_1k((a), (b), (c), 0, 0, 0)
DI bf16x8 cat8(s16x4 lo, s16x4 hi) { return __builtin_shufflevector(lo, hi, 0, 1, 2, 3, 4, 5, 6, 7); }

constexpr int A_XSLOT = 32768;
constexpr int A_WBASE = 3 * A_XSLOT;
template <int U>
constexpr int a_cw(int s, int grp) { return U == 4 ? 2 : (1 + (((s & 1) == grp) ? 1 : 0)); }
template <int U, int NWS>
constexpr int a_inflight(int j, int grp) {
  int cnt = 0;
  for (int t = j - 1; t >= -4; --t) {
    if ((t & 1) == 0) {
      int J = t / 2 + 2;
      if (t < 0) J = (t + 4) / 2;
      if (J >= 0 && J < 9) {
        if (J <= ((j + 1) >> 1)) return cnt;
        cnt += 4;
      }
    }
    int s = t + NWS - 1;
    if (s >= 0 && s < 18) {
      if (s <= j + 1) return cnt;
      cnt += a_cw<U>(s, grp);
    }
  }
  return cnt;
}

template <int U, int NWS, bool TRANS, int STATS>
DI void gemmA(char* lds, const char* __restrict__ Xb, int row0, const char* __restrict__ Wb, int ldw,
              const char* __restrict__ gw, int wave, int lane, int wm, int wn, f32x16 (&acc)[2][U],
              f32x4 (&sx)[2], f32x4 (&sxx)[2], f32x4 (&cs)[U]) {
  constexpr int NBW = U, NM = 2 * U, WSLOT = U * 4096;
  constexpr int GWOFF = A_WBASE + NWS * WSLOT;
  const unsigned lbase = lds_addr(lds);
  const int grp = wave >> 2;
  unsigned voffX[2], voffW;
  {
    int rl = lane >> 3;
#pragma unroll
    for (int i = 0; i < 2; ++i) {
      int row = wave * 32 + 8 * i + rl;
      int c = (lane & 7) ^ ((row >> 1) & 7);
      voffX[i] = (unsigned)((row0 + row) * 1152 + c * 16);
    }
    int c = (lane & 7) ^ (((rl >> 1) & 1) << 2);
    voffW = (unsigned)(rl * ldw + c * 16);
  }
  const int pi0 = wave, pi1 = U == 4 ? wave + 8 : 8 + (wave & 3);
  const int kb0 = pi0 / U, u0 = pi0 - kb0 * U, kb1 = pi1 / U, u1 = pi1 - kb1 * U;
  const char* wsrc0 = Wb + (size_t)kb0 * 8 * ldw + u0 * 128;
  const char* wsrc1 = Wb + (size_t)kb1 * 8 * ldw + u1 * 128;
  auto issueX = [&](int J, int i) {
    glds16s(Xb + J * 128 + (i >> 1) * (16 * 1152), voffX[i & 1], lbase + (J % 3) * A_XSLOT + (wave * 4 + i) * 1024);
  };
  auto issueW = [&](int s, int i) {
    unsigned dst = lbase + A_WBASE + (s % NWS) * WSLOT;
    if (i == 0) glds16s(wsrc0 + (size_t)s * 32 * ldw, voffW, dst + pi0 * 1024);
    else if (U == 4 || (s & 1) == grp) glds16s(wsrc1 + (size_t)s * 32 * ldw, voffW, dst + pi1 * 1024);
  };
  auto wait_top = [&](auto Jc) {
    constexpr int j = decltype(Jc)::v;
    constexpr int n0 = a_inflight<U, NWS>(j, 0), n1 = a_inflight<U, NWS>(j, 1);
    if constexpr (n0 == n1) wait_vm<n0>();
    else { if (grp == 0) wait_vm<n0>(); else wait_vm<n1>(); }
  };
  const int r = lane & 31, h = lane >> 5;
  const int q = (lane >> 2) & 3, p = lane & 3, g = (lane >> 4) & 1;
  const int xa = (wm * 64 + r) * 128 + ((h ^ ((r >> 1) & 7)) << 4);
  int wa[NBW];
#pragma unroll
  for (int nb = 0; nb < NBW; ++nb) {
    int b = wn * U + nb;
    wa[nb] = A_WBASE + (h * U + (b >> 1)) * 1024 + q * 128 + (((b & 1) ^ (q >> 1)) << 6) + g * 32 + p * 8;
  }
  const s16x4 ONES = {0x3F80, 0x3F80, 0x3F80, 0x3F80};

  STAMP(0, 0);
  if (STATS == 2) {
    if (wave == 0) glds16s(gw, lane * 16, lbase + GWOFF);
    else if (wave == 1 && lane < 8) glds16s(gw + 1024, lane * 16, lbase + GWOFF + 1024);
  }
  static_for<0, 4>([&](auto Tc) {
    constexpr int t = decltype(Tc)::v - 4;
    constexpr int s = t + NWS - 1;
    if constexpr (s >= 0) { issueW(s, 0); issueW(s, 1); }
    if constexpr ((t & 1) == 0) {
#pragma unroll
      for (int i = 0; i < 4; ++i) issueX((t + 4) / 2, i);
    }
  });
  wait_top(IC<-1>{});
  BARRIER();
  STAMP(0, 1);
  auto rd = [&](Frags<2, NBW>& f, s16x4 (&lo)[NBW], int j, int s, int n) {
    const char* xb = lds + ((j >> 1) % 3) * A_XSLOT;
    const char* wb = lds + (j % NWS) * WSLOT + 2 * s * U * 1024;
    const int ks = (j & 1) * 2 + s;
    if (n == 0) f.x[0] = *(const bf16x8*)(xb + (xa ^ (ks << 5)));
    else if (n == 2 * NBW + 1) {
      f.x[1] = *(const bf16x8*)(xb + (xa ^ (ks << 5)) + 4096);
      if (STATS == 2) f.g = *(const bf16x8*)(lds + GWOFF + (2 * j + s) * 32 + h * 16);
    }
    else if (n & 1) lo[(n - 1) >> 1] = tr_read(wb + wa[(n - 1) >> 1]);
    else f.w[(n - 2) >> 1] = cat8(lo[(n - 2) >> 1], tr_read(wb + wa[(n - 2) >> 1] + 512));
  };
  Frags<2, NBW> f0, f1;
  s16x4 lo[NBW];
#pragma unroll
  for (int n = 0; n < 2 * NBW + 2; ++n) rd(f0, lo, 0, 0, n);

  auto half = [&](auto Jc, auto Hc, Frags<2, NBW>& cur, Frags<2, NBW>& nxt) {
    constexpr int j = decltype(Jc)::v, hf = decltype(Hc)::v;
    constexpr int nj = hf ? j + 1 : j, ns = hf ? 0 : 1;
    static_for<0, NM>([&](auto Mc) {
      constexpr int m = decltype(Mc)::v;
      constexpr int mb = m / NBW, nb = m % NBW;
      if (TRANS) acc[mb][nb] = MFMA32(cur.w[nb], cur.x[mb], acc[mb][nb]);
      else acc[mb][nb] = MFMA32(cur.x[mb], cur.w[nb], acc[mb][nb]);
      if constexpr (nj < 18) {
        if constexpr (m == 0) { rd(nxt, lo, nj, ns, 0); rd(nxt, lo, nj, ns, 1); rd(nxt, lo, nj, ns, 2); }
        else if constexpr (m < NBW) { rd(nxt, lo, nj, ns, 2 * m + 1); rd(nxt, lo, nj, ns, 2 * m + 2); }
        else if constexpr (m == NBW) rd(nxt, lo, nj, ns, 2 * NBW + 1);
      }
      if constexpr (STATS != 0 && m == NBW + 1) {
        if (hf == wn) {
#pragma unroll
          for (int b = 0; b < 2; ++b) {
            s16x4 xl = __builtin_shufflevector(cur.x[b], cur.x[b], 0, 1, 2, 3);
            s16x4 xh = __builtin_shufflevector(cur.x[b], cur.x[b], 4, 5, 6, 7);
            if (STATS == 1) {
              sx[b] = MFMA4(ONES, xl, sx[b]);
              sxx[b] = MFMA4(xl, xl, sxx[b]);
              sx[b] = MFMA4(ONES, xh, sx[b]);
              sxx[b] = MFMA4(xh, xh, sxx[b]);
            } else {
              sx[b] = MFMA4(__builtin_shufflevector(cur.g, cur.g, 0, 1, 2, 3), xl, sx[b]);
              sxx[b] = MFMA4(__builtin_shufflevector(cur.g, cur.g, 4, 5, 6, 7), xh, sxx[b]);
            }
          }
          asm volatile("" : "+v"(sx[0]), "+v"(sxx[0]), "+v"(sx[1]), "+v"(sxx[1]));
        }
      }
      if constexpr (STATS == 1 && m == 2) {
        if (((2 * j + hf) & 3) == wm) {
#pragma unroll
          for (int b = 0; b < NBW; ++b) cs[b] = MFMA4(ONES, __builtin_shufflevector(cur.w[b], cur.w[b], 0, 1, 2, 3), cs[b]);
#pragma unroll
          for (int b = 0; b < NBW; ++b) cs[b] = MFMA4(ONES, __builtin_shufflevector(cur.w[b], cur.w[b], 4, 5, 6, 7), cs[b]);
        }
      }
      if constexpr (m == 1 || m == 3 || m == 5) {
        constexpr int e = hf * 3 + (m >> 1);
        constexpr int s = j + NWS - 1;
        if constexpr (e < 2) { if constexpr (s < 18) issueW(s, e); }
        else if constexpr ((j & 1) == 0 && j / 2 + 2 < 9) issueX(j / 2 + 2, e - 2);
      }
      SB();
    });
  };
  static_for<0, 18>([&](auto Jc) {
    if (decltype(Jc)::v == 4) STAMP(0, 2);
    if (decltype(Jc)::v == 9) STAMP(0, 3);
    if (decltype(Jc)::v == 14) STAMP(0, 4);
    wait_top(Jc);
    BARRIER();
    SB();
    half(Jc, IC<0>{}, f0, f1);
    half(Jc, IC<1>{}, f1, f0);
  });
  STAMP(0, 5);
}

template <int U, int NWS, int STATS>
DI void qkv_role(char* lds, const char* X, const char* Wqkv, const char* gw, char* T, float* G, int rt, int unit0,
                 int wave, int lane, int wm, int wn) {
  f32x16 acc[2][U];
#pragma unroll
  for (int a = 0; a < 2; ++a)
#pragma unroll
    for (int b = 0; b < U; ++b)
#pragma unroll
      for (int i = 0; i < 16; ++i) acc[a][b][i] = 0.f;
  const f32x4 z4 = {0.f, 0.f, 0.f, 0.f};
  f32x4 sx[2] = {z4, z4}, sxx[2] = {z4, z4}, cs[U];
  gemmA<U, NWS, false, STATS>(lds, X, rt * 256, Wqkv + unit0 * 128, NQKV * 2, gw, wave, lane, wm, wn, acc, sx, sxx, cs);
  const int r = lane & 31, h = lane >> 5;
  if (STATS == 2) {
    if (unit0 == 0) {
      float* gp = (float*)(lds + A_WBASE + NWS * U * 4096 + 1152);
#pragma unroll
      for (int b = 0; b < 2; ++b) {
        float v = sx[b][0] + sxx[b][0];
        v += xor32(v);
        if (h == 0) gp[wn * 256 + wm * 64 + b * 32 + r] = v;
      }
      __syncthreads();
      if (wave < 4) {
        float v = gp[wave * 64 + lane] + gp[256 + wave * 64 + lane];
        G[rt * 256 + wave * 64 + lane] = 1.0f / (1.0f + __builtin_amdgcn_exp2f(-1.4426950408889634f * v));
      }
    }
  }
  const float QSCALE = 0.23570226039551584f * 1.4426950408889634f;
#pragma unroll
  for (int nb = 0; nb < U; ++nb) {
    int n = unit0 * 64 + (wn * U + nb) * 32 + r;
    int part = n / 576;
    int rem = n - part * 576;
    int head = rem / 18;
    int d = rem - head * 18;
    float sc = part == 0 ? QSCALE : 1.0f;
    char* base = T + (size_t)part * TPART + head * THEAD + h * (TSTEP / 2) + d * 16;
#pragma unroll
    for (int mb = 0; mb < 2; ++mb)
#pragma unroll
      for (int s = 0; s < 2; ++s) {
        int step = (rt * 256 + wm * 64 + mb * 32) / 16 + s;
        u32x4 v;
#pragma unroll
        for (int i = 0; i < 4; ++i) v[i] = pk2(acc[mb][nb][8 * s + 2 * i] * sc, acc[mb][nb][8 * s + 2 * i + 1] * sc);
        *(u32x4*)(base + step * TSTEP) = v;
        if (part == 2 && d == 17) {
          u32x4 o = {0x3F803F80u, 0x3F803F80u, 0x3F803F80u, 0x3F803F80u};
          *(u32x4*)(base + step * TSTEP + 16) = o;
        }
      }
  }
}

DI unsigned short f2bf_rn(float f) { return (unsigned short)(pk2(f, 0.f) & 0xffffu); }

DI void mlp1_role(char* lds, const char* X, const char* W1, const unsigned short* b1, char* H, int rt, int ct,
                  int wave, int lane, int wm, int wn) {
  constexpr int NBW = 3;
  f32x16 acc[2][NBW];
#pragma unroll
  for (int a = 0; a < 2; ++a)
#pragma unroll
    for (int b = 0; b < NBW; ++b)
#pragma unroll
      for (int i = 0; i < 16; ++i) acc[a][b][i] = 0.f;
  const f32x4 z4 = {0.f, 0.f, 0.f, 0.f};
  f32x4 sx[2] = {z4, z4}, sxx[2] = {z4, z4}, cs[NBW] = {z4, z4, z4};
  const int r = lane & 31, h = lane >> 5;
  unsigned short bb[NBW];
#pragma unroll
  for (int nb = 0; nb < NBW; ++nb) bb[nb] = b1[ct * 192 + (wn * 3 + nb) * 32 + r];
  gemmA<3, 5, true, 1>(lds, X, rt * 256, W1 + ct * 384, F_ * 2, nullptr, wave, lane, wm, wn, acc, sx, sxx, cs);
  BARRIER();
  float* rowst = (float*)(lds + 4096);
  float* colp = (float*)lds;
#pragma unroll
  for (int b = 0; b < 2; ++b) {
    float a = sx[b][0];
    int i4 = lane & 3;
    float q2 = i4 == 0 ? sxx[b][0] : (i4 == 1 ? sxx[b][1] : (i4 == 2 ? sxx[b][2] : sxx[b][3]));
    a += xor32(a);
    q2 += xor32(q2);
    if (h == 0) {
      f32x2 v = {a, q2};
      *(f32x2*)(rowst + (wn * 256 + wm * 64 + b * 32 + r) * 2) = v;
    }
  }
#pragma unroll
  for (int nb = 0; nb < NBW; ++nb) {
    float c = cs[nb][0] + xor32(cs[nb][0]);
    if (h == 0) colp[wm * 192 + (wn * 3 + nb) * 32 + r] = c;
  }
  __syncthreads();
  bf16x8 wx[NBW], xx[2];
  float ca[2], cb[2], sd[2];
#pragma unroll
  for (int nb = 0; nb < NBW; ++nb) {
    int n = (wn * 3 + nb) * 32 + r;
    float c = -(colp[n] + colp[192 + n] + colp[384 + n] + colp[576 + n]);
    unsigned short chi = f2bf_rn(c);
    unsigned short clo = f2bf_rn(c - bf2f(chi));
    bf16x8 w = {(short)chi, (short)clo, (short)chi, (short)bb[nb], (short)bb[nb], 0, 0, 0};
    bf16x8 z = {0, 0, 0, 0, 0, 0, 0, 0};
    wx[nb] = h ? z : w;
  }
  const float C0 = -2.0f * 1.4426950408889634f * 0.7978845608028654f;
  const float C1 = C0 * 0.044715f;
#pragma unroll
  for (int mb = 0; mb < 2; ++mb) {
    f32x2 st = *(const f32x2*)(rowst + (wm * 64 + mb * 32 + r) * 2) +
               *(const f32x2*)(rowst + (256 + wm * 64 + mb * 32 + r) * 2);
    st[0] *= (1.0f / 576.0f);
    st[1] = __builtin_fmaxf(st[1] * (1.0f / 576.0f) - st[0] * st[0], 0.f);
    float rstd = __builtin_amdgcn_rsqf(st[1] + 1e-5f);
    float sdev = (st[1] + 1e-5f) * rstd;
    unsigned short mhi = f2bf_rn(st[0]);
    unsigned short mlo = f2bf_rn(st[0] - bf2f(mhi));
    unsigned short shi = f2bf_rn(sdev);
    unsigned short slo = f2bf_rn(sdev - bf2f(shi));
    bf16x8 x = {(short)mhi, (short)mhi, (short)mlo, (short)shi, (short)slo, 0, 0, 0};
    bf16x8 z = {0, 0, 0, 0, 0, 0, 0, 0};
    xx[mb] = h ? z : x;
    ca[mb] = C0 * rstd;
    cb[mb] = C1 * rstd * rstd * rstd;
    sd[mb] = sdev;
  }
#pragma unroll
  for (int mb = 0; mb < 2; ++mb)
#pragma unroll
    for (int nb = 0; nb < NBW; ++nb) acc[mb][nb] = MFMA32(wx[nb], xx[mb], acc[mb][nb]);
#pragma unroll
  for (int mb = 0; mb < 2; ++mb) {
    char* hb = H + ((size_t)(rt * 8 + wm * 2 + mb) * 288 + ct * 12 + wn * 6) * 1024 + lane * 16;
    const f32x2 a2 = {ca[mb], ca[mb]}, b2 = {cb[mb], cb[mb]}, s2 = {sd[mb], sd[mb]};
#pragma unroll
    for (int nb = 0; nb < NBW; ++nb)
#pragma unroll
      for (int gp = 0; gp < 2; ++gp) {
        u32x4 w;
#pragma unroll
        for (int i = 0; i < 4; ++i) {
          f32x2 v = {acc[mb][nb][8 * gp + 2 * i], acc[mb][nb][8 * gp + 2 * i + 1]};
          f32x2 t = v * __builtin_elementwise_fma(v * v, b2, a2);
          f32x2 e = {__builtin_amdgcn_exp2f(t[0]), __builtin_amdgcn_exp2f(t[1])};
          f32x2 d = __builtin_elementwise_fma(e, s2, s2);
          f32x2 rr = {__builtin_amdgcn_rcpf(d[0]), __builtin_amdgcn_rcpf(d[1])};
          f32x2 o = v * rr;
          w[i] = pk2(o[0], o[1]);
        }
        *(u32x4*)(hb + (nb * 2 + gp) * 1024) = w;
      }
  }
}

__global__ __launch_bounds__(512, 2) void kernelA(const char* __restrict__ X, const char* __restrict__ Wqkv,
                                                   const char* __restrict__ W1, const unsigned short* __restrict__ b1,
                                                   const char* __restrict__ gw, char* __restrict__ T,
                                                   char* __restrict__ H, float* __restrict__ G) {
  __shared__ __attribute__((aligned(16))) char lds[163840];
  const int wg = blockIdx.x;
  const int xcd = wg & 7, idx = wg >> 3;
  const int rt = (xcd & 1) * 4 + (idx & 3);
  const int ct = (xcd >> 1) + 4 * (idx >> 2);
  const int wave = __builtin_amdgcn_readfirstlane(threadIdx.x >> 6);
  const int lane = threadIdx.x & 63;
  const int wm = wave & 3, wn = wave >> 2;
  if (ct < 5) qkv_role<3, 5, 2>(lds, X, Wqkv, gw, T, G, rt, ct * 3, wave, lane, wm, wn);
  else if (ct < 8) qkv_role<4, 4, 0>(lds, X, Wqkv, gw, T, G, rt, 15 + (ct - 5) * 4, wave, lane, wm, wn);
  else mlp1_role(lds, X, W1, b1, H, rt, ct - 8, wave, lane, wm, wn);
  STAMP(0, 6);
  STAMP(0, 7);
}

DI f32x16 qk_tile(const char* kb, const char* ke, bf16x8 qf, s16x4 qe, f32x16 c) {
  s16x4 lo = tr_read(kb);
  s16x4 hi = tr_read(kb + 64);
  s16x4 ex = tr_read(ke);
  bf16x8 kf = __builtin_shufflevector(lo, hi, 0, 1, 2, 3, 4, 5, 6, 7);
  f32x16 s = MFMA32(kf, qf, c);
  s = MFMA32K8(ex, qe, s);
  return s;
}
DI f32x16 pv_tile(const char* vb, const f32x16& pe, f32x16 o) {
  bf16x8 v0 = *(const bf16x8*)(vb);
  bf16x8 v1 = *(const bf16x8*)(vb + TSTEP);
  u32x4 p0, p1;
#pragma unroll
  for (int i = 0; i < 4; ++i) {
    p0[i] = pk2(pe[2 * i], pe[2 * i + 1]);
    p1[i] = pk2(pe[8 + 2 * i], pe[8 + 2 * i + 1]);
  }
  o = MFMA32(v0, __builtin_bit_cast(bf16x8, p0), o);
  o = MFMA32(v1, __builtin_bit_cast(bf16x8, p1), o);
  return o;
}
struct KFrag { bf16x8 f; s16x4 e; };
struct VFrag { bf16x8 v0, v1; };
DI KFrag load_k(const char* kb, const char* ke) {
  KFrag k;
  s16x4 lo = tr_read(kb);
  s16x4 hi = tr_read(kb + 64);
  k.e = tr_read(ke);
  k.f = __builtin_shufflevector(lo, hi, 0, 1, 2, 3, 4, 5, 6, 7);
  return k;
}
DI VFrag load_v(const char* vb) {
  VFrag v;
  v.v0 = *(const bf16x8*)(vb);
  v.v1 = *(const bf16x8*)(vb + TSTEP);
  return v;
}
DI f32x16 exp_pv(const f32x16& s, const VFrag& v, f32x16 o) {
  u32x4 p0, p1;
#pragma unroll
  for (int i = 0; i < 4; ++i) p0[i] = pk2(__builtin_amdgcn_exp2f(s[2 * i]), __builtin_amdgcn_exp2f(s[2 * i + 1]));
  o = MFMA32(v.v0, __builtin_bit_cast(bf16x8, p0), o);
#pragma unroll
  for (int i = 0; i < 4; ++i)
    p1[i] = pk2(__builtin_amdgcn_exp2f(s[8 + 2 * i]), __builtin_amdgcn_exp2f(s[8 + 2 * i + 1]));
  o = MFMA32(v.v1, __builtin_bit_cast(bf16x8, p1), o);
  return o;
}
#define EXP2(x) __builtin_amdgcn_exp2f(x)
DI void tile_step(const f32x16& s, f32x16& sn, const KFrag& k, const VFrag& v, bf16x8 qf, s16x4 qe,
                  const f32x16& negm, f32x16& o) {
  u32x4 p0, p1;
  p0[0] = pk2(EXP2(s[0]), EXP2(s[1]));
  p0[1] = pk2(EXP2(s[2]), EXP2(s[3]));
  __builtin_amdgcn_sched_barrier(0);
  f32x16 t = MFMA32(k.f, qf, negm);
  __builtin_amdgcn_sched_barrier(0);
  p0[2] = pk2(EXP2(s[4]), EXP2(s[5]));
  p0[3] = pk2(EXP2(s[6]), EXP2(s[7]));
  __builtin_amdgcn_sched_barrier(0);
  o = MFMA32(v.v0, __builtin_bit_cast(bf16x8, p0), o);
  __builtin_amdgcn_sched_barrier(0);
  p1[0] = pk2(EXP2(s[8]), EXP2(s[9]));
  p1[1] = pk2(EXP2(s[10]), EXP2(s[11]));
  __builtin_amdgcn_sched_barrier(0);
  t = MFMA32K8(k.e, qe, t);
  __builtin_amdgcn_sched_barrier(0);
  p1[2] = pk2(EXP2(s[12]), EXP2(s[13]));
  p1[3] = pk2(EXP2(s[14]), EXP2(s[15]));
  __builtin_amdgcn_sched_barrier(0);
  o = MFMA32(v.v1, __builtin_bit_cast(bf16x8, p1), o);
  __builtin_amdgcn_sched_barrier(0);
  sn = t;
}
DI float rowmax16(const f32x16& s) {
  float m = __builtin_fmaxf(s[0], s[1]);
#pragma unroll
  for (int i = 2; i < 16; ++i) m = __builtin_fmaxf(m, s[i]);
  return __builtin_fmaxf(m, xor32(m));
}

__global__ __launch_bounds__(512, 2) void kernelAttn(const char* __restrict__ T, char* __restrict__ O) {
  __shared__ __attribute__((aligned(16))) char lds[2 * THEAD];
  const int wg = blockIdx.x;
  const int head = (wg & 7) + 8 * ((wg >> 3) & 3);
  const int qt = wg >> 5;
  const int wave = __builtin_amdgcn_readfirstlane(threadIdx.x >> 6);
  const int lane = threadIdx.x & 63;
  const int r = lane & 31, h = lane >> 5;
  const char* TK = T + (size_t)TPART + head * THEAD;
  const char* TV = T + (size_t)2 * TPART + head * THEAD;
  const int qi = qt * 256 + wave * 32 + r;
  const unsigned short* qp =
      (const unsigned short*)(T + head * THEAD + (qi >> 4) * TSTEP + ((qi >> 2) & 1) * (TSTEP / 2)) + (qi & 3) +
      4 * ((qi >> 3) & 1);
  unsigned short qv[8], qx[2];
#pragma unroll
  for (int j = 0; j < 8; ++j) qv[j] = qp[(8 * h + j) * 8];
  qx[0] = qp[16 * 8];
  qx[1] = qp[17 * 8];
  auto issue = [&](int c) {
#pragma unroll
    for (int i = 0; i < 5; ++i) {
      int pc = wave * 5 + i;
      int off = c * 20480 + (pc < 20 ? pc : pc - 20) * 1024;
      glds16s((pc < 20 ? TK : TV) + off, lane * 16, lds_addr(lds) + (pc < 20 ? 0 : THEAD) + off);
    }
  };
  issue(0);
  bf16x8 qf;
#pragma unroll
  for (int j = 0; j < 8; ++j) qf[j] = (short)qv[j];
  s16x4 qe;
  qe[0] = h ? (short)0 : (short)qx[0];
  qe[1] = h ? (short)0 : (short)qx[1];
  qe[2] = 0;
  qe[3] = 0;
  asm volatile("" : "+v"(qf), "+v"(qe));
  WAIT_VM(0);
  BARRIER();
  issue(1);
  issue(2);
  issue(3);

  const int q = (lane >> 2) & 3, p = lane & 3, g = (lane >> 4) & 1;
  const char* kb = lds + g * TSTEP + (p & 1) * (TSTEP / 2) + (8 * h + q) * 16 + (p >> 1) * 8;
  const char* ke = lds + g * TSTEP + (p & 1) * (TSTEP / 2) + (16 + (q & 1)) * 16 + (p >> 1) * 8;
  const char* vb = lds + THEAD + h * (TSTEP / 2) + (r < 18 ? r : 18) * 16;

  f32x16 zero;
#pragma unroll
  for (int i = 0; i < 16; ++i) zero[i] = 0.f;
  f32x16 o = zero;
  f32x16 sA = qk_tile(kb, ke, qf, qe, zero);
  float m0 = rowmax16(sA);
  f32x16 negm;
#pragma unroll
  for (int i = 0; i < 16; ++i) { negm[i] = -m0; sA[i] -= m0; }
  KFrag kA, kB;
  VFrag vA, vB;
  kB = load_k(kb + 1280, ke + 1280);
  vA = load_v(vb);
  f32x16 sB;
  for (int t = 0; t < 64; t += 2) {
    if ((t & 15) == 14) {
      if (t == 14) { WAIT_VM(10); } else if (t == 30) { WAIT_VM(5); } else { WAIT_VM(0); }
      BARRIER();
    }
    {
      int t2 = t + 2 < 64 ? t + 2 : 63;
      kA = load_k(kb + t2 * 1280, ke + t2 * 1280);
      vB = load_v(vb + (t + 1) * 1280);
      __builtin_amdgcn_sched_barrier(0);
      tile_step(sA, sB, kB, vA, qf, qe, negm, o);
    }
    {
      int t3 = t + 3 < 64 ? t + 3 : 63;
      int t2 = t + 2 < 64 ? t + 2 : 63;
      kB = load_k(kb + t3 * 1280, ke + t3 * 1280);
      VFrag vN = load_v(vb + t2 * 1280);
      __builtin_amdgcn_sched_barrier(0);
      tile_step(sB, sA, kA, vB, qf, qe, negm, o);
      vA = vN;
    }
  }
  float l = h ? o[8] : o[10];
  bool bad = !(l < 1.0e30f);
#pragma unroll
  for (int i = 0; i < 10; ++i) bad = bad || !(__builtin_fabsf(o[i]) < 1.0e37f);
  if (__any(bad)) {
    float m = -3.0e38f;
    o = zero;
    for (int t = 0; t < 64; ++t) {
      f32x16 s = qk_tile(kb + t * 1280, ke + t * 1280, qf, qe, zero);
      float mn = __builtin_fmaxf(m, rowmax16(s));
      float alpha = __builtin_amdgcn_exp2f(m - mn);
      m = mn;
      f32x16 pe;
#pragma unroll
      for (int i = 0; i < 16; ++i) { pe[i] = __builtin_amdgcn_exp2f(s[i] - mn); o[i] *= alpha; }
      o = pv_tile(vb + t * 1280, pe, o);
    }
    l = h ? o[8] : o[10];
  }
  float inv = 1.0f / l;
  char* ob = O + (size_t)(qt * 8 + wave) * 36 * 1024;
  u32x4 ov;
#pragma unroll
  for (int i = 0; i < 4; ++i) ov[i] = pk2(o[2 * i] * inv, o[2 * i + 1] * inv);
  *(u32x4*)(ob + head * 1024 + lane * 16) = ov;
  if (h == 0)
    *(unsigned*)(ob + (32 + (head >> 3)) * 1024 + (((head >> 2) & 1) * 32 + r) * 16 + (head & 3) * 4) =
        pk2(o[8] * inv, o[9] * inv);
}

constexpr int M_SLOT = 28672, M_NS = 5;
constexpr int m_cnt(int s, int grp) { return 3 + (((s & 1) == grp) ? 1 : 0); }
constexpr int m_inflight(int j, int grp) {
  int cnt = 0;
  for (int s = j + M_NS - 2; s > j + 1; --s)
    if (s < 18) cnt += m_cnt(s, grp);
  return cnt;
}
template <int NBW>
DI void mlp2_role(char* lds, const char* __restrict__ H, const char* __restrict__ W2, char* __restrict__ P, int rt,
                  int ct, int sp, int wave, int lane, int wm, int nb0) {
  f32x16 acc[NBW];
#pragma unroll
  for (int b = 0; b < NBW; ++b)
#pragma unroll
    for (int i = 0; i < 16; ++i) acc[b][i] = 0.f;
  const unsigned lbase = lds_addr(lds);
  const int grp = wave >> 2;
  const unsigned voffX = lane * 16;
  const unsigned voffW = (unsigned)((lane >> 3) * 1152 + (((lane & 7) ^ (((lane >> 4) & 1) << 2)) << 4));
  const char* xsrc = H + ((size_t)(rt * 4 + (wave >> 1)) * 288 + sp * 36 + (wave & 1)) * 1024;
  const char* wbase = W2 + (size_t)sp * 576 * 1152 + ct * 512;
  const int p0 = wave, p1 = wave + 8, p2 = 16 + (wave & 3);
  const char* ws0 = wbase + (p0 / 5) * (8 * 1152) + (p0 % 5) * 128;
  const char* ws1 = wbase + (p1 / 5) * (8 * 1152) + (p1 % 5) * 128;
  const char* ws2 = wbase + (p2 / 5) * (8 * 1152) + (p2 % 5) * 128;
  auto issue = [&](int s, int e) {
    unsigned dst = lbase + (s % M_NS) * M_SLOT;
    if (e == 0) glds16s(xsrc + s * 2048, voffX, dst + wave * 1024);
    else if (e == 1) glds16s(ws0 + s * (32 * 1152), voffW, dst + 8192 + p0 * 1024);
    else if (e == 2) glds16s(ws1 + s * (32 * 1152), voffW, dst + 8192 + p1 * 1024);
    else if ((s & 1) == grp) glds16s(ws2 + s * (32 * 1152), voffW, dst + 8192 + p2 * 1024);
  };
  auto wait_top = [&](auto Jc) {
    constexpr int j = decltype(Jc)::v;
    constexpr int n0 = m_inflight(j, 0), n1 = m_inflight(j, 1);
    if constexpr (n0 == n1) wait_vm<n0>();
    else { if (grp == 0) wait_vm<n0>(); else wait_vm<n1>(); }
  };
  const int h = lane >> 5;
  const int q = (lane >> 2) & 3, p = lane & 3, g = (lane >> 4) & 1;
  const int xa = wm * 2048 + lane * 16;
  int wa[NBW];
#pragma unroll
  for (int nb = 0; nb < NBW; ++nb) {
    int b = nb0 + nb + ct;
    wa[nb] = 8192 + (b >> 1) * 1024 + (4 * h + q) * 128 + (((b & 1) ^ (q >> 1)) << 6) + g * 32 + p * 8;
  }
  STAMP(2, 0);
#pragma unroll
  for (int s = 0; s < M_NS - 1; ++s)
#pragma unroll
    for (int e = 0; e < 4; ++e) issue(s, e);
  {
    constexpr int n0 = m_cnt(1, 0) + m_cnt(2, 0) + m_cnt(3, 0), n1 = m_cnt(1, 1) + m_cnt(2, 1) + m_cnt(3, 1);
    if (grp == 0) wait_vm<n0>(); else wait_vm<n1>();
  }
  BARRIER();
  STAMP(2, 1);
  auto rd = [&](Frags<1, NBW>& f, s16x4 (&lo)[NBW], int j, int s, int n) {
    const char* buf = lds + (j % M_NS) * M_SLOT;
    if (n == 0) f.x[0] = *(const bf16x8*)(buf + xa + s * 1024);
    else if (n & 1) lo[(n - 1) >> 1] = tr_read(buf + wa[(n - 1) >> 1] + s * 10240);
    else f.w[(n - 2) >> 1] = cat8(lo[(n - 2) >> 1], tr_read(buf + wa[(n - 2) >> 1] + s * 10240 + 5120));
  };
  Frags<1, NBW> f0, f1;
  s16x4 lo[NBW];
#pragma unroll
  for (int n = 0; n < 2 * NBW + 1; ++n) rd(f0, lo, 0, 0, n);
  auto half = [&](auto Jc, auto Hc, Frags<1, NBW>& cur, Frags<1, NBW>& nxt) {
    constexpr int j = decltype(Jc)::v, hf = decltype(Hc)::v;
    constexpr int nj = hf ? j + 1 : j, ns = hf ? 0 : 1;
    static_for<0, NBW>([&](auto Mc) {
      constexpr int m = decltype(Mc)::v;
      acc[m] = MFMA32(cur.w[m], cur.x[0], acc[m]);
      if constexpr (nj < 18) {
        if constexpr (m == 0) { rd(nxt, lo, nj, ns, 0); rd(nxt, lo, nj, ns, 1); rd(nxt, lo, nj, ns, 2); }
        else if constexpr (m == 1) { rd(nxt, lo, nj, ns, 3); rd(nxt, lo, nj, ns, 4); rd(nxt, lo, nj, ns, 5); }
        else if constexpr (m == 2) { rd(nxt, lo, nj, ns, 6); rd(nxt, lo, nj, ns, 7); rd(nxt, lo, nj, ns, 8); }
        else if constexpr (m == 3 && NBW == 5) { rd(nxt, lo, nj, ns, 9); rd(nxt, lo, nj, ns, 10); }
      }
      if constexpr (m == 1 || m == 3) {
        constexpr int s = j + M_NS - 1;
        if constexpr (s < 18) issue(s, hf * 2 + (m >> 1));
      }
      SB();
    });
  };
  static_for<0, 18>([&](auto Jc) {
    if (decltype(Jc)::v == 4) STAMP(2, 2);
    if (decltype(Jc)::v == 9) STAMP(2, 3);
    if (decltype(Jc)::v == 14) STAMP(2, 4);
    wait_top(Jc);
    BARRIER();
    SB();
    half(Jc, IC<0>{}, f0, f1);
    half(Jc, IC<1>{}, f1, f0);
  });
  STAMP(2, 5);
#pragma unroll
  for (int nb = 0; nb < NBW; ++nb) {
    char* pb = P + ((size_t)(sp * 64 + rt * 4 + wm) * 18 + ct * 9 + nb0 + nb) * 2048 + lane * 16;
#pragma unroll
    for (int gp = 0; gp < 2; ++gp) {
      u32x4 v;
#pragma unroll
      for (int i = 0; i < 4; ++i) v[i] = pk2(acc[nb][8 * gp + 2 * i], acc[nb][8 * gp + 2 * i + 1]);
      *(u32x4*)(pb + gp * 1024) = v;
    }
  }
}

__global__ __launch_bounds__(512, 2) void kernelMLP2(const char* __restrict__ H, const char* __restrict__ W2,
                                                      char* __restrict__ P) {
  __shared__ __attribute__((aligned(16))) char lds[M_NS * M_SLOT];
  const int wg = blockIdx.x;
  const int sp = wg & 7, idx = wg >> 3;
  const int ct = idx & 1, rt = idx >> 1;
  const int wave = __builtin_amdgcn_readfirstlane(threadIdx.x >> 6);
  const int lane = threadIdx.x & 63;
  const int wm = wave & 3, wn = wave >> 2;
  if (wn == 0) mlp2_role<5>(lds, H, W2, P, rt, ct, sp, wave, lane, wm, 0);
  else mlp2_role<4>(lds, H, W2, P, rt, ct, sp, wave, lane, wm, 5);
  STAMP(2, 6);
  STAMP(2, 7);
}

__global__ __launch_bounds__(256, 2) void kernelC(const char* __restrict__ O, const char* __restrict__ Wo,
                                                   const char* __restrict__ P, const float* __restrict__ G,
                                                   const unsigned short* __restrict__ b2, char* __restrict__ out) {
  __shared__ __attribute__((aligned(16))) char lds[73728];
  const int wg = blockIdx.x;
  const int ct = wg % 9, rt = wg / 9;
  const int wave = __builtin_amdgcn_readfirstlane(threadIdx.x >> 6);
  const int lane = threadIdx.x & 63;
  const int wm = wave & 1, wn = wave >> 1;
  const int r = lane & 31, h = lane >> 5;
  const unsigned lbase = lds_addr(lds);
  STAMP(3, 0);
  {
    const unsigned voffW = (unsigned)((lane >> 3) * 1152 + (((lane & 7) ^ (((lane >> 4) & 1) << 2)) << 4));
#pragma unroll
    for (int i = 0; i < 18; ++i) {
      int pi = i * 4 + wave;
      glds16s(Wo + (size_t)pi * (8 * 1152) + ct * 128, voffW, lbase + pi * 1024);
    }
  }
  const char* ob = O + (size_t)(rt * 2 + wm) * 36 * 1024 + lane * 16;
  bf16x8 of[36];
#pragma unroll
  for (int t = 0; t < 36; ++t) of[t] = *(const bf16x8*)(ob + t * 1024);
  u32x4 pv[16];
  {
    const char* pb = P + ((size_t)(rt * 2 + wm) * 18 + ct * 2 + wn) * 2048 + lane * 16;
#pragma unroll
    for (int sp = 0; sp < 8; ++sp)
#pragma unroll
      for (int gp = 0; gp < 2; ++gp) pv[sp * 2 + gp] = *(const u32x4*)(pb + (size_t)sp * 64 * 18 * 2048 + gp * 1024);
  }
  const float gg = G[rt * 64 + wm * 32 + r];
  unsigned short bb[16];
#pragma unroll
  for (int i = 0; i < 16; ++i) bb[i] = b2[ct * 64 + wn * 32 + 8 * (i >> 2) + 4 * h + (i & 3)];
  SB();
  STAMP(3, 1);
  WAIT_VM(0);
  STAMP(3, 2);
  BARRIER();
  STAMP(3, 3);
  const int q = (lane >> 2) & 3, p = lane & 3, g = (lane >> 4) & 1;
  f32x16 acc;
#pragma unroll
  for (int i = 0; i < 16; ++i) acc[i] = 0.f;
  const int cb = g * 32 + p * 8;
#pragma unroll
  for (int t = 0; t < 32; ++t) {
    int r0 = 18 * t + 4 * h + q, r1 = r0 + 8;
    s16x4 lo = tr_read(lds + r0 * 128 + ((wn ^ ((r0 >> 1) & 1)) << 6) + cb);
    s16x4 hi = tr_read(lds + r1 * 128 + ((wn ^ ((r1 >> 1) & 1)) << 6) + cb);
    acc = MFMA32(cat8(lo, hi), of[t], acc);
  }
#pragma unroll
  for (int u = 0; u < 4; ++u) {
    int r0 = (8 * u + 4 * h + (q >> 1)) * 18 + 16 + (q & 1), r1 = r0 + 36;
    s16x4 lo = tr_read(lds + r0 * 128 + ((wn ^ ((r0 >> 1) & 1)) << 6) + cb);
    s16x4 hi = tr_read(lds + r1 * 128 + ((wn ^ ((r1 >> 1) & 1)) << 6) + cb);
    acc = MFMA32(cat8(lo, hi), of[32 + u], acc);
  }
  asm volatile("" : "+v"(acc));
  STAMP(3, 4);
  f32x16 ps;
#pragma unroll
  for (int i = 0; i < 16; ++i) ps[i] = bf2f(bb[i]);
#pragma unroll
  for (int sp = 0; sp < 8; ++sp)
#pragma unroll
    for (int gp = 0; gp < 2; ++gp)
#pragma unroll
      for (int i = 0; i < 4; ++i) {
        unsigned w = pv[sp * 2 + gp][i];
        ps[8 * gp + 2 * i] += __uint_as_float(w << 16);
        ps[8 * gp + 2 * i + 1] += __uint_as_float(w & 0xffff0000u);
      }
  const float og = 1.0f - gg;
  int row = rt * 64 + wm * 32 + r;
  char* orow = out + (size_t)row * 1152 + (ct * 64 + wn * 32) * 2 + h * 16;
  u32x2 o2[4];
#pragma unroll
  for (int gq = 0; gq < 4; ++gq) {
    float v[4];
#pragma unroll
    for (int j = 0; j < 4; ++j) v[j] = gg * acc[4 * gq + j] + og * ps[4 * gq + j];
    o2[gq][0] = pk2(v[0], v[1]);
    o2[gq][1] = pk2(v[2], v[3]);
  }
#pragma unroll
  for (int gq = 0; gq < 4; gq += 2) {
    auto r0 = __builtin_amdgcn_permlane32_swap(o2[gq][0], o2[gq + 1][0], false, false);
    auto r1 = __builtin_amdgcn_permlane32_swap(o2[gq][1], o2[gq + 1][1], false, false);
    u32x4 w = {r0[0], r1[0], r0[1], r1[1]};
    *(u32x4*)(orow + gq * 16) = w;
  }
  STAMP(3, 5);
  STAMP(3, 6);
  STAMP(3, 7);
}

extern "C" void kernel_launch(void* const* d_in, const int* in_sizes, int n_in, void* d_out, int out_size, void* d_ws,
                              size_t ws_size, hipStream_t stream) {
  const char* X = (const char*)d_in[0];
  const char* Wqkv = (const char*)d_in[1];
  const char* Wo = (const char*)d_in[2];
  const char* W1 = (const char*)d_in[4];
  const unsigned short* b1 = (const unsigned short*)d_in[5];
  const char* W2 = (const char*)d_in[6];
  const unsigned short* b2 = (const unsigned short*)d_in[7];
  const char* gw = (const char*)d_in[8];
  char* ws = (char*)d_ws;
  char* T = ws + WS_T;
  char* H = ws + WS_H;
  char* O = ws + WS_O;
  char* P = ws + WS_P;
  float* G = (float*)(ws + WS_G);
  kernelA<<<256, 512, 0, stream>>>(X, Wqkv, W1, b1, gw, T, H, G);
  kernelAttn<<<256, 512, 0, stream>>>(T, O);
  kernelMLP2<<<256, 512, 0, stream>>>(H, W2, P);
  kernelC<<<288, 256, 0, stream>>>(O, Wo, P, G, b2, (char*)d_out);
}
```

```cpp
#include <hip/hip_runtime.h>
#include <stdint.h>

typedef __attribute__((ext_vector_type(8))) short bf16x8;
typedef __attribute__((ext_vector_type(4))) short s16x4;
typedef __attribute__((ext_vector_type(2))) __bf16 bf16x2_t;
typedef __attribute__((ext_vector_type(2))) float f32x2;
typedef __attribute__((ext_vector_type(4))) float f32x4;
typedef __attribute__((ext_vector_type(16))) float f32x16;
typedef __attribute__((ext_vector_type(4))) unsigned u32x4;
typedef __attribute__((ext_vector_type(2))) unsigned u32x2;
typedef __attribute__((address_space(3))) s16x4* lds_s16x4_p;
typedef __attribute__((address_space(3))) char* lds_p;

#define STAMP(k, i)
#define DI __device__ __forceinline__
#define MFMA32(a, b, c) __builtin_amdgcn_mfma_f32_32x32x16_bf16((a), (b), (c), 0, 0, 0)
#define MFMA32K8(a, b, c) __builtin_amdgcn_mfma_f32_32x32x8bf16_1k((a), (b), (c), 0, 0, 0)
#define WAIT_VM(n) asm volatile("s_waitcnt vmcnt(" #n ")" ::: "memory")
#define BARRIER() __builtin_amdgcn_s_barrier()

constexpr int S_ = 2048, D_ = 576, F_ = 4608, NQKV = 1728;
constexpr int TSTEP = 640;
constexpr int THEAD = 128 * TSTEP;
constexpr int TPART = 32 * THEAD;
constexpr size_t WS_T = 0;
constexpr size_t WS_H = 8u << 20;
constexpr size_t WS_O = 28u << 20;
constexpr size_t WS_P = 32u << 20;
constexpr size_t WS_G = 31u << 20;

DI unsigned pk2(float a, float b) {
  f32x2 v = {a, b};
  bf16x2_t r = __builtin_convertvector(v, bf16x2_t);
  return __builtin_bit_cast(unsigned, r);
}
DI float dot2bf(unsigned a, unsigned b, float acc) {
  return __builtin_amdgcn_fdot2_f32_bf16(__builtin_bit_cast(bf16x2_t, a), __builtin_bit_cast(bf16x2_t, b), acc, false);
}
DI float bf2f(unsigned short u) { return __uint_as_float(((unsigned)u) << 16); }
DI s16x4 tr_read(const char* p) {
  return __builtin_amdgcn_ds_read_tr16_b64_v4i16((lds_s16x4_p)(p));
}
DI void glds16(const char* g, char* l) {
  __builtin_amdgcn_global_load_lds((const unsigned*)g, (unsigned*)l, 16, 0, 0);
}
DI float xor32(float v) { return __shfl_xor(v, 32, 64); }

DI void glds16s(const char* sbase, unsigned voff, unsigned lds_dst) {
  unsigned keep;
  asm volatile("s_mov_b32 %0, m0\n\ts_mov_b32 m0, %3\n\ts_nop 0\n\tglobal_load_lds_dwordx4 %1, %2\n\ts_mov_b32 m0, %0"
               : "=&s"(keep)
               : "v"(voff), "s"(sbase), "s"(lds_dst)
               : "memory");
}
DI unsigned lds_addr(const void* p) { return (unsigned)(uintptr_t)(lds_p)p; }

DI void st16(void* p, u32x4 v) { asm volatile("global_store_dwordx4 %0, %1, off sc1\n\ts_nop 1" ::"v"(p), "v"(v) : "memory"); }
DI void st4(void* p, unsigned v) { asm volatile("global_store_dword %0, %1, off sc1" ::"v"(p), "v"(v) : "memory"); }

template <int I>
struct IC { static constexpr int v = I; };
template <int I, int N, class F>
DI void static_for(F&& f) {
  if constexpr (I < N) {
    f(IC<I>{});
    static_for<I + 1, N>(f);
  }
}
template <int N>
DI void wait_vm() { asm volatile("s_waitcnt vmcnt(%0)" ::"i"(N) : "memory"); }
#define SB() __builtin_amdgcn_sched_barrier(0)

template <int MB, int NBW>
struct Frags {
  bf16x8 x[MB];
  bf16x8 w[NBW];
  bf16x8 g;
};
#define MFMA4(a, b, c) __builtin_amdgcn_mfma_f32_4x4x4bf16_1k((a), (b), (c), 0, 0, 0)
DI bf16x8 cat8(s16x4 lo, s16x4 hi) { return __builtin_shufflevector(lo, hi, 0, 1, 2, 3, 4, 5, 6, 7); }

constexpr int A_XSLOT = 32768;
constexpr int A_WBASE = 3 * A_XSLOT;
template <int U>
constexpr int a_cw(int s, int grp) { return U == 4 ? 2 : (1 + (((s & 1) == grp) ? 1 : 0)); }
template <int U, int NWS>
constexpr int a_inflight(int j, int grp) {
  int cnt = 0;
  for (int t = j - 1; t >= -4; --t) {
    if ((t & 1) == 0) {
      int J = t / 2 + 2;
      if (t < 0) J = (t + 4) / 2;
      if (J >= 0 && J < 9) {
        if (J <= ((j + 1) >> 1)) return cnt;
        cnt += 4;
      }
    }
    int s = t + NWS - 1;
    if (s >= 0 && s < 18) {
      if (s <= j + 1) return cnt;
      cnt += a_cw<U>(s, grp);
    }
  }
  return cnt;
}

template <int U, int NWS, bool TRANS, int STATS>
DI void gemmA(char* lds, const char* __restrict__ Xb, int row0, const char* __restrict__ Wb, int ldw,
              const char* __restrict__ gw, int wave, int lane, int wm, int wn, f32x16 (&acc)[2][U],
              f32x4 (&sx)[2], f32x4 (&sxx)[2], f32x4 (&cs)[U]) {
  constexpr int NBW = U, NM = 2 * U, WSLOT = U * 4096;
  constexpr int GWOFF = A_WBASE + NWS * WSLOT;
  const unsigned lbase = lds_addr(lds);
  const int grp = wave >> 2;
  unsigned voffX[2], voffW;
  {
    int rl = lane >> 3;
#pragma unroll
    for (int i = 0; i < 2; ++i) {
      int row = wave * 32 + 8 * i + rl;
      int c = (lane & 7) ^ ((row >> 1) & 7);
      voffX[i] = (unsigned)((row0 + row) * 1152 + c * 16);
    }
    int c = (lane & 7) ^ (((rl >> 1) & 1) << 2);
    voffW = (unsigned)(rl * ldw + c * 16);
  }
  const int pi0 = wave, pi1 = U == 4 ? wave + 8 : 8 + (wave & 3);
  const int kb0 = pi0 / U, u0 = pi0 - kb0 * U, kb1 = pi1 / U, u1 = pi1 - kb1 * U;
  const char* wsrc0 = Wb + (size_t)kb0 * 8 * ldw + u0 * 128;
  const char* wsrc1 = Wb + (size_t)kb1 * 8 * ldw + u1 * 128;
  auto issueX = [&](int J, int i) {
    glds16s(Xb + J * 128 + (i >> 1) * (16 * 1152), voffX[i & 1], lbase + (J % 3) * A_XSLOT + (wave * 4 + i) * 1024);
  };
  auto issueW = [&](int s, int i) {
    unsigned dst = lbase + A_WBASE + (s % NWS) * WSLOT;
    if (i == 0) glds16s(wsrc0 + (size_t)s * 32 * ldw, voffW, dst + pi0 * 1024);
    else if (U == 4 || (s & 1) == grp) glds16s(wsrc1 + (size_t)s * 32 * ldw, voffW, dst + pi1 * 1024);
  };
  auto wait_top = [&](auto Jc) {
    constexpr int j = decltype(Jc)::v;
    constexpr int n0 = a_inflight<U, NWS>(j, 0), n1 = a_inflight<U, NWS>(j, 1);
    if constexpr (n0 == n1) wait_vm<n0>();
    else { if (grp == 0) wait_vm<n0>(); else wait_vm<n1>(); }
  };
  const int r = lane & 31, h = lane >> 5;
  const int q = (lane >> 2) & 3, p = lane & 3, g = (lane >> 4) & 1;
  const int xa = (wm * 64 + r) * 128 + ((h ^ ((r >> 1) & 7)) << 4);
  int wa[NBW];
#pragma unroll
  for (int nb = 0; nb < NBW; ++nb) {
    int b = wn * U + nb;
    wa[nb] = A_WBASE + (h * U + (b >> 1)) * 1024 + q * 128 + (((b & 1) ^ (q >> 1)) << 6) + g * 32 + p * 8;
  }
  const s16x4 ONES = {0x3F80, 0x3F80, 0x3F80, 0x3F80};

  STAMP(0, 0);
  if (STATS == 2) {
    if (wave == 0) glds16s(gw, lane * 16, lbase + GWOFF);
    else if (wave == 1 && lane < 8) glds16s(gw + 1024, lane * 16, lbase + GWOFF + 1024);
  }
  static_for<0, 4>([&](auto Tc) {
    constexpr int t = decltype(Tc)::v - 4;
    constexpr int s = t + NWS - 1;
    if constexpr (s >= 0) { issueW(s, 0); issueW(s, 1); }
    if constexpr ((t & 1) == 0) {
#pragma unroll
      for (int i = 0; i < 4; ++i) issueX((t + 4) / 2, i);
    }
  });
  wait_top(IC<-1>{});
  BARRIER();
  STAMP(0, 1);
  auto rd = [&](Frags<2, NBW>& f, s16x4 (&lo)[NBW], int j, int s, int n) {
    const char* xb = lds + ((j >> 1) % 3) * A_XSLOT;
    const char* wb = lds + (j % NWS) * WSLOT + 2 * s * U * 1024;
    const int ks = (j & 1) * 2 + s;
    if (n == 0) f.x[0] = *(const bf16x8*)(xb + (xa ^ (ks << 5)));
    else if (n == 2 * NBW + 1) {
      f.x[1] = *(const bf16x8*)(xb + (xa ^ (ks << 5)) + 4096);
      if (STATS == 2) f.g = *(const bf16x8*)(lds + GWOFF + (2 * j + s) * 32 + h * 16);
    }
    else if (n & 1) lo[(n - 1) >> 1] = tr_read(wb + wa[(n - 1) >> 1]);
    else f.w[(n - 2) >> 1] = cat8(lo[(n - 2) >> 1], tr_read(wb + wa[(n - 2) >> 1] + 512));
  };
  Frags<2, NBW> f0, f1;
  s16x4 lo[NBW];
#pragma unroll
  for (int n = 0; n < 2 * NBW + 2; ++n) rd(f0, lo, 0, 0, n);

  auto half = [&](auto Jc, auto Hc, Frags<2, NBW>& cur, Frags<2, NBW>& nxt) {
    constexpr int j = decltype(Jc)::v, hf = decltype(Hc)::v;
    constexpr int nj = hf ? j + 1 : j, ns = hf ? 0 : 1;
    static_for<0, NM>([&](auto Mc) {
      constexpr int m = decltype(Mc)::v;
      constexpr int mb = m / NBW, nb = m % NBW;
      if (TRANS) acc[mb][nb] = MFMA32(cur.w[nb], cur.x[mb], acc[mb][nb]);
      else acc[mb][nb] = MFMA32(cur.x[mb], cur.w[nb], acc[mb][nb]);
      if constexpr (nj < 18) {
        if constexpr (m == 0) { rd(nxt, lo, nj, ns, 0); rd(nxt, lo, nj, ns, 1); rd(nxt, lo, nj, ns, 2); }
        else if constexpr (m < NBW) { rd(nxt, lo, nj, ns, 2 * m + 1); rd(nxt, lo, nj, ns, 2 * m + 2); }
        else if constexpr (m == NBW) rd(nxt, lo, nj, ns, 2 * NBW + 1);
      }
      if constexpr (STATS != 0 && m == NBW + 1) {
        if (hf == wn) {
#pragma unroll
          for (int b = 0; b < 2; ++b) {
            s16x4 xl = __builtin_shufflevector(cur.x[b], cur.x[b], 0, 1, 2, 3);
            s16x4 xh = __builtin_shufflevector(cur.x[b], cur.x[b], 4, 5, 6, 7);
            if (STATS == 1) {
              sx[b] = MFMA4(ONES, xl, sx[b]);
              sxx[b] = MFMA4(xl, xl, sxx[b]);
              sx[b] = MFMA4(ONES, xh, sx[b]);
              sxx[b] = MFMA4(xh, xh, sxx[b]);
            } else {
              sx[b] = MFMA4(__builtin_shufflevector(cur.g, cur.g, 0, 1, 2, 3), xl, sx[b]);
              sxx[b] = MFMA4(__builtin_shufflevector(cur.g, cur.g, 4, 5, 6, 7), xh, sxx[b]);
            }
          }
          asm volatile("" : "+v"(sx[0]), "+v"(sxx[0]), "+v"(sx[1]), "+v"(sxx[1]));
        }
      }
      if constexpr (STATS == 1 && m == 2) {
        if (((2 * j + hf) & 3) == wm) {
#pragma unroll
          for (int b = 0; b < NBW; ++b) cs[b] = MFMA4(ONES, __builtin_shufflevector(cur.w[b], cur.w[b], 0, 1, 2, 3), cs[b]);
#pragma unroll
          for (int b = 0; b < NBW; ++b) cs[b] = MFMA4(ONES, __builtin_shufflevector(cur.w[b], cur.w[b], 4, 5, 6, 7), cs[b]);
        }
      }
      if constexpr (m == 1 || m == 3 || m == 5) {
        constexpr int e = hf * 3 + (m >> 1);
        constexpr int s = j + NWS - 1;
        if constexpr (e < 2) { if constexpr (s < 18) issueW(s, e); }
        else if constexpr ((j & 1) == 0 && j / 2 + 2 < 9) issueX(j / 2 + 2, e - 2);
      }
      SB();
    });
  };
  static_for<0, 18>([&](auto Jc) {
    if (decltype(Jc)::v == 4) STAMP(0, 2);
    if (decltype(Jc)::v == 9) STAMP(0, 3);
    if (decltype(Jc)::v == 14) STAMP(0, 4);
    wait_top(Jc);
    BARRIER();
    SB();
    half(Jc, IC<0>{}, f0, f1);
    half(Jc, IC<1>{}, f1, f0);
  });
  STAMP(0, 5);
}

template <int U, int NWS, int STATS>
DI void qkv_role(char* lds, const char* X, const char* Wqkv, const char* gw, char* T, float* G, int rt, int unit0,
                 int wave, int lane, int wm, int wn) {
  f32x16 acc[2][U];
#pragma unroll
  for (int a = 0; a < 2; ++a)
#pragma unroll
    for (int b = 0; b < U; ++b)
#pragma unroll
      for (int i = 0; i < 16; ++i) acc[a][b][i] = 0.f;
  const f32x4 z4 = {0.f, 0.f, 0.f, 0.f};
  f32x4 sx[2] = {z4, z4}, sxx[2] = {z4, z4}, cs[U];
  gemmA<U, NWS, false, STATS>(lds, X, rt * 256, Wqkv + unit0 * 128, NQKV * 2, gw, wave, lane, wm, wn, acc, sx, sxx, cs);
  const int r = lane & 31, h = lane >> 5;
  if (STATS == 2) {
    if (unit0 == 0) {
      float* gp = (float*)(lds + A_WBASE + NWS * U * 4096 + 1152);
#pragma unroll
      for (int b = 0; b < 2; ++b) {
        float v = sx[b][0] + sxx[b][0];
        v += xor32(v);
        if (h == 0) gp[wn * 256 + wm * 64 + b * 32 + r] = v;
      }
      __syncthreads();
      if (wave < 4) {
        float v = gp[wave * 64 + lane] + gp[256 + wave * 64 + lane];
        G[rt * 256 + wave * 64 + lane] = 1.0f / (1.0f + __builtin_amdgcn_exp2f(-1.4426950408889634f * v));
      }
    }
  }
  const float QSCALE = 0.23570226039551584f * 1.4426950408889634f;
#pragma unroll
  for (int nb = 0; nb < U; ++nb) {
    int n = unit0 * 64 + (wn * U + nb) * 32 + r;
    int part = n / 576;
    int rem = n - part * 576;
    int head = rem / 18;
    int d = rem - head * 18;
    float sc = part == 0 ? QSCALE : 1.0f;
    char* base = T + (size_t)part * TPART + head * THEAD + h * (TSTEP / 2) + d * 16;
#pragma unroll
    for (int mb = 0; mb < 2; ++mb)
#pragma unroll
      for (int s = 0; s < 2; ++s) {
        int step = (rt * 256 + wm * 64 + mb * 32) / 16 + s;
        u32x4 v;
#pragma unroll
        for (int i = 0; i < 4; ++i) v[i] = pk2(acc[mb][nb][8 * s + 2 * i] * sc, acc[mb][nb][8 * s + 2 * i + 1] * sc);
        st16(base + step * TSTEP, v);
        if (part == 2 && d == 17) {
          u32x4 o = {0x3F803F80u, 0x3F803F80u, 0x3F803F80u, 0x3F803F80u};
          st16(base + step * TSTEP + 16, o);
          u32x4 z = {0u, 0u, 0u, 0u};
          st16(base + step * TSTEP + 32, z);
        }
      }
  }
}

DI unsigned short f2bf_rn(float f) { return (unsigned short)(pk2(f, 0.f) & 0xffffu); }

DI void mlp1_role(char* lds, const char* X, const char* W1, const unsigned short* b1, char* H, int rt, int ct,
                  int wave, int lane, int wm, int wn) {
  constexpr int NBW = 3;
  f32x16 acc[2][NBW];
#pragma unroll
  for (int a = 0; a < 2; ++a)
#pragma unroll
    for (int b = 0; b < NBW; ++b)
#pragma unroll
      for (int i = 0; i < 16; ++i) acc[a][b][i] = 0.f;
  const f32x4 z4 = {0.f, 0.f, 0.f, 0.f};
  f32x4 sx[2] = {z4, z4}, sxx[2] = {z4, z4}, cs[NBW] = {z4, z4, z4};
  const int r = lane & 31, h = lane >> 5;
  unsigned short bb[NBW];
#pragma unroll
  for (int nb = 0; nb < NBW; ++nb) bb[nb] = b1[ct * 192 + (wn * 3 + nb) * 32 + r];
  gemmA<3, 5, true, 1>(lds, X, rt * 256, W1 + ct * 384, F_ * 2, nullptr, wave, lane, wm, wn, acc, sx, sxx, cs);
  BARRIER();
  float* rowst = (float*)(lds + 4096);
  float* colp = (float*)lds;
#pragma unroll
  for (int b = 0; b < 2; ++b) {
    float a = sx[b][0];
    int i4 = lane & 3;
    float q2 = i4 == 0 ? sxx[b][0] : (i4 == 1 ? sxx[b][1] : (i4 == 2 ? sxx[b][2] : sxx[b][3]));
    a += xor32(a);
    q2 += xor32(q2);
    if (h == 0) {
      f32x2 v = {a, q2};
      *(f32x2*)(rowst + (wn * 256 + wm * 64 + b * 32 + r) * 2) = v;
    }
  }
#pragma unroll
  for (int nb = 0; nb < NBW; ++nb) {
    float c = cs[nb][0] + xor32(cs[nb][0]);
    if (h == 0) colp[wm * 192 + (wn * 3 + nb) * 32 + r] = c;
  }
  __syncthreads();
  bf16x8 wx[NBW], xx[2];
  float ca[2], cb[2], sd[2];
#pragma unroll
  for (int nb = 0; nb < NBW; ++nb) {
    int n = (wn * 3 + nb) * 32 + r;
    float c = -(colp[n] + colp[192 + n] + colp[384 + n] + colp[576 + n]);
    unsigned short chi = f2bf_rn(c);
    unsigned short clo = f2bf_rn(c - bf2f(chi));
    bf16x8 w = {(short)chi, (short)clo, (short)chi, (short)bb[nb], (short)bb[nb], 0, 0, 0};
    bf16x8 z = {0, 0, 0, 0, 0, 0, 0, 0};
    wx[nb] = h ? z : w;
  }
  const float C0 = -2.0f * 1.4426950408889634f * 0.7978845608028654f;
  const float C1 = C0 * 0.044715f;
#pragma unroll
  for (int mb = 0; mb < 2; ++mb) {
    f32x2 st = *(const f32x2*)(rowst + (wm * 64 + mb * 32 + r) * 2) +
               *(const f32x2*)(rowst + (256 + wm * 64 + mb * 32 + r) * 2);
    st[0] *= (1.0f / 576.0f);
    st[1] = __builtin_fmaxf(st[1] * (1.0f / 576.0f) - st[0] * st[0], 0.f);
    float rstd = __builtin_amdgcn_rsqf(st[1] + 1e-5f);
    float sdev = (st[1] + 1e-5f) * rstd;
    unsigned short mhi = f2bf_rn(st[0]);
    unsigned short mlo = f2bf_rn(st[0] - bf2f(mhi));
    unsigned short shi = f2bf_rn(sdev);
    unsigned short slo = f2bf_rn(sdev - bf2f(shi));
    bf16x8 x = {(short)mhi, (short)mhi, (short)mlo, (short)shi, (short)slo, 0, 0, 0};
    bf16x8 z = {0, 0, 0, 0, 0, 0, 0, 0};
    xx[mb] = h ? z : x;
    ca[mb] = C0 * rstd;
    cb[mb] = C1 * rstd * rstd * rstd;
    sd[mb] = sdev;
  }
#pragma unroll
  for (int mb = 0; mb < 2; ++mb)
#pragma unroll
    for (int nb = 0; nb < NBW; ++nb) acc[mb][nb] = MFMA32(wx[nb], xx[mb], acc[mb][nb]);
#pragma unroll
  for (int mb = 0; mb < 2; ++mb) {
    char* hb = H + ((size_t)(rt * 8 + wm * 2 + mb) * 288 + ct * 12 + wn * 6) * 1024 + lane * 16;
    const f32x2 a2 = {ca[mb], ca[mb]}, b2 = {cb[mb], cb[mb]}, s2 = {sd[mb], sd[mb]};
#pragma unroll
    for (int nb = 0; nb < NBW; ++nb)
#pragma unroll
      for (int gp = 0; gp < 2; ++gp) {
        u32x4 w;
#pragma unroll
        for (int i = 0; i < 4; ++i) {
          f32x2 v = {acc[mb][nb][8 * gp + 2 * i], acc[mb][nb][8 * gp + 2 * i + 1]};
          f32x2 t = v * __builtin_elementwise_fma(v * v, b2, a2);
          f32x2 e = {__builtin_amdgcn_exp2f(t[0]), __builtin_amdgcn_exp2f(t[1])};
          f32x2 d = __builtin_elementwise_fma(e, s2, s2);
          f32x2 rr = {__builtin_amdgcn_rcpf(d[0]), __builtin_amdgcn_rcpf(d[1])};
          f32x2 o = v * rr;
          w[i] = pk2(o[0], o[1]);
        }
        st16(hb + (nb * 2 + gp) * 1024, w);
      }
  }
}

__global__ __launch_bounds__(512, 2) void kernelA(const char* __restrict__ X, const char* __restrict__ Wqkv,
                                                   const char* __restrict__ W1, const unsigned short* __restrict__ b1,
                                                   const char* __restrict__ gw, char* __restrict__ T,
                                                   char* __restrict__ H, float* __restrict__ G) {
  __shared__ __attribute__((aligned(16))) char lds[163840];
  const int wg = blockIdx.x;
  const int xcd = wg & 7, idx = wg >> 3;
  const int rt = (xcd & 1) * 4 + (idx & 3);
  const int ct = (xcd >> 1) + 4 * (idx >> 2);
  const int wave = __builtin_amdgcn_readfirstlane(threadIdx.x >> 6);
  const int lane = threadIdx.x & 63;
  const int wm = wave & 3, wn = wave >> 2;
  if (ct < 5) qkv_role<3, 5, 2>(lds, X, Wqkv, gw, T, G, rt, ct * 3, wave, lane, wm, wn);
  else if (ct < 8) qkv_role<4, 4, 0>(lds, X, Wqkv, gw, T, G, rt, 15 + (ct - 5) * 4, wave, lane, wm, wn);
  else mlp1_role(lds, X, W1, b1, H, rt, ct - 8, wave, lane, wm, wn);
  STAMP(0, 6);
  STAMP(0, 7);
}

typedef __attribute__((address_space(3))) const bf16x8* lds_bf16x8_p;
DI s16x4 trl(lds_p p) { return __builtin_amdgcn_ds_read_tr16_b64_v4i16((lds_s16x4_p)p); }
DI bf16x8 ld8(lds_p p) { return *(lds_bf16x8_p)p; }
DI f32x16 qk_tile(lds_p kb, lds_p ke, bf16x8 qf, s16x4 qe, f32x16 c) {
  s16x4 lo = trl(kb);
  s16x4 hi = trl(kb + 64);
  s16x4 ex = trl(ke);
  bf16x8 kf = __builtin_shufflevector(lo, hi, 0, 1, 2, 3, 4, 5, 6, 7);
  f32x16 s = MFMA32(kf, qf, c);
  s = MFMA32K8(ex, qe, s);
  return s;
}
DI f32x16 pv_tile(lds_p vb, const f32x16& pe, f32x16 o) {
  bf16x8 v0 = ld8(vb);
  bf16x8 v1 = ld8(vb + TSTEP);
  u32x4 p0, p1;
#pragma unroll
  for (int i = 0; i < 4; ++i) {
    p0[i] = pk2(pe[2 * i], pe[2 * i + 1]);
    p1[i] = pk2(pe[8 + 2 * i], pe[8 + 2 * i + 1]);
  }
  o = MFMA32(v0, __builtin_bit_cast(bf16x8, p0), o);
  o = MFMA32(v1, __builtin_bit_cast(bf16x8, p1), o);
  return o;
}
struct KFrag { bf16x8 f; s16x4 e; };
struct VFrag { bf16x8 v0, v1; };
DI KFrag load_k(lds_p kb, lds_p ke) {
  KFrag k;
  s16x4 lo = trl(kb);
  s16x4 hi = trl(kb + 64);
  k.e = trl(ke);
  k.f = __builtin_shufflevector(lo, hi, 0, 1, 2, 3, 4, 5, 6, 7);
  return k;
}
DI VFrag load_v(lds_p vb) {
  VFrag v;
  v.v0 = ld8(vb);
  v.v1 = ld8(vb + TSTEP);
  return v;
}
#define EXP2(x) __builtin_amdgcn_exp2f(x)
DI void tile_step(const f32x16& s, f32x16& sn, const KFrag& k, const VFrag& v, bf16x8 qf, s16x4 qe,
                  const f32x16& negm, f32x16& o) {
  u32x4 p0, p1;
  p0[0] = pk2(EXP2(s[0]), EXP2(s[1]));
  p0[1] = pk2(EXP2(s[2]), EXP2(s[3]));
  SB();
  f32x16 t = MFMA32(k.f, qf, negm);
  SB();
  p0[2] = pk2(EXP2(s[4]), EXP2(s[5]));
  p0[3] = pk2(EXP2(s[6]), EXP2(s[7]));
  SB();
  o = MFMA32(v.v0, __builtin_bit_cast(bf16x8, p0), o);
  SB();
  p1[0] = pk2(EXP2(s[8]), EXP2(s[9]));
  p1[1] = pk2(EXP2(s[10]), EXP2(s[11]));
  SB();
  t = MFMA32K8(k.e, qe, t);
  SB();
  p1[2] = pk2(EXP2(s[12]), EXP2(s[13]));
  p1[3] = pk2(EXP2(s[14]), EXP2(s[15]));
  SB();
  o = MFMA32(v.v1, __builtin_bit_cast(bf16x8, p1), o);
  SB();
  sn = t;
}
DI float rowmax16(const f32x16& s) {
  float m = __builtin_fmaxf(s[0], s[1]);
#pragma unroll
  for (int i = 2; i < 16; ++i) m = __builtin_fmaxf(m, s[i]);
  return __builtin_fmaxf(m, xor32(m));
}

constexpr int AT_SLOT = 20480;
constexpr int at_cnt(int c, int grp) { return 2 + (((c & 1) == grp) ? 1 : 0); }
constexpr int at_off(int tt) { return ((tt >> 3) & 1) * AT_SLOT + (tt & 7) * 1280; }

DI void attn_role(char* lds, const char* __restrict__ T, char* __restrict__ O, int wg, int wave, int lane) {
  STAMP(1, 0);
  const int head = (wg & 7) + 8 * ((wg >> 3) & 3);
  const int qt = wg >> 5;
  const int r = lane & 31, h = lane >> 5;
  const int grp = wave >> 2;
  const unsigned lbase = lds_addr(lds);
  const char* TK = T + (size_t)TPART + head * THEAD;
  const char* TV = T + (size_t)2 * TPART + head * THEAD;
  const int qi = qt * 256 + wave * 32 + r;
  const unsigned short* qp =
      (const unsigned short*)(T + head * THEAD + (qi >> 4) * TSTEP + ((qi >> 2) & 1) * (TSTEP / 2)) + (qi & 3) +
      4 * ((qi >> 3) & 1);
  unsigned short qv[8], qx[2];
#pragma unroll
  for (int j = 0; j < 8; ++j) qv[j] = qp[(8 * h + j) * 8];
  qx[0] = qp[16 * 8];
  qx[1] = qp[17 * 8];
  const int p1 = wave + 8, p2 = 16 + (wave & 3);
  const char* s0 = TK + wave * 1024;
  const char* s1 = p1 < 10 ? TK + p1 * 1024 : TV + (p1 - 10) * 1024;
  const char* s2 = TV + (p2 - 10) * 1024;
  const unsigned voff = lane * 16;
  auto issue = [&](int c) {
    unsigned dst = lbase + (c & 3) * AT_SLOT;
    glds16s(s0 + c * 10240, voff, dst + wave * 1024);
    glds16s(s1 + c * 10240, voff, dst + p1 * 1024);
    if ((c & 1) == grp) glds16s(s2 + c * 10240, voff, dst + p2 * 1024);
  };
  issue(0);
  bf16x8 qf;
#pragma unroll
  for (int j = 0; j < 8; ++j) qf[j] = (short)qv[j];
  s16x4 qe;
  qe[0] = h ? (short)0 : (short)qx[0];
  qe[1] = h ? (short)0 : (short)qx[1];
  qe[2] = 0;
  qe[3] = 0;
  asm volatile("" : "+v"(qf), "+v"(qe));
  wait_vm<0>();
  issue(1);
  issue(2);
  BARRIER();
  STAMP(1, 1);

  const int q = (lane >> 2) & 3, p = lane & 3, g = (lane >> 4) & 1;
  const lds_p L = (lds_p)lds;
  const lds_p kb = L + g * TSTEP + (p & 1) * (TSTEP / 2) + (8 * h + q) * 16 + (p >> 1) * 8;
  const lds_p ke = L + g * TSTEP + (p & 1) * (TSTEP / 2) + (16 + (q & 1)) * 16 + (p >> 1) * 8;
  const lds_p vb = L + 10240 + h * (TSTEP / 2) + (r < 19 ? r : 19) * 16;
  lds_p kb2 = kb + 2 * AT_SLOT, ke2 = ke + 2 * AT_SLOT, vb2 = vb + 2 * AT_SLOT;
  asm volatile("" : "+v"(kb2), "+v"(ke2), "+v"(vb2));

  f32x16 zero;
#pragma unroll
  for (int i = 0; i < 16; ++i) zero[i] = 0.f;
  f32x16 o = zero;
  f32x16 sA = qk_tile(kb, ke, qf, qe, zero);
  float m0 = rowmax16(sA);
  f32x16 negm;
#pragma unroll
  for (int i = 0; i < 16; ++i) { negm[i] = -m0; sA[i] -= m0; }
  KFrag kA, kB;
  VFrag vA, vB;
  kB = load_k(kb + 1280, ke + 1280);
  vA = load_v(vb);
  f32x16 sB;
#pragma unroll 1
  for (int cc = 0; cc < 8; cc += 4) {
    static_for<0, 32>([&](auto Tc) {
      constexpr int tt = decltype(Tc)::v, ci = tt >> 3;
      if constexpr ((tt & 7) == 6) {
        if (cc == 0 || ci < 2) {
          if (grp == 0) wait_vm<at_cnt(ci + 2, 0)>(); else wait_vm<at_cnt(ci + 2, 1)>();
        } else if (ci == 2) wait_vm<0>();
        if (cc == 0 || ci < 3) BARRIER();
        if (cc == 0 || ci == 0) issue(cc + ci + 3);
      }
      constexpr int t1 = (tt + 1) & 31, t2 = (tt + 2) & 31;
      const lds_p kp = (t2 < 16 ? kb : kb2) + at_off(t2);
      const lds_p ep = (t2 < 16 ? ke : ke2) + at_off(t2);
      const lds_p vp = (t1 < 16 ? vb : vb2) + at_off(t1);
      if constexpr ((tt & 1) == 0) {
        kA = load_k(kp, ep);
        vB = load_v(vp);
        SB();
        tile_step(sA, sB, kB, vA, qf, qe, negm, o);
      } else {
        kB = load_k(kp, ep);
        vA = load_v(vp);
        SB();
        tile_step(sB, sA, kA, vB, qf, qe, negm, o);
      }
    });
    if (cc == 0) STAMP(1, 2);
  }
  STAMP(1, 5);
  float l = xor32(o[10]);
  l = h ? l : o[10];
  bool bad = !(l < 1.0e30f);
#pragma unroll
  for (int i = 0; i < 10; ++i) bad = bad || !(__builtin_fabsf(o[i]) < 1.0e37f);
  {
    int* fl = (int*)(lds + AT_SLOT);
    int anyb = __any(bad) ? 1 : 0;
    if (lane == 0) fl[wave] = anyb;
    __syncthreads();
    int v = fl[lane & 7];
    bad = __any(v != 0);
  }
  if (bad) {
    float m = -3.0e38f;
    o = zero;
#pragma unroll 1
    for (int c = 0; c < 8; ++c) {
      __syncthreads();
      {
        unsigned dst = lbase;
        glds16s(s0 + c * 10240, voff, dst + wave * 1024);
        glds16s(s1 + c * 10240, voff, dst + p1 * 1024);
        if (grp == 0) glds16s(s2 + c * 10240, voff, dst + p2 * 1024);
      }
      wait_vm<0>();
      __syncthreads();
#pragma unroll 1
      for (int t = 0; t < 8; ++t) {
        f32x16 s = qk_tile(kb + t * 1280, ke + t * 1280, qf, qe, zero);
        float mn = __builtin_fmaxf(m, rowmax16(s));
        float alpha = __builtin_amdgcn_exp2f(m - mn);
        m = mn;
        f32x16 pe;
#pragma unroll
        for (int i = 0; i < 16; ++i) { pe[i] = __builtin_amdgcn_exp2f(s[i] - mn); o[i] *= alpha; }
        o = pv_tile(vb + t * 1280, pe, o);
      }
    }
    l = xor32(o[10]);
    l = h ? l : o[10];
  }
  float inv = 1.0f / l;
  char* ob = O + (size_t)(qt * 8 + wave) * 36 * 1024;
  u32x4 ov;
#pragma unroll
  for (int i = 0; i < 4; ++i) ov[i] = pk2(o[2 * i] * inv, o[2 * i + 1] * inv);
  st16(ob + head * 1024 + lane * 16, ov);
  if (h == 0)
    st4(ob + (32 + (head >> 3)) * 1024 + (((head >> 2) & 1) * 32 + r) * 16 + (head & 3) * 4,
        pk2(o[8] * inv, o[9] * inv));
  STAMP(1, 6);
  STAMP(1, 7);
}

constexpr int M_SLOT = 20480, M_IT = 36;
template <int U>
constexpr int m_cnt(int s, int grp) { return s >= M_IT ? 0 : (U == 2 ? 2 : 2 + (((s & 1) == grp) ? 1 : 0)); }

template <int NBW, int U>
DI void mlp2_role(char* lds, const char* __restrict__ H, const char* __restrict__ W2, char* __restrict__ P, int rt,
                  int sp, int unit0, int boff, int wave, int lane) {
  f32x16 acc[NBW];
#pragma unroll
  for (int b = 0; b < NBW; ++b)
#pragma unroll
    for (int i = 0; i < 16; ++i) acc[b][i] = 0.f;
  const unsigned lbase = lds_addr(lds);
  const int wm = wave & 3, kh = wave >> 2;
  const unsigned voffX = lane * 16;
  const unsigned voffW = (unsigned)((lane >> 3) * 1152 + (((lane & 7) ^ (((lane >> 4) & 1) << 2)) << 4));
  const char* xsrc = H + ((size_t)(rt * 4 + wm) * 288 + sp * 72 + kh) * 1024;
  const char* wbase = W2 + (size_t)sp * 1152 * 1152 + unit0 * 128;
  const int p0 = wave, p2 = 8 + (wave & 3);
  const char* ws0 = wbase + (p0 / U) * (8 * 1152) + (p0 % U) * 128;
  const char* ws2 = wbase + (p2 / U) * (8 * 1152) + (p2 % U) * 128;
  auto issue = [&](int s, int sl, int e) {
    unsigned dst = lbase + sl * M_SLOT;
    if (e == 0) glds16s(xsrc + s * 2048, voffX, dst + wave * 1024);
    else if (e == 1) glds16s(ws0 + s * (32 * 1152), voffW, dst + 8192 + p0 * 1024);
    else if (U == 3 && (sl & 1) == kh) glds16s(ws2 + s * (32 * 1152), voffW, dst + 8192 + p2 * 1024);
  };
  const int h = lane >> 5;
  const int q = (lane >> 2) & 3, p = lane & 3, g = (lane >> 4) & 1;
  const lds_p L = (lds_p)lds;
  const lds_p xa = L + wave * 1024 + lane * 16;
  lds_p wa[2];
#pragma unroll
  for (int e = 0; e < 2; ++e) {
    int b = boff + e;
    wa[e] = L + 8192 + (2 * kh * U + (b >> 1)) * 1024 + (4 * h + q) * 128 + (((b & 1) ^ (q >> 1)) << 6) + g * 32 + p * 8;
  }
  lds_p xa2 = xa + 2 * M_SLOT, wa2[2] = {wa[0] + 2 * M_SLOT, wa[1] + 2 * M_SLOT};
  asm volatile("" : "+v"(xa2), "+v"(wa2[0]), "+v"(wa2[1]));
  STAMP(2, 0);
#pragma unroll
  for (int s = 0; s < 4; ++s)
#pragma unroll
    for (int e = 0; e < 3; ++e) issue(s, s, e);
  {
    constexpr int n0 = m_cnt<U>(1, 0) + m_cnt<U>(2, 0) + m_cnt<U>(3, 0), n1 = m_cnt<U>(1, 1) + m_cnt<U>(2, 1) + m_cnt<U>(3, 1);
    if (kh == 0) wait_vm<n0>(); else wait_vm<n1>();
  }
  BARRIER();
  STAMP(2, 1);
  bf16x8 w[NBW], x0, x1;
  auto rdx = [&](int sl) { return ld8((sl < 2 ? xa : xa2) + (sl & 1) * M_SLOT); };
  auto rdw = [&](int sl, int m) {
    const lds_p a = (sl < 2 ? wa[m & 1] : wa2[m & 1]) + (sl & 1) * M_SLOT + (m >> 1) * 1024;
    return cat8(trl(a), trl(a + U * 1024));
  };
  x0 = rdx(0);
#pragma unroll
  for (int m = 0; m < NBW; ++m) w[m] = rdw(0, m);
  auto iter = [&](int jb, auto Lc, auto Ic) {
    constexpr int last = decltype(Lc)::v, i = decltype(Ic)::v;
    constexpr int js = last ? 32 + i : i;
    constexpr int n0 = m_cnt<U>(js + 2, 0) + m_cnt<U>(js + 3, 0), n1 = m_cnt<U>(js + 2, 1) + m_cnt<U>(js + 3, 1);
    if constexpr (n0 == n1) wait_vm<n0>();
    else { if (kh == 0) wait_vm<n0>(); else wait_vm<n1>(); }
    asm volatile("s_waitcnt lgkmcnt(0)" ::: "memory");
    BARRIER();
    SB();
    bf16x8& xc = (i & 1) ? x1 : x0;
    bf16x8& xn = (i & 1) ? x0 : x1;
    static_for<0, NBW>([&](auto Mc) {
      constexpr int m = decltype(Mc)::v;
      acc[m] = MFMA32(w[m], xc, acc[m]);
      SB();
      if constexpr (js + 1 < M_IT) {
        if constexpr (m == 0) xn = rdx((i + 1) & 3);
        w[m] = rdw((i + 1) & 3, m);
      }
      if constexpr (!last && m >= 1 && m <= 3) issue(jb + i + 4, i, m - 1);
      SB();
    });
  };
#pragma unroll 1
  for (int jb = 0; jb < 32; jb += 4) {
    static_for<0, 4>([&](auto Ic) { iter(jb, IC<0>{}, Ic); });
    if (jb == 8) STAMP(2, 2);
    if (jb == 16) STAMP(2, 3);
    if (jb == 24) STAMP(2, 4);
  }
  static_for<0, 4>([&](auto Ic) { iter(32, IC<1>{}, Ic); });
  STAMP(2, 5);
  constexpr int NA = (NBW + 1) / 2;
  char* ex = lds + wm * (NBW * 4096) + lane * 16;
  if (kh == 0) {
#pragma unroll
    for (int b = NA; b < NBW; ++b)
#pragma unroll
      for (int c = 0; c < 4; ++c) {
        f32x4 v = {acc[b][4 * c], acc[b][4 * c + 1], acc[b][4 * c + 2], acc[b][4 * c + 3]};
        *(f32x4*)(ex + b * 4096 + c * 1024) = v;
      }
  } else {
#pragma unroll
    for (int b = 0; b < NA; ++b)
#pragma unroll
      for (int c = 0; c < 4; ++c) {
        f32x4 v = {acc[b][4 * c], acc[b][4 * c + 1], acc[b][4 * c + 2], acc[b][4 * c + 3]};
        *(f32x4*)(ex + b * 4096 + c * 1024) = v;
      }
  }
  __syncthreads();
  char* pb = P + ((size_t)(sp * 64 + rt * 4 + wm) * 18 + unit0 * 2 + boff) * 2048 + lane * 16;
  auto fin = [&](int b) {
#pragma unroll
    for (int gp = 0; gp < 2; ++gp) {
      f32x4 a = *(const f32x4*)(ex + b * 4096 + (2 * gp) * 1024);
      f32x4 c = *(const f32x4*)(ex + b * 4096 + (2 * gp + 1) * 1024);
      u32x4 v;
      v[0] = pk2(acc[b][8 * gp] + a[0], acc[b][8 * gp + 1] + a[1]);
      v[1] = pk2(acc[b][8 * gp + 2] + a[2], acc[b][8 * gp + 3] + a[3]);
      v[2] = pk2(acc[b][8 * gp + 4] + c[0], acc[b][8 * gp + 5] + c[1]);
      v[3] = pk2(acc[b][8 * gp + 6] + c[2], acc[b][8 * gp + 7] + c[3]);
      st16(pb + b * 2048 + gp * 1024, v);
    }
  };
  if (kh == 0) {
#pragma unroll
    for (int b = 0; b < NA; ++b) fin(b);
  } else {
#pragma unroll
    for (int b = NA; b < NBW; ++b) fin(b);
  }
  STAMP(2, 6);
  STAMP(2, 7);
}

__global__ __launch_bounds__(512, 4) void kernelB(const char* __restrict__ T, char* __restrict__ O,
                                                   const char* __restrict__ H, const char* __restrict__ W2,
                                                   char* __restrict__ P) {
  __shared__ __attribute__((aligned(16))) char lds[81920];
  const int wave = __builtin_amdgcn_readfirstlane(threadIdx.x >> 6);
  const int lane = threadIdx.x & 63;
  const int bid = blockIdx.x;
  if (bid < 256) {
    attn_role(lds, T, O, bid, wave, lane);
  } else {
    const int wg = bid - 256;
    const int xcd = wg & 7, idx = wg >> 3;
    const int sp = xcd >> 1, ct = idx & 3, rt = (xcd & 1) * 8 + (idx >> 2);
    if (ct == 0 || ct == 3) mlp2_role<4, 2>(lds, H, W2, P, rt, sp, ct == 0 ? 0 : 7, 0, wave, lane);
    else mlp2_role<5, 3>(lds, H, W2, P, rt, sp, 2 * ct, ct - 1, wave, lane);
  }
}

__global__ __launch_bounds__(256, 2) void kernelC(const char* __restrict__ O, const char* __restrict__ Wo,
                                                   const char* __restrict__ P, const float* __restrict__ G,
                                                   const char* __restrict__ b2, char* __restrict__ out) {
  __shared__ __attribute__((aligned(16))) char lds[73728];
  const int wg = blockIdx.x;
  const int idx = wg >> 3;
  const int ct = idx % 9, rt = (wg & 7) * 4 + idx / 9;
  const int wave = __builtin_amdgcn_readfirstlane(threadIdx.x >> 6);
  const int lane = threadIdx.x & 63;
  const int wm = wave & 1, kh = wave >> 1;
  const int r = lane & 31, h = lane >> 5;
  const unsigned lbase = lds_addr(lds);
  STAMP(3, 0);
  {
    const unsigned voffW = (unsigned)((lane >> 3) * 1152 + (((lane & 7) ^ (((lane >> 4) & 1) << 2)) << 4));
#pragma unroll
    for (int i = 0; i < 18; ++i) {
      int pi = kh * 36 + i * 2 + wm;
      glds16s(Wo + (size_t)pi * (8 * 1152) + ct * 128, voffW, lbase + pi * 1024);
    }
  }
  const char* ob = O + (size_t)(rt * 2 + wm) * 36 * 1024 + lane * 16;
  bf16x8 of[18];
#pragma unroll
  for (int t = 0; t < 16; ++t) of[t] = *(const bf16x8*)(ob + (kh * 16 + t) * 1024);
#pragma unroll
  for (int u = 0; u < 2; ++u) of[16 + u] = *(const bf16x8*)(ob + (32 + kh * 2 + u) * 1024);
  u32x4 pv[8];
  {
    const char* pb = P + ((size_t)(rt * 2 + wm) * 18 + ct * 2 + kh) * 2048 + lane * 16;
#pragma unroll
    for (int sp = 0; sp < 4; ++sp)
#pragma unroll
      for (int gp = 0; gp < 2; ++gp) pv[sp * 2 + gp] = *(const u32x4*)(pb + (size_t)sp * 64 * 18 * 2048 + gp * 1024);
  }
  const float gg = G[rt * 64 + wm * 32 + r];
  u32x2 bb[4];
#pragma unroll
  for (int i = 0; i < 4; ++i) bb[i] = *(const u32x2*)(b2 + (ct * 64 + kh * 32 + 8 * i + 4 * h) * 2);
  SB();
  STAMP(3, 1);
  WAIT_VM(0);
  STAMP(3, 2);
  BARRIER();
  STAMP(3, 3);
  const int q = (lane >> 2) & 3, p = lane & 3, g = (lane >> 4) & 1;
  f32x16 acc[2];
#pragma unroll
  for (int i = 0; i < 16; ++i) { acc[0][i] = 0.f; acc[1][i] = 0.f; }
  const char* lb = lds + kh * (288 * 128) + g * 32 + p * 8;
#pragma unroll
  for (int t = 0; t < 16; ++t) {
    int r0 = 18 * t + 4 * h + q, r1 = r0 + 8;
#pragma unroll
    for (int n = 0; n < 2; ++n) {
      s16x4 lo = tr_read(lb + r0 * 128 + ((n ^ ((r0 >> 1) & 1)) << 6));
      s16x4 hi = tr_read(lb + r1 * 128 + ((n ^ ((r1 >> 1) & 1)) << 6));
      acc[n] = MFMA32(cat8(lo, hi), of[t], acc[n]);
    }
  }
#pragma unroll
  for (int u = 0; u < 2; ++u) {
    int r0 = (8 * u + 4 * h + (q >> 1)) * 18 + 16 + (q & 1), r1 = r0 + 36;
#pragma unroll
    for (int n = 0; n < 2; ++n) {
      s16x4 lo = tr_read(lb + r0 * 128 + ((n ^ ((r0 >> 1) & 1)) << 6));
      s16x4 hi = tr_read(lb + r1 * 128 + ((n ^ ((r1 >> 1) & 1)) << 6));
      acc[n] = MFMA32(cat8(lo, hi), of[16 + u], acc[n]);
    }
  }
  STAMP(3, 4);
  f32x16 ps;
#pragma unroll
  for (int i = 0; i < 4; ++i) {
    ps[4 * i] = __uint_as_float(bb[i][0] << 16);
    ps[4 * i + 1] = __uint_as_float(bb[i][0] & 0xffff0000u);
    ps[4 * i + 2] = __uint_as_float(bb[i][1] << 16);
    ps[4 * i + 3] = __uint_as_float(bb[i][1] & 0xffff0000u);
  }
#pragma unroll
  for (int sp = 0; sp < 4; ++sp)
#pragma unroll
    for (int gp = 0; gp < 2; ++gp)
#pragma unroll
      for (int i = 0; i < 4; ++i) {
        unsigned w = pv[sp * 2 + gp][i];
        ps[8 * gp + 2 * i] += __uint_as_float(w << 16);
        ps[8 * gp + 2 * i + 1] += __uint_as_float(w & 0xffff0000u);
      }
  __syncthreads();
  {
    char* ex = lds + wave * 4096 + lane * 16;
#pragma unroll
    for (int c = 0; c < 4; ++c) {
      f32x4 v;
#pragma unroll
      for (int j = 0; j < 4; ++j) v[j] = kh ? acc[0][4 * c + j] : acc[1][4 * c + j];
      *(f32x4*)(ex + c * 1024) = v;
    }
  }
  __syncthreads();
  f32x16 a;
  {
    const char* ex = lds + (wave ^ 2) * 4096 + lane * 16;
#pragma unroll
    for (int c = 0; c < 4; ++c) {
      f32x4 v = *(const f32x4*)(ex + c * 1024);
#pragma unroll
      for (int j = 0; j < 4; ++j) a[4 * c + j] = v[j] + (kh ? acc[1][4 * c + j] : acc[0][4 * c + j]);
    }
  }
  const float og = 1.0f - gg;
  int row = rt * 64 + wm * 32 + r;
  char* orow = out + (size_t)row * 1152 + (ct * 64 + kh * 32) * 2 + h * 16;
  u32x2 o2[4];
#pragma unroll
  for (int gq = 0; gq < 4; ++gq) {
    float v[4];
#pragma unroll
    for (int j = 0; j < 4; ++j) v[j] = gg * a[4 * gq + j] + og * ps[4 * gq + j];
    o2[gq][0] = pk2(v[0], v[1]);
    o2[gq][1] = pk2(v[2], v[3]);
  }
#pragma unroll
  for (int gq = 0; gq < 4; gq += 2) {
    auto r0 = __builtin_amdgcn_permlane32_swap(o2[gq][0], o2[gq + 1][0], false, false);
    auto r1 = __builtin_amdgcn_permlane32_swap(o2[gq][1], o2[gq + 1][1], false, false);
    u32x4 w = {r0[0], r1[0], r0[1], r1[1]};
    *(u32x4*)(orow + gq * 16) = w;
  }
  STAMP(3, 5);
  STAMP(3, 6);
  STAMP(3, 7);
}

extern "C" void kernel_launch(void* const* d_in, const int* in_sizes, int n_in, void* d_out, int out_size, void* d_ws,
                              size_t ws_size, hipStream_t stream) {
  const char* X = (const char*)d_in[0];
  const char* Wqkv = (const char*)d_in[1];
  const char* Wo = (const char*)d_in[2];
  const char* W1 = (const char*)d_in[4];
  const unsigned short* b1 = (const unsigned short*)d_in[5];
  const char* W2 = (const char*)d_in[6];
  const char* b2 = (const char*)d_in[7];
  const char* gw = (const char*)d_in[8];
  char* ws = (char*)d_ws;
  char* T = ws + WS_T;
  char* H = ws + WS_H;
  char* O = ws + WS_O;
  char* P = ws + WS_P;
  float* G = (float*)(ws + WS_G);
  kernelA<<<256, 512, 0, stream>>>(X, Wqkv, W1, b1, gw, T, H, G);
  kernelB<<<512, 512, 0, stream>>>(T, O, H, W2, P);
  kernelC<<<288, 256, 0, stream>>>(O, Wo, P, G, b2, (char*)d_out);
}
```

```cpp
#include <hip/hip_runtime.h>
#include <stdint.h>

typedef __attribute__((ext_vector_type(8))) short bf16x8;
typedef __attribute__((ext_vector_type(4))) short s16x4;
typedef __attribute__((ext_vector_type(2))) __bf16 bf16x2_t;
typedef __attribute__((ext_vector_type(2))) float f32x2;
typedef __attribute__((ext_vector_type(4))) float f32x4;
typedef __attribute__((ext_vector_type(16))) float f32x16;
typedef __attribute__((ext_vector_type(4))) unsigned u32x4;
typedef __attribute__((ext_vector_type(2))) unsigned u32x2;
typedef __attribute__((address_space(3))) s16x4* lds_s16x4_p;
typedef __attribute__((address_space(3))) char* lds_p;

#define STAMP(k, i)
#define DI __device__ __forceinline__
#define MFMA32(a, b, c) __builtin_amdgcn_mfma_f32_32x32x16_bf16((a), (b), (c), 0, 0, 0)
#define MFMA32K8(a, b, c) __builtin_amdgcn_mfma_f32_32x32x8bf16_1k((a), (b), (c), 0, 0, 0)
#define WAIT_VM(n) asm volatile("s_waitcnt vmcnt(" #n ")" ::: "memory")
#define BARRIER() __builtin_amdgcn_s_barrier()

constexpr int S_ = 2048, D_ = 576, F_ = 4608, NQKV = 1728;
constexpr int TSTEP = 640;
constexpr int THEAD = 128 * TSTEP;
constexpr int TPART = 32 * THEAD;
constexpr size_t WS_T = 0;
constexpr size_t WS_H = 8u << 20;
constexpr size_t WS_O = 28u << 20;
constexpr size_t WS_P = 32u << 20;
constexpr size_t WS_G = 31u << 20;

DI unsigned pk2(float a, float b) {
  f32x2 v = {a, b};
  bf16x2_t r = __builtin_convertvector(v, bf16x2_t);
  return __builtin_bit_cast(unsigned, r);
}
DI float dot2bf(unsigned a, unsigned b, float acc) {
  return __builtin_amdgcn_fdot2_f32_bf16(__builtin_bit_cast(bf16x2_t, a), __builtin_bit_cast(bf16x2_t, b), acc, false);
}
DI float bf2f(unsigned short u) { return __uint_as_float(((unsigned)u) << 16); }
DI s16x4 tr_read(const char* p) {
  return __builtin_amdgcn_ds_read_tr16_b64_v4i16((lds_s16x4_p)(p));
}
DI void glds16(const char* g, char* l) {
  __builtin_amdgcn_global_load_lds((const unsigned*)g, (unsigned*)l, 16, 0, 0);
}
DI float xor32(float v) { return __shfl_xor(v, 32, 64); }

DI void glds16s(const char* sbase, unsigned voff, unsigned lds_dst) {
  unsigned keep;
  asm volatile("s_mov_b32 %0, m0\n\ts_mov_b32 m0, %3\n\ts_nop 0\n\tglobal_load_lds_dwordx4 %1, %2\n\ts_mov_b32 m0, %0"
               : "=&s"(keep)
               : "v"(voff), "s"(sbase), "s"(lds_dst)
               : "memory");
}
DI unsigned lds_addr(const void* p) { return (unsigned)(uintptr_t)(lds_p)p; }

DI void st16(void* p, u32x4 v) { asm volatile("global_store_dwordx4 %0, %1, off sc1\n\ts_nop 1" ::"v"(p), "v"(v) : "memory"); }
DI void st16p(void* p, u32x4 v) { *(u32x4*)p = v; }
DI void st4(void* p, unsigned v) { asm volatile("global_store_dword %0, %1, off" ::"v"(p), "v"(v) : "memory"); }

template <int I>
struct IC { static constexpr int v = I; };
template <int I, int N, class F>
DI void static_for(F&& f) {
  if constexpr (I < N) {
    f(IC<I>{});
    static_for<I + 1, N>(f);
  }
}
template <int N>
DI void wait_vm() { asm volatile("s_waitcnt vmcnt(%0)" ::"i"(N) : "memory"); }
#define SB() __builtin_amdgcn_sched_barrier(0)

template <int MB, int NBW>
struct Frags {
  bf16x8 x[MB];
  bf16x8 w[NBW];
  bf16x8 g;
};
#define MFMA4(a, b, c) __builtin_amdgcn_mfma_f32_4x4x4bf16_1k((a), (b), (c), 0, 0, 0)
DI bf16x8 cat8(s16x4 lo, s16x4 hi) { return __builtin_shufflevector(lo, hi, 0, 1, 2, 3, 4, 5, 6, 7); }

constexpr int A_XSLOT = 32768;
constexpr int A_WBASE = 3 * A_XSLOT;
template <int U>
constexpr int a_cw(int s, int grp) { return U == 4 ? 2 : (1 + (((s & 1) == grp) ? 1 : 0)); }
template <int U, int NWS>
constexpr int a_inflight(int j, int grp) {
  int cnt = 0;
  for (int t = j - 1; t >= -4; --t) {
    if ((t & 1) == 0) {
      int J = t / 2 + 2;
      if (t < 0) J = (t + 4) / 2;
      if (J >= 0 && J < 9) {
        if (J <= ((j + 1) >> 1)) return cnt;
        cnt += 4;
      }
    }
    int s = t + NWS - 1;
    if (s >= 0 && s < 18) {
      if (s <= j + 1) return cnt;
      cnt += a_cw<U>(s, grp);
    }
  }
  return cnt;
}

template <int U, int NWS, bool TRANS, int STATS>
DI void gemmA(char* lds, const char* __restrict__ Xb, int row0, const char* __restrict__ Wb, int ldw,
              const char* __restrict__ gw, int wave, int lane, int wm, int wn, f32x16 (&acc)[2][U],
              f32x4 (&sx)[2], f32x4 (&sxx)[2], f32x4 (&cs)[U]) {
  constexpr int NBW = U, NM = 2 * U, WSLOT = U * 4096;
  constexpr int GWOFF = A_WBASE + NWS * WSLOT;
  const unsigned lbase = lds_addr(lds);
  const int grp = wave >> 2;
  unsigned voffX[2], voffW;
  {
    int rl = lane >> 3;
#pragma unroll
    for (int i = 0; i < 2; ++i) {
      int row = wave * 32 + 8 * i + rl;
      int c = (lane & 7) ^ ((row >> 1) & 7);
      voffX[i] = (unsigned)((row0 + row) * 1152 + c * 16);
    }
    int c = (lane & 7) ^ (((rl >> 1) & 1) << 2);
    voffW = (unsigned)(rl * ldw + c * 16);
  }
  const int pi0 = wave, pi1 = U == 4 ? wave + 8 : 8 + (wave & 3);
  const int kb0 = pi0 / U, u0 = pi0 - kb0 * U, kb1 = pi1 / U, u1 = pi1 - kb1 * U;
  const char* wsrc0 = Wb + (size_t)kb0 * 8 * ldw + u0 * 128;
  const char* wsrc1 = Wb + (size_t)kb1 * 8 * ldw + u1 * 128;
  auto issueX = [&](int J, int i) {
    glds16s(Xb + J * 128 + (i >> 1) * (16 * 1152), voffX[i & 1], lbase + (J % 3) * A_XSLOT + (wave * 4 + i) * 1024);
  };
  auto issueW = [&](int s, int i) {
    unsigned dst = lbase + A_WBASE + (s % NWS) * WSLOT;
    if (i == 0) glds16s(wsrc0 + (size_t)s * 32 * ldw, voffW, dst + pi0 * 1024);
    else if (U == 4 || (s & 1) == grp) glds16s(wsrc1 + (size_t)s * 32 * ldw, voffW, dst + pi1 * 1024);
  };
  auto wait_top = [&](auto Jc) {
    constexpr int j = decltype(Jc)::v;
    constexpr int n0 = a_inflight<U, NWS>(j, 0), n1 = a_inflight<U, NWS>(j, 1);
    if constexpr (n0 == n1) wait_vm<n0>();
    else { if (grp == 0) wait_vm<n0>(); else wait_vm<n1>(); }
  };
  const int r = lane & 31, h = lane >> 5;
  const int q = (lane >> 2) & 3, p = lane & 3, g = (lane >> 4) & 1;
  const int xa = (wm * 64 + r) * 128 + ((h ^ ((r >> 1) & 7)) << 4);
  int wa[NBW];
#pragma unroll
  for (int nb = 0; nb < NBW; ++nb) {
    int b = wn * U + nb;
    wa[nb] = A_WBASE + (h * U + (b >> 1)) * 1024 + q * 128 + (((b & 1) ^ (q >> 1)) << 6) + g * 32 + p * 8;
  }
  const s16x4 ONES = {0x3F80, 0x3F80, 0x3F80, 0x3F80};

  STAMP(0, 0);
  if (STATS == 2) {
    if (wave == 0) glds16s(gw, lane * 16, lbase + GWOFF);
    else if (wave == 1 && lane < 8) glds16s(gw + 1024, lane * 16, lbase + GWOFF + 1024);
  }
  static_for<0, 4>([&](auto Tc) {
    constexpr int t = decltype(Tc)::v - 4;
    constexpr int s = t + NWS - 1;
    if constexpr (s >= 0) { issueW(s, 0); issueW(s, 1); }
    if constexpr ((t & 1) == 0) {
#pragma unroll
      for (int i = 0; i < 4; ++i) issueX((t + 4) / 2, i);
    }
  });
  wait_top(IC<-1>{});
  BARRIER();
  STAMP(0, 1);
  auto rd = [&](Frags<2, NBW>& f, s16x4 (&lo)[NBW], int j, int s, int n) {
    const char* xb = lds + ((j >> 1) % 3) * A_XSLOT;
    const char* wb = lds + (j % NWS) * WSLOT + 2 * s * U * 1024;
    const int ks = (j & 1) * 2 + s;
    if (n == 0) f.x[0] = *(const bf16x8*)(xb + (xa ^ (ks << 5)));
    else if (n == 2 * NBW + 1) {
      f.x[1] = *(const bf16x8*)(xb + (xa ^ (ks << 5)) + 4096);
      if (STATS == 2) f.g = *(const bf16x8*)(lds + GWOFF + (2 * j + s) * 32 + h * 16);
    }
    else if (n & 1) lo[(n - 1) >> 1] = tr_read(wb + wa[(n - 1) >> 1]);
    else f.w[(n - 2) >> 1] = cat8(lo[(n - 2) >> 1], tr_read(wb + wa[(n - 2) >> 1] + 512));
  };
  Frags<2, NBW> f0, f1;
  s16x4 lo[NBW];
#pragma unroll
  for (int n = 0; n < 2 * NBW + 2; ++n) rd(f0, lo, 0, 0, n);

  auto half = [&](auto Jc, auto Hc, Frags<2, NBW>& cur, Frags<2, NBW>& nxt) {
    constexpr int j = decltype(Jc)::v, hf = decltype(Hc)::v;
    constexpr int nj = hf ? j + 1 : j, ns = hf ? 0 : 1;
    static_for<0, NM>([&](auto Mc) {
      constexpr int m = decltype(Mc)::v;
      constexpr int mb = m / NBW, nb = m % NBW;
      if (TRANS) acc[mb][nb] = MFMA32(cur.w[nb], cur.x[mb], acc[mb][nb]);
      else acc[mb][nb] = MFMA32(cur.x[mb], cur.w[nb], acc[mb][nb]);
      if constexpr (nj < 18) {
        if constexpr (m == 0) { rd(nxt, lo, nj, ns, 0); rd(nxt, lo, nj, ns, 1); rd(nxt, lo, nj, ns, 2); }
        else if constexpr (m < NBW) { rd(nxt, lo, nj, ns, 2 * m + 1); rd(nxt, lo, nj, ns, 2 * m + 2); }
        else if constexpr (m == NBW) rd(nxt, lo, nj, ns, 2 * NBW + 1);
      }
      if constexpr (STATS != 0 && m == NBW + 1) {
        if (hf == wn) {
#pragma unroll
          for (int b = 0; b < 2; ++b) {
            s16x4 xl = __builtin_shufflevector(cur.x[b], cur.x[b], 0, 1, 2, 3);
            s16x4 xh = __builtin_shufflevector(cur.x[b], cur.x[b], 4, 5, 6, 7);
            if (STATS == 1) {
              sx[b] = MFMA4(ONES, xl, sx[b]);
              sxx[b] = MFMA4(xl, xl, sxx[b]);
              sx[b] = MFMA4(ONES, xh, sx[b]);
              sxx[b] = MFMA4(xh, xh, sxx[b]);
            } else {
              sx[b] = MFMA4(__builtin_shufflevector(cur.g, cur.g, 0, 1, 2, 3), xl, sx[b]);
              sxx[b] = MFMA4(__builtin_shufflevector(cur.g, cur.g, 4, 5, 6, 7), xh, sxx[b]);
            }
          }
          asm volatile("" : "+v"(sx[0]), "+v"(sxx[0]), "+v"(sx[1]), "+v"(sxx[1]));
        }
      }
      if constexpr (STATS == 1 && m == 2) {
        if (((2 * j + hf) & 3) == wm) {
#pragma unroll
          for (int b = 0; b < NBW; ++b) cs[b] = MFMA4(ONES, __builtin_shufflevector(cur.w[b], cur.w[b], 0, 1, 2, 3), cs[b]);
#pragma unroll
          for (int b = 0; b < NBW; ++b) cs[b] = MFMA4(ONES, __builtin_shufflevector(cur.w[b], cur.w[b], 4, 5, 6, 7), cs[b]);
        }
      }
      if constexpr (m == 1 || m == 3 || m == 5) {
        constexpr int e = hf * 3 + (m >> 1);
        constexpr int s = j + NWS - 1;
        if constexpr (e < 2) { if constexpr (s < 18) issueW(s, e); }
        else if constexpr ((j & 1) == 0 && j / 2 + 2 < 9) issueX(j / 2 + 2, e - 2);
      }
      SB();
    });
  };
  static_for<0, 18>([&](auto Jc) {
    if (decltype(Jc)::v == 4) STAMP(0, 2);
    if (decltype(Jc)::v == 9) STAMP(0, 3);
    if (decltype(Jc)::v == 14) STAMP(0, 4);
    wait_top(Jc);
    BARRIER();
    SB();
    half(Jc, IC<0>{}, f0, f1);
    half(Jc, IC<1>{}, f1, f0);
  });
  STAMP(0, 5);
}

template <int U, int NWS, int STATS>
DI void qkv_role(char* lds, const char* X, const char* Wqkv, const char* gw, char* T, float* G, int rt, int unit0,
                 int wave, int lane, int wm, int wn) {
  f32x16 acc[2][U];
#pragma unroll
  for (int a = 0; a < 2; ++a)
#pragma unroll
    for (int b = 0; b < U; ++b)
#pragma unroll
      for (int i = 0; i < 16; ++i) acc[a][b][i] = 0.f;
  const f32x4 z4 = {0.f, 0.f, 0.f, 0.f};
  f32x4 sx[2] = {z4, z4}, sxx[2] = {z4, z4}, cs[U];
  gemmA<U, NWS, false, STATS>(lds, X, rt * 256, Wqkv + unit0 * 128, NQKV * 2, gw, wave, lane, wm, wn, acc, sx, sxx, cs);
  const int r = lane & 31, h = lane >> 5;
  if (STATS == 2) {
    if (unit0 == 0) {
      float* gp = (float*)(lds + A_WBASE + NWS * U * 4096 + 1152);
#pragma unroll
      for (int b = 0; b < 2; ++b) {
        float v = sx[b][0] + sxx[b][0];
        v += xor32(v);
        if (h == 0) gp[wn * 256 + wm * 64 + b * 32 + r] = v;
      }
      __syncthreads();
      if (wave < 4) {
        float v = gp[wave * 64 + lane] + gp[256 + wave * 64 + lane];
        G[rt * 256 + wave * 64 + lane] = 1.0f / (1.0f + __builtin_amdgcn_exp2f(-1.4426950408889634f * v));
      }
    }
  }
  const float QSCALE = 0.23570226039551584f * 1.4426950408889634f;
#pragma unroll
  for (int nb = 0; nb < U; ++nb) {
    int n = unit0 * 64 + (wn * U + nb) * 32 + r;
    int part = n / 576;
    int rem = n - part * 576;
    int head = rem / 18;
    int d = rem - head * 18;
    float sc = part == 0 ? QSCALE : 1.0f;
    char* base = T + (size_t)part * TPART + head * THEAD + h * (TSTEP / 2) + d * 16;
#pragma unroll
    for (int mb = 0; mb < 2; ++mb)
#pragma unroll
      for (int s = 0; s < 2; ++s) {
        int step = (rt * 256 + wm * 64 + mb * 32) / 16 + s;
        u32x4 v;
#pragma unroll
        for (int i = 0; i < 4; ++i) v[i] = pk2(acc[mb][nb][8 * s + 2 * i] * sc, acc[mb][nb][8 * s + 2 * i + 1] * sc);
        st16p(base + step * TSTEP, v);
        if (part == 2 && d == 17) {
          u32x4 o = {0x3F803F80u, 0x3F803F80u, 0x3F803F80u, 0x3F803F80u};
          st16p(base + step * TSTEP + 16, o);
          u32x4 z = {0u, 0u, 0u, 0u};
          st16p(base + step * TSTEP + 32, z);
        }
      }
  }
}

DI unsigned short f2bf_rn(float f) { return (unsigned short)(pk2(f, 0.f) & 0xffffu); }

DI void mlp1_role(char* lds, const char* X, const char* W1, const unsigned short* b1, char* H, int rt, int ct,
                  int wave, int lane, int wm, int wn) {
  constexpr int NBW = 3;
  f32x16 acc[2][NBW];
#pragma unroll
  for (int a = 0; a < 2; ++a)
#pragma unroll
    for (int b = 0; b < NBW; ++b)
#pragma unroll
      for (int i = 0; i < 16; ++i) acc[a][b][i] = 0.f;
  const f32x4 z4 = {0.f, 0.f, 0.f, 0.f};
  f32x4 sx[2] = {z4, z4}, sxx[2] = {z4, z4}, cs[NBW] = {z4, z4, z4};
  const int r = lane & 31, h = lane >> 5;
  unsigned short bb[NBW];
#pragma unroll
  for (int nb = 0; nb < NBW; ++nb) bb[nb] = b1[ct * 192 + (wn * 3 + nb) * 32 + r];
  gemmA<3, 5, true, 1>(lds, X, rt * 256, W1 + ct * 384, F_ * 2, nullptr, wave, lane, wm, wn, acc, sx, sxx, cs);
  BARRIER();
  float* rowst = (float*)(lds + 4096);
  float* colp = (float*)lds;
#pragma unroll
  for (int b = 0; b < 2; ++b) {
    float a = sx[b][0];
    int i4 = lane & 3;
    float q2 = i4 == 0 ? sxx[b][0] : (i4 == 1 ? sxx[b][1] : (i4 == 2 ? sxx[b][2] : sxx[b][3]));
    a += xor32(a);
    q2 += xor32(q2);
    if (h == 0) {
      f32x2 v = {a, q2};
      *(f32x2*)(rowst + (wn * 256 + wm * 64 + b * 32 + r) * 2) = v;
    }
  }
#pragma unroll
  for (int nb = 0; nb < NBW; ++nb) {
    float c = cs[nb][0] + xor32(cs[nb][0]);
    if (h == 0) colp[wm * 192 + (wn * 3 + nb) * 32 + r] = c;
  }
  __syncthreads();
  bf16x8 wx[NBW], xx[2];
  float ca[2], cb[2], sd[2];
#pragma unroll
  for (int nb = 0; nb < NBW; ++nb) {
    int n = (wn * 3 + nb) * 32 + r;
    float c = -(colp[n] + colp[192 + n] + colp[384 + n] + colp[576 + n]);
    unsigned short chi = f2bf_rn(c);
    unsigned short clo = f2bf_rn(c - bf2f(chi));
    bf16x8 w = {(short)chi, (short)clo, (short)chi, (short)bb[nb], (short)bb[nb], 0, 0, 0};
    bf16x8 z = {0, 0, 0, 0, 0, 0, 0, 0};
    wx[nb] = h ? z : w;
  }
  const float C0 = -2.0f * 1.4426950408889634f * 0.7978845608028654f;
  const float C1 = C0 * 0.044715f;
#pragma unroll
  for (int mb = 0; mb < 2; ++mb) {
    f32x2 st = *(const f32x2*)(rowst + (wm * 64 + mb * 32 + r) * 2) +
               *(const f32x2*)(rowst + (256 + wm * 64 + mb * 32 + r) * 2);
    st[0] *= (1.0f / 576.0f);
    st[1] = __builtin_fmaxf(st[1] * (1.0f / 576.0f) - st[0] * st[0], 0.f);
    float rstd = __builtin_amdgcn_rsqf(st[1] + 1e-5f);
    float sdev = (st[1] + 1e-5f) * rstd;
    unsigned short mhi = f2bf_rn(st[0]);
    unsigned short mlo = f2bf_rn(st[0] - bf2f(mhi));
    unsigned short shi = f2bf_rn(sdev);
    unsigned short slo = f2bf_rn(sdev - bf2f(shi));
    bf16x8 x = {(short)mhi, (short)mhi, (short)mlo, (short)shi, (short)slo, 0, 0, 0};
    bf16x8 z = {0, 0, 0, 0, 0, 0, 0, 0};
    xx[mb] = h ? z : x;
    ca[mb] = C0 * rstd;
    cb[mb] = C1 * rstd * rstd * rstd;
    sd[mb] = sdev;
  }
#pragma unroll
  for (int mb = 0; mb < 2; ++mb)
#pragma unroll
    for (int nb = 0; nb < NBW; ++nb) acc[mb][nb] = MFMA32(wx[nb], xx[mb], acc[mb][nb]);
#pragma unroll
  for (int mb = 0; mb < 2; ++mb) {
    char* hb = H + ((size_t)(rt * 8 + wm * 2 + mb) * 288 + ct * 12 + wn * 6) * 1024 + lane * 16;
    const f32x2 a2 = {ca[mb], ca[mb]}, b2 = {cb[mb], cb[mb]}, s2 = {sd[mb], sd[mb]};
#pragma unroll
    for (int nb = 0; nb < NBW; ++nb)
#pragma unroll
      for (int gp = 0; gp < 2; ++gp) {
        u32x4 w;
#pragma unroll
        for (int i = 0; i < 4; ++i) {
          f32x2 v = {acc[mb][nb][8 * gp + 2 * i], acc[mb][nb][8 * gp + 2 * i + 1]};
          f32x2 t = v * __builtin_elementwise_fma(v * v, b2, a2);
          f32x2 e = {__builtin_amdgcn_exp2f(t[0]), __builtin_amdgcn_exp2f(t[1])};
          f32x2 d = __builtin_elementwise_fma(e, s2, s2);
          f32x2 rr = {__builtin_amdgcn_rcpf(d[0]), __builtin_amdgcn_rcpf(d[1])};
          f32x2 o = v * rr;
          w[i] = pk2(o[0], o[1]);
        }
        st16(hb + (nb * 2 + gp) * 1024, w);
      }
  }
}

__global__ __launch_bounds__(512, 2) void kernelA(const char* __restrict__ X, const char* __restrict__ Wqkv,
                                                   const char* __restrict__ W1, const unsigned short* __restrict__ b1,
                                                   const char* __restrict__ gw, char* __restrict__ T,
                                                   char* __restrict__ H, float* __restrict__ G) {
  __shared__ __attribute__((aligned(16))) char lds[163840];
  const int wg = blockIdx.x;
  const int xcd = wg & 7, idx = wg >> 3;
  const int rt = (xcd & 1) * 4 + (idx & 3);
  const int ct = (xcd >> 1) + 4 * (idx >> 2);
  const int wave = __builtin_amdgcn_readfirstlane(threadIdx.x >> 6);
  const int lane = threadIdx.x & 63;
  const int wm = wave & 3, wn = wave >> 2;
  if (ct < 5) qkv_role<3, 5, 2>(lds, X, Wqkv, gw, T, G, rt, ct * 3, wave, lane, wm, wn);
  else if (ct < 8) qkv_role<4, 4, 0>(lds, X, Wqkv, gw, T, G, rt, 15 + (ct - 5) * 4, wave, lane, wm, wn);
  else mlp1_role(lds, X, W1, b1, H, rt, ct - 8, wave, lane, wm, wn);
  STAMP(0, 6);
  STAMP(0, 7);
}

typedef __attribute__((address_space(3))) const bf16x8* lds_bf16x8_p;
DI s16x4 trl(lds_p p) { return __builtin_amdgcn_ds_read_tr16_b64_v4i16((lds_s16x4_p)p); }
DI bf16x8 ld8(lds_p p) { return *(lds_bf16x8_p)p; }
DI f32x16 qk_tile(lds_p kb, lds_p ke, bf16x8 qf, s16x4 qe, f32x16 c) {
  s16x4 lo = trl(kb);
  s16x4 hi = trl(kb + 64);
  s16x4 ex = trl(ke);
  bf16x8 kf = __builtin_shufflevector(lo, hi, 0, 1, 2, 3, 4, 5, 6, 7);
  f32x16 s = MFMA32(kf, qf, c);
  s = MFMA32K8(ex, qe, s);
  return s;
}
DI f32x16 pv_tile(lds_p vb, const f32x16& pe, f32x16 o) {
  bf16x8 v0 = ld8(vb);
  bf16x8 v1 = ld8(vb + TSTEP);
  u32x4 p0, p1;
#pragma unroll
  for (int i = 0; i < 4; ++i) {
    p0[i] = pk2(pe[2 * i], pe[2 * i + 1]);
    p1[i] = pk2(pe[8 + 2 * i], pe[8 + 2 * i + 1]);
  }
  o = MFMA32(v0, __builtin_bit_cast(bf16x8, p0), o);
  o = MFMA32(v1, __builtin_bit_cast(bf16x8, p1), o);
  return o;
}
struct KFrag { bf16x8 f; s16x4 e; };
struct VFrag { bf16x8 v0, v1; };
DI KFrag load_k(lds_p kb, lds_p ke) {
  KFrag k;
  s16x4 lo = trl(kb);
  s16x4 hi = trl(kb + 64);
  k.e = trl(ke);
  k.f = __builtin_shufflevector(lo, hi, 0, 1, 2, 3, 4, 5, 6, 7);
  return k;
}
DI VFrag load_v(lds_p vb) {
  VFrag v;
  v.v0 = ld8(vb);
  v.v1 = ld8(vb + TSTEP);
  return v;
}
#define EXP2(x) __builtin_amdgcn_exp2f(x)
DI void tile_step(const f32x16& s, f32x16& sn, const KFrag& k, const VFrag& v, bf16x8 qf, s16x4 qe,
                  const f32x16& negm, f32x16& o) {
  u32x4 p0, p1;
  p0[0] = pk2(EXP2(s[0]), EXP2(s[1]));
  p0[1] = pk2(EXP2(s[2]), EXP2(s[3]));
  SB();
  f32x16 t = MFMA32(k.f, qf, negm);
  SB();
  p0[2] = pk2(EXP2(s[4]), EXP2(s[5]));
  p0[3] = pk2(EXP2(s[6]), EXP2(s[7]));
  SB();
  o = MFMA32(v.v0, __builtin_bit_cast(bf16x8, p0), o);
  SB();
  p1[0] = pk2(EXP2(s[8]), EXP2(s[9]));
  p1[1] = pk2(EXP2(s[10]), EXP2(s[11]));
  SB();
  t = MFMA32K8(k.e, qe, t);
  SB();
  p1[2] = pk2(EXP2(s[12]), EXP2(s[13]));
  p1[3] = pk2(EXP2(s[14]), EXP2(s[15]));
  SB();
  o = MFMA32(v.v1, __builtin_bit_cast(bf16x8, p1), o);
  SB();
  sn = t;
}
DI float rowmax16(const f32x16& s) {
  float m = __builtin_fmaxf(s[0], s[1]);
#pragma unroll
  for (int i = 2; i < 16; ++i) m = __builtin_fmaxf(m, s[i]);
  return __builtin_fmaxf(m, xor32(m));
}

constexpr int AT_SLOT = 20480;
constexpr int at_cnt(int c, int grp) { return 2 + (((c & 1) == grp) ? 1 : 0); }
constexpr int at_off(int tt) { return ((tt >> 3) & 1) * AT_SLOT + (tt & 7) * 1280; }

DI void attn_role(char* lds, const char* __restrict__ T, char* __restrict__ O, int wg, int wave, int lane) {
  STAMP(1, 0);
  const int head = (wg & 7) + 8 * ((wg >> 3) & 3);
  const int qt = wg >> 5;
  const int r = lane & 31, h = lane >> 5;
  const int grp = wave >> 2;
  const unsigned lbase = lds_addr(lds);
  const char* TK = T + (size_t)TPART + head * THEAD;
  const char* TV = T + (size_t)2 * TPART + head * THEAD;
  const int qi = qt * 256 + wave * 32 + r;
  const unsigned short* qp =
      (const unsigned short*)(T + head * THEAD + (qi >> 4) * TSTEP + ((qi >> 2) & 1) * (TSTEP / 2)) + (qi & 3) +
      4 * ((qi >> 3) & 1);
  unsigned short qv[8], qx[2];
#pragma unroll
  for (int j = 0; j < 8; ++j) qv[j] = qp[(8 * h + j) * 8];
  qx[0] = qp[16 * 8];
  qx[1] = qp[17 * 8];
  const int p1 = wave + 8, p2 = 16 + (wave & 3);
  const char* s0 = TK + wave * 1024;
  const char* s1 = p1 < 10 ? TK + p1 * 1024 : TV + (p1 - 10) * 1024;
  const char* s2 = TV + (p2 - 10) * 1024;
  const unsigned voff = lane * 16;
  auto issue = [&](int c) {
    unsigned dst = lbase + (c & 3) * AT_SLOT;
    glds16s(s0 + c * 10240, voff, dst + wave * 1024);
    glds16s(s1 + c * 10240, voff, dst + p1 * 1024);
    if ((c & 1) == grp) glds16s(s2 + c * 10240, voff, dst + p2 * 1024);
  };
  issue(0);
  bf16x8 qf;
#pragma unroll
  for (int j = 0; j < 8; ++j) qf[j] = (short)qv[j];
  s16x4 qe;
  qe[0] = h ? (short)0 : (short)qx[0];
  qe[1] = h ? (short)0 : (short)qx[1];
  qe[2] = 0;
  qe[3] = 0;
  asm volatile("" : "+v"(qf), "+v"(qe));
  wait_vm<0>();
  issue(1);
  issue(2);
  BARRIER();
  STAMP(1, 1);

  const int q = (lane >> 2) & 3, p = lane & 3, g = (lane >> 4) & 1;
  const lds_p L = (lds_p)lds;
  const lds_p kb = L + g * TSTEP + (p & 1) * (TSTEP / 2) + (8 * h + q) * 16 + (p >> 1) * 8;
  const lds_p ke = L + g * TSTEP + (p & 1) * (TSTEP / 2) + (16 + (q & 1)) * 16 + (p >> 1) * 8;
  const lds_p vb = L + 10240 + h * (TSTEP / 2) + (r < 19 ? r : 19) * 16;
  lds_p kb2 = kb + 2 * AT_SLOT, ke2 = ke + 2 * AT_SLOT, vb2 = vb + 2 * AT_SLOT;
  asm volatile("" : "+v"(kb2), "+v"(ke2), "+v"(vb2));

  f32x16 zero;
#pragma unroll
  for (int i = 0; i < 16; ++i) zero[i] = 0.f;
  f32x16 o = zero;
  f32x16 sA = qk_tile(kb, ke, qf, qe, zero);
  float m0 = rowmax16(sA);
  f32x16 negm;
#pragma unroll
  for (int i = 0; i < 16; ++i) { negm[i] = -m0; sA[i] -= m0; }
  KFrag kA, kB;
  VFrag vA, vB;
  kB = load_k(kb + 1280, ke + 1280);
  vA = load_v(vb);
  f32x16 sB;
#pragma unroll 1
  for (int cc = 0; cc < 8; cc += 4) {
    static_for<0, 32>([&](auto Tc) {
      constexpr int tt = decltype(Tc)::v, ci = tt >> 3;
      if constexpr ((tt & 7) == 6) {
        if (cc == 0 || ci < 2) {
          if (grp == 0) wait_vm<at_cnt(ci + 2, 0)>(); else wait_vm<at_cnt(ci + 2, 1)>();
        } else if (ci == 2) wait_vm<0>();
        if (cc == 0 || ci < 3) BARRIER();
        if (cc == 0 || ci == 0) issue(cc + ci + 3);
      }
      constexpr int t1 = (tt + 1) & 31, t2 = (tt + 2) & 31;
      const lds_p kp = (t2 < 16 ? kb : kb2) + at_off(t2);
      const lds_p ep = (t2 < 16 ? ke : ke2) + at_off(t2);
      const lds_p vp = (t1 < 16 ? vb : vb2) + at_off(t1);
      if constexpr ((tt & 1) == 0) {
        kA = load_k(kp, ep);
        vB = load_v(vp);
        SB();
        tile_step(sA, sB, kB, vA, qf, qe, negm, o);
      } else {
        kB = load_k(kp, ep);
        vA = load_v(vp);
        SB();
        tile_step(sB, sA, kA, vB, qf, qe, negm, o);
      }
    });
    if (cc == 0) STAMP(1, 2);
  }
  STAMP(1, 5);
  float l = xor32(o[10]);
  l = h ? l : o[10];
  bool bad = !(l < 1.0e30f);
#pragma unroll
  for (int i = 0; i < 10; ++i) bad = bad || !(__builtin_fabsf(o[i]) < 1.0e37f);
  {
    int* fl = (int*)(lds + AT_SLOT);
    int anyb = __any(bad) ? 1 : 0;
    if (lane == 0) fl[wave] = anyb;
    __syncthreads();
    int v = fl[lane & 7];
    bad = __any(v != 0);
  }
  if (bad) {
    float m = -3.0e38f;
    o = zero;
#pragma unroll 1
    for (int c = 0; c < 8; ++c) {
      __syncthreads();
      {
        unsigned dst = lbase;
        glds16s(s0 + c * 10240, voff, dst + wave * 1024);
        glds16s(s1 + c * 10240, voff, dst + p1 * 1024);
        if (grp == 0) glds16s(s2 + c * 10240, voff, dst + p2 * 1024);
      }
      wait_vm<0>();
      __syncthreads();
#pragma unroll 1
      for (int t = 0; t < 8; ++t) {
        f32x16 s = qk_tile(kb + t * 1280, ke + t * 1280, qf, qe, zero);
        float mn = __builtin_fmaxf(m, rowmax16(s));
        float alpha = __builtin_amdgcn_exp2f(m - mn);
        m = mn;
        f32x16 pe;
#pragma unroll
        for (int i = 0; i < 16; ++i) { pe[i] = __builtin_amdgcn_exp2f(s[i] - mn); o[i] *= alpha; }
        o = pv_tile(vb + t * 1280, pe, o);
      }
    }
    l = xor32(o[10]);
    l = h ? l : o[10];
  }
  float inv = 1.0f / l;
  char* ob = O + (size_t)(qt * 8 + wave) * 36 * 1024;
  u32x4 ov;
#pragma unroll
  for (int i = 0; i < 4; ++i) ov[i] = pk2(o[2 * i] * inv, o[2 * i + 1] * inv);
  st16p(ob + head * 1024 + lane * 16, ov);
  if (h == 0)
    st4(ob + (32 + (head >> 3)) * 1024 + (((head >> 2) & 1) * 32 + r) * 16 + (head & 3) * 4,
        pk2(o[8] * inv, o[9] * inv));
  STAMP(1, 6);
  STAMP(1, 7);
}

constexpr int M_SLOT = 20480, M_IT = 36;
template <int U>
constexpr int m_cnt(int s, int grp) { return s >= M_IT ? 0 : (U == 2 ? 2 : 2 + (((s & 1) == grp) ? 1 : 0)); }

template <int NBW, int U>
DI void mlp2_role(char* lds, const char* __restrict__ H, const char* __restrict__ W2, char* __restrict__ P, int rt,
                  int sp, int unit0, int boff, int wave, int lane) {
  f32x16 acc[NBW];
#pragma unroll
  for (int b = 0; b < NBW; ++b)
#pragma unroll
    for (int i = 0; i < 16; ++i) acc[b][i] = 0.f;
  const unsigned lbase = lds_addr(lds);
  const int wm = wave & 3, kh = wave >> 2;
  const unsigned voffX = lane * 16;
  const unsigned voffW = (unsigned)((lane >> 3) * 1152 + (((lane & 7) ^ (((lane >> 4) & 1) << 2)) << 4));
  const char* xsrc = H + ((size_t)(rt * 4 + wm) * 288 + sp * 72 + kh) * 1024;
  const char* wbase = W2 + (size_t)sp * 1152 * 1152 + unit0 * 128;
  const int p0 = wave, p2 = 8 + (wave & 3);
  const char* ws0 = wbase + (p0 / U) * (8 * 1152) + (p0 % U) * 128;
  const char* ws2 = wbase + (p2 / U) * (8 * 1152) + (p2 % U) * 128;
  auto issue = [&](int s, int sl, int e) {
    unsigned dst = lbase + sl * M_SLOT;
    if (e == 0) glds16s(xsrc + s * 2048, voffX, dst + wave * 1024);
    else if (e == 1) glds16s(ws0 + s * (32 * 1152), voffW, dst + 8192 + p0 * 1024);
    else if (U == 3 && (sl & 1) == kh) glds16s(ws2 + s * (32 * 1152), voffW, dst + 8192 + p2 * 1024);
  };
  const int h = lane >> 5;
  const int q = (lane >> 2) & 3, p = lane & 3, g = (lane >> 4) & 1;
  const lds_p L = (lds_p)lds;
  const lds_p xa = L + wave * 1024 + lane * 16;
  lds_p wa[2];
#pragma unroll
  for (int e = 0; e < 2; ++e) {
    int b = boff + e;
    wa[e] = L + 8192 + (2 * kh * U + (b >> 1)) * 1024 + (4 * h + q) * 128 + (((b & 1) ^ (q >> 1)) << 6) + g * 32 + p * 8;
  }
  lds_p xa2 = xa + 2 * M_SLOT, wa2[2] = {wa[0] + 2 * M_SLOT, wa[1] + 2 * M_SLOT};
  asm volatile("" : "+v"(xa2), "+v"(wa2[0]), "+v"(wa2[1]));
  STAMP(2, 0);
#pragma unroll
  for (int s = 0; s < 4; ++s)
#pragma unroll
    for (int e = 0; e < 3; ++e) issue(s, s, e);
  {
    constexpr int n0 = m_cnt<U>(1, 0) + m_cnt<U>(2, 0) + m_cnt<U>(3, 0), n1 = m_cnt<U>(1, 1) + m_cnt<U>(2, 1) + m_cnt<U>(3, 1);
    if (kh == 0) wait_vm<n0>(); else wait_vm<n1>();
  }
  BARRIER();
  STAMP(2, 1);
  bf16x8 w[NBW], x0, x1;
  auto rdx = [&](int sl) { return ld8((sl < 2 ? xa : xa2) + (sl & 1) * M_SLOT); };
  auto rdw = [&](int sl, int m) {
    const lds_p a = (sl < 2 ? wa[m & 1] : wa2[m & 1]) + (sl & 1) * M_SLOT + (m >> 1) * 1024;
    return cat8(trl(a), trl(a + U * 1024));
  };
  x0 = rdx(0);
#pragma unroll
  for (int m = 0; m < NBW; ++m) w[m] = rdw(0, m);
  auto iter = [&](int jb, auto Lc, auto Ic) {
    constexpr int last = decltype(Lc)::v, i = decltype(Ic)::v;
    constexpr int js = last ? 32 + i : i;
    constexpr int n0 = m_cnt<U>(js + 2, 0) + m_cnt<U>(js + 3, 0), n1 = m_cnt<U>(js + 2, 1) + m_cnt<U>(js + 3, 1);
    if constexpr (n0 == n1) wait_vm<n0>();
    else { if (kh == 0) wait_vm<n0>(); else wait_vm<n1>(); }
    asm volatile("s_waitcnt lgkmcnt(0)" ::: "memory");
    BARRIER();
    SB();
    bf16x8& xc = (i & 1) ? x1 : x0;
    bf16x8& xn = (i & 1) ? x0 : x1;
    static_for<0, NBW>([&](auto Mc) {
      constexpr int m = decltype(Mc)::v;
      acc[m] = MFMA32(w[m], xc, acc[m]);
      SB();
      if constexpr (js + 1 < M_IT) {
        if constexpr (m == 0) xn = rdx((i + 1) & 3);
        w[m] = rdw((i + 1) & 3, m);
      }
      if constexpr (!last && m >= 1 && m <= 3) issue(jb + i + 4, i, m - 1);
      SB();
    });
  };
#pragma unroll 1
  for (int jb = 0; jb < 32; jb += 4) {
    static_for<0, 4>([&](auto Ic) { iter(jb, IC<0>{}, Ic); });
    if (jb == 8) STAMP(2, 2);
    if (jb == 16) STAMP(2, 3);
    if (jb == 24) STAMP(2, 4);
  }
  static_for<0, 4>([&](auto Ic) { iter(32, IC<1>{}, Ic); });
  STAMP(2, 5);
  constexpr int NA = (NBW + 1) / 2;
  char* ex = lds + wm * (NBW * 4096) + lane * 16;
  if (kh == 0) {
#pragma unroll
    for (int b = NA; b < NBW; ++b)
#pragma unroll
      for (int c = 0; c < 4; ++c) {
        f32x4 v = {acc[b][4 * c], acc[b][4 * c + 1], acc[b][4 * c + 2], acc[b][4 * c + 3]};
        *(f32x4*)(ex + b * 4096 + c * 1024) = v;
      }
  } else {
#pragma unroll
    for (int b = 0; b < NA; ++b)
#pragma unroll
      for (int c = 0; c < 4; ++c) {
        f32x4 v = {acc[b][4 * c], acc[b][4 * c + 1], acc[b][4 * c + 2], acc[b][4 * c + 3]};
        *(f32x4*)(ex + b * 4096 + c * 1024) = v;
      }
  }
  __syncthreads();
  char* pb = P + ((size_t)(sp * 64 + rt * 4 + wm) * 18 + unit0 * 2 + boff) * 2048 + lane * 16;
  auto fin = [&](int b) {
#pragma unroll
    for (int gp = 0; gp < 2; ++gp) {
      f32x4 a = *(const f32x4*)(ex + b * 4096 + (2 * gp) * 1024);
      f32x4 c = *(const f32x4*)(ex + b * 4096 + (2 * gp + 1) * 1024);
      u32x4 v;
      v[0] = pk2(acc[b][8 * gp] + a[0], acc[b][8 * gp + 1] + a[1]);
      v[1] = pk2(acc[b][8 * gp + 2] + a[2], acc[b][8 * gp + 3] + a[3]);
      v[2] = pk2(acc[b][8 * gp + 4] + c[0], acc[b][8 * gp + 5] + c[1]);
      v[3] = pk2(acc[b][8 * gp + 6] + c[2], acc[b][8 * gp + 7] + c[3]);
      st16(pb + b * 2048 + gp * 1024, v);
    }
  };
  if (kh == 0) {
#pragma unroll
    for (int b = 0; b < NA; ++b) fin(b);
  } else {
#pragma unroll
    for (int b = NA; b < NBW; ++b) fin(b);
  }
  STAMP(2, 6);
  STAMP(2, 7);
}

__global__ __launch_bounds__(512, 4) void kernelB(const char* __restrict__ T, char* __restrict__ O,
                                                   const char* __restrict__ H, const char* __restrict__ W2,
                                                   char* __restrict__ P) {
  __shared__ __attribute__((aligned(16))) char lds[81920];
  const int wave = __builtin_amdgcn_readfirstlane(threadIdx.x >> 6);
  const int lane = threadIdx.x & 63;
  const int bid = blockIdx.x;
  if (bid < 256) {
    attn_role(lds, T, O, bid, wave, lane);
  } else {
    const int wg = bid - 256;
    const int xcd = wg & 7, idx = wg >> 3;
    const int sp = xcd >> 1, ct = idx & 3, rt = (xcd & 1) * 8 + (idx >> 2);
    if (ct == 0 || ct == 3) mlp2_role<4, 2>(lds, H, W2, P, rt, sp, ct == 0 ? 0 : 7, 0, wave, lane);
    else mlp2_role<5, 3>(lds, H, W2, P, rt, sp, 2 * ct, ct - 1, wave, lane);
  }
}

__global__ __launch_bounds__(256, 2) void kernelC(const char* __restrict__ O, const char* __restrict__ Wo,
                                                   const char* __restrict__ P, const float* __restrict__ G,
                                                   const char* __restrict__ b2, char* __restrict__ out) {
  __shared__ __attribute__((aligned(16))) char lds[73728];
  const int wg = blockIdx.x;
  const int idx = wg >> 3;
  const int ct = idx % 9, rt = (wg & 7) * 4 + idx / 9;
  const int wave = __builtin_amdgcn_readfirstlane(threadIdx.x >> 6);
  const int lane = threadIdx.x & 63;
  const int wm = wave & 1, kh = wave >> 1;
  const int r = lane & 31, h = lane >> 5;
  const unsigned lbase = lds_addr(lds);
  STAMP(3, 0);
  {
    const unsigned voffW = (unsigned)((lane >> 3) * 1152 + (((lane & 7) ^ (((lane >> 4) & 1) << 2)) << 4));
#pragma unroll
    for (int i = 0; i < 18; ++i) {
      int pi = kh * 36 + i * 2 + wm;
      glds16s(Wo + (size_t)pi * (8 * 1152) + ct * 128, voffW, lbase + pi * 1024);
    }
  }
  const char* ob = O + (size_t)(rt * 2 + wm) * 36 * 1024 + lane * 16;
  bf16x8 of[18];
#pragma unroll
  for (int t = 0; t < 16; ++t) of[t] = *(const bf16x8*)(ob + (kh * 16 + t) * 1024);
#pragma unroll
  for (int u = 0; u < 2; ++u) of[16 + u] = *(const bf16x8*)(ob + (32 + kh * 2 + u) * 1024);
  u32x4 pv[8];
  {
    const char* pb = P + ((size_t)(rt * 2 + wm) * 18 + ct * 2 + kh) * 2048 + lane * 16;
#pragma unroll
    for (int sp = 0; sp < 4; ++sp)
#pragma unroll
      for (int gp = 0; gp < 2; ++gp) pv[sp * 2 + gp] = *(const u32x4*)(pb + (size_t)sp * 64 * 18 * 2048 + gp * 1024);
  }
  const float gg = G[rt * 64 + wm * 32 + r];
  u32x2 bb[4];
#pragma unroll
  for (int i = 0; i < 4; ++i) bb[i] = *(const u32x2*)(b2 + (ct * 64 + kh * 32 + 8 * i + 4 * h) * 2);
  SB();
  STAMP(3, 1);
  WAIT_VM(0);
  STAMP(3, 2);
  BARRIER();
  STAMP(3, 3);
  const int q = (lane >> 2) & 3, p = lane & 3, g = (lane >> 4) & 1;
  f32x16 acc[2];
#pragma unroll
  for (int i = 0; i < 16; ++i) { acc[0][i] = 0.f; acc[1][i] = 0.f; }
  const char* lb = lds + kh * (288 * 128) + g * 32 + p * 8;
#pragma unroll
  for (int t = 0; t < 16; ++t) {
    int r0 = 18 * t + 4 * h + q, r1 = r0 + 8;
#pragma unroll
    for (int n = 0; n < 2; ++n) {
      s16x4 lo = tr_read(lb + r0 * 128 + ((n ^ ((r0 >> 1) & 1)) << 6));
      s16x4 hi = tr_read(lb + r1 * 128 + ((n ^ ((r1 >> 1) & 1)) << 6));
      acc[n] = MFMA32(cat8(lo, hi), of[t], acc[n]);
    }
  }
#pragma unroll
  for (int u = 0; u < 2; ++u) {
    int r0 = (8 * u + 4 * h + (q >> 1)) * 18 + 16 + (q & 1), r1 = r0 + 36;
#pragma unroll
    for (int n = 0; n < 2; ++n) {
      s16x4 lo = tr_read(lb + r0 * 128 + ((n ^ ((r0 >> 1) & 1)) << 6));
      s16x4 hi = tr_read(lb + r1 * 128 + ((n ^ ((r1 >> 1) & 1)) << 6));
      acc[n] = MFMA32(cat8(lo, hi), of[16 + u], acc[n]);
    }
  }
  STAMP(3, 4);
  f32x16 ps;
#pragma unroll
  for (int i = 0; i < 4; ++i) {
    ps[4 * i] = __uint_as_float(bb[i][0] << 16);
    ps[4 * i + 1] = __uint_as_float(bb[i][0] & 0xffff0000u);
    ps[4 * i + 2] = __uint_as_float(bb[i][1] << 16);
    ps[4 * i + 3] = __uint_as_float(bb[i][1] & 0xffff0000u);
  }
#pragma unroll
  for (int sp = 0; sp < 4; ++sp)
#pragma unroll
    for (int gp = 0; gp < 2; ++gp)
#pragma unroll
      for (int i = 0; i < 4; ++i) {
        unsigned w = pv[sp * 2 + gp][i];
        ps[8 * gp + 2 * i] += __uint_as_float(w << 16);
        ps[8 * gp + 2 * i + 1] += __uint_as_float(w & 0xffff0000u);
      }
  __syncthreads();
  {
    char* ex = lds + wave * 4096 + lane * 16;
#pragma unroll
    for (int c = 0; c < 4; ++c) {
      f32x4 v;
#pragma unroll
      for (int j = 0; j < 4; ++j) v[j] = kh ? acc[0][4 * c + j] : acc[1][4 * c + j];
      *(f32x4*)(ex + c * 1024) = v;
    }
  }
  __syncthreads();
  f32x16 a;
  {
    const char* ex = lds + (wave ^ 2) * 4096 + lane * 16;
#pragma unroll
    for (int c = 0; c < 4; ++c) {
      f32x4 v = *(const f32x4*)(ex + c * 1024);
#pragma unroll
      for (int j = 0; j < 4; ++j) a[4 * c + j] = v[j] + (kh ? acc[1][4 * c + j] : acc[0][4 * c + j]);
    }
  }
  const float og = 1.0f - gg;
  int row = rt * 64 + wm * 32 + r;
  char* orow = out + (size_t)row * 1152 + (ct * 64 + kh * 32) * 2 + h * 16;
  u32x2 o2[4];
#pragma unroll
  for (int gq = 0; gq < 4; ++gq) {
    float v[4];
#pragma unroll
    for (int j = 0; j < 4; ++j) v[j] = gg * a[4 * gq + j] + og * ps[4 * gq + j];
    o2[gq][0] = pk2(v[0], v[1]);
    o2[gq][1] = pk2(v[2], v[3]);
  }
#pragma unroll
  for (int gq = 0; gq < 4; gq += 2) {
    auto r0 = __builtin_amdgcn_permlane32_swap(o2[gq][0], o2[gq + 1][0], false, false);
    auto r1 = __builtin_amdgcn_permlane32_swap(o2[gq][1], o2[gq + 1][1], false, false);
    u32x4 w = {r0[0], r1[0], r0[1], r1[1]};
    *(u32x4*)(orow + gq * 16) = w;
  }
  STAMP(3, 5);
  STAMP(3, 6);
  STAMP(3, 7);
}

extern "C" void kernel_launch(void* const* d_in, const int* in_sizes, int n_in, void* d_out, int out_size, void* d_ws,
                              size_t ws_size, hipStream_t stream) {
  const char* X = (const char*)d_in[0];
  const char* Wqkv = (const char*)d_in[1];
  const char* Wo = (const char*)d_in[2];
  const char* W1 = (const char*)d_in[4];
  const unsigned short* b1 = (const unsigned short*)d_in[5];
  const char* W2 = (const char*)d_in[6];
  const char* b2 = (const char*)d_in[7];
  const char* gw = (const char*)d_in[8];
  char* ws = (char*)d_ws;
  char* T = ws + WS_T;
  char* H = ws + WS_H;
  char* O = ws + WS_O;
  char* P = ws + WS_P;
  float* G = (float*)(ws + WS_G);
  kernelA<<<256, 512, 0, stream>>>(X, Wqkv, W1, b1, gw, T, H, G);
  kernelB<<<512, 512, 0, stream>>>(T, O, H, W2, P);
  kernelC<<<288, 256, 0, stream>>>(O, Wo, P, G, b2, (char*)d_out);
}
```

```cpp
#include <hip/hip_runtime.h>
#include <stdint.h>

typedef __attribute__((ext_vector_type(8))) short bf16x8;
typedef __attribute__((ext_vector_type(4))) short s16x4;
typedef __attribute__((ext_vector_type(2))) __bf16 bf16x2_t;
typedef __attribute__((ext_vector_type(2))) float f32x2;
typedef __attribute__((ext_vector_type(4))) float f32x4;
typedef __attribute__((ext_vector_type(16))) float f32x16;
typedef __attribute__((ext_vector_type(4))) unsigned u32x4;
typedef __attribute__((ext_vector_type(2))) unsigned u32x2;
typedef __attribute__((address_space(3))) s16x4* lds_s16x4_p;
typedef __attribute__((address_space(3))) char* lds_p;

#define STAMP(k, i)
#define DI __device__ __forceinline__
#define MFMA32(a, b, c) __builtin_amdgcn_mfma_f32_32x32x16_bf16((a), (b), (c), 0, 0, 0)
#define MFMA32K8(a, b, c) __builtin_amdgcn_mfma_f32_32x32x8bf16_1k((a), (b), (c), 0, 0, 0)
#define WAIT_VM(n) asm volatile("s_waitcnt vmcnt(" #n ")" ::: "memory")
#define BARRIER() __builtin_amdgcn_s_barrier()

constexpr int S_ = 2048, D_ = 576, F_ = 4608, NQKV = 1728;
constexpr int TSTEP = 640;
constexpr int TP = 1736 * 16;
constexpr int OEX = 64 * 32 * 1024;
constexpr size_t WS_T = 0;
constexpr size_t WS_H = 8u << 20;
constexpr size_t WS_O = 28u << 20;
constexpr size_t WS_P = 32u << 20;
constexpr size_t WS_G = 31u << 20;

DI unsigned pk2(float a, float b) {
  f32x2 v = {a, b};
  bf16x2_t r = __builtin_convertvector(v, bf16x2_t);
  return __builtin_bit_cast(unsigned, r);
}
DI float dot2bf(unsigned a, unsigned b, float acc) {
  return __builtin_amdgcn_fdot2_f32_bf16(__builtin_bit_cast(bf16x2_t, a), __builtin_bit_cast(bf16x2_t, b), acc, false);
}
DI float bf2f(unsigned short u) { return __uint_as_float(((unsigned)u) << 16); }
DI s16x4 tr_read(const char* p) {
  return __builtin_amdgcn_ds_read_tr16_b64_v4i16((lds_s16x4_p)(p));
}
DI void glds16(const char* g, char* l) {
  __builtin_amdgcn_global_load_lds((const unsigned*)g, (unsigned*)l, 16, 0, 0);
}
DI float xor32(float v) { return __shfl_xor(v, 32, 64); }

DI void glds16s(const char* sbase, unsigned voff, unsigned lds_dst) {
  unsigned keep;
  asm volatile("s_mov_b32 %0, m0\n\ts_mov_b32 m0, %3\n\ts_nop 0\n\tglobal_load_lds_dwordx4 %1, %2\n\ts_mov_b32 m0, %0"
               : "=&s"(keep)
               : "v"(voff), "s"(sbase), "s"(lds_dst)
               : "memory");
}
DI unsigned lds_addr(const void* p) { return (unsigned)(uintptr_t)(lds_p)p; }

DI void st16(void* p, u32x4 v) { asm volatile("global_store_dwordx4 %0, %1, off sc1\n\ts_nop 1" ::"v"(p), "v"(v) : "memory"); }
DI void st4(void* p, unsigned v) { asm volatile("global_store_dword %0, %1, off sc1" ::"v"(p), "v"(v) : "memory"); }

template <int I>
struct IC { static constexpr int v = I; };
template <int I, int N, class F>
DI void static_for(F&& f) {
  if constexpr (I < N) {
    f(IC<I>{});
    static_for<I + 1, N>(f);
  }
}
template <int N>
DI void wait_vm() { asm volatile("s_waitcnt vmcnt(%0)" ::"i"(N) : "memory"); }
#define SB() __builtin_amdgcn_sched_barrier(0)

template <int MB, int NBW>
struct Frags {
  bf16x8 x[MB];
  bf16x8 w[NBW];
  bf16x8 g;
};
#define MFMA4(a, b, c) __builtin_amdgcn_mfma_f32_4x4x4bf16_1k((a), (b), (c), 0, 0, 0)
DI bf16x8 cat8(s16x4 lo, s16x4 hi) { return __builtin_shufflevector(lo, hi, 0, 1, 2, 3, 4, 5, 6, 7); }

constexpr int A_XSLOT = 32768;
constexpr int A_WBASE = 3 * A_XSLOT;
template <int U>
constexpr int a_cw(int s, int grp) { return U == 4 ? 2 : (1 + (((s & 1) == grp) ? 1 : 0)); }
template <int U, int NWS>
constexpr int a_inflight(int j, int grp) {
  int cnt = 0;
  for (int t = j - 1; t >= -4; --t) {
    if ((t & 1) == 0) {
      int J = t / 2 + 2;
      if (t < 0) J = (t + 4) / 2;
      if (J >= 0 && J < 9) {
        if (J <= ((j + 1) >> 1)) return cnt;
        cnt += 4;
      }
    }
    int s = t + NWS - 1;
    if (s >= 0 && s < 18) {
      if (s <= j + 1) return cnt;
      cnt += a_cw<U>(s, grp);
    }
  }
  return cnt;
}

template <int U, int NWS, bool TRANS, int STATS>
DI void gemmA(char* lds, const char* __restrict__ Xb, int row0, const char* __restrict__ Wb, int ldw,
              const char* __restrict__ gw, int wave, int lane, int wm, int wn, f32x16 (&acc)[2][U],
              f32x4 (&sx)[2], f32x4 (&sxx)[2], f32x4 (&cs)[U]) {
  constexpr int NBW = U, NM = 2 * U, WSLOT = U * 4096;
  constexpr int GWOFF = A_WBASE + NWS * WSLOT;
  const unsigned lbase = lds_addr(lds);
  const int grp = wave >> 2;
  unsigned voffX[2], voffW;
  {
    int rl = lane >> 3;
#pragma unroll
    for (int i = 0; i < 2; ++i) {
      int row = wave * 32 + 8 * i + rl;
      int c = (lane & 7) ^ ((row >> 1) & 7);
      voffX[i] = (unsigned)((row0 + row) * 1152 + c * 16);
    }
    int c = (lane & 7) ^ (((rl >> 1) & 1) << 2);
    voffW = (unsigned)(rl * ldw + c * 16);
  }
  const int pi0 = wave, pi1 = U == 4 ? wave + 8 : 8 + (wave & 3);
  const int kb0 = pi0 / U, u0 = pi0 - kb0 * U, kb1 = pi1 / U, u1 = pi1 - kb1 * U;
  const char* wsrc0 = Wb + (size_t)kb0 * 8 * ldw + u0 * 128;
  const char* wsrc1 = Wb + (size_t)kb1 * 8 * ldw + u1 * 128;
  auto issueX = [&](int J, int i) {
    glds16s(Xb + J * 128 + (i >> 1) * (16 * 1152), voffX[i & 1], lbase + (J % 3) * A_XSLOT + (wave * 4 + i) * 1024);
  };
  auto issueW = [&](int s, int i) {
    unsigned dst = lbase + A_WBASE + (s % NWS) * WSLOT;
    if (i == 0) glds16s(wsrc0 + (size_t)s * 32 * ldw, voffW, dst + pi0 * 1024);
    else if (U == 4 || (s & 1) == grp) glds16s(wsrc1 + (size_t)s * 32 * ldw, voffW, dst + pi1 * 1024);
  };
  auto wait_top = [&](auto Jc) {
    constexpr int j = decltype(Jc)::v;
    constexpr int n0 = a_inflight<U, NWS>(j, 0), n1 = a_inflight<U, NWS>(j, 1);
    if constexpr (n0 == n1) wait_vm<n0>();
    else { if (grp == 0) wait_vm<n0>(); else wait_vm<n1>(); }
  };
  const int r = lane & 31, h = lane >> 5;
  const int q = (lane >> 2) & 3, p = lane & 3, g = (lane >> 4) & 1;
  const int xa = (wm * 64 + r) * 128 + ((h ^ ((r >> 1) & 7)) << 4);
  int wa[NBW];
#pragma unroll
  for (int nb = 0; nb < NBW; ++nb) {
    int b = wn * U + nb;
    wa[nb] = A_WBASE + (h * U + (b >> 1)) * 1024 + q * 128 + (((b & 1) ^ (q >> 1)) << 6) + g * 32 + p * 8;
  }
  const s16x4 ONES = {0x3F80, 0x3F80, 0x3F80, 0x3F80};

  STAMP(0, 0);
  if (STATS == 2) {
    if (wave == 0) glds16s(gw, lane * 16, lbase + GWOFF);
    else if (wave == 1 && lane < 8) glds16s(gw + 1024, lane * 16, lbase + GWOFF + 1024);
  }
  static_for<0, 4>([&](auto Tc) {
    constexpr int t = decltype(Tc)::v - 4;
    constexpr int s = t + NWS - 1;
    if constexpr (s >= 0) { issueW(s, 0); issueW(s, 1); }
    if constexpr ((t & 1) == 0) {
#pragma unroll
      for (int i = 0; i < 4; ++i) issueX((t + 4) / 2, i);
    }
  });
  wait_top(IC<-1>{});
  BARRIER();
  STAMP(0, 1);
  auto rd = [&](Frags<2, NBW>& f, s16x4 (&lo)[NBW], int j, int s, int n) {
    const char* xb = lds + ((j >> 1) % 3) * A_XSLOT;
    const char* wb = lds + (j % NWS) * WSLOT + 2 * s * U * 1024;
    const int ks = (j & 1) * 2 + s;
    if (n == 0) f.x[0] = *(const bf16x8*)(xb + (xa ^ (ks << 5)));
    else if (n == 2 * NBW + 1) {
      f.x[1] = *(const bf16x8*)(xb + (xa ^ (ks << 5)) + 4096);
      if (STATS == 2) f.g = *(const bf16x8*)(lds + GWOFF + (2 * j + s) * 32 + h * 16);
    }
    else if (n & 1) lo[(n - 1) >> 1] = tr_read(wb + wa[(n - 1) >> 1]);
    else f.w[(n - 2) >> 1] = cat8(lo[(n - 2) >> 1], tr_read(wb + wa[(n - 2) >> 1] + 512));
  };
  Frags<2, NBW> f0, f1;
  s16x4 lo[NBW];
#pragma unroll
  for (int n = 0; n < 2 * NBW + 2; ++n) rd(f0, lo, 0, 0, n);

  auto half = [&](auto Jc, auto Hc, Frags<2, NBW>& cur, Frags<2, NBW>& nxt) {
    constexpr int j = decltype(Jc)::v, hf = decltype(Hc)::v;
    constexpr int nj = hf ? j + 1 : j, ns = hf ? 0 : 1;
    static_for<0, NM>([&](auto Mc) {
      constexpr int m = decltype(Mc)::v;
      constexpr int mb = m / NBW, nb = m % NBW;
      if (TRANS) acc[mb][nb] = MFMA32(cur.w[nb], cur.x[mb], acc[mb][nb]);
      else acc[mb][nb] = MFMA32(cur.x[mb], cur.w[nb], acc[mb][nb]);
      if constexpr (nj < 18) {
        if constexpr (m == 0) { rd(nxt, lo, nj, ns, 0); rd(nxt, lo, nj, ns, 1); rd(nxt, lo, nj, ns, 2); }
        else if constexpr (m < NBW) { rd(nxt, lo, nj, ns, 2 * m + 1); rd(nxt, lo, nj, ns, 2 * m + 2); }
        else if constexpr (m == NBW) rd(nxt, lo, nj, ns, 2 * NBW + 1);
      }
      if constexpr (STATS != 0 && m == NBW + 1) {
        if (hf == wn) {
#pragma unroll
          for (int b = 0; b < 2; ++b) {
            s16x4 xl = __builtin_shufflevector(cur.x[b], cur.x[b], 0, 1, 2, 3);
            s16x4 xh = __builtin_shufflevector(cur.x[b], cur.x[b], 4, 5, 6, 7);
            if (STATS == 1) {
              sx[b] = MFMA4(ONES, xl, sx[b]);
              sxx[b] = MFMA4(xl, xl, sxx[b]);
              sx[b] = MFMA4(ONES, xh, sx[b]);
              sxx[b] = MFMA4(xh, xh, sxx[b]);
            } else {
              sx[b] = MFMA4(__builtin_shufflevector(cur.g, cur.g, 0, 1, 2, 3), xl, sx[b]);
              sxx[b] = MFMA4(__builtin_shufflevector(cur.g, cur.g, 4, 5, 6, 7), xh, sxx[b]);
            }
          }
          asm volatile("" : "+v"(sx[0]), "+v"(sxx[0]), "+v"(sx[1]), "+v"(sxx[1]));
        }
      }
      if constexpr (STATS == 1 && m == 2) {
        if (((2 * j + hf) & 3) == wm) {
#pragma unroll
          for (int b = 0; b < NBW; ++b) cs[b] = MFMA4(ONES, __builtin_shufflevector(cur.w[b], cur.w[b], 0, 1, 2, 3), cs[b]);
#pragma unroll
          for (int b = 0; b < NBW; ++b) cs[b] = MFMA4(ONES, __builtin_shufflevector(cur.w[b], cur.w[b], 4, 5, 6, 7), cs[b]);
        }
      }
      if constexpr (m == 1 || m == 3 || m == 5) {
        constexpr int e = hf * 3 + (m >> 1);
        constexpr int s = j + NWS - 1;
        if constexpr (e < 2) { if constexpr (s < 18) issueW(s, e); }
        else if constexpr ((j & 1) == 0 && j / 2 + 2 < 9) issueX(j / 2 + 2, e - 2);
      }
      SB();
    });
  };
  static_for<0, 18>([&](auto Jc) {
    if (decltype(Jc)::v == 4) STAMP(0, 2);
    if (decltype(Jc)::v == 9) STAMP(0, 3);
    if (decltype(Jc)::v == 14) STAMP(0, 4);
    wait_top(Jc);
    BARRIER();
    SB();
    half(Jc, IC<0>{}, f0, f1);
    half(Jc, IC<1>{}, f1, f0);
  });
  STAMP(0, 5);
}

template <int U, int NWS, int STATS>
DI void qkv_role(char* lds, const char* X, const char* Wqkv, const char* gw, char* T, float* G, int rt, int unit0,
                 int wave, int lane, int wm, int wn) {
  f32x16 acc[2][U];
#pragma unroll
  for (int a = 0; a < 2; ++a)
#pragma unroll
    for (int b = 0; b < U; ++b)
#pragma unroll
      for (int i = 0; i < 16; ++i) acc[a][b][i] = 0.f;
  const f32x4 z4 = {0.f, 0.f, 0.f, 0.f};
  f32x4 sx[2] = {z4, z4}, sxx[2] = {z4, z4}, cs[U];
  gemmA<U, NWS, false, STATS>(lds, X, rt * 256, Wqkv + unit0 * 128, NQKV * 2, gw, wave, lane, wm, wn, acc, sx, sxx, cs);
  const int r = lane & 31, h = lane >> 5;
  if (STATS == 2) {
    if (unit0 == 0) {
      float* gp = (float*)(lds + A_WBASE + NWS * U * 4096 + 1152);
#pragma unroll
      for (int b = 0; b < 2; ++b) {
        float v = sx[b][0] + sxx[b][0];
        v += xor32(v);
        if (h == 0) gp[wn * 256 + wm * 64 + b * 32 + r] = v;
      }
      __syncthreads();
      if (wave < 4) {
        float v = gp[wave * 64 + lane] + gp[256 + wave * 64 + lane];
        G[rt * 256 + wave * 64 + lane] = 1.0f / (1.0f + __builtin_amdgcn_exp2f(-1.4426950408889634f * v));
      }
    }
  }
  const float QSCALE = 0.23570226039551584f * 1.4426950408889634f;
#pragma unroll
  for (int nb = 0; nb < U; ++nb) {
    int n0 = unit0 * 64 + (wn * U + nb) * 32;
    float sc = n0 < 576 ? QSCALE : 1.0f;
    char* base = T + (size_t)h * TP + (n0 + r) * 16;
#pragma unroll
    for (int mb = 0; mb < 2; ++mb)
#pragma unroll
      for (int s = 0; s < 2; ++s) {
        int step = (rt * 256 + wm * 64 + mb * 32) / 16 + s;
        u32x4 v;
#pragma unroll
        for (int i = 0; i < 4; ++i) v[i] = pk2(acc[mb][nb][8 * s + 2 * i] * sc, acc[mb][nb][8 * s + 2 * i + 1] * sc);
        st16(base + (size_t)step * (2 * TP), v);
      }
  }
  if (U == 4 && unit0 == 23 && wave < 4) {
    int idx = wave * 64 + lane;
    unsigned c = (idx & 7) == 0 ? 0x3F803F80u : 0u;
    u32x4 v = {c, c, c, c};
    st16(T + (size_t)(rt * 32 + (idx >> 3)) * TP + (1728 + (idx & 7)) * 16, v);
  }
}

DI unsigned short f2bf_rn(float f) { return (unsigned short)(pk2(f, 0.f) & 0xffffu); }

DI void mlp1_role(char* lds, const char* X, const char* W1, const unsigned short* b1, char* H, int rt, int ct,
                  int wave, int lane, int wm, int wn) {
  constexpr int NBW = 3;
  f32x16 acc[2][NBW];
#pragma unroll
  for (int a = 0; a < 2; ++a)
#pragma unroll
    for (int b = 0; b < NBW; ++b)
#pragma unroll
      for (int i = 0; i < 16; ++i) acc[a][b][i] = 0.f;
  const f32x4 z4 = {0.f, 0.f, 0.f, 0.f};
  f32x4 sx[2] = {z4, z4}, sxx[2] = {z4, z4}, cs[NBW] = {z4, z4, z4};
  const int r = lane & 31, h = lane >> 5;
  unsigned short bb[NBW];
#pragma unroll
  for (int nb = 0; nb < NBW; ++nb) bb[nb] = b1[ct * 192 + (wn * 3 + nb) * 32 + r];
  gemmA<3, 5, true, 1>(lds, X, rt * 256, W1 + ct * 384, F_ * 2, nullptr, wave, lane, wm, wn, acc, sx, sxx, cs);
  BARRIER();
  float* rowst = (float*)(lds + 4096);
  float* colp = (float*)lds;
#pragma unroll
  for (int b = 0; b < 2; ++b) {
    float a = sx[b][0];
    int i4 = lane & 3;
    float q2 = i4 == 0 ? sxx[b][0] : (i4 == 1 ? sxx[b][1] : (i4 == 2 ? sxx[b][2] : sxx[b][3]));
    a += xor32(a);
    q2 += xor32(q2);
    if (h == 0) {
      f32x2 v = {a, q2};
      *(f32x2*)(rowst + (wn * 256 + wm * 64 + b * 32 + r) * 2) = v;
    }
  }
#pragma unroll
  for (int nb = 0; nb < NBW; ++nb) {
    float c = cs[nb][0] + xor32(cs[nb][0]);
    if (h == 0) colp[wm * 192 + (wn * 3 + nb) * 32 + r] = c;
  }
  __syncthreads();
  bf16x8 wx[NBW], xx[2];
  float ca[2], cb[2], sd[2];
#pragma unroll
  for (int nb = 0; nb < NBW; ++nb) {
    int n = (wn * 3 + nb) * 32 + r;
    float c = -(colp[n] + colp[192 + n] + colp[384 + n] + colp[576 + n]);
    unsigned short chi = f2bf_rn(c);
    unsigned short clo = f2bf_rn(c - bf2f(chi));
    bf16x8 w = {(short)chi, (short)clo, (short)chi, (short)bb[nb], (short)bb[nb], 0, 0, 0};
    bf16x8 z = {0, 0, 0, 0, 0, 0, 0, 0};
    wx[nb] = h ? z : w;
  }
  const float C0 = -2.0f * 1.4426950408889634f * 0.7978845608028654f;
  const float C1 = C0 * 0.044715f;
#pragma unroll
  for (int mb = 0; mb < 2; ++mb) {
    f32x2 st = *(const f32x2*)(rowst + (wm * 64 + mb * 32 + r) * 2) +
               *(const f32x2*)(rowst + (256 + wm * 64 + mb * 32 + r) * 2);
    st[0] *= (1.0f / 576.0f);
    st[1] = __builtin_fmaxf(st[1] * (1.0f / 576.0f) - st[0] * st[0], 0.f);
    float rstd = __builtin_amdgcn_rsqf(st[1] + 1e-5f);
    float sdev = (st[1] + 1e-5f) * rstd;
    unsigned short mhi = f2bf_rn(st[0]);
    unsigned short mlo = f2bf_rn(st[0] - bf2f(mhi));
    unsigned short shi = f2bf_rn(sdev);
    unsigned short slo = f2bf_rn(sdev - bf2f(shi));
    bf16x8 x = {(short)mhi, (short)mhi, (short)mlo, (short)shi, (short)slo, 0, 0, 0};
    bf16x8 z = {0, 0, 0, 0, 0, 0, 0, 0};
    xx[mb] = h ? z : x;
    ca[mb] = C0 * rstd;
    cb[mb] = C1 * rstd * rstd * rstd;
    sd[mb] = sdev;
  }
#pragma unroll
  for (int mb = 0; mb < 2; ++mb)
#pragma unroll
    for (int nb = 0; nb < NBW; ++nb) acc[mb][nb] = MFMA32(wx[nb], xx[mb], acc[mb][nb]);
#pragma unroll
  for (int mb = 0; mb < 2; ++mb) {
    char* hb = H + ((size_t)(rt * 8 + wm * 2 + mb) * 288 + ct * 12 + wn * 6) * 1024 + lane * 16;
    const f32x2 a2 = {ca[mb], ca[mb]}, b2 = {cb[mb], cb[mb]}, s2 = {sd[mb], sd[mb]};
#pragma unroll
    for (int nb = 0; nb < NBW; ++nb)
#pragma unroll
      for (int gp = 0; gp < 2; ++gp) {
        u32x4 w;
#pragma unroll
        for (int i = 0; i < 4; ++i) {
          f32x2 v = {acc[mb][nb][8 * gp + 2 * i], acc[mb][nb][8 * gp + 2 * i + 1]};
          f32x2 t = v * __builtin_elementwise_fma(v * v, b2, a2);
          f32x2 e = {__builtin_amdgcn_exp2f(t[0]), __builtin_amdgcn_exp2f(t[1])};
          f32x2 d = __builtin_elementwise_fma(e, s2, s2);
          f32x2 rr = {__builtin_amdgcn_rcpf(d[0]), __builtin_amdgcn_rcpf(d[1])};
          f32x2 o = v * rr;
          w[i] = pk2(o[0], o[1]);
        }
        st16(hb + (nb * 2 + gp) * 1024, w);
      }
  }
}

__global__ __launch_bounds__(512, 2) void kernelA(const char* __restrict__ X, const char* __restrict__ Wqkv,
                                                   const char* __restrict__ W1, const unsigned short* __restrict__ b1,
                                                   const char* __restrict__ gw, char* __restrict__ T,
                                                   char* __restrict__ H, float* __restrict__ G) {
  __shared__ __attribute__((aligned(16))) char lds[163840];
  const int wg = blockIdx.x;
  const int xcd = wg & 7, idx = wg >> 3;
  const int rt = (xcd & 1) * 4 + (idx & 3);
  const int ct = (xcd >> 1) + 4 * (idx >> 2);
  const int wave = __builtin_amdgcn_readfirstlane(threadIdx.x >> 6);
  const int lane = threadIdx.x & 63;
  const int wm = wave & 3, wn = wave >> 2;
  if (ct < 5) qkv_role<3, 5, 2>(lds, X, Wqkv, gw, T, G, rt, ct * 3, wave, lane, wm, wn);
  else if (ct < 8) qkv_role<4, 4, 0>(lds, X, Wqkv, gw, T, G, rt, 15 + (ct - 5) * 4, wave, lane, wm, wn);
  else mlp1_role(lds, X, W1, b1, H, rt, ct - 8, wave, lane, wm, wn);
  STAMP(0, 6);
  STAMP(0, 7);
}

typedef __attribute__((address_space(3))) const bf16x8* lds_bf16x8_p;
DI s16x4 trl(lds_p p) { return __builtin_amdgcn_ds_read_tr16_b64_v4i16((lds_s16x4_p)p); }
DI bf16x8 ld8(lds_p p) { return *(lds_bf16x8_p)p; }
DI f32x16 qk_tile(lds_p kb, lds_p ke, bf16x8 qf, s16x4 qe, f32x16 c) {
  s16x4 lo = trl(kb);
  s16x4 hi = trl(kb + 64);
  s16x4 ex = trl(ke);
  bf16x8 kf = __builtin_shufflevector(lo, hi, 0, 1, 2, 3, 4, 5, 6, 7);
  f32x16 s = MFMA32(kf, qf, c);
  s = MFMA32K8(ex, qe, s);
  return s;
}
DI f32x16 pv_tile(lds_p vb, const f32x16& pe, f32x16 o) {
  bf16x8 v0 = ld8(vb);
  bf16x8 v1 = ld8(vb + TSTEP);
  u32x4 p0, p1;
#pragma unroll
  for (int i = 0; i < 4; ++i) {
    p0[i] = pk2(pe[2 * i], pe[2 * i + 1]);
    p1[i] = pk2(pe[8 + 2 * i], pe[8 + 2 * i + 1]);
  }
  o = MFMA32(v0, __builtin_bit_cast(bf16x8, p0), o);
  o = MFMA32(v1, __builtin_bit_cast(bf16x8, p1), o);
  return o;
}
struct KFrag { bf16x8 f; s16x4 e; };
struct VFrag { bf16x8 v0, v1; };
DI KFrag load_k(lds_p kb, lds_p ke) {
  KFrag k;
  s16x4 lo = trl(kb);
  s16x4 hi = trl(kb + 64);
  k.e = trl(ke);
  k.f = __builtin_shufflevector(lo, hi, 0, 1, 2, 3, 4, 5, 6, 7);
  return k;
}
DI VFrag load_v(lds_p vb) {
  VFrag v;
  v.v0 = ld8(vb);
  v.v1 = ld8(vb + TSTEP);
  return v;
}
#define EXP2(x) __builtin_amdgcn_exp2f(x)
DI void tile_step(const f32x16& s, f32x16& sn, const KFrag& k, const VFrag& v, bf16x8 qf, s16x4 qe,
                  const f32x16& negm, f32x16& o) {
  u32x4 p0, p1;
  p0[0] = pk2(EXP2(s[0]), EXP2(s[1]));
  p0[1] = pk2(EXP2(s[2]), EXP2(s[3]));
  SB();
  f32x16 t = MFMA32(k.f, qf, negm);
  SB();
  p0[2] = pk2(EXP2(s[4]), EXP2(s[5]));
  p0[3] = pk2(EXP2(s[6]), EXP2(s[7]));
  SB();
  o = MFMA32(v.v0, __builtin_bit_cast(bf16x8, p0), o);
  SB();
  p1[0] = pk2(EXP2(s[8]), EXP2(s[9]));
  p1[1] = pk2(EXP2(s[10]), EXP2(s[11]));
  SB();
  t = MFMA32K8(k.e, qe, t);
  SB();
  p1[2] = pk2(EXP2(s[12]), EXP2(s[13]));
  p1[3] = pk2(EXP2(s[14]), EXP2(s[15]));
  SB();
  o = MFMA32(v.v1, __builtin_bit_cast(bf16x8, p1), o);
  SB();
  sn = t;
}
DI float rowmax16(const f32x16& s) {
  float m = __builtin_fmaxf(s[0], s[1]);
#pragma unroll
  for (int i = 2; i < 16; ++i) m = __builtin_fmaxf(m, s[i]);
  return __builtin_fmaxf(m, xor32(m));
}

constexpr int AT_SLOT = 20480;
constexpr int at_cnt(int c, int grp) { return 2 + (((c & 1) == grp) ? 1 : 0); }
constexpr int at_off(int tt) { return ((tt >> 3) & 1) * AT_SLOT + (tt & 7) * 1280; }

DI void attn_role(char* lds, const char* __restrict__ T, char* __restrict__ O, int wg, int wave, int lane) {
  STAMP(1, 0);
  const int head = (wg & 7) * 4 + ((wg >> 3) & 3);
  const int qt = wg >> 5;
  const int r = lane & 31, h = lane >> 5;
  const int grp = wave >> 2;
  const unsigned lbase = lds_addr(lds);
  const int qi = qt * 256 + wave * 32 + r;
  const unsigned short* qp =
      (const unsigned short*)(T + (size_t)((qi >> 4) * 2 + ((qi >> 2) & 1)) * TP + head * 288) + (qi & 3) +
      4 * ((qi >> 3) & 1);
  unsigned short qv[8], qx[2];
#pragma unroll
  for (int j = 0; j < 8; ++j) qv[j] = qp[(8 * h + j) * 8];
  qx[0] = qp[16 * 8];
  qx[1] = qp[17 * 8];
  const int p1 = wave + 8, p2 = 16 + (wave & 3);
  auto mkoff = [&](int pc) {
    int isv = pc >= 10;
    int sl = (pc - 10 * isv) * 64 + lane;
    int seg = sl / 20, row = sl - seg * 20;
    int slot = row < 18 ? 576 + 576 * isv + head * 18 + row : 1728 + row - 18;
    return (unsigned)(seg * TP + slot * 16);
  };
  const unsigned v0 = mkoff(wave), v1 = mkoff(p1), v2 = mkoff(p2);
  auto issue = [&](int c) {
    unsigned dst = lbase + (c & 3) * AT_SLOT;
    const char* src = T + (size_t)c * (32 * TP);
    glds16s(src, v0, dst + wave * 1024);
    glds16s(src, v1, dst + p1 * 1024);
    if ((c & 1) == grp) glds16s(src, v2, dst + p2 * 1024);
  };
  issue(0);
  bf16x8 qf;
#pragma unroll
  for (int j = 0; j < 8; ++j) qf[j] = (short)qv[j];
  s16x4 qe;
  qe[0] = h ? (short)0 : (short)qx[0];
  qe[1] = h ? (short)0 : (short)qx[1];
  qe[2] = 0;
  qe[3] = 0;
  asm volatile("" : "+v"(qf), "+v"(qe));
  wait_vm<0>();
  issue(1);
  issue(2);
  BARRIER();
  STAMP(1, 1);

  const int q = (lane >> 2) & 3, p = lane & 3, g = (lane >> 4) & 1;
  const lds_p L = (lds_p)lds;
  const lds_p kb = L + g * TSTEP + (p & 1) * (TSTEP / 2) + (8 * h + q) * 16 + (p >> 1) * 8;
  const lds_p ke = L + g * TSTEP + (p & 1) * (TSTEP / 2) + (16 + (q & 1)) * 16 + (p >> 1) * 8;
  const lds_p vb = L + 10240 + h * (TSTEP / 2) + (r < 19 ? r : 19) * 16;
  lds_p kb2 = kb + 2 * AT_SLOT, ke2 = ke + 2 * AT_SLOT, vb2 = vb + 2 * AT_SLOT;
  asm volatile("" : "+v"(kb2), "+v"(ke2), "+v"(vb2));

  f32x16 zero;
#pragma unroll
  for (int i = 0; i < 16; ++i) zero[i] = 0.f;
  f32x16 o = zero;
  f32x16 sA = qk_tile(kb, ke, qf, qe, zero);
  float m0 = rowmax16(sA);
  f32x16 negm;
#pragma unroll
  for (int i = 0; i < 16; ++i) { negm[i] = -m0; sA[i] -= m0; }
  KFrag kA, kB;
  VFrag vA, vB;
  kB = load_k(kb + 1280, ke + 1280);
  vA = load_v(vb);
  f32x16 sB;
#pragma unroll 1
  for (int cc = 0; cc < 8; cc += 4) {
    static_for<0, 32>([&](auto Tc) {
      constexpr int tt = decltype(Tc)::v, ci = tt >> 3;
      if constexpr ((tt & 7) == 6) {
        if (cc == 0 || ci < 2) {
          if (grp == 0) wait_vm<at_cnt(ci + 2, 0)>(); else wait_vm<at_cnt(ci + 2, 1)>();
        } else if (ci == 2) wait_vm<0>();
        if (cc == 0 || ci < 3) BARRIER();
        if (cc == 0 || ci == 0) issue(cc + ci + 3);
      }
      constexpr int t1 = (tt + 1) & 31, t2 = (tt + 2) & 31;
      const lds_p kp = (t2 < 16 ? kb : kb2) + at_off(t2);
      const lds_p ep = (t2 < 16 ? ke : ke2) + at_off(t2);
      const lds_p vp = (t1 < 16 ? vb : vb2) + at_off(t1);
      if constexpr ((tt & 1) == 0) {
        kA = load_k(kp, ep);
        vB = load_v(vp);
        SB();
        tile_step(sA, sB, kB, vA, qf, qe, negm, o);
      } else {
        kB = load_k(kp, ep);
        vA = load_v(vp);
        SB();
        tile_step(sB, sA, kA, vB, qf, qe, negm, o);
      }
    });
    if (cc == 0) STAMP(1, 2);
  }
  STAMP(1, 5);
  float l = xor32(o[10]);
  l = h ? l : o[10];
  bool bad = !(l < 1.0e30f);
#pragma unroll
  for (int i = 0; i < 10; ++i) bad = bad || !(__builtin_fabsf(o[i]) < 1.0e37f);
  {
    int* fl = (int*)(lds + AT_SLOT);
    int anyb = __any(bad) ? 1 : 0;
    if (lane == 0) fl[wave] = anyb;
    __syncthreads();
    int v = fl[lane & 7];
    bad = __any(v != 0);
  }
  if (bad) {
    float m = -3.0e38f;
    o = zero;
#pragma unroll 1
    for (int c = 0; c < 8; ++c) {
      __syncthreads();
      {
        const char* src = T + (size_t)c * (32 * TP);
        glds16s(src, v0, lbase + wave * 1024);
        glds16s(src, v1, lbase + p1 * 1024);
        if (grp == 0) glds16s(src, v2, lbase + p2 * 1024);
      }
      wait_vm<0>();
      __syncthreads();
#pragma unroll 1
      for (int t = 0; t < 8; ++t) {
        f32x16 s = qk_tile(kb + t * 1280, ke + t * 1280, qf, qe, zero);
        float mn = __builtin_fmaxf(m, rowmax16(s));
        float alpha = __builtin_amdgcn_exp2f(m - mn);
        m = mn;
        f32x16 pe;
#pragma unroll
        for (int i = 0; i < 16; ++i) { pe[i] = __builtin_amdgcn_exp2f(s[i] - mn); o[i] *= alpha; }
        o = pv_tile(vb + t * 1280, pe, o);
      }
    }
    l = xor32(o[10]);
    l = h ? l : o[10];
  }
  float inv = 1.0f / l;
  const int ob = (qt * 8 + wave) * 32 + head;
  u32x4 ov;
#pragma unroll
  for (int i = 0; i < 4; ++i) ov[i] = pk2(o[2 * i] * inv, o[2 * i + 1] * inv);
  st16(O + (size_t)ob * 1024 + lane * 16, ov);
  if (h == 0) st4(O + OEX + ob * 128 + r * 4, pk2(o[8] * inv, o[9] * inv));
  STAMP(1, 6);
  STAMP(1, 7);
}

constexpr int M_SLOT = 20480, M_IT = 36;
template <int U>
constexpr int m_cnt(int s, int grp) { return s >= M_IT ? 0 : (U == 2 ? 2 : 2 + (((s & 1) == grp) ? 1 : 0)); }

template <int NBW, int U>
DI void mlp2_role(char* lds, const char* __restrict__ H, const char* __restrict__ W2, char* __restrict__ P, int rt,
                  int sp, int unit0, int boff, int wave, int lane) {
  f32x16 acc[NBW];
#pragma unroll
  for (int b = 0; b < NBW; ++b)
#pragma unroll
    for (int i = 0; i < 16; ++i) acc[b][i] = 0.f;
  const unsigned lbase = lds_addr(lds);
  const int wm = wave & 3, kh = wave >> 2;
  const unsigned voffX = lane * 16;
  const unsigned voffW = (unsigned)((lane >> 3) * 1152 + (((lane & 7) ^ (((lane >> 4) & 1) << 2)) << 4));
  const char* xsrc = H + ((size_t)(rt * 4 + wm) * 288 + sp * 72 + kh) * 1024;
  const char* wbase = W2 + (size_t)sp * 1152 * 1152 + unit0 * 128;
  const int p0 = wave, p2 = 8 + (wave & 3);
  const char* ws0 = wbase + (p0 / U) * (8 * 1152) + (p0 % U) * 128;
  const char* ws2 = wbase + (p2 / U) * (8 * 1152) + (p2 % U) * 128;
  auto issue = [&](int s, int sl, int e) {
    unsigned dst = lbase + sl * M_SLOT;
    if (e == 0) glds16s(xsrc + s * 2048, voffX, dst + wave * 1024);
    else if (e == 1) glds16s(ws0 + s * (32 * 1152), voffW, dst + 8192 + p0 * 1024);
    else if (U == 3 && (sl & 1) == kh) glds16s(ws2 + s * (32 * 1152), voffW, dst + 8192 + p2 * 1024);
  };
  const int h = lane >> 5;
  const int q = (lane >> 2) & 3, p = lane & 3, g = (lane >> 4) & 1;
  const lds_p L = (lds_p)lds;
  const lds_p xa = L + wave * 1024 + lane * 16;
  lds_p wa[2];
#pragma unroll
  for (int e = 0; e < 2; ++e) {
    int b = boff + e;
    wa[e] = L + 8192 + (2 * kh * U + (b >> 1)) * 1024 + (4 * h + q) * 128 + (((b & 1) ^ (q >> 1)) << 6) + g * 32 + p * 8;
  }
  lds_p xa2 = xa + 2 * M_SLOT, wa2[2] = {wa[0] + 2 * M_SLOT, wa[1] + 2 * M_SLOT};
  asm volatile("" : "+v"(xa2), "+v"(wa2[0]), "+v"(wa2[1]));
  STAMP(2, 0);
#pragma unroll
  for (int s = 0; s < 4; ++s)
#pragma unroll
    for (int e = 0; e < 3; ++e) issue(s, s, e);
  {
    constexpr int n0 = m_cnt<U>(1, 0) + m_cnt<U>(2, 0) + m_cnt<U>(3, 0), n1 = m_cnt<U>(1, 1) + m_cnt<U>(2, 1) + m_cnt<U>(3, 1);
    if (kh == 0) wait_vm<n0>(); else wait_vm<n1>();
  }
  BARRIER();
  STAMP(2, 1);
  bf16x8 w[NBW], x0, x1;
  auto rdx = [&](int sl) { return ld8((sl < 2 ? xa : xa2) + (sl & 1) * M_SLOT); };
  auto rdw = [&](int sl, int m) {
    const lds_p a = (sl < 2 ? wa[m & 1] : wa2[m & 1]) + (sl & 1) * M_SLOT + (m >> 1) * 1024;
    return cat8(trl(a), trl(a + U * 1024));
  };
  x0 = rdx(0);
#pragma unroll
  for (int m = 0; m < NBW; ++m) w[m] = rdw(0, m);
  auto iter = [&](int jb, auto Lc, auto Ic) {
    constexpr int last = decltype(Lc)::v, i = decltype(Ic)::v;
    constexpr int js = last ? 32 + i : i;
    constexpr int n0 = m_cnt<U>(js + 2, 0) + m_cnt<U>(js + 3, 0), n1 = m_cnt<U>(js + 2, 1) + m_cnt<U>(js + 3, 1);
    if constexpr (n0 == n1) wait_vm<n0>();
    else { if (kh == 0) wait_vm<n0>(); else wait_vm<n1>(); }
    asm volatile("s_waitcnt lgkmcnt(0)" ::: "memory");
    BARRIER();
    SB();
    bf16x8& xc = (i & 1) ? x1 : x0;
    bf16x8& xn = (i & 1) ? x0 : x1;
    static_for<0, NBW>([&](auto Mc) {
      constexpr int m = decltype(Mc)::v;
      acc[m] = MFMA32(w[m], xc, acc[m]);
      SB();
      if constexpr (js + 1 < M_IT) {
        if constexpr (m == 0) xn = rdx((i + 1) & 3);
        w[m] = rdw((i + 1) & 3, m);
      }
      if constexpr (!last && m >= 1 && m <= 3) issue(jb + i + 4, i, m - 1);
      SB();
    });
  };
#pragma unroll 1
  for (int jb = 0; jb < 32; jb += 4) {
    static_for<0, 4>([&](auto Ic) { iter(jb, IC<0>{}, Ic); });
    if (jb == 8) STAMP(2, 2);
    if (jb == 16) STAMP(2, 3);
    if (jb == 24) STAMP(2, 4);
  }
  static_for<0, 4>([&](auto Ic) { iter(32, IC<1>{}, Ic); });
  STAMP(2, 5);
  constexpr int NA = (NBW + 1) / 2;
  char* ex = lds + wm * (NBW * 4096) + lane * 16;
  if (kh == 0) {
#pragma unroll
    for (int b = NA; b < NBW; ++b)
#pragma unroll
      for (int c = 0; c < 4; ++c) {
        f32x4 v = {acc[b][4 * c], acc[b][4 * c + 1], acc[b][4 * c + 2], acc[b][4 * c + 3]};
        *(f32x4*)(ex + b * 4096 + c * 1024) = v;
      }
  } else {
#pragma unroll
    for (int b = 0; b < NA; ++b)
#pragma unroll
      for (int c = 0; c < 4; ++c) {
        f32x4 v = {acc[b][4 * c], acc[b][4 * c + 1], acc[b][4 * c + 2], acc[b][4 * c + 3]};
        *(f32x4*)(ex + b * 4096 + c * 1024) = v;
      }
  }
  __syncthreads();
  char* pb = P + ((size_t)(sp * 64 + rt * 4 + wm) * 18 + unit0 * 2 + boff) * 2048 + lane * 16;
  auto fin = [&](int b) {
#pragma unroll
    for (int gp = 0; gp < 2; ++gp) {
      f32x4 a = *(const f32x4*)(ex + b * 4096 + (2 * gp) * 1024);
      f32x4 c = *(const f32x4*)(ex + b * 4096 + (2 * gp + 1) * 1024);
      u32x4 v;
      v[0] = pk2(acc[b][8 * gp] + a[0], acc[b][8 * gp + 1] + a[1]);
      v[1] = pk2(acc[b][8 * gp + 2] + a[2], acc[b][8 * gp + 3] + a[3]);
      v[2] = pk2(acc[b][8 * gp + 4] + c[0], acc[b][8 * gp + 5] + c[1]);
      v[3] = pk2(acc[b][8 * gp + 6] + c[2], acc[b][8 * gp + 7] + c[3]);
      st16(pb + b * 2048 + gp * 1024, v);
    }
  };
  if (kh == 0) {
#pragma unroll
    for (int b = 0; b < NA; ++b) fin(b);
  } else {
#pragma unroll
    for (int b = NA; b < NBW; ++b) fin(b);
  }
  STAMP(2, 6);
  STAMP(2, 7);
}

__global__ __launch_bounds__(512, 4) void kernelB(const char* __restrict__ T, char* __restrict__ O,
                                                   const char* __restrict__ H, const char* __restrict__ W2,
                                                   char* __restrict__ P) {
  __shared__ __attribute__((aligned(16))) char lds[81920];
  const int wave = __builtin_amdgcn_readfirstlane(threadIdx.x >> 6);
  const int lane = threadIdx.x & 63;
  const int bid = blockIdx.x;
  if (bid < 256) {
    attn_role(lds, T, O, bid, wave, lane);
  } else {
    const int wg = bid - 256;
    const int xcd = wg & 7, idx = wg >> 3;
    const int sp = xcd >> 1, ct = idx & 3, rt = (xcd & 1) * 8 + (idx >> 2);
    if (ct == 0 || ct == 3) mlp2_role<4, 2>(lds, H, W2, P, rt, sp, ct == 0 ? 0 : 7, 0, wave, lane);
    else mlp2_role<5, 3>(lds, H, W2, P, rt, sp, 2 * ct, ct - 1, wave, lane);
  }
}

__global__ __launch_bounds__(256, 2) void kernelC(const char* __restrict__ O, const char* __restrict__ Wo,
                                                   const char* __restrict__ P, const float* __restrict__ G,
                                                   const char* __restrict__ b2, char* __restrict__ out) {
  __shared__ __attribute__((aligned(16))) char lds[73728];
  const int wg = blockIdx.x;
  const int idx = wg >> 3;
  const int ct = idx % 9, rt = (wg & 7) * 4 + idx / 9;
  const int wave = __builtin_amdgcn_readfirstlane(threadIdx.x >> 6);
  const int lane = threadIdx.x & 63;
  const int wm = wave & 1, kh = wave >> 1;
  const int r = lane & 31, h = lane >> 5;
  const unsigned lbase = lds_addr(lds);
  STAMP(3, 0);
  {
    const unsigned voffW = (unsigned)((lane >> 3) * 1152 + (((lane & 7) ^ (((lane >> 4) & 1) << 2)) << 4));
#pragma unroll
    for (int i = 0; i < 18; ++i) {
      int pi = kh * 36 + i * 2 + wm;
      glds16s(Wo + (size_t)pi * (8 * 1152) + ct * 128, voffW, lbase + pi * 1024);
    }
  }
  const char* ob = O + (size_t)((rt * 2 + wm) * 32 + kh * 16) * 1024 + lane * 16;
  bf16x8 of[18];
#pragma unroll
  for (int t = 0; t < 16; ++t) of[t] = *(const bf16x8*)(ob + t * 1024);
  {
    const char* oe = O + OEX + ((rt * 2 + wm) * 32 + kh * 16 + 4 * h) * 128 + r * 4;
#pragma unroll
    for (int u = 0; u < 2; ++u) {
      u32x4 v;
#pragma unroll
      for (int j = 0; j < 4; ++j) v[j] = *(const unsigned*)(oe + (8 * u + j) * 128);
      of[16 + u] = __builtin_bit_cast(bf16x8, v);
    }
  }
  u32x4 pv[8];
  {
    const char* pb = P + ((size_t)(rt * 2 + wm) * 18 + ct * 2 + kh) * 2048 + lane * 16;
#pragma unroll
    for (int sp = 0; sp < 4; ++sp)
#pragma unroll
      for (int gp = 0; gp < 2; ++gp) pv[sp * 2 + gp] = *(const u32x4*)(pb + (size_t)sp * 64 * 18 * 2048 + gp * 1024);
  }
  const float gg = G[rt * 64 + wm * 32 + r];
  u32x2 bb[4];
#pragma unroll
  for (int i = 0; i < 4; ++i) bb[i] = *(const u32x2*)(b2 + (ct * 64 + kh * 32 + 8 * i + 4 * h) * 2);
  SB();
  STAMP(3, 1);
  WAIT_VM(0);
  STAMP(3, 2);
  BARRIER();
  STAMP(3, 3);
  const int q = (lane >> 2) & 3, p = lane & 3, g = (lane >> 4) & 1;
  f32x16 acc[2];
#pragma unroll
  for (int i = 0; i < 16; ++i) { acc[0][i] = 0.f; acc[1][i] = 0.f; }
  const char* lb = lds + kh * (288 * 128) + g * 32 + p * 8;
#pragma unroll
  for (int t = 0; t < 16; ++t) {
    int r0 = 18 * t + 4 * h + q, r1 = r0 + 8;
#pragma unroll
    for (int n = 0; n < 2; ++n) {
      s16x4 lo = tr_read(lb + r0 * 128 + ((n ^ ((r0 >> 1) & 1)) << 6));
      s16x4 hi = tr_read(lb + r1 * 128 + ((n ^ ((r1 >> 1) & 1)) << 6));
      acc[n] = MFMA32(cat8(lo, hi), of[t], acc[n]);
    }
  }
#pragma unroll
  for (int u = 0; u < 2; ++u) {
    int r0 = (8 * u + 4 * h + (q >> 1)) * 18 + 16 + (q & 1), r1 = r0 + 36;
#pragma unroll
    for (int n = 0; n < 2; ++n) {
      s16x4 lo = tr_read(lb + r0 * 128 + ((n ^ ((r0 >> 1) & 1)) << 6));
      s16x4 hi = tr_read(lb + r1 * 128 + ((n ^ ((r1 >> 1) & 1)) << 6));
      acc[n] = MFMA32(cat8(lo, hi), of[16 + u], acc[n]);
    }
  }
  STAMP(3, 4);
  f32x16 ps;
#pragma unroll
  for (int i = 0; i < 4; ++i) {
    ps[4 * i] = __uint_as_float(bb[i][0] << 16);
    ps[4 * i + 1] = __uint_as_float(bb[i][0] & 0xffff0000u);
    ps[4 * i + 2] = __uint_as_float(bb[i][1] << 16);
    ps[4 * i + 3] = __uint_as_float(bb[i][1] & 0xffff0000u);
  }
#pragma unroll
  for (int sp = 0; sp < 4; ++sp)
#pragma unroll
    for (int gp = 0; gp < 2; ++gp)
#pragma unroll
      for (int i = 0; i < 4; ++i) {
        unsigned w = pv[sp * 2 + gp][i];
        ps[8 * gp + 2 * i] += __uint_as_float(w << 16);
        ps[8 * gp + 2 * i + 1] += __uint_as_float(w & 0xffff0000u);
      }
  __syncthreads();
  {
    char* ex = lds + wave * 4096 + lane * 16;
#pragma unroll
    for (int c = 0; c < 4; ++c) {
      f32x4 v;
#pragma unroll
      for (int j = 0; j < 4; ++j) v[j] = kh ? acc[0][4 * c + j] : acc[1][4 * c + j];
      *(f32x4*)(ex + c * 1024) = v;
    }
  }
  __syncthreads();
  f32x16 a;
  {
    const char* ex = lds + (wave ^ 2) * 4096 + lane * 16;
#pragma unroll
    for (int c = 0; c < 4; ++c) {
      f32x4 v = *(const f32x4*)(ex + c * 1024);
#pragma unroll
      for (int j = 0; j < 4; ++j) a[4 * c + j] = v[j] + (kh ? acc[1][4 * c + j] : acc[0][4 * c + j]);
    }
  }
  const float og = 1.0f - gg;
  int row = rt * 64 + wm * 32 + r;
  char* orow = out + (size_t)row * 1152 + (ct * 64 + kh * 32) * 2 + h * 16;
  u32x2 o2[4];
#pragma unroll
  for (int gq = 0; gq < 4; ++gq) {
    float v[4];
#pragma unroll
    for (int j = 0; j < 4; ++j) v[j] = gg * a[4 * gq + j] + og * ps[4 * gq + j];
    o2[gq][0] = pk2(v[0], v[1]);
    o2[gq][1] = pk2(v[2], v[3]);
  }
#pragma unroll
  for (int gq = 0; gq < 4; gq += 2) {
    auto r0 = __builtin_amdgcn_permlane32_swap(o2[gq][0], o2[gq + 1][0], false, false);
    auto r1 = __builtin_amdgcn_permlane32_swap(o2[gq][1], o2[gq + 1][1], false, false);
    u32x4 w = {r0[0], r1[0], r0[1], r1[1]};
    *(u32x4*)(orow + gq * 16) = w;
  }
  STAMP(3, 5);
  STAMP(3, 6);
  STAMP(3, 7);
}

extern "C" void kernel_launch(void* const* d_in, const int* in_sizes, int n_in, void* d_out, int out_size, void* d_ws,
                              size_t ws_size, hipStream_t stream) {
  const char* X = (const char*)d_in[0];
  const char* Wqkv = (const char*)d_in[1];
  const char* Wo = (const char*)d_in[2];
  const char* W1 = (const char*)d_in[4];
  const unsigned short* b1 = (const unsigned short*)d_in[5];
  const char* W2 = (const char*)d_in[6];
  const char* b2 = (const char*)d_in[7];
  const char* gw = (const char*)d_in[8];
  char* ws = (char*)d_ws;
  char* T = ws + WS_T;
  char* H = ws + WS_H;
  char* O = ws + WS_O;
  char* P = ws + WS_P;
  float* G = (float*)(ws + WS_G);
  kernelA<<<256, 512, 0, stream>>>(X, Wqkv, W1, b1, gw, T, H, G);
  kernelB<<<512, 512, 0, stream>>>(T, O, H, W2, P);
  kernelC<<<288, 256, 0, stream>>>(O, Wo, P, G, b2, (char*)d_out);
}
```

```cpp
#include <hip/hip_runtime.h>
#include <stdint.h>

typedef __attribute__((ext_vector_type(8))) short bf16x8;
typedef __attribute__((ext_vector_type(4))) short s16x4;
typedef __attribute__((ext_vector_type(2))) __bf16 bf16x2_t;
typedef __attribute__((ext_vector_type(2))) float f32x2;
typedef __attribute__((ext_vector_type(4))) float f32x4;
typedef __attribute__((ext_vector_type(16))) float f32x16;
typedef __attribute__((ext_vector_type(4))) unsigned u32x4;
typedef __attribute__((ext_vector_type(2))) unsigned u32x2;
typedef __attribute__((address_space(3))) s16x4* lds_s16x4_p;
typedef __attribute__((address_space(3))) char* lds_p;
typedef __attribute__((address_space(3))) const bf16x8* lds_bf16x8_p;

#define STAMP(k, i)
#define DI __device__ __forceinline__
#define MFMA32(a, b, c) __builtin_amdgcn_mfma_f32_32x32x16_bf16((a), (b), (c), 0, 0, 0)
#define MFMA32K8(a, b, c) __builtin_amdgcn_mfma_f32_32x32x8bf16_1k((a), (b), (c), 0, 0, 0)
#define WAIT_VM(n) asm volatile("s_waitcnt vmcnt(" #n ")" ::: "memory")
#define BARRIER() __builtin_amdgcn_s_barrier()
#define SB() __builtin_amdgcn_sched_barrier(0)

constexpr int S_ = 2048, D_ = 576, F_ = 4608, NQKV = 1728;
constexpr int TSTEP = 640;
constexpr int TP = 1736 * 16;
constexpr int OEX = 64 * 32 * 1024;
constexpr size_t WS_T = 0;
constexpr size_t WS_H = 8u << 20;
constexpr size_t WS_O = 28u << 20;
constexpr size_t WS_G = 31u << 20;
constexpr size_t WS_FL = (31u << 20) + (64u << 10);
constexpr size_t WS_P = 32u << 20;
constexpr size_t WS_XN = 44u << 20;

DI unsigned pk2(float a, float b) {
  f32x2 v = {a, b};
  bf16x2_t r = __builtin_convertvector(v, bf16x2_t);
  return __builtin_bit_cast(unsigned, r);
}
DI float bf2f(unsigned short u) { return __uint_as_float(((unsigned)u) << 16); }
DI s16x4 tr_read(const char* p) {
  return __builtin_amdgcn_ds_read_tr16_b64_v4i16((lds_s16x4_p)(p));
}
DI s16x4 trl(lds_p p) { return __builtin_amdgcn_ds_read_tr16_b64_v4i16((lds_s16x4_p)p); }
DI bf16x8 ld8(lds_p p) { return *(lds_bf16x8_p)p; }
DI float xor32(float v) { return __shfl_xor(v, 32, 64); }

DI void glds16s(const char* sbase, unsigned voff, unsigned lds_dst) {
  unsigned keep;
  asm volatile("s_mov_b32 %0, m0\n\ts_mov_b32 m0, %3\n\ts_nop 0\n\tglobal_load_lds_dwordx4 %1, %2\n\ts_mov_b32 m0, %0"
               : "=&s"(keep)
               : "v"(voff), "s"(sbase), "s"(lds_dst)
               : "memory");
}
DI void glds16c(const char* sbase, unsigned voff, unsigned lds_dst) {
  unsigned keep;
  asm volatile("s_mov_b32 %0, m0\n\ts_mov_b32 m0, %3\n\ts_nop 0\n\tglobal_load_lds_dwordx4 %1, %2 sc1\n\ts_mov_b32 m0, %0"
               : "=&s"(keep)
               : "v"(voff), "s"(sbase), "s"(lds_dst)
               : "memory");
}
DI unsigned lds_addr(const void* p) { return (unsigned)(uintptr_t)(lds_p)p; }

DI void st16(void* p, u32x4 v) { asm volatile("global_store_dwordx4 %0, %1, off sc1\n\ts_nop 1" ::"v"(p), "v"(v) : "memory"); }
DI void st4(void* p, unsigned v) { asm volatile("global_store_dword %0, %1, off sc1" ::"v"(p), "v"(v) : "memory"); }

DI int fresh_lane() {
  int l;
  asm volatile("v_mbcnt_lo_u32_b32 %0, -1, 0\n\tv_mbcnt_hi_u32_b32 %0, -1, %0" : "=v"(l));
  return l;
}
template <int I>
struct IC { static constexpr int v = I; };
template <int I, int N, class F>
DI void static_for(F&& f) {
  if constexpr (I < N) {
    f(IC<I>{});
    static_for<I + 1, N>(f);
  }
}
template <int N>
DI void wait_vm() { asm volatile("s_waitcnt vmcnt(%0)" ::"i"(N) : "memory"); }
DI bf16x8 cat8(s16x4 lo, s16x4 hi) { return __builtin_shufflevector(lo, hi, 0, 1, 2, 3, 4, 5, 6, 7); }

constexpr int Q_SLOT = 40960;
DI void qkv_role(char* lds, const char* __restrict__ X, const char* __restrict__ Wqkv, char* __restrict__ T, int rt,
                 int ct, int joff, int wave, int lane) {
  f32x16 acc[2];
#pragma unroll
  for (int i = 0; i < 16; ++i) { acc[0][i] = 0.f; acc[1][i] = 0.f; }
  const unsigned lbase = lds_addr(lds);
  unsigned voffX[2];
#pragma unroll
  for (int i = 0; i < 2; ++i) {
    int row = wave * 32 + 8 * i + (lane >> 3);
    int c = (lane & 7) ^ ((row >> 1) & 7);
    voffX[i] = (unsigned)((rt * 256 + row) * 1152 + c * 16);
  }
  const unsigned voffW = (unsigned)((lane >> 3) * 3456 + (((lane & 7) ^ (((lane >> 4) & 1) << 2)) << 4));
  const char* wsrc = Wqkv + (size_t)wave * 8 * 3456 + ct * 128;
  auto issue = [&](int J, int e) {
    unsigned dst = lbase + (J & 3) * Q_SLOT;
    unsigned Jr = J + joff;
    Jr = Jr >= 9 ? Jr - 9 : Jr;
    if (e == 0) glds16s(wsrc + Jr * (64u * 3456u), voffW, dst + 32768 + wave * 1024);
    else glds16s(X + Jr * 128u + ((e - 1) >> 1) * (16 * 1152), voffX[(e - 1) & 1], dst + (wave * 4 + e - 1) * 1024);
  };
  STAMP(0, 0);
#pragma unroll
  for (int J = 0; J < 3; ++J)
#pragma unroll
    for (int e = 0; e < 5; ++e) issue(J, e);
  const int r = lane & 31, h = lane >> 5;
  const int q = (lane >> 2) & 3, p = lane & 3, g = (lane >> 4) & 1;
  const lds_p L = (lds_p)lds;
  lds_p xa[4], wa[2][2];
#pragma unroll
  for (int ks = 0; ks < 4; ++ks) xa[ks] = L + (wave * 32 + r) * 128 + (((2 * ks + h) ^ ((r >> 1) & 7)) << 4);
#pragma unroll
  for (int b = 0; b < 2; ++b) {
    wa[0][b] = L + 32768 + h * 1024 + q * 128 + ((b ^ (q >> 1)) << 6) + g * 32 + p * 8;
    wa[1][b] = wa[0][b] + 2 * Q_SLOT;
  }
  lds_p xb[4];
#pragma unroll
  for (int ks = 0; ks < 4; ++ks) xb[ks] = xa[ks] + 2 * Q_SLOT;
  asm volatile("" : "+v"(xb[0]), "+v"(xb[1]), "+v"(xb[2]), "+v"(xb[3]), "+v"(wa[1][0]), "+v"(wa[1][1]));
  wait_vm<10>();
  BARRIER();
  STAMP(0, 1);
  bf16x8 fx[2], fw[2][2];
  auto rd = [&](int buf, int J, int ks) {
    const int sl = J & 3;
    fx[buf] = ld8((sl < 2 ? xa[ks] : xb[ks]) + (sl & 1) * Q_SLOT);
#pragma unroll
    for (int b = 0; b < 2; ++b) {
      const lds_p a = wa[sl >> 1][b] + (sl & 1) * Q_SLOT + ks * 2048;
      fw[buf][b] = cat8(trl(a), trl(a + 512));
    }
  };
  rd(0, 0, 0);
  static_for<0, 9>([&](auto Jc) {
    constexpr int J = decltype(Jc)::v;
    if constexpr (J + 1 < 9) {
      wait_vm<(J + 2 < 9) ? 5 : 0>();
      BARRIER();
    }
    if (J == 3) STAMP(0, 2);
    if (J == 6) STAMP(0, 3);
    SB();
    static_for<0, 4>([&](auto Kc) {
      constexpr int ks = decltype(Kc)::v, cur = ks & 1;
      acc[0] = MFMA32(fx[cur], fw[cur][0], acc[0]);
      SB();
      if constexpr (ks < 3) rd(cur ^ 1, J, ks + 1);
      else if constexpr (J + 1 < 9) rd(cur ^ 1, J + 1, 0);
      SB();
      acc[1] = MFMA32(fx[cur], fw[cur][1], acc[1]);
      SB();
      if constexpr (J + 3 < 9) {
        if constexpr (ks == 0) { issue(J + 3, 0); issue(J + 3, 1); }
        else issue(J + 3, ks + 1);
      }
      SB();
    });
  });
  STAMP(0, 5);
  const float QSCALE = 0.23570226039551584f * 1.4426950408889634f;
  const float sc = ct < 9 ? QSCALE : 1.0f;
#pragma unroll
  for (int nb = 0; nb < 2; ++nb) {
    char* base = T + (size_t)h * TP + (ct * 64 + nb * 32 + r) * 16;
#pragma unroll
    for (int s = 0; s < 2; ++s) {
      int step = rt * 16 + wave * 2 + s;
      u32x4 v;
#pragma unroll
      for (int i = 0; i < 4; ++i) v[i] = pk2(acc[nb][8 * s + 2 * i] * sc, acc[nb][8 * s + 2 * i + 1] * sc);
      st16(base + (size_t)step * (2 * TP), v);
    }
  }
  if (ct == 26 && wave < 4) {
    int idx = wave * 64 + lane;
    unsigned c = (idx & 7) == 0 ? 0x3F803F80u : 0u;
    u32x4 v = {c, c, c, c};
    st16(T + (size_t)(rt * 32 + (idx >> 3)) * TP + (1728 + (idx & 7)) * 16, v);
  }
}

DI void ln_role(char* lds, const char* __restrict__ X, const char* __restrict__ gw, char* __restrict__ XN,
                float* __restrict__ G, char* __restrict__ FL, int id, int wave, int lane) {
  const int r = lane & 31, h = lane >> 5;
  const int rb = wave >> 2, kq = wave & 3;
  const int row = id * 64 + rb * 32 + r;
  u32x4 xv[9], gv[9];
#pragma unroll
  for (int i = 0; i < 9; ++i) xv[i] = *(const u32x4*)(X + (size_t)row * 1152 + ((kq * 9 + i) * 2 + h) * 16);
#pragma unroll
  for (int i = 0; i < 9; ++i) gv[i] = *(const u32x4*)(gw + ((kq * 9 + i) * 2 + h) * 16);
  if (wave == 0) {
    u32x4 z = {0u, 0u, 0u, 0u};
    st16(FL + id * 1024 + lane * 16, z);
  }
  float* red = (float*)lds;
  float s = 0.f, d = 0.f;
#pragma unroll
  for (int i = 0; i < 9; ++i)
#pragma unroll
    for (int j = 0; j < 4; ++j) {
      float a = __uint_as_float(xv[i][j] << 16), b = __uint_as_float(xv[i][j] & 0xffff0000u);
      s += a + b;
      d = __builtin_fmaf(a, __uint_as_float(gv[i][j] << 16), d);
      d = __builtin_fmaf(b, __uint_as_float(gv[i][j] & 0xffff0000u), d);
    }
  s += xor32(s);
  d += xor32(d);
  if (h == 0) { red[wave * 32 + r] = s; red[256 + wave * 32 + r] = d; }
  __syncthreads();
  const float* rs = red + rb * 128 + r;
  const float mean = (rs[0] + rs[32] + rs[64] + rs[96]) * (1.0f / 576.0f);
  if (kq == 0 && h == 0) {
    float v = rs[256] + rs[288] + rs[320] + rs[352];
    G[row] = 1.0f / (1.0f + __builtin_amdgcn_exp2f(-1.4426950408889634f * v));
  }
  float q2 = 0.f;
#pragma unroll
  for (int i = 0; i < 9; ++i)
#pragma unroll
    for (int j = 0; j < 4; ++j) {
      float a = __uint_as_float(xv[i][j] << 16) - mean, b = __uint_as_float(xv[i][j] & 0xffff0000u) - mean;
      q2 = __builtin_fmaf(a, a, q2);
      q2 = __builtin_fmaf(b, b, q2);
    }
  q2 += xor32(q2);
  if (h == 0) red[512 + wave * 32 + r] = q2;
  __syncthreads();
  const float var = (rs[512] + rs[544] + rs[576] + rs[608]) * (1.0f / 576.0f);
  const float rstd = 1.0f / __builtin_sqrtf(var + 1e-5f);
  char* dst = XN + ((size_t)(id * 2 + rb) * 36 + kq * 9) * 1024 + lane * 16;
#pragma unroll
  for (int i = 0; i < 9; ++i) {
    u32x4 v;
#pragma unroll
    for (int j = 0; j < 4; ++j) {
      float a = __uint_as_float(xv[i][j] << 16) - mean, b = __uint_as_float(xv[i][j] & 0xffff0000u) - mean;
      v[j] = pk2(a * rstd, b * rstd);
    }
    st16(dst + i * 1024, v);
  }
}

__global__ __launch_bounds__(512, 2) void kernelQ(const char* __restrict__ X, const char* __restrict__ Wqkv,
                                                   const char* __restrict__ gw, char* __restrict__ T,
                                                   char* __restrict__ XN, float* __restrict__ G,
                                                   char* __restrict__ FL) {
  __shared__ __attribute__((aligned(16))) char lds[163840];
  const int wg = blockIdx.x;
  const int wave = __builtin_amdgcn_readfirstlane(threadIdx.x >> 6);
  const int lane = threadIdx.x & 63;
  if (wg < 216) {
    const int xcd = wg & 7, t = (xcd >> 2) * 27 + (wg >> 3);
    const int joff = ((t >> 1) * 3 + (t & 1) * 4) % 9;
    qkv_role(lds, X, Wqkv, T, (xcd & 3) * 2 + (t & 1), t >> 1, joff, wave, lane);
  } else {
    STAMP(0, 0);
    ln_role(lds, X, gw, XN, G, FL, wg - 216, wave, lane);
  }
  STAMP(0, 6);
  STAMP(0, 7);
}

DI f32x16 qk_tile(lds_p kb, lds_p ke, bf16x8 qf, s16x4 qe, f32x16 c) {
  s16x4 lo = trl(kb);
  s16x4 hi = trl(kb + 64);
  s16x4 ex = trl(ke);
  bf16x8 kf = __builtin_shufflevector(lo, hi, 0, 1, 2, 3, 4, 5, 6, 7);
  f32x16 s = MFMA32(kf, qf, c);
  s = MFMA32K8(ex, qe, s);
  return s;
}
DI f32x16 pv_tile(lds_p vb, const f32x16& pe, f32x16 o) {
  bf16x8 v0 = ld8(vb);
  bf16x8 v1 = ld8(vb + TSTEP);
  u32x4 p0, p1;
#pragma unroll
  for (int i = 0; i < 4; ++i) {
    p0[i] = pk2(pe[2 * i], pe[2 * i + 1]);
    p1[i] = pk2(pe[8 + 2 * i], pe[8 + 2 * i + 1]);
  }
  o = MFMA32(v0, __builtin_bit_cast(bf16x8, p0), o);
  o = MFMA32(v1, __builtin_bit_cast(bf16x8, p1), o);
  return o;
}
struct KFrag { bf16x8 f; s16x4 e; };
struct VFrag { bf16x8 v0, v1; };
DI KFrag load_k(lds_p kb, lds_p ke) {
  KFrag k;
  s16x4 lo = trl(kb);
  s16x4 hi = trl(kb + 64);
  k.e = trl(ke);
  k.f = __builtin_shufflevector(lo, hi, 0, 1, 2, 3, 4, 5, 6, 7);
  return k;
}
DI VFrag load_v(lds_p vb) {
  VFrag v;
  v.v0 = ld8(vb);
  v.v1 = ld8(vb + TSTEP);
  return v;
}
#define EXP2(x) __builtin_amdgcn_exp2f(x)
#define PHI()
#define PLO()
DI void tile_step(const f32x16& s, f32x16& sn, const KFrag& k, const VFrag& v, bf16x8 qf, s16x4 qe,
                  const f32x16& negm, f32x16& o) {
  u32x4 p0, p1;
  p0[0] = pk2(EXP2(s[0]), EXP2(s[1]));
  p0[1] = pk2(EXP2(s[2]), EXP2(s[3]));
  SB();
  PHI();
  SB();
  f32x16 t = MFMA32(k.f, qf, negm);
  SB();
  PLO();
  SB();
  p0[2] = pk2(EXP2(s[4]), EXP2(s[5]));
  p0[3] = pk2(EXP2(s[6]), EXP2(s[7]));
  SB();
  PHI();
  SB();
  o = MFMA32(v.v0, __builtin_bit_cast(bf16x8, p0), o);
  SB();
  PLO();
  SB();
  p1[0] = pk2(EXP2(s[8]), EXP2(s[9]));
  p1[1] = pk2(EXP2(s[10]), EXP2(s[11]));
  SB();
  PHI();
  SB();
  t = MFMA32K8(k.e, qe, t);
  SB();
  PLO();
  SB();
  p1[2] = pk2(EXP2(s[12]), EXP2(s[13]));
  p1[3] = pk2(EXP2(s[14]), EXP2(s[15]));
  SB();
  PHI();
  SB();
  o = MFMA32(v.v1, __builtin_bit_cast(bf16x8, p1), o);
  SB();
  PLO();
  SB();
  sn = t;
}
DI float rowmax16(const f32x16& s) {
  float m = __builtin_fmaxf(s[0], s[1]);
#pragma unroll
  for (int i = 2; i < 16; ++i) m = __builtin_fmaxf(m, s[i]);
  return __builtin_fmaxf(m, xor32(m));
}

constexpr int AT_SLOT = 20480;
constexpr int at_cnt(int c, int grp) { return 2 + (((c & 1) == grp) ? 1 : 0); }
constexpr int at_off(int tt) { return ((tt >> 3) & 1) * AT_SLOT + (tt & 7) * 1280; }

DI void attn_role(char* lds, const char* __restrict__ T, char* __restrict__ O, int wg, int wave, int lane) {
  STAMP(1, 0);
  const int head = (wg & 7) * 4 + ((wg >> 3) & 3);
  const int qt = wg >> 5;
  const int r = lane & 31, h = lane >> 5;
  const int grp = wave >> 2;
  const unsigned lbase = lds_addr(lds);
  const int qi = qt * 256 + wave * 32 + r;
  const unsigned short* qp =
      (const unsigned short*)(T + (size_t)((qi >> 4) * 2 + ((qi >> 2) & 1)) * TP + head * 288) + (qi & 3) +
      4 * ((qi >> 3) & 1);
  unsigned short qv[8], qx[2];
#pragma unroll
  for (int j = 0; j < 8; ++j) qv[j] = qp[(8 * h + j) * 8];
  qx[0] = qp[16 * 8];
  qx[1] = qp[17 * 8];
  const int p1 = wave + 8, p2 = 16 + (wave & 3);
  auto mkoff = [&](int pc) {
    int isv = pc >= 10;
    int sl = (pc - 10 * isv) * 64 + lane;
    int seg = sl / 20, row = sl - seg * 20;
    int slot = row < 18 ? 576 + 576 * isv + head * 18 + row : 1728 + row - 18;
    return (unsigned)(seg * TP + slot * 16);
  };
  const unsigned v0 = mkoff(wave), v1 = mkoff(p1), v2 = mkoff(p2);
  auto issue = [&](int c) {
    unsigned dst = lbase + (c & 3) * AT_SLOT;
    const char* src = T + (size_t)c * (32 * TP);
    glds16s(src, v0, dst + wave * 1024);
    glds16s(src, v1, dst + p1 * 1024);
    if ((c & 1) == grp) glds16s(src, v2, dst + p2 * 1024);
  };
  issue(0);
  bf16x8 qf;
#pragma unroll
  for (int j = 0; j < 8; ++j) qf[j] = (short)qv[j];
  s16x4 qe;
  qe[0] = h ? (short)0 : (short)qx[0];
  qe[1] = h ? (short)0 : (short)qx[1];
  qe[2] = 0;
  qe[3] = 0;
  asm volatile("" : "+v"(qf), "+v"(qe));
  wait_vm<0>();
  issue(1);
  issue(2);
  BARRIER();
  STAMP(1, 1);

  const int q = (lane >> 2) & 3, p = lane & 3, g = (lane >> 4) & 1;
  const lds_p L = (lds_p)lds;
  const lds_p kb = L + g * TSTEP + (p & 1) * (TSTEP / 2) + (8 * h + q) * 16 + (p >> 1) * 8;
  const lds_p ke = L + g * TSTEP + (p & 1) * (TSTEP / 2) + (16 + (q & 1)) * 16 + (p >> 1) * 8;
  const lds_p vb = L + 10240 + h * (TSTEP / 2) + (r < 19 ? r : 19) * 16;
  lds_p kb2 = kb + 2 * AT_SLOT, ke2 = ke + 2 * AT_SLOT, vb2 = vb + 2 * AT_SLOT;
  asm volatile("" : "+v"(kb2), "+v"(ke2), "+v"(vb2));

  f32x16 zero;
#pragma unroll
  for (int i = 0; i < 16; ++i) zero[i] = 0.f;
  f32x16 o = zero;
  f32x16 sA = qk_tile(kb, ke, qf, qe, zero);
  float m0 = rowmax16(sA);
  f32x16 negm;
#pragma unroll
  for (int i = 0; i < 16; ++i) { negm[i] = -m0; sA[i] -= m0; }
  KFrag kA, kB;
  VFrag vA, vB;
  kB = load_k(kb + 1280, ke + 1280);
  vA = load_v(vb);
  f32x16 sB;
#pragma unroll 1
  for (int cc = 0; cc < 8; cc += 4) {
    static_for<0, 32>([&](auto Tc) {
      constexpr int tt = decltype(Tc)::v, ci = tt >> 3;
      if constexpr ((tt & 7) == 6) {
        if (cc == 0 || ci < 2) {
          if (grp == 0) wait_vm<at_cnt(ci + 2, 0)>(); else wait_vm<at_cnt(ci + 2, 1)>();
        } else if (ci == 2) wait_vm<0>();
        if (cc == 0 || ci < 3) BARRIER();
        if (cc == 0 || ci == 0) issue(cc + ci + 3);
      }
      constexpr int t1 = (tt + 1) & 31, t2 = (tt + 2) & 31;
      const lds_p kp = (t2 < 16 ? kb : kb2) + at_off(t2);
      const lds_p ep = (t2 < 16 ? ke : ke2) + at_off(t2);
      const lds_p vp = (t1 < 16 ? vb : vb2) + at_off(t1);
      if constexpr ((tt & 1) == 0) {
        kA = load_k(kp, ep);
        vB = load_v(vp);
        SB();
        tile_step(sA, sB, kB, vA, qf, qe, negm, o);
      } else {
        kB = load_k(kp, ep);
        vA = load_v(vp);
        SB();
        tile_step(sB, sA, kA, vB, qf, qe, negm, o);
      }
    });
    if (cc == 0) STAMP(1, 2);
  }
  STAMP(1, 5);
  float l = xor32(o[10]);
  l = h ? l : o[10];
  bool bad = !(l < 1.0e30f);
#pragma unroll
  for (int i = 0; i < 10; ++i) bad = bad || !(__builtin_fabsf(o[i]) < 1.0e37f);
  {
    int* fl = (int*)(lds + AT_SLOT);
    int anyb = __any(bad) ? 1 : 0;
    if (lane == 0) fl[wave] = anyb;
    __syncthreads();
    int v = fl[lane & 7];
    bad = __any(v != 0);
  }
  if (bad) {
    float m = -3.0e38f;
    o = zero;
#pragma unroll 1
    for (int c = 0; c < 8; ++c) {
      __syncthreads();
      {
        const char* src = T + (size_t)c * (32 * TP);
        glds16s(src, v0, lbase + wave * 1024);
        glds16s(src, v1, lbase + p1 * 1024);
        if (grp == 0) glds16s(src, v2, lbase + p2 * 1024);
      }
      wait_vm<0>();
      __syncthreads();
#pragma unroll 1
      for (int t = 0; t < 8; ++t) {
        f32x16 s = qk_tile(kb + t * 1280, ke + t * 1280, qf, qe, zero);
        float mn = __builtin_fmaxf(m, rowmax16(s));
        float alpha = __builtin_amdgcn_exp2f(m - mn);
        m = mn;
        f32x16 pe;
#pragma unroll
        for (int i = 0; i < 16; ++i) { pe[i] = __builtin_amdgcn_exp2f(s[i] - mn); o[i] *= alpha; }
        o = pv_tile(vb + t * 1280, pe, o);
      }
    }
    l = xor32(o[10]);
    l = h ? l : o[10];
  }
  float inv = 1.0f / l;
  const int ob = (qt * 8 + wave) * 32 + head;
  u32x4 ov;
#pragma unroll
  for (int i = 0; i < 4; ++i) ov[i] = pk2(o[2 * i] * inv, o[2 * i + 1] * inv);
  st16(O + (size_t)ob * 1024 + lane * 16, ov);
  if (h == 0) st4(O + OEX + ob * 128 + r * 4, pk2(o[8] * inv, o[9] * inv));
  STAMP(1, 6);
  STAMP(1, 7);
}

#define INV 0
#define MLP_PRIO 1
constexpr int M1_NS = 5;
constexpr int M_IT = 36;
template <int U2>
constexpr int m_cnt(int s, int grp) { return s >= M_IT ? 0 : (U2 == 2 ? 2 : 2 + (((s & 1) == grp) ? 1 : 0)); }

template <int U>
DI void mlp_track(char* lds, const char* __restrict__ XN, const char* __restrict__ W1,
                  const unsigned short* __restrict__ b1, char* H, const char* __restrict__ W2, char* __restrict__ P,
                  char* FLg, int rt, int sp, int j4, int koff, int wave, int lane) {
  constexpr int NBW = 9 - U, U2 = U == 5 ? 2 : 3;
  const bool up = j4 < 2;
  const int unit0 = sp * 18 + (U == 5 ? (up ? 0 : 13) : (up ? 5 : 9));
  const unsigned lbase = lds_addr(lds);
  const lds_p L = (lds_p)lds;
  const int wm = wave & 3, wn = wave >> 2;
  f32x16 acc[5];
#pragma unroll
  for (int b = 0; b < 5; ++b)
#pragma unroll
    for (int i = 0; i < 16; ++i) acc[b][i] = 0.f;
  bf16x8 w[5], x0, x1;
  {
    constexpr int WS = 2 * U * 1024, XB = M1_NS * WS, NP = 4 + 2 * U;
    const int h = lane >> 5;
    const unsigned voffW = (unsigned)((lane >> 3) * 9216 + (((lane & 7) ^ (((lane >> 4) & 1) << 2)) << 4));
    const unsigned voff0 = wave < 4 ? (unsigned)(lane * 16) : voffW;
    const int q0 = wave - 4, q1 = wave + 4;
    const char* wb = W1 + unit0 * 128;
    const char* src0 =
        wave < 4 ? XN + (size_t)(rt * 4 + wave) * (36 * 1024) : wb + (q0 / U) * (8 * 9216) + (q0 % U) * 128;
    const char* src1 = wb + (q1 / U) * (8 * 9216) + (q1 % U) * 128;
    const unsigned st0 = wave < 4 ? 1024u : 147456u;
    const bool two = wave + 8 < NP;
    const unsigned dst0 = lbase + (wave < 4 ? XB + wave * 1024 : q0 * 1024), ds0 = wave < 4 ? 4096u : (unsigned)WS;
    auto issue = [&](int s, int e) {
      int ks = s + koff;
      ks = ks >= 36 ? ks - 36 : ks;
      if (e == 0) glds16s(src0 + (unsigned)ks * st0, voff0, dst0 + (s % M1_NS) * ds0);
      else if (two) glds16s(src1 + (unsigned)ks * 147456u, voffW, lbase + (s % M1_NS) * WS + q1 * 1024);
    };
    auto waitn = [&](auto Nc) {
      constexpr int N = decltype(Nc)::v;
      if (two) wait_vm<2 * N>(); else wait_vm<N>();
    };
    STAMP(4, 0);
#pragma unroll
    for (int s = 0; s < M1_NS; ++s) { issue(s, 0); issue(s, 1); }
    const int q = (lane >> 2) & 3, p = lane & 3, g = (lane >> 4) & 1;
    lds_p xa = L + XB + wm * 1024 + lane * 16;
    lds_p wa[2];
#pragma unroll
    for (int e = 0; e < 2; ++e) {
      int b = wn * U + e;
      wa[e] = L + (h * U + (b >> 1)) * 1024 + q * 128 + (((b & 1) ^ (q >> 1)) << 6) + g * 32 + p * 8;
    }
    asm volatile("" : "+v"(xa), "+v"(wa[0]), "+v"(wa[1]));
    waitn(IC<M1_NS - 1>{});
    BARRIER();
    __builtin_amdgcn_s_setprio(MLP_PRIO);
    STAMP(4, 1);
    auto rdx = [&](int sl) { return ld8(xa + sl * 4096); };
    auto rdw = [&](int sl, int m) {
      const lds_p a = wa[m & 1] + sl * WS + (m >> 1) * 1024;
      return cat8(trl(a), trl(a + 512));
    };
    x0 = rdx(0);
#pragma unroll
    for (int m = 0; m < U; ++m) w[m] = rdw(0, m);
    unsigned short bb[U];
    static_for<0, 36>([&](auto Jc) {
      constexpr int j = decltype(Jc)::v;
      if constexpr (j + 1 < 36) {
        constexpr int fl = (35 - (j + 1)) < (M1_NS - 2) ? (35 - (j + 1)) : (M1_NS - 2);
        waitn(IC<fl>{});
        asm volatile("s_waitcnt lgkmcnt(0)" ::: "memory");
        BARRIER();
      }
      if (j == 12) STAMP(4, 2);
      if (j == 24) STAMP(4, 3);
      SB();
      bf16x8& xc = (j & 1) ? x1 : x0;
      bf16x8& xn = (j & 1) ? x0 : x1;
      static_for<0, U>([&](auto Mc) {
        constexpr int m = decltype(Mc)::v;
        acc[m] = MFMA32(w[m], xc, acc[m]);
        SB();
        if constexpr (j + 1 < 36) {
          if constexpr (m == 0) xn = rdx((j + 1) % M1_NS);
          w[m] = rdw((j + 1) % M1_NS, m);
        }
        if constexpr (j + M1_NS < 36 && (m == 1 || m == 2)) issue(j + M1_NS, m - 1);
        if constexpr (j == 34 && m == 0) {
          const int l2 = fresh_lane();
#pragma unroll
          for (int b = 0; b < U; ++b) bb[b] = b1[(unit0 * 2 + wn * U + b) * 32 + (l2 & 31)];
        }
        SB();
      });
    });
    STAMP(4, 4);
    const bf16x8 xx = {(short)(fresh_lane() >> 5 ? 0 : 0x3F80), 0, 0, 0, 0, 0, 0, 0};
#pragma unroll
    for (int b = 0; b < U; ++b) {
      const bf16x8 wx = {(short)bb[b], 0, 0, 0, 0, 0, 0, 0};
      acc[b] = MFMA32(wx, xx, acc[b]);
    }
    asm volatile("" : "+v"(acc[0]), "+v"(acc[U - 1]));
  }
  BARRIER();
  constexpr int B1 = 2 * U, B2 = B1 + 8, B3 = 26;
  const int d0 = (U == 5 ? (up ? 0 : 26) : (up ? 10 : 18));
  const int d1 = (U == 5 ? (up ? 10 : 18) : (up ? 18 : 10)) - B1;
  const int d2 = (U == 5 ? (up ? 18 : 10) : (up ? 26 : 0)) - B2;
  const int d3 = (U == 5 ? (up ? 26 : 0) : (up ? 0 : 26)) - B3;
  const int kh = wn;
  const int unit2 = U == 5 ? (up ? 0 : 7) : 2 * j4, boff = U == 5 ? 0 : j4 - 1;
  const int ln = fresh_lane();
  const unsigned voffX = ln * 16;
  const unsigned voffW = (unsigned)((ln >> 3) * 1152 + (((ln & 7) ^ (((ln >> 4) & 1) << 2)) << 4));
  const char* xsrc = H + ((size_t)(rt * 4 + wm) * 288 + sp * 72 + kh) * 1024;
  const char* wbase = W2 + (size_t)sp * 1152 * 1152 + unit2 * 128;
  const int p0 = wave, p2 = 8 + (wave & 3);
  const char* ws0 = wbase + (p0 / U2) * (8 * 1152) + (p0 % U2) * 128;
  const char* ws2 = wbase + (p2 / U2) * (8 * 1152) + (p2 % U2) * 128;
  auto issue = [&](int s, int e) {
    const int sl = s & 3;
    const unsigned gs =
        (unsigned)(s < B1 ? d0 + (s & 1) * U + (s >> 1) : s + (s < B2 ? d1 : (s < B3 ? d2 : d3)));
    if (e == 0) glds16c(xsrc + gs * 2048u, voffX, lbase + 49152 + sl * 8192 + wave * 1024);
    else if (e == 1) glds16s(ws0 + gs * (32u * 1152u), voffW, lbase + sl * 12288 + p0 * 1024);
    else if (U2 == 3 && (sl & 1) == kh) glds16s(ws2 + gs * (32u * 1152u), voffW, lbase + sl * 12288 + p2 * 1024);
  };
#pragma unroll
  for (int s = 0; s < 4; ++s) { issue(s, 1); issue(s, 2); }
  {
    const float C0 = -2.0f * 1.4426950408889634f * 0.7978845608028654f;
    const float C1 = C0 * 0.044715f;
    char* hb = H + ((size_t)(rt * 4 + wm) * 288 + (unit0 * 2 + wn * U) * 2) * 1024 + voffX;
    const lds_p hx = L + 49152 + wn * 8192 + wm * 1024 + voffX;
#pragma unroll
    for (int bi = 0; bi < U; ++bi) {
      const int b = (bi + 2) % U;
#pragma unroll
      for (int gp = 0; gp < 2; ++gp) {
        u32x4 o4;
#pragma unroll
        for (int i = 0; i < 4; ++i) {
          float o[2];
#pragma unroll
          for (int e = 0; e < 2; ++e) {
            float v = acc[b][8 * gp + 2 * i + e];
            float t = v * __builtin_fmaf(v * v, C1, C0);
            o[e] = v * __builtin_amdgcn_rcpf(1.0f + __builtin_amdgcn_exp2f(t));
          }
          o4[i] = pk2(o[0], o[1]);
        }
        st16(hb + (b * 2 + gp) * 1024, o4);
        if (b < 2) *(__attribute__((address_space(3))) u32x4*)(hx + b * 16384 + gp * 4096) = o4;
      }
    }
  }
  STAMP(4, 5);
#pragma unroll
  for (int b = 0; b < NBW; ++b)
#pragma unroll
    for (int i = 0; i < 16; ++i) acc[b][i] = 0.f;
  lds_p xa = L + 49152 + wave * 1024 + voffX;
  lds_p wa[2];
  {
    const int h = ln >> 5, q = (ln >> 2) & 3, p = ln & 3, g = (ln >> 4) & 1;
#pragma unroll
    for (int e = 0; e < 2; ++e) {
      int b = boff + e;
      wa[e] = L + (2 * kh * U2 + (b >> 1)) * 1024 + (4 * h + q) * 128 + (((b & 1) ^ (q >> 1)) << 6) + g * 32 + p * 8;
    }
  }
  asm volatile("" : "+v"(xa), "+v"(wa[0]), "+v"(wa[1]));
  STAMP(2, 0);
  wait_vm<4>();
  asm volatile("s_waitcnt lgkmcnt(0)" ::: "memory");
  BARRIER();
  STAMP(4, 6);
  STAMP(4, 7);
  STAMP(2, 1);
  auto rdx = [&](int sl) { return ld8(xa + sl * 8192); };
  auto rdw = [&](int sl, int m) {
    const lds_p a = wa[m & 1] + sl * 12288 + (m >> 1) * 1024;
    return cat8(trl(a), trl(a + U2 * 1024));
  };
  x0 = rdx(0);
#pragma unroll
  for (int m = 0; m < NBW; ++m) w[m] = rdw(0, m);
  unsigned fv = 0;
  auto poll = [&]() {
    unsigned vo = voffX;
    asm volatile("" : "+v"(vo));
    vo = (vo & 48u) << 3;
    asm volatile("global_load_dword %0, %1, %2 sc1" : "=v"(fv) : "v"(vo), "s"(FLg) : "memory");
  };
  auto check = [&](unsigned long long mask) {
    mask &= ~(1ull << j4);
    asm volatile("" : "+v"(fv));
    if (__ballot(fv != 1u) & mask) {
#pragma unroll 1
      for (int it = 0; it < (1 << 20); ++it) {
        __builtin_amdgcn_s_sleep(2);
        poll();
        wait_vm<0>();
        asm volatile("" : "+v"(fv));
        if (!(__ballot(fv != 1u) & mask)) break;
      }
    }
  };
  constexpr int LAG = INV ? 2 : 0;
  constexpr int CK1 = B1 - 4 - LAG, CK2 = (U == 5 ? B3 : B2) - 4 - LAG;
  static_for<0, M_IT>([&](auto Jc) {
    constexpr int j = decltype(Jc)::v;
    if constexpr (j >= 3) {
      constexpr int n0 = m_cnt<U2>(j + 2, 0) + m_cnt<U2>(j + 3, 0), n1 = m_cnt<U2>(j + 2, 1) + m_cnt<U2>(j + 3, 1);
      if constexpr (n0 == n1) wait_vm<n0>();
      else { if (kh == 0) wait_vm<n0>(); else wait_vm<n1>(); }
    }
    asm volatile("s_waitcnt lgkmcnt(0)" ::: "memory");
    BARRIER();
    if constexpr (j == 3) {
      if (wave == 0 && voffX == 0) st4(FLg + j4 * 128, 1u);
    }
    if (j == 9) STAMP(2, 2);
    if (j == 18) STAMP(2, 3);
    if (j == 27) STAMP(2, 4);
    if constexpr (j == CK1 - 2 || j == CK2 - 2) poll();
    if constexpr (j == CK1) {
      if constexpr (j < 3) wait_vm<m_cnt<U2>(CK1 + 2, 0) + m_cnt<U2>(CK1 + 3, 0) - (U2 == 3 ? 1 : 0)>();
      check(6ull);
    }
    if constexpr (j == CK2) check(15ull);
    SB();
    bf16x8& xc = (j & 1) ? x1 : x0;
    bf16x8& xn = (j & 1) ? x0 : x1;
    static_for<0, NBW>([&](auto Mc) {
      constexpr int m = decltype(Mc)::v;
      acc[m] = MFMA32(w[m], xc, acc[m]);
      SB();
      if constexpr (j + 1 < M_IT) {
        if constexpr (m == 0) xn = rdx((j + 1) & 3);
        w[m] = rdw((j + 1) & 3, m);
      }
      if constexpr (j + 4 < M_IT && m >= 1 && m <= 3) issue(j + 4, m - 1);
      SB();
    });
  });
  STAMP(2, 5);
  constexpr int NA = (NBW + 1) / 2;
  const int l2 = fresh_lane();
  char* ex = lds + wm * (NBW * 4096) + l2 * 16;
  if (kh == 0) {
#pragma unroll
    for (int b = NA; b < NBW; ++b)
#pragma unroll
      for (int c = 0; c < 4; ++c) {
        f32x4 v = {acc[b][4 * c], acc[b][4 * c + 1], acc[b][4 * c + 2], acc[b][4 * c + 3]};
        *(f32x4*)(ex + b * 4096 + c * 1024) = v;
      }
  } else {
#pragma unroll
    for (int b = 0; b < NA; ++b)
#pragma unroll
      for (int c = 0; c < 4; ++c) {
        f32x4 v = {acc[b][4 * c], acc[b][4 * c + 1], acc[b][4 * c + 2], acc[b][4 * c + 3]};
        *(f32x4*)(ex + b * 4096 + c * 1024) = v;
      }
  }
  __syncthreads();
  char* pb = P + ((size_t)(sp * 64 + rt * 4 + wm) * 18 + unit2 * 2 + boff) * 2048 + l2 * 16;
  auto fin = [&](int b) {
#pragma unroll
    for (int gp = 0; gp < 2; ++gp) {
      f32x4 a = *(const f32x4*)(ex + b * 4096 + (2 * gp) * 1024);
      f32x4 c = *(const f32x4*)(ex + b * 4096 + (2 * gp + 1) * 1024);
      u32x4 v;
      v[0] = pk2(acc[b][8 * gp] + a[0], acc[b][8 * gp + 1] + a[1]);
      v[1] = pk2(acc[b][8 * gp + 2] + a[2], acc[b][8 * gp + 3] + a[3]);
      v[2] = pk2(acc[b][8 * gp + 4] + c[0], acc[b][8 * gp + 5] + c[1]);
      v[3] = pk2(acc[b][8 * gp + 6] + c[2], acc[b][8 * gp + 7] + c[3]);
      st16(pb + b * 2048 + gp * 1024, v);
    }
  };
  if (kh == 0) {
#pragma unroll
    for (int b = 0; b < NA; ++b) fin(b);
  } else {
#pragma unroll
    for (int b = NA; b < NBW; ++b) fin(b);
  }
  STAMP(2, 6);
  STAMP(2, 7);
}

__global__ __launch_bounds__(512, 4) void kernelB(const char* __restrict__ T, char* __restrict__ O,
                                                   const char* __restrict__ XN, const char* __restrict__ W1,
                                                   const unsigned short* __restrict__ b1, char* H,
                                                   const char* __restrict__ W2, char* __restrict__ P, char* FL) {
  __shared__ __attribute__((aligned(16))) char lds[81920];
  const int wave = __builtin_amdgcn_readfirstlane(threadIdx.x >> 6);
  const int lane = threadIdx.x & 63;
  const int bid = blockIdx.x;
  if (bid < 256) {
    attn_role(lds, T, O, bid, wave, lane);
  } else {
    const int wg = bid - 256;
    const int xcd = wg & 7, idx = wg >> 3;
    const int sp = xcd >> 1, j4 = idx & 3, r8 = idx >> 2, rt = (xcd & 1) * 8 + r8;
    char* FLg = FL + (size_t)(rt * 4 + sp) * 512;
    const int koff = (r8 * 9 / 2 + j4 * 9) % 36;
    if (j4 == 0 || j4 == 3) mlp_track<5>(lds, XN, W1, b1, H, W2, P, FLg, rt, sp, j4, koff, wave, lane);
    else mlp_track<4>(lds, XN, W1, b1, H, W2, P, FLg, rt, sp, j4, koff, wave, lane);
  }
}

__global__ __launch_bounds__(256, 2) void kernelC(const char* __restrict__ O, const char* __restrict__ Wo,
                                                   const char* __restrict__ P, const float* __restrict__ G,
                                                   const char* __restrict__ b2, char* __restrict__ out) {
  __shared__ __attribute__((aligned(16))) char lds[73728];
  const int wg = blockIdx.x;
  const int idx = wg >> 3;
  const int ct = idx % 9, rt = (wg & 7) * 4 + idx / 9;
  const int wave = __builtin_amdgcn_readfirstlane(threadIdx.x >> 6);
  const int lane = threadIdx.x & 63;
  const int wm = wave & 1, kh = wave >> 1;
  const int r = lane & 31, h = lane >> 5;
  const unsigned lbase = lds_addr(lds);
  STAMP(3, 0);
  {
    const unsigned voffW = (unsigned)((lane >> 3) * 1152 + (((lane & 7) ^ (((lane >> 4) & 1) << 2)) << 4));
#pragma unroll
    for (int i = 0; i < 18; ++i) {
      int pi = kh * 36 + i * 2 + wm;
      glds16s(Wo + (size_t)pi * (8 * 1152) + ct * 128, voffW, lbase + pi * 1024);
    }
  }
  const char* ob = O + (size_t)((rt * 2 + wm) * 32 + kh * 16) * 1024 + lane * 16;
  bf16x8 of[18];
#pragma unroll
  for (int t = 0; t < 16; ++t) of[t] = *(const bf16x8*)(ob + t * 1024);
  {
    const char* oe = O + OEX + ((rt * 2 + wm) * 32 + kh * 16 + 4 * h) * 128 + r * 4;
#pragma unroll
    for (int u = 0; u < 2; ++u) {
      u32x4 v;
#pragma unroll
      for (int j = 0; j < 4; ++j) v[j] = *(const unsigned*)(oe + (8 * u + j) * 128);
      of[16 + u] = __builtin_bit_cast(bf16x8, v);
    }
  }
  u32x4 pv[8];
  {
    const char* pb = P + ((size_t)(rt * 2 + wm) * 18 + ct * 2 + kh) * 2048 + lane * 16;
#pragma unroll
    for (int sp = 0; sp < 4; ++sp)
#pragma unroll
      for (int gp = 0; gp < 2; ++gp) pv[sp * 2 + gp] = *(const u32x4*)(pb + (size_t)sp * 64 * 18 * 2048 + gp * 1024);
  }
  const float gg = G[rt * 64 + wm * 32 + r];
  u32x2 bb[4];
#pragma unroll
  for (int i = 0; i < 4; ++i) bb[i] = *(const u32x2*)(b2 + (ct * 64 + kh * 32 + 8 * i + 4 * h) * 2);
  SB();
  STAMP(3, 1);
  WAIT_VM(0);
  STAMP(3, 2);
  BARRIER();
  STAMP(3, 3);
  const int q = (lane >> 2) & 3, p = lane & 3, g = (lane >> 4) & 1;
  f32x16 acc[2];
#pragma unroll
  for (int i = 0; i < 16; ++i) { acc[0][i] = 0.f; acc[1][i] = 0.f; }
  const char* lb = lds + kh * (288 * 128) + g * 32 + p * 8;
#pragma unroll
  for (int t = 0; t < 16; ++t) {
    int r0 = 18 * t + 4 * h + q, r1 = r0 + 8;
#pragma unroll
    for (int n = 0; n < 2; ++n) {
      s16x4 lo = tr_read(lb + r0 * 128 + ((n ^ ((r0 >> 1) & 1)) << 6));
      s16x4 hi = tr_read(lb + r1 * 128 + ((n ^ ((r1 >> 1) & 1)) << 6));
      acc[n] = MFMA32(cat8(lo, hi), of[t], acc[n]);
    }
  }
#pragma unroll
  for (int u = 0; u < 2; ++u) {
    int r0 = (8 * u + 4 * h + (q >> 1)) * 18 + 16 + (q & 1), r1 = r0 + 36;
#pragma unroll
    for (int n = 0; n < 2; ++n) {
      s16x4 lo = tr_read(lb + r0 * 128 + ((n ^ ((r0 >> 1) & 1)) << 6));
      s16x4 hi = tr_read(lb + r1 * 128 + ((n ^ ((r1 >> 1) & 1)) << 6));
      acc[n] = MFMA32(cat8(lo, hi), of[16 + u], acc[n]);
    }
  }
  STAMP(3, 4);
  f32x16 ps;
#pragma unroll
  for (int i = 0; i < 4; ++i) {
    ps[4 * i] = __uint_as_float(bb[i][0] << 16);
    ps[4 * i + 1] = __uint_as_float(bb[i][0] & 0xffff0000u);
    ps[4 * i + 2] = __uint_as_float(bb[i][1] << 16);
    ps[4 * i + 3] = __uint_as_float(bb[i][1] & 0xffff0000u);
  }
#pragma unroll
  for (int sp = 0; sp < 4; ++sp)
#pragma unroll
    for (int gp = 0; gp < 2; ++gp)
#pragma unroll
      for (int i = 0; i < 4; ++i) {
        unsigned w = pv[sp * 2 + gp][i];
        ps[8 * gp + 2 * i] += __uint_as_float(w << 16);
        ps[8 * gp + 2 * i + 1] += __uint_as_float(w & 0xffff0000u);
      }
  __syncthreads();
  {
    char* ex = lds + wave * 4096 + lane * 16;
#pragma unroll
    for (int c = 0; c < 4; ++c) {
      f32x4 v;
#pragma unroll
      for (int j = 0; j < 4; ++j) v[j] = kh ? acc[0][4 * c + j] : acc[1][4 * c + j];
      *(f32x4*)(ex + c * 1024) = v;
    }
  }
  __syncthreads();
  f32x16 a;
  {
    const char* ex = lds + (wave ^ 2) * 4096 + lane * 16;
#pragma unroll
    for (int c = 0; c < 4; ++c) {
      f32x4 v = *(const f32x4*)(ex + c * 1024);
#pragma unroll
      for (int j = 0; j < 4; ++j) a[4 * c + j] = v[j] + (kh ? acc[1][4 * c + j] : acc[0][4 * c + j]);
    }
  }
  const float og = 1.0f - gg;
  int row = rt * 64 + wm * 32 + r;
  char* orow = out + (size_t)row * 1152 + (ct * 64 + kh * 32) * 2 + h * 16;
  u32x2 o2[4];
#pragma unroll
  for (int gq = 0; gq < 4; ++gq) {
    float v[4];
#pragma unroll
    for (int j = 0; j < 4; ++j) v[j] = gg * a[4 * gq + j] + og * ps[4 * gq + j];
    o2[gq][0] = pk2(v[0], v[1]);
    o2[gq][1] = pk2(v[2], v[3]);
  }
#pragma unroll
  for (int gq = 0; gq < 4; gq += 2) {
    auto r0 = __builtin_amdgcn_permlane32_swap(o2[gq][0], o2[gq + 1][0], false, false);
    auto r1 = __builtin_amdgcn_permlane32_swap(o2[gq][1], o2[gq + 1][1], false, false);
    u32x4 w = {r0[0], r1[0], r0[1], r1[1]};
    *(u32x4*)(orow + gq * 16) = w;
  }
  STAMP(3, 5);
  STAMP(3, 6);
  STAMP(3, 7);
}

extern "C" void kernel_launch(void* const* d_in, const int* in_sizes, int n_in, void* d_out, int out_size, void* d_ws,
                              size_t ws_size, hipStream_t stream) {
  const char* X = (const char*)d_in[0];
  const char* Wqkv = (const char*)d_in[1];
  const char* Wo = (const char*)d_in[2];
  const char* W1 = (const char*)d_in[4];
  const unsigned short* b1 = (const unsigned short*)d_in[5];
  const char* W2 = (const char*)d_in[6];
  const char* b2 = (const char*)d_in[7];
  const char* gw = (const char*)d_in[8];
  char* ws = (char*)d_ws;
  char* T = ws + WS_T;
  char* H = ws + WS_H;
  char* O = ws + WS_O;
  char* P = ws + WS_P;
  char* XN = ws + WS_XN;
  char* FL = ws + WS_FL;
  float* G = (float*)(ws + WS_G);
  kernelQ<<<248, 512, 0, stream>>>(X, Wqkv, gw, T, XN, G, FL);
  kernelB<<<512, 512, 0, stream>>>(T, O, XN, W1, b1, H, W2, P, FL);
  kernelC<<<288, 256, 0, stream>>>(O, Wo, P, G, b2, (char*)d_out);
}
```

```cpp
#include <hip/hip_runtime.h>
#include <stdint.h>

typedef __attribute__((ext_vector_type(8))) short bf16x8;
typedef __attribute__((ext_vector_type(4))) short s16x4;
typedef __attribute__((ext_vector_type(2))) __bf16 bf16x2_t;
typedef __attribute__((ext_vector_type(2))) float f32x2;
typedef __attribute__((ext_vector_type(4))) float f32x4;
typedef __attribute__((ext_vector_type(16))) float f32x16;
typedef __attribute__((ext_vector_type(4))) unsigned u32x4;
typedef __attribute__((ext_vector_type(2))) unsigned u32x2;
typedef __attribute__((address_space(3))) s16x4* lds_s16x4_p;
typedef __attribute__((address_space(3))) char* lds_p;
typedef __attribute__((address_space(3))) const bf16x8* lds_bf16x8_p;

#define STAMP(k, i)
#define STAMP2(i)
#define DI __device__ __forceinline__
#define MFMA32(a, b, c) __builtin_amdgcn_mfma_f32_32x32x16_bf16((a), (b), (c), 0, 0, 0)
#define MFMA32K8(a, b, c) __builtin_amdgcn_mfma_f32_32x32x8bf16_1k((a), (b), (c), 0, 0, 0)
#define WAIT_VM(n) asm volatile("s_waitcnt vmcnt(" #n ")" ::: "memory")
#define BARRIER() __builtin_amdgcn_s_barrier()
#define SB() __builtin_amdgcn_sched_barrier(0)

constexpr int S_ = 2048, D_ = 576, F_ = 4608, NQKV = 1728;
constexpr int TSTEP = 640;
constexpr int TP = 1736 * 16;
constexpr int OEX = 64 * 32 * 1024;
constexpr size_t WS_T = 0;
constexpr size_t WS_H = 8u << 20;
constexpr size_t WS_O = 28u << 20;
constexpr size_t WS_G = 31u << 20;
constexpr size_t WS_FL = (31u << 20) + (64u << 10);
constexpr size_t WS_P = 32u << 20;
constexpr size_t WS_XN = 44u << 20;
constexpr size_t WS_XF = 47u << 20;
constexpr int FL_Q = 32768, FL_O = 36864, FL_P = 45056;

DI unsigned pk2(float a, float b) {
  f32x2 v = {a, b};
  bf16x2_t r = __builtin_convertvector(v, bf16x2_t);
  return __builtin_bit_cast(unsigned, r);
}
DI float bf2f(unsigned short u) { return __uint_as_float(((unsigned)u) << 16); }
DI s16x4 tr_read(const char* p) {
  return __builtin_amdgcn_ds_read_tr16_b64_v4i16((lds_s16x4_p)(p));
}
DI s16x4 trl(lds_p p) { return __builtin_amdgcn_ds_read_tr16_b64_v4i16((lds_s16x4_p)p); }
DI bf16x8 ld8(lds_p p) { return *(lds_bf16x8_p)p; }
DI float xor32(float v) { return __shfl_xor(v, 32, 64); }

DI void glds16s(const char* sbase, unsigned voff, unsigned lds_dst) {
  unsigned keep;
  asm volatile("s_mov_b32 %0, m0\n\ts_mov_b32 m0, %3\n\ts_nop 0\n\tglobal_load_lds_dwordx4 %1, %2\n\ts_mov_b32 m0, %0"
               : "=&s"(keep)
               : "v"(voff), "s"(sbase), "s"(lds_dst)
               : "memory");
}
DI void glds16c(const char* sbase, unsigned voff, unsigned lds_dst) {
  unsigned keep;
  asm volatile("s_mov_b32 %0, m0\n\ts_mov_b32 m0, %3\n\ts_nop 0\n\tglobal_load_lds_dwordx4 %1, %2 sc1\n\ts_mov_b32 m0, %0"
               : "=&s"(keep)
               : "v"(voff), "s"(sbase), "s"(lds_dst)
               : "memory");
}
DI void touch(const char* sbase, unsigned voff, unsigned lds_dst) {
  unsigned keep;
  asm volatile("s_mov_b32 %0, m0\n\ts_mov_b32 m0, %3\n\ts_nop 0\n\tglobal_load_lds_dword %1, %2\n\ts_mov_b32 m0, %0"
               : "=&s"(keep)
               : "v"(voff), "s"(sbase), "s"(lds_dst)
               : "memory");
}
DI void gld16(bf16x8& d, const char* sbase, unsigned voff) {
  asm volatile("global_load_dwordx4 %0, %1, %2" : "=v"(d) : "v"(voff), "s"(sbase) : "memory");
}
DI void gld16c(bf16x8& d, const char* sbase, unsigned voff) {
  asm volatile("global_load_dwordx4 %0, %1, %2 sc1" : "=v"(d) : "v"(voff), "s"(sbase) : "memory");
}
DI void gld4c(unsigned& d, const char* sbase, unsigned voff) {
  asm volatile("global_load_dword %0, %1, %2 sc1" : "=v"(d) : "v"(voff), "s"(sbase) : "memory");
}
DI void gld4(unsigned& d, const char* sbase, unsigned voff) {
  asm volatile("global_load_dword %0, %1, %2" : "=v"(d) : "v"(voff), "s"(sbase) : "memory");
}
DI unsigned lds_addr(const void* p) { return (unsigned)(uintptr_t)(lds_p)p; }

DI void st16(void* p, u32x4 v) { asm volatile("global_store_dwordx4 %0, %1, off sc1\n\ts_nop 1" ::"v"(p), "v"(v) : "memory"); }
DI void st16p(void* p, u32x4 v) { asm volatile("global_store_dwordx4 %0, %1, off\n\ts_nop 1" ::"v"(p), "v"(v) : "memory"); }
DI void st4(void* p, unsigned v) { asm volatile("global_store_dword %0, %1, off sc1" ::"v"(p), "v"(v) : "memory"); }

DI int fresh_lane() {
  int l;
  asm volatile("v_mbcnt_lo_u32_b32 %0, -1, 0\n\tv_mbcnt_hi_u32_b32 %0, -1, %0" : "=v"(l));
  return l;
}
template <int I>
struct IC { static constexpr int v = I; };
template <int I, int N, class F>
DI void static_for(F&& f) {
  if constexpr (I < N) {
    f(IC<I>{});
    static_for<I + 1, N>(f);
  }
}
template <int N>
DI void wait_vm() { asm volatile("s_waitcnt vmcnt(%0)" ::"i"(N) : "memory"); }
template <int N>
DI void wait_vm1(bf16x8& a) { asm volatile("s_waitcnt vmcnt(%1)" : "+v"(a) : "i"(N) : "memory"); }
DI void spin_flags(const char* base, unsigned voff) {
#pragma unroll 1
  for (int it = 0; it < (1 << 17); ++it) {
    unsigned f;
    gld4c(f, base, voff);
    asm volatile("s_waitcnt vmcnt(0)" : "+v"(f)::"memory");
    if (!__ballot(f != 1u)) break;
    __builtin_amdgcn_s_sleep(4);
  }
}
DI bf16x8 cat8(s16x4 lo, s16x4 hi) { return __builtin_shufflevector(lo, hi, 0, 1, 2, 3, 4, 5, 6, 7); }

constexpr int Q_SLOT = 40960;
#define PF1 0
DI void qkv_role(char* lds, const char* __restrict__ X, const char* __restrict__ Wqkv, const char* __restrict__ pf, int pfl,
                 char* __restrict__ T, int rt, int ct, int joff, int wave, int lane) {
  f32x16 acc[2];
#pragma unroll
  for (int i = 0; i < 16; ++i) { acc[0][i] = 0.f; acc[1][i] = 0.f; }
  const unsigned lbase = lds_addr(lds);
  unsigned voffX[2];
#pragma unroll
  for (int i = 0; i < 2; ++i) {
    int row = wave * 32 + 8 * i + (lane >> 3);
    int c = (lane & 7) ^ ((row >> 1) & 7);
    voffX[i] = (unsigned)((rt * 256 + row) * 1152 + c * 16);
  }
  const unsigned voffW = (unsigned)((lane >> 3) * 3456 + (((lane & 7) ^ (((lane >> 4) & 1) << 2)) << 4));
  const char* wsrc = Wqkv + (size_t)wave * 8 * 3456 + ct * 128;
  auto issue = [&](int J, int e) {
    unsigned dst = lbase + (J & 3) * Q_SLOT;
    unsigned Jr = J + joff;
    Jr = Jr >= 9 ? Jr - 9 : Jr;
    if (e == 0) glds16s(wsrc + Jr * (64u * 3456u), voffW, dst + 32768 + wave * 1024);
    else glds16s(X + Jr * 128u + ((e - 1) >> 1) * (16 * 1152), voffX[(e - 1) & 1], dst + (wave * 4 + e - 1) * 1024);
  };
  STAMP(0, 0);
#pragma unroll
  for (int J = 0; J < 3; ++J)
#pragma unroll
    for (int e = 0; e < 5; ++e) issue(J, e);
  const int r = lane & 31, h = lane >> 5;
  const int q = (lane >> 2) & 3, p = lane & 3, g = (lane >> 4) & 1;
  const lds_p L = (lds_p)lds;
  lds_p xa[4], wa[2][2];
#pragma unroll
  for (int ks = 0; ks < 4; ++ks) xa[ks] = L + (wave * 32 + r) * 128 + (((2 * ks + h) ^ ((r >> 1) & 7)) << 4);
#pragma unroll
  for (int b = 0; b < 2; ++b) {
    wa[0][b] = L + 32768 + h * 1024 + q * 128 + ((b ^ (q >> 1)) << 6) + g * 32 + p * 8;
    wa[1][b] = wa[0][b] + 2 * Q_SLOT;
  }
  lds_p xb[4];
#pragma unroll
  for (int ks = 0; ks < 4; ++ks) xb[ks] = xa[ks] + 2 * Q_SLOT;
  asm volatile("" : "+v"(xb[0]), "+v"(xb[1]), "+v"(xb[2]), "+v"(xb[3]), "+v"(wa[1][0]), "+v"(wa[1][1]));
  wait_vm<10>();
  BARRIER();
  STAMP(0, 1);
  bf16x8 fx[2], fw[2][2];
  auto rd = [&](int buf, int J, int ks) {
    const int sl = J & 3;
    fx[buf] = ld8((sl < 2 ? xa[ks] : xb[ks]) + (sl & 1) * Q_SLOT);
#pragma unroll
    for (int b = 0; b < 2; ++b) {
      const lds_p a = wa[sl >> 1][b] + (sl & 1) * Q_SLOT + ks * 2048;
      fw[buf][b] = cat8(trl(a), trl(a + 512));
    }
  };
  rd(0, 0, 0);
  unsigned pfd = 0;
  static_for<0, 9>([&](auto Jc) {
    constexpr int J = decltype(Jc)::v;
    if constexpr (J + 1 < 9) {
      wait_vm<((J + 2 < 9) ? 5 : 0) + (J >= 6 ? PF1 : 0)>();
      BARRIER();
    }
    if (J == 3) STAMP(0, 2);
    if (J == 6) STAMP(0, 3);
    SB();
    static_for<0, 4>([&](auto Kc) {
      constexpr int ks = decltype(Kc)::v, cur = ks & 1;
      acc[0] = MFMA32(fx[cur], fw[cur][0], acc[0]);
      SB();
      if constexpr (ks < 3) rd(cur ^ 1, J, ks + 1);
      else if constexpr (J + 1 < 9) rd(cur ^ 1, J + 1, 0);
      SB();
      acc[1] = MFMA32(fx[cur], fw[cur][1], acc[1]);
      SB();
      if constexpr (J + 3 < 9) {
        if constexpr (ks == 0) { issue(J + 3, 0); issue(J + 3, 1); }
        else issue(J + 3, ks + 1);
      }
      SB();
    });
  });
  STAMP(0, 5);
  const float QSCALE = 0.23570226039551584f * 1.4426950408889634f;
  const float sc = ct < 9 ? QSCALE : 1.0f;
#pragma unroll
  for (int nb = 0; nb < 2; ++nb) {
    char* base = T + (size_t)h * TP + (ct * 64 + nb * 32 + r) * 16;
#pragma unroll
    for (int s = 0; s < 2; ++s) {
      int step = rt * 16 + wave * 2 + s;
      u32x4 v;
#pragma unroll
      for (int i = 0; i < 4; ++i) v[i] = pk2(acc[nb][8 * s + 2 * i] * sc, acc[nb][8 * s + 2 * i + 1] * sc);
      st16(base + (size_t)step * (2 * TP), v);
    }
  }
  if (ct == 26 && wave < 4) {
    int idx = wave * 64 + lane;
    unsigned c = (idx & 7) == 0 ? 0x3F803F80u : 0u;
    u32x4 v = {c, c, c, c};
    st16(T + (size_t)(rt * 32 + (idx >> 3)) * TP + (1728 + (idx & 7)) * 16, v);
  }
  asm volatile("s_waitcnt vmcnt(0)" : "+v"(pfd)::"memory");
}

DI void ln_role(char* lds, const char* __restrict__ X, const char* __restrict__ gw, char* __restrict__ XN,
                float* __restrict__ G, char* __restrict__ FL, int id, int wave, int lane) {
  const int r = lane & 31, h = lane >> 5;
  const int rb = wave >> 2, kq = wave & 3;
  const int row = id * 64 + rb * 32 + r;
  u32x4 xv[9], gv[9];
#pragma unroll
  for (int i = 0; i < 9; ++i) xv[i] = *(const u32x4*)(X + (size_t)row * 1152 + ((kq * 9 + i) * 2 + h) * 16);
#pragma unroll
  for (int i = 0; i < 9; ++i) gv[i] = *(const u32x4*)(gw + ((kq * 9 + i) * 2 + h) * 16);
  if (wave < 2) {
    u32x4 z = {0u, 0u, 0u, 0u};
    st16(FL + id * 2048 + wave * 1024 + lane * 16, z);
  }
  float* red = (float*)lds;
  float s = 0.f, d = 0.f;
#pragma unroll
  for (int i = 0; i < 9; ++i)
#pragma unroll
    for (int j = 0; j < 4; ++j) {
      float a = __uint_as_float(xv[i][j] << 16), b = __uint_as_float(xv[i][j] & 0xffff0000u);
      s += a + b;
      d = __builtin_fmaf(a, __uint_as_float(gv[i][j] << 16), d);
      d = __builtin_fmaf(b, __uint_as_float(gv[i][j] & 0xffff0000u), d);
    }
  s += xor32(s);
  d += xor32(d);
  if (h == 0) { red[wave * 32 + r] = s; red[256 + wave * 32 + r] = d; }
  __syncthreads();
  const float* rs = red + rb * 128 + r;
  const float mean = (rs[0] + rs[32] + rs[64] + rs[96]) * (1.0f / 576.0f);
  if (kq == 0 && h == 0) {
    float v = rs[256] + rs[288] + rs[320] + rs[352];
    G[row] = 1.0f / (1.0f + __builtin_amdgcn_exp2f(-1.4426950408889634f * v));
  }
  float q2 = 0.f;
#pragma unroll
  for (int i = 0; i < 9; ++i)
#pragma unroll
    for (int j = 0; j < 4; ++j) {
      float a = __uint_as_float(xv[i][j] << 16) - mean, b = __uint_as_float(xv[i][j] & 0xffff0000u) - mean;
      q2 = __builtin_fmaf(a, a, q2);
      q2 = __builtin_fmaf(b, b, q2);
    }
  q2 += xor32(q2);
  if (h == 0) red[512 + wave * 32 + r] = q2;
  __syncthreads();
  const float var = (rs[512] + rs[544] + rs[576] + rs[608]) * (1.0f / 576.0f);
  const float rstd = 1.0f / __builtin_sqrtf(var + 1e-5f);
  char* dst = XN + ((size_t)(id * 2 + rb) * 36 + kq * 9) * 1024 + lane * 16;
#pragma unroll
  for (int i = 0; i < 9; ++i) {
    u32x4 v;
#pragma unroll
    for (int j = 0; j < 4; ++j) {
      float a = __uint_as_float(xv[i][j] << 16) - mean, b = __uint_as_float(xv[i][j] & 0xffff0000u) - mean;
      v[j] = pk2(a * rstd, b * rstd);
    }
    st16(dst + i * 1024, v);
  }
}

__global__ __launch_bounds__(512, 2) void kernelQ(const char* __restrict__ X, const char* __restrict__ Wqkv,
                                                   const char* __restrict__ gw, const char* __restrict__ W1,
                                                   char* __restrict__ T,
                                                   char* __restrict__ XN, float* __restrict__ G,
                                                   char* __restrict__ FL) {
  __shared__ __attribute__((aligned(16))) char lds[163840];
  const int wg = blockIdx.x;
  const int wave = __builtin_amdgcn_readfirstlane(threadIdx.x >> 6);
  const int lane = threadIdx.x & 63;
  if (wg < 216) {
    const int xcd = wg & 7, t = (xcd >> 2) * 27 + (wg >> 3);
    const int joff = ((t >> 1) * 3 + (t & 1) * 4) % 9;
    qkv_role(lds, X, Wqkv, W1 + (xcd >> 1) * 2304, (wg >> 3) * 384, T, (xcd & 3) * 2 + (t & 1), t >> 1, joff, wave, lane);
  } else {
    STAMP(0, 0);
    ln_role(lds, X, gw, XN, G, FL, wg - 216, wave, lane);
  }
  STAMP(0, 6);
  STAMP(0, 7);
}

DI f32x16 qk_tile(lds_p kb, lds_p ke, bf16x8 qf, s16x4 qe, f32x16 c) {
  s16x4 lo = trl(kb);
  s16x4 hi = trl(kb + 64);
  s16x4 ex = trl(ke);
  bf16x8 kf = __builtin_shufflevector(lo, hi, 0, 1, 2, 3, 4, 5, 6, 7);
  f32x16 s = MFMA32(kf, qf, c);
  s = MFMA32K8(ex, qe, s);
  return s;
}
DI f32x16 pv_tile(lds_p vb, const f32x16& pe, f32x16 o) {
  bf16x8 v0 = ld8(vb);
  bf16x8 v1 = ld8(vb + TSTEP);
  u32x4 p0, p1;
#pragma unroll
  for (int i = 0; i < 4; ++i) {
    p0[i] = pk2(pe[2 * i], pe[2 * i + 1]);
    p1[i] = pk2(pe[8 + 2 * i], pe[8 + 2 * i + 1]);
  }
  o = MFMA32(v0, __builtin_bit_cast(bf16x8, p0), o);
  o = MFMA32(v1, __builtin_bit_cast(bf16x8, p1), o);
  return o;
}
struct KFrag { bf16x8 f; s16x4 e; };
struct VFrag { bf16x8 v0, v1; };
DI KFrag load_k(lds_p kb, lds_p ke) {
  KFrag k;
  s16x4 lo = trl(kb);
  s16x4 hi = trl(kb + 64);
  k.e = trl(ke);
  k.f = __builtin_shufflevector(lo, hi, 0, 1, 2, 3, 4, 5, 6, 7);
  return k;
}
DI VFrag load_v(lds_p vb) {
  VFrag v;
  v.v0 = ld8(vb);
  v.v1 = ld8(vb + TSTEP);
  return v;
}
#define EXP2(x) __builtin_amdgcn_exp2f(x)
#define APM 2
#define APMASK 0x77
#define PHI(i) if constexpr ((PM >> (i)) & 1) __builtin_amdgcn_s_setprio(APM)
#define PLO(i) if constexpr ((PM >> (i)) & 1) __builtin_amdgcn_s_setprio(0)
template <int PM>
DI void tile_step(const f32x16& s, f32x16& sn, const KFrag& k, const VFrag& v, bf16x8 qf, s16x4 qe,
                  f32x16& o) {
  f32x16 negm;
#pragma unroll
  for (int i = 0; i < 16; ++i) negm[i] = 0.f;
  u32x4 p0, p1;
  p0[0] = pk2(EXP2(s[0]), EXP2(s[1]));
  p0[1] = pk2(EXP2(s[2]), EXP2(s[3]));
  SB();
  PHI(0);
  SB();
  f32x16 t = MFMA32(k.f, qf, negm);
  SB();
  PLO(0);
  SB();
  p0[2] = pk2(EXP2(s[4]), EXP2(s[5]));
  p0[3] = pk2(EXP2(s[6]), EXP2(s[7]));
  SB();
  PHI(1);
  SB();
  o = MFMA32(v.v0, __builtin_bit_cast(bf16x8, p0), o);
  SB();
  PLO(1);
  SB();
  p1[0] = pk2(EXP2(s[8]), EXP2(s[9]));
  p1[1] = pk2(EXP2(s[10]), EXP2(s[11]));
  SB();
  PHI(2);
  SB();
  t = MFMA32K8(k.e, qe, t);
  SB();
  PLO(2);
  SB();
  p1[2] = pk2(EXP2(s[12]), EXP2(s[13]));
  p1[3] = pk2(EXP2(s[14]), EXP2(s[15]));
  SB();
  PHI(3);
  SB();
  o = MFMA32(v.v1, __builtin_bit_cast(bf16x8, p1), o);
  SB();
  PLO(3);
  SB();
  sn = t;
}
DI float rowmax16(const f32x16& s) {
  float m = __builtin_fmaxf(s[0], s[1]);
#pragma unroll
  for (int i = 2; i < 16; ++i) m = __builtin_fmaxf(m, s[i]);
  return __builtin_fmaxf(m, xor32(m));
}

constexpr int AT_SLOT = 20480;
constexpr int at_cnt(int c, int grp) { return 2 + (((c & 1) == grp) ? 1 : 0); }
constexpr int at_off(int tt) { return ((tt >> 3) & 1) * AT_SLOT + (tt & 7) * 1280; }

#define TOUCH_AT 9
DI void attn_role(char* lds, const char* T, char* O, const char* __restrict__ G, char* FL, const char* __restrict__ W2,
                  const char* __restrict__ Wo, int wg, int wave, int lane) {
  const int head = (wg & 7) * 4 + ((wg >> 3) & 3);
  const int qt = wg >> 5;
  const int r = lane & 31, h = lane >> 5;
  const int grp = wave >> 2;
  const unsigned lbase = lds_addr(lds);
  const int p1 = wave + 8, p2 = 16 + (wave & 3);
  auto mkoff = [&](int pc, int first) {
    int isv = pc >= 10;
    int sl = (pc - 10 * isv) * 64 + lane;
    int seg = sl / 20, row = sl - seg * 20;
    int slot = row < 18 ? first + 576 * isv + head * 18 + row : 1728 + row - 18;
    return (unsigned)(seg * TP + slot * 16);
  };
  const unsigned v0 = mkoff(wave, 576), v1 = mkoff(p1, 576), v2 = mkoff(p2, 576);
  auto issue = [&](int c) {
    unsigned dst = lbase + (c & 3) * AT_SLOT;
    const char* src = T + (size_t)c * (32 * TP);
    glds16s(src, v0, dst + wave * 1024);
    glds16s(src, v1, dst + p1 * 1024);
    if ((c & 1) == grp) glds16s(src, v2, dst + p2 * 1024);
  };
  STAMP(1, 1);
  unsigned gbits;
  gld4(gbits, G + (qt * 256 + wave * 32) * 4, r * 4);
  {
    const char* src = T + (size_t)qt * (32 * TP);
    glds16s(src, mkoff(wave, 0), lbase + 3 * AT_SLOT + wave * 1024);
    if (wave < 2) glds16s(src, mkoff(p1, 0), lbase + 3 * AT_SLOT + p1 * 1024);
  }
  issue(0);
  issue(1);
  issue(2);
  const int q = (lane >> 2) & 3, p = lane & 3, g = (lane >> 4) & 1;
  const lds_p L = (lds_p)lds;
  const lds_p kb = L + g * TSTEP + (p & 1) * (TSTEP / 2) + (8 * h + q) * 16 + (p >> 1) * 8;
  const lds_p ke = L + g * TSTEP + (p & 1) * (TSTEP / 2) + (16 + q) * 16 + (p >> 1) * 8;
  const lds_p vb = L + 10240 + h * (TSTEP / 2) + (r < 19 ? r : 19) * 16;
  lds_p kb2 = kb + 2 * AT_SLOT, ke2 = ke + 2 * AT_SLOT, vb2 = vb + 2 * AT_SLOT;
  asm volatile("" : "+v"(kb2), "+v"(ke2), "+v"(vb2));
  asm volatile("s_waitcnt vmcnt(5)" : "+v"(gbits)::"memory");
  BARRIER();
  STAMP(1, 2);
  bf16x8 qf;
  s16x4 qe;
  {
    KFrag qq = load_k(kb2 + AT_SLOT + wave * 1280, ke2 + AT_SLOT + wave * 1280);
    qf = qq.f;
    qe[0] = h ? (short)0 : qq.e[0];
    qe[1] = h ? (short)0 : qq.e[1];
    qe[2] = 0;
    qe[3] = 0;
  }

  const float gate = __uint_as_float(gbits);
  f32x16 zero;
#pragma unroll
  for (int i = 0; i < 16; ++i) zero[i] = 0.f;
  f32x16 o = zero;
  f32x16 sA = qk_tile(kb, ke, qf, qe, zero);
  {
    const float nx = -rowmax16(sA);
    const unsigned mb = (__float_as_uint(nx) + (nx > 0.f ? 0xffffu : 0u)) >> 16;
    const float nm = __uint_as_float(mb << 16);
#pragma unroll
    for (int i = 0; i < 16; ++i) sA[i] += nm;
    qe[2] = h ? (short)0 : (short)mb;
  }
  KFrag kA, kB;
  VFrag vA, vB;
  kB = load_k(kb + 1280, ke + 1280);
  vA = load_v(vb);
  f32x16 sB;
#pragma unroll 1
  for (int cc = 0; cc < 8; cc += 4) {
    static_for<0, 32>([&](auto Tc) {
      constexpr int tt = decltype(Tc)::v, ci = tt >> 3;
      if constexpr ((tt & 7) == 6) {
        if (cc == 0 || ci < 2) {
          if (grp == 0) wait_vm<at_cnt(ci + 2, 0)>(); else wait_vm<at_cnt(ci + 2, 1)>();
        } else if (ci == 2) wait_vm<0>();
        if (cc == 0 || ci < 3) BARRIER();
        if constexpr (ci == TOUCH_AT) {
          if (cc == 0) {
            const int l2 = fresh_lane();
            const char* src = W2 + (size_t)((wg & 7) >> 1) * (1152 * 1152) + (wg >> 3) * 41472;
            const unsigned dst = lbase + ((ci + 3) & 3) * AT_SLOT + wave * 1024;
            touch(src, (wave * 41 + l2 - (l2 > 40 ? 24 : 0) - (wave >> 2) * 4) * 128u, dst);
            if (wave == 0) touch(Wo + ((wg & 7) >> 1) * (144 * 1152) + (wg >> 3) * 5184, (l2 > 39 ? ((wg & 8) ? 39 : 40) : l2) * 128u + ((wg & 8) ? 64u : 0u), dst);
          }
        }
        if (cc == 0 || ci == 0) issue(cc + ci + 3);
      }
      constexpr int t1 = (tt + 1) & 31, t2 = (tt + 2) & 31;
      const lds_p kp = (t2 < 16 ? kb : kb2) + at_off(t2);
      const lds_p ep = (t2 < 16 ? ke : ke2) + at_off(t2);
      const lds_p vp = (t1 < 16 ? vb : vb2) + at_off(t1);
      if constexpr ((tt & 1) == 0) {
        kA = load_k(kp, ep);
        vB = load_v(vp);
        SB();
        tile_step<APMASK & 15>(sA, sB, kB, vA, qf, qe, o);
      } else {
        kB = load_k(kp, ep);
        vA = load_v(vp);
        SB();
        tile_step<(APMASK >> 4) & 15>(sB, sA, kA, vB, qf, qe, o);
      }
    });
    if (cc == 0) STAMP(1, 3);
  }
  STAMP(1, 4);
  STAMP(1, 5);
  float l = xor32(o[10]);
  l = h ? l : o[10];
  bool bad = !(l < 1.0e30f);
#pragma unroll
  for (int i = 0; i < 10; ++i) bad = bad || !(__builtin_fabsf(o[i]) < 1.0e37f);
  const int ob = (qt * 8 + wave) * 32 + head;
  auto publish = [&]() {
    const float inv = gate / l;
    u32x4 ov;
#pragma unroll
    for (int i = 0; i < 4; ++i) ov[i] = pk2(o[2 * i] * inv, o[2 * i + 1] * inv);
    st16(O + (size_t)ob * 1024 + lane * 16, ov);
    if (h == 0) st4(O + OEX + (ob >> 3) * 1024 + ((head >> 2) & 1) * 512 + r * 16 + (head & 3) * 4, pk2(o[8] * inv, o[9] * inv));
    STAMP(1, 6);
    wait_vm<0>();
    if (lane == 0) st4(FL + FL_O + ob * 4, 1u);
  };
  const bool mybad = __any(bad);
  if (!mybad) publish();
  {
    int* fl = (int*)(lds + AT_SLOT);
    if (lane == 0) fl[wave] = mybad ? 1 : 0;
    __syncthreads();
    int v = fl[lane & 7];
    bad = __any(v != 0);
  }
  if (bad) {
    float m = -3.0e38f;
    o = zero;
    qe[2] = 0;
#pragma unroll 1
    for (int c = 0; c < 8; ++c) {
      __syncthreads();
      {
        const char* src = T + (size_t)c * (32 * TP);
        glds16s(src, v0, lbase + wave * 1024);
        glds16s(src, v1, lbase + p1 * 1024);
        if (grp == 0) glds16s(src, v2, lbase + p2 * 1024);
      }
      wait_vm<0>();
      __syncthreads();
#pragma unroll 1
      for (int t = 0; t < 8; ++t) {
        f32x16 s = qk_tile(kb + t * 1280, ke + t * 1280, qf, qe, zero);
        float mn = __builtin_fmaxf(m, rowmax16(s));
        float alpha = __builtin_amdgcn_exp2f(m - mn);
        m = mn;
        f32x16 pe;
#pragma unroll
        for (int i = 0; i < 16; ++i) { pe[i] = __builtin_amdgcn_exp2f(s[i] - mn); o[i] *= alpha; }
        o = pv_tile(vb + t * 1280, pe, o);
      }
    }
    l = xor32(o[10]);
    l = h ? l : o[10];
    if (mybad) publish();
  }
  STAMP(1, 7);
}

#define QKV_PRIO 2
#define ATT_PRIO 0
#define MLP_PRIO 1
#define PF2_AT 20
constexpr int M_IT = 36;
constexpr int M2_NT = 41;
struct M2S { int slot[M2_NT]; int it[M2_NT]; };
constexpr M2S m2_sched(int U) {
  M2S r{};
  const int E = 2 * U - 2;
  int next = 0;
  for (; next < 4; ++next) { r.slot[next] = next; r.it[next] = -1; }
  for (int j = 1; next < M2_NT; ++j) {
    r.slot[next] = r.slot[j - 1];
    r.it[next] = j;
    ++next;
    if (next < M2_NT && (j == E || j == E + 1)) { r.slot[next] = 4 + (j - E); r.it[next] = j; ++next; }
  }
  return r;
}
constexpr int m2_ck1(int U) { return 2 * U - 4; }
constexpr int m2_ck2(int U) { return (U == 5 ? 26 : 2 * U + 8) - 4; }
constexpr int m2_wait(int U, int kh, int j, int mode) {
  const M2S sc = m2_sched(U);
  const int U2 = U == 5 ? 2 : 3;
  int n = 0, last = -1;
  for (int i = -1; i < j + mode; ++i) {
    if (i == m2_ck1(U) - 2 || i == m2_ck2(U) - 2) { if (mode == 1 && i == j - 2) last = n; ++n; }
    for (int s = 0; s < M2_NT; ++s)
      if (sc.it[s] == i) {
        const int c = (U2 == 3 && (s & 1) == kh) ? 2 : 1;
        n += c;
        if (mode == 0 && s <= j + 1) last = n - 1;
      }
    if (i == -1) n += 2 * U;
    if (i >= 0 && i < j && i + 4 >= 2 * U && i + 4 < M_IT) { if (mode == 0 && i + 4 <= j) last = n; ++n; }
  }
  return n - 1 - last;
}
constexpr bool m2_need(int U, int j) { return j >= 3; }

template <int N>
DI void pin_acc(f32x16* a) {
  if constexpr (N == 5)
    asm volatile("" : "+{v[0:15]}"(a[0]), "+{v[16:31]}"(a[1]), "+{v[32:47]}"(a[2]), "+{v[48:63]}"(a[3]), "+{v[64:79]}"(a[4]));
  else
    asm volatile("" : "+{v[0:15]}"(a[0]), "+{v[16:31]}"(a[1]), "+{v[32:47]}"(a[2]), "+{v[48:63]}"(a[3]));
}
#define XLD(R) asm volatile("global_load_dwordx4 %0, %1, %2 sc1" : "={" R "}"(d) : "v"(voff), "s"(sbase) : "memory")
template <int I>
DI void xload(bf16x8& d, const char* sbase, unsigned voff) {
  if constexpr (I == 0) XLD("v[80:83]");
  else if constexpr (I == 1) XLD("v[84:87]");
  else if constexpr (I == 2) XLD("v[88:91]");
  else XLD("v[92:95]");
}
template <int I>
DI void pload(bf16x8& d, const char* sbase, unsigned voff) {
  if constexpr (I == 0) XLD("v[48:51]");
  else if constexpr (I == 1) XLD("v[52:55]");
  else if constexpr (I == 2) XLD("v[56:59]");
  else if constexpr (I == 3) XLD("v[60:63]");
  else if constexpr (I == 4) XLD("v[64:67]");
  else if constexpr (I == 5) XLD("v[68:71]");
  else if constexpr (I == 6) XLD("v[72:75]");
  else if constexpr (I == 7) XLD("v[76:79]");
  else if constexpr (I == 8) XLD("v[80:83]");
  else if constexpr (I == 9) XLD("v[84:87]");
  else if constexpr (I == 10) XLD("v[88:91]");
  else if constexpr (I == 11) XLD("v[92:95]");
  else if constexpr (I == 12) XLD("v[96:99]");
  else if constexpr (I == 13) XLD("v[100:103]");
  else if constexpr (I == 14) XLD("v[104:107]");
  else if constexpr (I == 15) XLD("v[108:111]");
  else if constexpr (I == 16) XLD("v[112:115]");
  else if constexpr (I == 17) XLD("v[116:119]");
}
template <int I>
DI void ppin(bf16x8& d) {
  if constexpr (I == 0) asm volatile("" : "+{v[48:51]}"(d));
  else if constexpr (I == 1) asm volatile("" : "+{v[52:55]}"(d));
  else if constexpr (I == 2) asm volatile("" : "+{v[56:59]}"(d));
  else if constexpr (I == 3) asm volatile("" : "+{v[60:63]}"(d));
  else if constexpr (I == 4) asm volatile("" : "+{v[64:67]}"(d));
  else if constexpr (I == 5) asm volatile("" : "+{v[68:71]}"(d));
  else if constexpr (I == 6) asm volatile("" : "+{v[72:75]}"(d));
  else if constexpr (I == 7) asm volatile("" : "+{v[76:79]}"(d));
  else if constexpr (I == 8) asm volatile("" : "+{v[80:83]}"(d));
  else if constexpr (I == 9) asm volatile("" : "+{v[84:87]}"(d));
  else if constexpr (I == 10) asm volatile("" : "+{v[88:91]}"(d));
  else if constexpr (I == 11) asm volatile("" : "+{v[92:95]}"(d));
  else if constexpr (I == 12) asm volatile("" : "+{v[96:99]}"(d));
  else if constexpr (I == 13) asm volatile("" : "+{v[100:103]}"(d));
  else if constexpr (I == 14) asm volatile("" : "+{v[104:107]}"(d));
  else if constexpr (I == 15) asm volatile("" : "+{v[108:111]}"(d));
  else if constexpr (I == 16) asm volatile("" : "+{v[112:115]}"(d));
  else if constexpr (I == 17) asm volatile("" : "+{v[116:119]}"(d));
}
#define XDS(R) asm volatile("ds_read_b128 %0, %1 offset:%2" : "={" R "}"(d) : "v"(a), "i"(OFF) : "memory")
template <int I, int OFF>
DI void xstash(bf16x8& d, unsigned a) {
  if constexpr (I == 0) XDS("v[80:83]");
  else if constexpr (I == 1) XDS("v[84:87]");
  else if constexpr (I == 2) XDS("v[88:91]");
  else XDS("v[92:95]");
}
template <int I>
DI void pin_x1(bf16x8& x) {
  if constexpr (I == 0) asm volatile("" : "+{v[80:83]}"(x));
  else if constexpr (I == 1) asm volatile("" : "+{v[84:87]}"(x));
  else if constexpr (I == 2) asm volatile("" : "+{v[88:91]}"(x));
  else asm volatile("" : "+{v[92:95]}"(x));
}
template <int I>
DI void pin_w(bf16x8& w) {
  if constexpr (I == 0) asm volatile("" : "+{v[96:99]}"(w));
  else if constexpr (I == 1) asm volatile("" : "+{v[100:103]}"(w));
  else if constexpr (I == 2) asm volatile("" : "+{v[104:107]}"(w));
  else if constexpr (I == 3) asm volatile("" : "+{v[108:111]}"(w));
  else asm volatile("" : "+{v[112:115]}"(w));
}
template <int U>
DI void mlp_track(char* lds, const char* __restrict__ XN, const char* __restrict__ W1,
                  const unsigned short* __restrict__ b1, char* H, const char* __restrict__ W2,
                  const unsigned short* __restrict__ b2, const char* __restrict__ Wo, const char* O,
                  const char* __restrict__ G, char* P, char* FL, char* __restrict__ out, int rt, int sp, int j4,
                  int koff, int wave, int lane) {
  constexpr int NBW = 9 - U, U2 = U == 5 ? 2 : 3;
  constexpr int NS1 = U == 5 ? 5 : 6;
  const bool up = j4 < 2;
  const int unit0 = sp * 18 + (U == 5 ? (up ? 0 : 13) : (up ? 5 : 9));
  const unsigned lbase = lds_addr(lds);
  const lds_p L = (lds_p)lds;
  const int wm = wave & 3, wn = wave >> 2;
  char* FLg = FL + (size_t)(rt * 4 + sp) * 512;
  f32x16 acc[5];
#pragma unroll
  for (int b = 0; b < 5; ++b)
#pragma unroll
    for (int i = 0; i < 16; ++i) acc[b][i] = 0.f;
  bf16x8 w[5];
  {
    constexpr int WS = 2 * U * 1024, XB = NS1 * WS, NP = 4 + 2 * U;
    const int h = lane >> 5;
    const unsigned voffW = (unsigned)((lane >> 3) * 9216 + (((lane & 7) ^ (((lane >> 4) & 1) << 2)) << 4));
    const unsigned voff0 = wave < 4 ? (unsigned)(lane * 16) : voffW;
    const int q0 = wave - 4, q1 = wave + 4;
    const char* wb = W1 + unit0 * 128;
    const char* src0 =
        wave < 4 ? XN + (size_t)(rt * 4 + wave) * (36 * 1024) : wb + (q0 / U) * (8 * 9216) + (q0 % U) * 128;
    const char* src1 = wb + (q1 / U) * (8 * 9216) + (q1 % U) * 128;
    const unsigned st0 = wave < 4 ? 1024u : 147456u;
    const bool two = wave + 8 < NP;
    const unsigned dst0 = lbase + (wave < 4 ? XB + wave * 1024 : q0 * 1024), ds0 = wave < 4 ? 4096u : (unsigned)WS;
    auto issue = [&](int s, int e) {
      int ks = s + koff;
      ks = ks >= 36 ? ks - 36 : ks;
      if (e == 0) glds16s(src0 + (unsigned)ks * st0, voff0, dst0 + (s % NS1) * ds0);
      else if (two) glds16s(src1 + (unsigned)ks * 147456u, voffW, lbase + (s % NS1) * WS + q1 * 1024);
    };
    auto waitn = [&](auto Nc) {
      constexpr int N = decltype(Nc)::v;
      if (two) wait_vm<2 * N>(); else wait_vm<N>();
    };
#pragma unroll
    for (int s = 0; s < NS1; ++s) { issue(s, 0); issue(s, 1); }
    const int q = (lane >> 2) & 3, p = lane & 3, g = (lane >> 4) & 1;
    lds_p xa = L + XB + wm * 1024 + lane * 16;
    lds_p wa[2];
#pragma unroll
    for (int e = 0; e < 2; ++e) {
      int b = wn * U + e;
      wa[e] = L + (h * U + (b >> 1)) * 1024 + q * 128 + (((b & 1) ^ (q >> 1)) << 6) + g * 32 + p * 8;
    }
    asm volatile("" : "+v"(xa), "+v"(wa[0]), "+v"(wa[1]));
    waitn(IC<NS1 - 1>{});
    BARRIER();
    __builtin_amdgcn_s_setprio(MLP_PRIO);
    STAMP(4, 1);
    auto rdx = [&](int sl) { return ld8(xa + sl * 4096); };
    auto rdw = [&](int sl, int m) {
      const lds_p a = wa[m & 1] + sl * WS + (m >> 1) * 1024;
      return cat8(trl(a), trl(a + 512));
    };
    bf16x8 x0, x1;
    x0 = rdx(0);
#pragma unroll
    for (int m = 0; m < U; ++m) w[m] = rdw(0, m);
    unsigned short bb[U];
    unsigned pfd;
    static_for<0, 36>([&](auto Jc) {
      constexpr int j = decltype(Jc)::v;
      if constexpr (j + 1 < 36) {
        constexpr int top = j == 0 ? NS1 - 1 : (j + NS1 - 2 < 35 ? j + NS1 - 2 : 35);
        waitn(IC<top - (j + 1)>{});
        BARRIER();
      }
      STAMP2(j);
      if (j == 12) STAMP(4, 2);
      if (j == 24) STAMP(4, 3);
      SB();
      bf16x8& xc = (j & 1) ? x1 : x0;
      bf16x8& xn = (j & 1) ? x0 : x1;
      static_for<0, U>([&](auto Mc) {
        constexpr int m = decltype(Mc)::v;
        acc[m] = MFMA32(w[m], xc, acc[m]);
        SB();
        if constexpr (j + 1 < 36) {
          if constexpr (m == 0) xn = rdx((j + 1) % NS1);
          w[m] = rdw((j + 1) % NS1, m);
        }
        if constexpr (j >= 1 && j + NS1 - 1 < 36 && (m == 1 || m == 2)) issue(j + NS1 - 1, m - 1);
        if constexpr (j == PF2_AT && m == 3) {
          const unsigned l2 = fresh_lane(), li = l2 < 18 * U2 ? l2 : l2 - 32, row = li / U2;
          const unsigned vo = row * 1152u + (li - row * U2) * 128u;
          const int u2 = U == 5 ? (up ? 0 : 7) : 2 * j4;
          gld4(pfd, W2 + (size_t)(sp * 1152 + (rt & 7) * 144 + wave * 18) * 1152 + u2 * 128, vo);
          if (wave == 7) gld4(pfd, Wo + (sp * 144 + (rt & 7) * 18) * 1152 + u2 * 128, vo);
        }
        if constexpr (j == PF2_AT + NS1 + 1 && m == 0) asm volatile("" ::"v"(pfd));
        if constexpr (j == 34 && m == 0) {
          const int l2 = fresh_lane();
#pragma unroll
          for (int b = 0; b < U; ++b) bb[b] = b1[(unit0 * 2 + wn * U + b) * 32 + (l2 & 31)];
        }
        SB();
      });
    });
    STAMP2(36);
    STAMP(4, 4);
    const bf16x8 xx = {(short)(fresh_lane() >> 5 ? 0 : 0x3F80), 0, 0, 0, 0, 0, 0, 0};
#pragma unroll
    for (int b = 0; b < U; ++b) {
      const bf16x8 wx = {(short)bb[b], 0, 0, 0, 0, 0, 0, 0};
      acc[b] = MFMA32(wx, xx, acc[b]);
    }
    asm volatile("" : "+v"(acc[0]), "+v"(acc[U - 1]));
  }
  BARRIER();
  constexpr int B1 = 2 * U, B2 = B1 + 8, B3 = 26;
  constexpr M2S SC = m2_sched(U);
  const int e0 = 2 * (U == 5 ? (up ? 0 : 26) : (up ? 10 : 18));
  const int e1 = 2 * (U == 5 ? (up ? 10 : 18) : (up ? 18 : 10)) - B1;
  const int e2 = 2 * (U == 5 ? (up ? 18 : 10) : (up ? 26 : 0)) - B2;
  const int e3 = 2 * (U == 5 ? (up ? 26 : 0) : (up ? 0 : 26)) - B3;
  auto kstep = [&](int s, int half) {
    const int uc2 = s < B1 ? 2 * U : (s < B2 ? 8 : (s < B3 ? (U == 5 ? 8 : 10) : 10));
    return (unsigned)(s + (s < B1 ? e0 : (s < B2 ? e1 : (s < B3 ? e2 : e3))) + half * uc2);
  };
  const int kh = wn;
  const int unit2 = U == 5 ? (up ? 0 : 7) : 2 * j4, boff = U == 5 ? 0 : j4 - 1;
  const int ln = fresh_lane();
  const unsigned voffX = ln * 16;
  const unsigned voffW = (unsigned)((ln >> 3) * 1152 + (((ln & 7) ^ (((ln >> 4) & 1) << 2)) << 4));
  const char* xsrc = H + ((size_t)(rt * 4 + wm) * 288 + sp * 72) * 1024;
  const int p0 = wave, p2 = 8 + (wave & 3);
  const int kb0 = p0 / U2, kb2 = p2 / U2;
  const unsigned c0 = (kb0 & 1) * (8 * 1152) + (unit2 + p0 % U2) * 128, c2 = (kb2 & 1) * (8 * 1152) + (unit2 + p2 % U2) * 128;
  const char* w2s = W2 + (size_t)sp * 1152 * 1152;
  const char* wos = Wo + sp * (8 * 18 * 1152);
  constexpr int SL2 = U2 * 4096;
  auto issueW = [&](auto Sc, int e) {
    constexpr int s = decltype(Sc)::v;
    const unsigned dst = lbase + SC.slot[s] * SL2 + (e ? p2 : p0) * 1024;
    if (e == 1 && !(U2 == 3 && (s & 1) == kh)) return;
    const int half = e ? 1 : kb0 >> 1;
    const unsigned c = e ? c2 : c0;
    if constexpr (s < M_IT) glds16s(w2s + (c + kstep(s, half) * (16u * 1152u)), voffW, dst);
    else if constexpr (s < M2_NT - 1) glds16s(wos + (c + (unsigned)(4 * half + s - M_IT) * (18u * 1152u)), voffW, dst);
    else {
      const int l2 = fresh_lane(), row = l2 >> 3;
      const unsigned ve = (unsigned)(((4 * (row >> 2) + ((row & 3) >> 1)) * 18 + (row & 1)) * 1152 + (((l2 & 7) ^ (((l2 >> 4) & 1) << 2)) << 4));
      const int kbl = (e ? kb2 : kb0) & 1;
      glds16s(wos + (c + (unsigned)(kbl * (36 - 8) + 16) * 1152u), ve, dst);
    }
  };
  unsigned b2v[NBW];
  const bool addb = sp == 0 && kh == 0;
#pragma unroll
  for (int b = 0; b < NBW; ++b)
    asm volatile("global_load_ushort %0, %1, %2" : "=v"(b2v[b]) : "v"((ln & 31) * 2), "s"(b2 + (unit2 * 2 + boff + b) * 32) : "memory");
  static_for<0, 4>([&](auto Sc) { issueW(Sc, 0); issueW(Sc, 1); });
  bf16x8 xr[4];
  const unsigned stash = lbase + 4 * SL2 + wave * ((2 * U - 4) * 1024) + voffX;
  {
    const float C0 = -2.0f * 1.4426950408889634f * 0.7978845608028654f;
    const float C1 = C0 * 0.044715f;
    char* hb = H + ((size_t)(rt * 4 + wm) * 288 + (unit0 * 2 + wn * U) * 2) * 1024 + fresh_lane() * 16;
#pragma unroll
    for (int b = 0; b < U; ++b) {
#pragma unroll
      for (int gp = 0; gp < 2; ++gp) {
        u32x4 o4;
#pragma unroll
        for (int i = 0; i < 4; ++i) {
          float o[2];
#pragma unroll
          for (int e = 0; e < 2; ++e) {
            float v = acc[b][8 * gp + 2 * i + e];
            float t = v * __builtin_fmaf(v * v, C1, C0);
            o[e] = v * __builtin_amdgcn_rcpf(1.0f + __builtin_amdgcn_exp2f(t));
          }
          o4[i] = pk2(o[0], o[1]);
        }
        st16(hb + (b * 2 + gp) * 1024, o4);
        if (b < 2) xr[b * 2 + gp] = __builtin_bit_cast(bf16x8, o4);
        else *(__attribute__((address_space(3))) u32x4*)(uintptr_t)(stash + (b * 2 + gp - 4) * 1024) = o4;
      }
    }
  }
  STAMP(4, 5);
  lds_p wa[2];
  {
    const int h = ln >> 5, q = (ln >> 2) & 3, p = ln & 3, g = (ln >> 4) & 1;
#pragma unroll
    for (int e = 0; e < 2; ++e) {
      int b = boff + e;
      wa[e] = L + (2 * kh * U2 + (b >> 1)) * 1024 + (4 * h + q) * 128 + (((b & 1) ^ (q >> 1)) << 6) + g * 32 + p * 8;
    }
  }
  asm volatile("" : "+v"(wa[0]), "+v"(wa[1]));
  wait_vm<2 * U>();
  {
    f32x16 zero;
#pragma unroll
    for (int i = 0; i < 16; ++i) zero[i] = 0.f;
    const bf16x8 xx = {(short)((ln >> 5) || !addb ? 0 : 0x3F80), 0, 0, 0, 0, 0, 0, 0};
#pragma unroll
    for (int b = 0; b < NBW; ++b) {
      asm volatile("" : "+v"(b2v[b]));
      const bf16x8 wx = {(short)b2v[b], 0, 0, 0, 0, 0, 0, 0};
      acc[b] = MFMA32(wx, xx, zero);
    }
  }
  BARRIER();
  STAMP(4, 6);
  STAMP(4, 7);
  STAMP(2, 1);
  auto rdw = [&](int s, int m) {
    const lds_p a = wa[m & 1] + SC.slot[s] * SL2 + (m >> 1) * 1024;
    return cat8(trl(a), trl(a + U2 * 1024));
  };
#pragma unroll
  for (int m = 0; m < NBW; ++m) w[m] = rdw(0, m);
  unsigned fv = 0;
  auto poll = [&]() {
    unsigned vo = voffX;
    asm volatile("" : "+v"(vo));
    vo = (vo & 48u) << 3;
    gld4c(fv, FLg, vo);
  };
  auto check = [&](unsigned long long mask) {
    mask &= ~(1ull << j4);
    asm volatile("" : "+v"(fv));
    if (__ballot(fv != 1u) & mask) {
#pragma unroll 1
      for (int it = 0; it < (1 << 17); ++it) {
        __builtin_amdgcn_s_sleep(4);
        poll();
        wait_vm<0>();
        asm volatile("" : "+v"(fv));
        if (!(__ballot(fv != 1u) & mask)) break;
      }
    }
  };
  auto waitk = [&](auto N0, auto N1) {
    constexpr int n0 = decltype(N0)::v, n1 = decltype(N1)::v;
    if constexpr (n0 == n1) wait_vm<n0>();
    else { if (kh == 0) wait_vm<n0>(); else wait_vm<n1>(); }
  };
  constexpr int CK1 = m2_ck1(U), CK2 = m2_ck2(U);
  unsigned gv;
  static_for<0, M_IT>([&](auto Jc) {
    constexpr int j = decltype(Jc)::v;
    if constexpr (m2_need(U, j)) waitk(IC<m2_wait(U, 0, j, 0)>{}, IC<m2_wait(U, 1, j, 0)>{});
    if constexpr (j > 0) BARRIER();
    if constexpr (j > 0) pin_acc<NBW>(acc);
    static_for<0, 4>([&](auto Ic) {
      constexpr int i = decltype(Ic)::v;
      if constexpr (j + ((i - j) & 3) < M_IT) pin_x1<i>(xr[i]);
    });
    if constexpr (j == 3) {
      if (wave == 0 && voffX == 0) st4(FLg + j4 * 128, 1u);
    }
    STAMP2(40 + j);
    if (j == 9) STAMP(2, 2);
    if (j == 18) STAMP(2, 3);
    if (j == 27) STAMP(2, 4);
    if constexpr (j == CK1 - 2 || j == CK2 - 2) poll();
    SB();
    static_for<0, NBW>([&](auto Mc) {
      constexpr int m = decltype(Mc)::v;
      pin_w<m>(w[m]);
      acc[m] = MFMA32(w[m], xr[j & 3], acc[m]);
      SB();
      w[m] = rdw(j + 1, m);
      static_for<j + 3, (j + 7 < M2_NT ? j + 7 : M2_NT)>([&](auto Sc) {
        constexpr int s = decltype(Sc)::v;
        if constexpr (SC.it[s] == j) {
          constexpr bool second = s > 0 && SC.it[s - 1] == j;
          if constexpr (m == (second ? 2 : 0)) issueW(Sc, 0);
          if constexpr (m == (second ? 3 : 1)) issueW(Sc, 1);
        }
      });
      SB();
    });
    if constexpr (j == CK1 || j == CK2) {
      waitk(IC<m2_wait(U, 0, j, 1)>{}, IC<m2_wait(U, 1, j, 1)>{});
      check(j == CK1 ? 6ull : 15ull);
    }
    if constexpr (j + 4 < B1) xstash<(j & 3), j * 1024>(xr[j & 3], stash);
    else if constexpr (j + 4 < M_IT) xload<(j & 3)>(xr[j & 3], xsrc + kstep(j + 4, kh) * 1024u, voffX);
    if constexpr (j == 32) gld4(gv, G + (rt * 4 + wm) * 128, (voffX >> 2) & 124u);
    SB();
  });
  STAMP2(76);
  STAMP(2, 5);
  asm volatile("s_waitcnt vmcnt(0)" : "+v"(gv)::"memory");
  pin_acc<NBW>(acc);
  {
    const float og = 1.0f - __uint_as_float(gv);
#pragma unroll
    for (int b = 0; b < NBW; ++b)
#pragma unroll
      for (int i = 0; i < 16; ++i) acc[b][i] *= og;
  }
  pin_acc<NBW>(acc);
  static_for<0, NBW>([&](auto Mc) { pin_w<decltype(Mc)::v>(w[decltype(Mc)::v]); });
  if (wave == 0) {
    const int l2 = fresh_lane();
    spin_flags(FL + FL_O + rt * 512 + sp * 32, (unsigned)(((l2 >> 3) & 3) * 128 + (l2 & 7) * 4));
  }
  BARRIER();
  {
    bf16x8 xe;
    const char* osrc = O + (size_t)((rt * 4 + wm) * 32 + sp * 8 + kh * 4) * 1024;
    static_for<0, 4>([&](auto Ic) { xload<decltype(Ic)::v>(xr[decltype(Ic)::v], osrc + decltype(Ic)::v * 1024, voffX); });
    gld16c(xe, O + OEX + ((rt * 4 + wm) * 4 + sp) * 1024, voffX);
    static_for<0, 4>([&](auto Tc) {
      constexpr int t = decltype(Tc)::v;
      wait_vm<4 - t>();
      pin_x1<t>(xr[t]);
      pin_acc<NBW>(acc);
      static_for<0, NBW>([&](auto Mc) {
        constexpr int m = decltype(Mc)::v;
        pin_w<m>(w[m]);
        acc[m] = MFMA32(w[m], xr[t], acc[m]);
        SB();
        w[m] = rdw(M_IT + t + 1, m);
        SB();
      });
    });
    wait_vm1<0>(xe);
    pin_acc<NBW>(acc);
    static_for<0, NBW>([&](auto Mc) { pin_w<decltype(Mc)::v>(w[decltype(Mc)::v]); });
    if (kh == 0) {
#pragma unroll
      for (int m = 0; m < NBW; ++m) acc[m] = MFMA32(w[m], xe, acc[m]);
    }
    pin_acc<NBW>(acc);
  }
  BARRIER();
  STAMP(2, 6);
  constexpr int NA = (NBW + 1) / 2;
  const int l2 = fresh_lane();
  char* ex = lds + wm * (NBW * 4096) + l2 * 16;
  if (kh == 0) {
#pragma unroll
    for (int b = NA; b < NBW; ++b)
#pragma unroll
      for (int c = 0; c < 4; ++c) {
        f32x4 v = {acc[b][4 * c], acc[b][4 * c + 1], acc[b][4 * c + 2], acc[b][4 * c + 3]};
        *(f32x4*)(ex + b * 4096 + c * 1024) = v;
      }
  } else {
#pragma unroll
    for (int b = 0; b < NA; ++b)
#pragma unroll
      for (int c = 0; c < 4; ++c) {
        f32x4 v = {acc[b][4 * c], acc[b][4 * c + 1], acc[b][4 * c + 2], acc[b][4 * c + 3]};
        *(f32x4*)(ex + b * 4096 + c * 1024) = v;
      }
    acc[0] = acc[NA];
    acc[1] = acc[NA + 1];
  }
  auto pin_r = [&]() {
    if constexpr (NA == 3) asm volatile("" : "+{v[0:15]}"(acc[0]), "+{v[16:31]}"(acc[1]), "+{v[32:47]}"(acc[2]));
    else asm volatile("" : "+{v[0:15]}"(acc[0]), "+{v[16:31]}"(acc[1]));
  };
  pin_r();
  __syncthreads();
  const int bb0 = kh ? NA : 0;
  const bool third = kh == 0;
  ex += bb0 * 4096;
#pragma unroll
  for (int b = 0; b < NA; ++b) {
    if (b == 2 && !third) break;
#pragma unroll
    for (int c = 0; c < 4; ++c) {
      f32x4 a = *(const f32x4*)(ex + b * 4096 + c * 1024);
#pragma unroll
      for (int i = 0; i < 4; ++i) acc[b][4 * c + i] += a[i];
    }
  }
  pin_r();
  const char* flp = FL + FL_P + (((rt * 4 + j4) * 4 + wm) * 2 + kh) * 16;
  const size_t pofs = ((size_t)(rt * 4 + wm) * 18 + unit2 * 2 + boff + bb0) * 2048;
  if (wm != sp) {
    char* pb = P + (size_t)sp * (64 * 18 * 2048) + pofs + l2 * 16;
#pragma unroll
    for (int b = 0; b < NA; ++b) {
      if (b == 2 && !third) break;
#pragma unroll
      for (int gp = 0; gp < 2; ++gp) {
        u32x4 v;
#pragma unroll
        for (int i = 0; i < 4; ++i) v[i] = pk2(acc[b][8 * gp + 2 * i], acc[b][8 * gp + 2 * i + 1]);
        st16(pb + b * 2048 + gp * 1024, v);
      }
    }
    wait_vm<0>();
    if (l2 == 0) st4((char*)flp + sp * 4, 1u);
  } else {
    spin_flags(flp, (unsigned)(((sp + 1 + (l2 % 3)) & 3) * 4));
    bf16x8 pv[NA][3][2];
    static_for<0, NA * 6>([&](auto Ic) {
      constexpr int i = decltype(Ic)::v, b = i / 6, o = (i >> 1) % 3, gp = i & 1;
      const char* pb = P + (size_t)((sp + 1 + o) & 3) * (64 * 18 * 2048) + pofs + (b == 2 && !third ? 1 : b) * 2048 + gp * 1024;
      pload<i>(pv[b][o][gp], pb, l2 * 16);
    });
    char* orow = out + (size_t)((rt * 4 + wm) * 32 + (l2 & 31)) * 1152 + (unit2 * 2 + boff + bb0) * 64 + (l2 >> 5) * 16;
    static_for<0, NA>([&](auto Bc) {
      constexpr int b = decltype(Bc)::v;
      wait_vm<(NA - 1 - b) * 6 + 2 * b>();
      if (b < 2 || third) {
        static_for<0, 6>([&](auto Ic) {
          constexpr int i = decltype(Ic)::v, gp = i & 1;
          ppin<b * 6 + i>(pv[b][i >> 1][gp]);
          const u32x4 v = __builtin_bit_cast(u32x4, pv[b][i >> 1][gp]);
#pragma unroll
          for (int k = 0; k < 4; ++k) {
            acc[b][8 * gp + 2 * k] += __uint_as_float(v[k] << 16);
            acc[b][8 * gp + 2 * k + 1] += __uint_as_float(v[k] & 0xffff0000u);
          }
        });
        u32x2 o2[4];
#pragma unroll
        for (int gq = 0; gq < 4; ++gq) {
          o2[gq][0] = pk2(acc[b][4 * gq], acc[b][4 * gq + 1]);
          o2[gq][1] = pk2(acc[b][4 * gq + 2], acc[b][4 * gq + 3]);
        }
#pragma unroll
        for (int gq = 0; gq < 4; gq += 2) {
          auto r0 = __builtin_amdgcn_permlane32_swap(o2[gq][0], o2[gq + 1][0], false, false);
          auto r1 = __builtin_amdgcn_permlane32_swap(o2[gq][1], o2[gq + 1][1], false, false);
          u32x4 wv = {r0[0], r1[0], r0[1], r1[1]};
          st16p(orow + b * 64 + gq * 16, wv);
        }
      }
    });
  }
  STAMP(2, 7);
}

__global__ __launch_bounds__(512, 4) void kernel1(const char* __restrict__ T,
                                                   char* O, const char* __restrict__ XN,
                                                   const char* __restrict__ W1, const unsigned short* __restrict__ b1,
                                                   char* H, const char* __restrict__ W2,
                                                   const unsigned short* __restrict__ b2, const char* __restrict__ Wo,
                                                   const char* __restrict__ G, char* P, char* FL,
                                                   char* __restrict__ out) {
  __shared__ __attribute__((aligned(16))) char lds[81920];
  const int wave = __builtin_amdgcn_readfirstlane(threadIdx.x >> 6);
  const int lane = threadIdx.x & 63;
  const int bid = blockIdx.x;
  if (bid < 256) {
    STAMP(1, 0);
    STAMP(5, 0);
    __builtin_amdgcn_s_setprio(ATT_PRIO);
    attn_role(lds, T, O, G, FL, W2, Wo, bid, wave, fresh_lane());
  } else {
    STAMP(2, 0);
    STAMP(4, 0);
    const int wg = bid - 256;
    const int xcd = wg & 7, idx = wg >> 3;
    const int sp = xcd >> 1, j4 = idx & 3, r8 = idx >> 2, rt = (xcd & 1) * 8 + r8;
    const int koff = (r8 * 9 / 2 + j4 * 9) % 36;
    if (j4 == 0 || j4 == 3)
      mlp_track<5>(lds, XN, W1, b1, H, W2, b2, Wo, O, G, P, FL, out, rt, sp, j4, koff, wave, lane);
    else
      mlp_track<4>(lds, XN, W1, b1, H, W2, b2, Wo, O, G, P, FL, out, rt, sp, j4, koff, wave, lane);
  }
}

extern "C" void kernel_launch(void* const* d_in, const int* in_sizes, int n_in, void* d_out, int out_size, void* d_ws,
                              size_t ws_size, hipStream_t stream) {
  const char* X = (const char*)d_in[0];
  const char* Wqkv = (const char*)d_in[1];
  const char* Wo = (const char*)d_in[2];
  const char* W1 = (const char*)d_in[4];
  const unsigned short* b1 = (const unsigned short*)d_in[5];
  const char* W2 = (const char*)d_in[6];
  const unsigned short* b2 = (const unsigned short*)d_in[7];
  const char* gw = (const char*)d_in[8];
  char* ws = (char*)d_ws;
  char* T = ws + WS_T;
  char* H = ws + WS_H;
  char* O = ws + WS_O;
  char* P = ws + WS_P;
  char* XN = ws + WS_XN;
  char* FL = ws + WS_FL;
  float* G = (float*)(ws + WS_G);
  kernelQ<<<248, 512, 0, stream>>>(X, Wqkv, gw, W1, T, XN, G, FL);
  kernel1<<<512, 512, 0, stream>>>(T, O, XN, W1, b1, H, W2, b2, Wo, (const char*)G, P, FL, (char*)d_out);
}
```

```cpp
#include <hip/hip_runtime.h>
#include <stdint.h>

typedef __attribute__((ext_vector_type(8))) short bf16x8;
typedef __attribute__((ext_vector_type(4))) short s16x4;
typedef __attribute__((ext_vector_type(2))) __bf16 bf16x2_t;
typedef __attribute__((ext_vector_type(2))) float f32x2;
typedef __attribute__((ext_vector_type(4))) float f32x4;
typedef __attribute__((ext_vector_type(16))) float f32x16;
typedef __attribute__((ext_vector_type(4))) unsigned u32x4;
typedef __attribute__((ext_vector_type(2))) unsigned u32x2;
typedef __attribute__((address_space(3))) s16x4* lds_s16x4_p;
typedef __attribute__((address_space(3))) char* lds_p;
typedef __attribute__((address_space(3))) const bf16x8* lds_bf16x8_p;

#define STAMP(k, i)
#define STAMP2(i)
#define SST(i)
#define SSD()
#define SSW()
#define DI __device__ __forceinline__
#define MFMA32(a, b, c) __builtin_amdgcn_mfma_f32_32x32x16_bf16((a), (b), (c), 0, 0, 0)
#define MFMA32K8(a, b, c) __builtin_amdgcn_mfma_f32_32x32x8bf16_1k((a), (b), (c), 0, 0, 0)
#define WAIT_VM(n) asm volatile("s_waitcnt vmcnt(" #n ")" ::: "memory")
#define BARRIER() __builtin_amdgcn_s_barrier()
#define SB() __builtin_amdgcn_sched_barrier(0)

constexpr int S_ = 2048, D_ = 576, F_ = 4608, NQKV = 1728;
constexpr int TSTEP = 640;
constexpr int TP = 1736 * 16;
constexpr int OEX = 64 * 32 * 1024;
constexpr size_t WS_T = 0;
constexpr size_t WS_H = 8u << 20;
constexpr size_t WS_O = 28u << 20;
constexpr size_t WS_G = 31u << 20;
constexpr size_t WS_FL = (31u << 20) + (64u << 10);
constexpr size_t WS_P = 32u << 20;
constexpr size_t WS_XN = 44u << 20;
constexpr size_t WS_XF = 47u << 20;
constexpr int FL_Q = 32768, FL_O = 36864, FL_P = 45056;

DI unsigned pk2(float a, float b) {
  f32x2 v = {a, b};
  bf16x2_t r = __builtin_convertvector(v, bf16x2_t);
  return __builtin_bit_cast(unsigned, r);
}
DI float bf2f(unsigned short u) { return __uint_as_float(((unsigned)u) << 16); }
DI s16x4 tr_read(const char* p) {
  return __builtin_amdgcn_ds_read_tr16_b64_v4i16((lds_s16x4_p)(p));
}
DI s16x4 trl(lds_p p) { return __builtin_amdgcn_ds_read_tr16_b64_v4i16((lds_s16x4_p)p); }
DI bf16x8 ld8(lds_p p) { return *(lds_bf16x8_p)p; }
DI float xor32(float v) { return __shfl_xor(v, 32, 64); }

DI void glds16s(const char* sbase, unsigned voff, unsigned lds_dst) {
  unsigned keep;
  asm volatile("s_mov_b32 %0, m0\n\ts_mov_b32 m0, %3\n\ts_nop 0\n\tglobal_load_lds_dwordx4 %1, %2\n\ts_mov_b32 m0, %0"
               : "=&s"(keep)
               : "v"(voff), "s"(sbase), "s"(lds_dst)
               : "memory");
}
DI void glds16c(const char* sbase, unsigned voff, unsigned lds_dst) {
  unsigned keep;
  asm volatile("s_mov_b32 %0, m0\n\ts_mov_b32 m0, %3\n\ts_nop 0\n\tglobal_load_lds_dwordx4 %1, %2 sc1\n\ts_mov_b32 m0, %0"
               : "=&s"(keep)
               : "v"(voff), "s"(sbase), "s"(lds_dst)
               : "memory");
}
DI void touch(const char* sbase, unsigned voff, unsigned lds_dst) {
  unsigned keep;
  asm volatile("s_mov_b32 %0, m0\n\ts_mov_b32 m0, %3\n\ts_nop 0\n\tglobal_load_lds_dword %1, %2\n\ts_mov_b32 m0, %0"
               : "=&s"(keep)
               : "v"(voff), "s"(sbase), "s"(lds_dst)
               : "memory");
}
DI void gld16(bf16x8& d, const char* sbase, unsigned voff) {
  asm volatile("global_load_dwordx4 %0, %1, %2" : "=v"(d) : "v"(voff), "s"(sbase) : "memory");
}
DI void gld16c(bf16x8& d, const char* sbase, unsigned voff) {
  asm volatile("global_load_dwordx4 %0, %1, %2 sc1" : "=v"(d) : "v"(voff), "s"(sbase) : "memory");
}
DI void gld4c(unsigned& d, const char* sbase, unsigned voff) {
  asm volatile("global_load_dword %0, %1, %2 sc1" : "=v"(d) : "v"(voff), "s"(sbase) : "memory");
}
DI void gld4(unsigned& d, const char* sbase, unsigned voff) {
  asm volatile("global_load_dword %0, %1, %2" : "=v"(d) : "v"(voff), "s"(sbase) : "memory");
}
DI unsigned lds_addr(const void* p) { return (unsigned)(uintptr_t)(lds_p)p; }

DI void st16(void* p, u32x4 v) { asm volatile("global_store_dwordx4 %0, %1, off sc1\n\ts_nop 1" ::"v"(p), "v"(v) : "memory"); }
DI void st16p(void* p, u32x4 v) { asm volatile("global_store_dwordx4 %0, %1, off\n\ts_nop 1" ::"v"(p), "v"(v) : "memory"); }
DI void st4(void* p, unsigned v) { asm volatile("global_store_dword %0, %1, off sc1" ::"v"(p), "v"(v) : "memory"); }

DI int fresh_lane() {
  int l;
  asm volatile("v_mbcnt_lo_u32_b32 %0, -1, 0\n\tv_mbcnt_hi_u32_b32 %0, -1, %0" : "=v"(l));
  return l;
}
template <int I>
struct IC { static constexpr int v = I; };
template <int I, int N, class F>
DI void static_for(F&& f) {
  if constexpr (I < N) {
    f(IC<I>{});
    static_for<I + 1, N>(f);
  }
}
template <int N>
DI void wait_vm() { asm volatile("s_waitcnt vmcnt(%0)" ::"i"(N) : "memory"); }
template <int N>
DI void wait_vm1(bf16x8& a) { asm volatile("s_waitcnt vmcnt(%1)" : "+v"(a) : "i"(N) : "memory"); }
DI void spin_flags(const char* base, unsigned voff) {
#pragma unroll 1
  for (int it = 0; it < (1 << 17); ++it) {
    unsigned f;
    gld4c(f, base, voff);
    asm volatile("s_waitcnt vmcnt(0)" : "+v"(f)::"memory");
    if (!__ballot(f != 1u)) break;
    __builtin_amdgcn_s_sleep(4);
  }
}
DI bf16x8 cat8(s16x4 lo, s16x4 hi) { return __builtin_shufflevector(lo, hi, 0, 1, 2, 3, 4, 5, 6, 7); }

DI f32x16 qk_tile(lds_p kb, lds_p ke, bf16x8 qf, s16x4 qe, f32x16 c) {
  s16x4 lo = trl(kb);
  s16x4 hi = trl(kb + 64);
  s16x4 ex = trl(ke);
  bf16x8 kf = __builtin_shufflevector(lo, hi, 0, 1, 2, 3, 4, 5, 6, 7);
  f32x16 s = MFMA32(kf, qf, c);
  s = MFMA32K8(ex, qe, s);
  return s;
}
DI f32x16 pv_tile(lds_p vb, const f32x16& pe, f32x16 o) {
  bf16x8 v0 = ld8(vb);
  bf16x8 v1 = ld8(vb + TSTEP);
  u32x4 p0, p1;
#pragma unroll
  for (int i = 0; i < 4; ++i) {
    p0[i] = pk2(pe[2 * i], pe[2 * i + 1]);
    p1[i] = pk2(pe[8 + 2 * i], pe[8 + 2 * i + 1]);
  }
  o = MFMA32(v0, __builtin_bit_cast(bf16x8, p0), o);
  o = MFMA32(v1, __builtin_bit_cast(bf16x8, p1), o);
  return o;
}
struct KFrag { bf16x8 f; s16x4 e; };
struct VFrag { bf16x8 v0, v1; };
DI KFrag load_k(lds_p kb, lds_p ke) {
  KFrag k;
  s16x4 lo = trl(kb);
  s16x4 hi = trl(kb + 64);
  k.e = trl(ke);
  k.f = __builtin_shufflevector(lo, hi, 0, 1, 2, 3, 4, 5, 6, 7);
  return k;
}
DI VFrag load_v(lds_p vb) {
  VFrag v;
  v.v0 = ld8(vb);
  v.v1 = ld8(vb + TSTEP);
  return v;
}
#define EXP2(x) __builtin_amdgcn_exp2f(x)
#define APM 2
#define APMASK 0x77
#define PHI(i) if constexpr ((PM >> (i)) & 1) __builtin_amdgcn_s_setprio(APM)
#define PLO(i) if constexpr ((PM >> (i)) & 1) __builtin_amdgcn_s_setprio(0)
template <int PM>
DI void tile_step(const f32x16& s, f32x16& sn, const KFrag& k, const VFrag& v, bf16x8 qf, s16x4 qe,
                  f32x16& o) {
  f32x16 negm;
#pragma unroll
  for (int i = 0; i < 16; ++i) negm[i] = 0.f;
  u32x4 p0, p1;
  p0[0] = pk2(EXP2(s[0]), EXP2(s[1]));
  p0[1] = pk2(EXP2(s[2]), EXP2(s[3]));
  SB();
  PHI(0);
  SB();
  f32x16 t = MFMA32(k.f, qf, negm);
  SB();
  PLO(0);
  SB();
  p0[2] = pk2(EXP2(s[4]), EXP2(s[5]));
  p0[3] = pk2(EXP2(s[6]), EXP2(s[7]));
  SB();
  PHI(1);
  SB();
  o = MFMA32(v.v0, __builtin_bit_cast(bf16x8, p0), o);
  SB();
  PLO(1);
  SB();
  p1[0] = pk2(EXP2(s[8]), EXP2(s[9]));
  p1[1] = pk2(EXP2(s[10]), EXP2(s[11]));
  SB();
  PHI(2);
  SB();
  t = MFMA32K8(k.e, qe, t);
  SB();
  PLO(2);
  SB();
  p1[2] = pk2(EXP2(s[12]), EXP2(s[13]));
  p1[3] = pk2(EXP2(s[14]), EXP2(s[15]));
  SB();
  PHI(3);
  SB();
  o = MFMA32(v.v1, __builtin_bit_cast(bf16x8, p1), o);
  SB();
  PLO(3);
  SB();
  sn = t;
}
DI float rowmax16(const f32x16& s) {
  float m = __builtin_fmaxf(s[0], s[1]);
#pragma unroll
  for (int i = 2; i < 16; ++i) m = __builtin_fmaxf(m, s[i]);
  return __builtin_fmaxf(m, xor32(m));
}

constexpr int AT_SLOT = 20480;
constexpr int at_cnt(int c, int grp) { return 2 + (((c & 1) == grp) ? 1 : 0); }
constexpr int at_off(int tt) { return ((tt >> 3) & 1) * AT_SLOT + (tt & 7) * 1280; }

#define TOUCH_AT 9
DI void attn_role(char* lds, const char* T, char* O, const char* __restrict__ G, char* FL, const char* __restrict__ W2,
                  const char* __restrict__ Wo, int wg, int wave, int lane) {
  const int head = (wg & 7) * 4 + ((wg >> 3) & 3);
  const int qt = wg >> 5;
  const int r = lane & 31, h = lane >> 5;
  const int grp = wave >> 2;
  const unsigned lbase = lds_addr(lds);
  const int p1 = wave + 8, p2 = 16 + (wave & 3);
  auto mkoff = [&](int pc, int first) {
    int isv = pc >= 10;
    int sl = (pc - 10 * isv) * 64 + lane;
    int seg = sl / 20, row = sl - seg * 20;
    int slot = row < 18 ? first + 576 * isv + head * 18 + row : 1728 + row - 18;
    return (unsigned)(seg * TP + slot * 16);
  };
  const unsigned v0 = mkoff(wave, 576), v1 = mkoff(p1, 576), v2 = mkoff(p2, 576);
  auto issue = [&](int c) {
    unsigned dst = lbase + (c & 3) * AT_SLOT;
    const char* src = T + (size_t)c * (32 * TP);
    glds16s(src, v0, dst + wave * 1024);
    glds16s(src, v1, dst + p1 * 1024);
    if ((c & 1) == grp) glds16s(src, v2, dst + p2 * 1024);
  };
  STAMP(1, 1);
  unsigned gbits;
  gld4(gbits, G + (qt * 256 + wave * 32) * 4, r * 4);
  {
    const char* src = T + (size_t)qt * (32 * TP);
    glds16s(src, mkoff(wave, 0), lbase + 3 * AT_SLOT + wave * 1024);
    if (wave < 2) glds16s(src, mkoff(p1, 0), lbase + 3 * AT_SLOT + p1 * 1024);
  }
  issue(0);
  issue(1);
  issue(2);
  const int q = (lane >> 2) & 3, p = lane & 3, g = (lane >> 4) & 1;
  const lds_p L = (lds_p)lds;
  const lds_p kb = L + g * TSTEP + (p & 1) * (TSTEP / 2) + (8 * h + q) * 16 + (p >> 1) * 8;
  const lds_p ke = L + g * TSTEP + (p & 1) * (TSTEP / 2) + (16 + q) * 16 + (p >> 1) * 8;
  const lds_p vb = L + 10240 + h * (TSTEP / 2) + (r < 19 ? r : 19) * 16;
  lds_p kb2 = kb + 2 * AT_SLOT, ke2 = ke + 2 * AT_SLOT, vb2 = vb + 2 * AT_SLOT;
  asm volatile("" : "+v"(kb2), "+v"(ke2), "+v"(vb2));
  asm volatile("s_waitcnt vmcnt(5)" : "+v"(gbits)::"memory");
  BARRIER();
  STAMP(1, 2);
  bf16x8 qf;
  s16x4 qe;
  {
    KFrag qq = load_k(kb2 + AT_SLOT + wave * 1280, ke2 + AT_SLOT + wave * 1280);
    qf = qq.f;
    qe[0] = h ? (short)0 : qq.e[0];
    qe[1] = h ? (short)0 : qq.e[1];
    qe[2] = 0;
    qe[3] = 0;
  }

  const float gate = __uint_as_float(gbits);
  f32x16 zero;
#pragma unroll
  for (int i = 0; i < 16; ++i) zero[i] = 0.f;
  f32x16 o = zero;
  f32x16 sA = qk_tile(kb, ke, qf, qe, zero);
  {
    const float nx = -rowmax16(sA);
    const unsigned mb = (__float_as_uint(nx) + (nx > 0.f ? 0xffffu : 0u)) >> 16;
    const float nm = __uint_as_float(mb << 16);
#pragma unroll
    for (int i = 0; i < 16; ++i) sA[i] += nm;
    qe[2] = h ? (short)0 : (short)mb;
  }
  KFrag kA, kB;
  VFrag vA, vB;
  kB = load_k(kb + 1280, ke + 1280);
  vA = load_v(vb);
  f32x16 sB;
#pragma unroll 1
  for (int cc = 0; cc < 8; cc += 4) {
    static_for<0, 32>([&](auto Tc) {
      constexpr int tt = decltype(Tc)::v, ci = tt >> 3;
      if constexpr ((tt & 7) == 6) {
        if (cc == 0 || ci < 2) {
          if (grp == 0) wait_vm<at_cnt(ci + 2, 0)>(); else wait_vm<at_cnt(ci + 2, 1)>();
        } else if (ci == 2) wait_vm<0>();
        if (cc == 0 || ci < 3) BARRIER();
        if constexpr (ci == TOUCH_AT) {
          if (cc == 0) {
            const int l2 = fresh_lane();
            const char* src = W2 + (size_t)((wg & 7) >> 1) * (1152 * 1152) + (wg >> 3) * 41472;
            const unsigned dst = lbase + ((ci + 3) & 3) * AT_SLOT + wave * 1024;
            touch(src, (wave * 41 + l2 - (l2 > 40 ? 24 : 0) - (wave >> 2) * 4) * 128u, dst);
            if (wave == 0) touch(Wo + ((wg & 7) >> 1) * (144 * 1152) + (wg >> 3) * 5184, (l2 > 39 ? ((wg & 8) ? 39 : 40) : l2) * 128u + ((wg & 8) ? 64u : 0u), dst);
          }
        }
        if (cc == 0 || ci == 0) issue(cc + ci + 3);
      }
      constexpr int t1 = (tt + 1) & 31, t2 = (tt + 2) & 31;
      const lds_p kp = (t2 < 16 ? kb : kb2) + at_off(t2);
      const lds_p ep = (t2 < 16 ? ke : ke2) + at_off(t2);
      const lds_p vp = (t1 < 16 ? vb : vb2) + at_off(t1);
      if constexpr ((tt & 1) == 0) {
        kA = load_k(kp, ep);
        vB = load_v(vp);
        SB();
        tile_step<APMASK & 15>(sA, sB, kB, vA, qf, qe, o);
      } else {
        kB = load_k(kp, ep);
        vA = load_v(vp);
        SB();
        tile_step<(APMASK >> 4) & 15>(sB, sA, kA, vB, qf, qe, o);
      }
    });
    if (cc == 0) STAMP(1, 3);
  }
  STAMP(1, 4);
  STAMP(1, 5);
  float l = xor32(o[10]);
  l = h ? l : o[10];
  bool bad = !(l < 1.0e30f);
#pragma unroll
  for (int i = 0; i < 10; ++i) bad = bad || !(__builtin_fabsf(o[i]) < 1.0e37f);
  const int ob = (qt * 8 + wave) * 32 + head;
  auto publish = [&]() {
    const float inv = gate / l;
    u32x4 ov;
#pragma unroll
    for (int i = 0; i < 4; ++i) ov[i] = pk2(o[2 * i] * inv, o[2 * i + 1] * inv);
    st16(O + (size_t)ob * 1024 + lane * 16, ov);
    if (h == 0) st4(O + OEX + (ob >> 3) * 1024 + ((head >> 2) & 1) * 512 + r * 16 + (head & 3) * 4, pk2(o[8] * inv, o[9] * inv));
    STAMP(1, 6);
    wait_vm<0>();
    if (lane == 0) st4(FL + FL_O + ob * 4, 1u);
  };
  const bool mybad = __any(bad);
  if (!mybad) publish();
  {
    int* fl = (int*)(lds + AT_SLOT);
    if (lane == 0) fl[wave] = mybad ? 1 : 0;
    __syncthreads();
    int v = fl[lane & 7];
    bad = __any(v != 0);
  }
  if (bad) {
    float m = -3.0e38f;
    o = zero;
    qe[2] = 0;
#pragma unroll 1
    for (int c = 0; c < 8; ++c) {
      __syncthreads();
      {
        const char* src = T + (size_t)c * (32 * TP);
        glds16s(src, v0, lbase + wave * 1024);
        glds16s(src, v1, lbase + p1 * 1024);
        if (grp == 0) glds16s(src, v2, lbase + p2 * 1024);
      }
      wait_vm<0>();
      __syncthreads();
#pragma unroll 1
      for (int t = 0; t < 8; ++t) {
        f32x16 s = qk_tile(kb + t * 1280, ke + t * 1280, qf, qe, zero);
        float mn = __builtin_fmaxf(m, rowmax16(s));
        float alpha = __builtin_amdgcn_exp2f(m - mn);
        m = mn;
        f32x16 pe;
#pragma unroll
        for (int i = 0; i < 16; ++i) { pe[i] = __builtin_amdgcn_exp2f(s[i] - mn); o[i] *= alpha; }
        o = pv_tile(vb + t * 1280, pe, o);
      }
    }
    l = xor32(o[10]);
    l = h ? l : o[10];
    if (mybad) publish();
  }
  STAMP(1, 7);
}

#define QKV_PRIO 2
#define ATT_PRIO 0
#define MLP_PRIO 1
#define PF2_AT 20
constexpr int M_IT = 36;
constexpr int M2_NT = 41;
struct M2S { int slot[M2_NT]; int it[M2_NT]; };
constexpr M2S m2_sched(int U) {
  M2S r{};
  const int E = 2 * U - 2;
  int next = 0;
  for (; next < 4; ++next) { r.slot[next] = next; r.it[next] = -1; }
  for (int j = 1; next < M2_NT; ++j) {
    r.slot[next] = r.slot[j - 1];
    r.it[next] = j;
    ++next;
    if (next < M2_NT && (j == E || j == E + 1)) { r.slot[next] = 4 + (j - E); r.it[next] = j; ++next; }
  }
  return r;
}
constexpr int m2_ck1(int U) { return 2 * U - 4; }
constexpr int m2_ck2(int U) { return (U == 5 ? 26 : 2 * U + 8) - 4; }
constexpr int m2_wait(int U, int kh, int j, int mode) {
  const M2S sc = m2_sched(U);
  const int U2 = U == 5 ? 2 : 3;
  int n = 0, last = -1;
  for (int i = -1; i < j + mode; ++i) {
    if (i == m2_ck1(U) - 2 || i == m2_ck2(U) - 2) { if (mode == 1 && i == j - 2) last = n; ++n; }
    for (int s = 0; s < M2_NT; ++s)
      if (sc.it[s] == i) {
        const int c = (U2 == 3 && (s & 1) == kh) ? 2 : 1;
        n += c;
        if (mode == 0 && s <= j + 1) last = n - 1;
      }
    if (i == -1) n += 2 * U;
    if (i >= 0 && i < j && i + 4 >= 2 * U && i + 4 < M_IT) { if (mode == 0 && i + 4 <= j) last = n; ++n; }
  }
  return n - 1 - last;
}
constexpr bool m2_need(int U, int j) { return j >= 3; }

template <int N>
DI void pin_acc(f32x16* a) {
  if constexpr (N == 5)
    asm volatile("" : "+{v[0:15]}"(a[0]), "+{v[16:31]}"(a[1]), "+{v[32:47]}"(a[2]), "+{v[48:63]}"(a[3]), "+{v[64:79]}"(a[4]));
  else
    asm volatile("" : "+{v[0:15]}"(a[0]), "+{v[16:31]}"(a[1]), "+{v[32:47]}"(a[2]), "+{v[48:63]}"(a[3]));
}
#define XLD(R) asm volatile("global_load_dwordx4 %0, %1, %2 sc1" : "={" R "}"(d) : "v"(voff), "s"(sbase) : "memory")
template <int I>
DI void xload(bf16x8& d, const char* sbase, unsigned voff) {
  if constexpr (I == 0) XLD("v[80:83]");
  else if constexpr (I == 1) XLD("v[84:87]");
  else if constexpr (I == 2) XLD("v[88:91]");
  else XLD("v[92:95]");
}
template <int I>
DI void pload(bf16x8& d, const char* sbase, unsigned voff) {
  if constexpr (I == 0) XLD("v[48:51]");
  else if constexpr (I == 1) XLD("v[52:55]");
  else if constexpr (I == 2) XLD("v[56:59]");
  else if constexpr (I == 3) XLD("v[60:63]");
  else if constexpr (I == 4) XLD("v[64:67]");
  else if constexpr (I == 5) XLD("v[68:71]");
  else if constexpr (I == 6) XLD("v[72:75]");
  else if constexpr (I == 7) XLD("v[76:79]");
  else if constexpr (I == 8) XLD("v[80:83]");
  else if constexpr (I == 9) XLD("v[84:87]");
  else if constexpr (I == 10) XLD("v[88:91]");
  else if constexpr (I == 11) XLD("v[92:95]");
  else if constexpr (I == 12) XLD("v[96:99]");
  else if constexpr (I == 13) XLD("v[100:103]");
  else if constexpr (I == 14) XLD("v[104:107]");
  else if constexpr (I == 15) XLD("v[108:111]");
  else if constexpr (I == 16) XLD("v[112:115]");
  else if constexpr (I == 17) XLD("v[116:119]");
}
template <int I>
DI void ppin(bf16x8& d) {
  if constexpr (I == 0) asm volatile("" : "+{v[48:51]}"(d));
  else if constexpr (I == 1) asm volatile("" : "+{v[52:55]}"(d));
  else if constexpr (I == 2) asm volatile("" : "+{v[56:59]}"(d));
  else if constexpr (I == 3) asm volatile("" : "+{v[60:63]}"(d));
  else if constexpr (I == 4) asm volatile("" : "+{v[64:67]}"(d));
  else if constexpr (I == 5) asm volatile("" : "+{v[68:71]}"(d));
  else if constexpr (I == 6) asm volatile("" : "+{v[72:75]}"(d));
  else if constexpr (I == 7) asm volatile("" : "+{v[76:79]}"(d));
  else if constexpr (I == 8) asm volatile("" : "+{v[80:83]}"(d));
  else if constexpr (I == 9) asm volatile("" : "+{v[84:87]}"(d));
  else if constexpr (I == 10) asm volatile("" : "+{v[88:91]}"(d));
  else if constexpr (I == 11) asm volatile("" : "+{v[92:95]}"(d));
  else if constexpr (I == 12) asm volatile("" : "+{v[96:99]}"(d));
  else if constexpr (I == 13) asm volatile("" : "+{v[100:103]}"(d));
  else if constexpr (I == 14) asm volatile("" : "+{v[104:107]}"(d));
  else if constexpr (I == 15) asm volatile("" : "+{v[108:111]}"(d));
  else if constexpr (I == 16) asm volatile("" : "+{v[112:115]}"(d));
  else if constexpr (I == 17) asm volatile("" : "+{v[116:119]}"(d));
}
#define XDS(R) asm volatile("ds_read_b128 %0, %1 offset:%2" : "={" R "}"(d) : "v"(a), "i"(OFF) : "memory")
template <int I, int OFF>
DI void xstash(bf16x8& d, unsigned a) {
  if constexpr (I == 0) XDS("v[80:83]");
  else if constexpr (I == 1) XDS("v[84:87]");
  else if constexpr (I == 2) XDS("v[88:91]");
  else XDS("v[92:95]");
}
template <int I>
DI void pin_x1(bf16x8& x) {
  if constexpr (I == 0) asm volatile("" : "+{v[80:83]}"(x));
  else if constexpr (I == 1) asm volatile("" : "+{v[84:87]}"(x));
  else if constexpr (I == 2) asm volatile("" : "+{v[88:91]}"(x));
  else asm volatile("" : "+{v[92:95]}"(x));
}
template <int I>
DI void pin_w(bf16x8& w) {
  if constexpr (I == 0) asm volatile("" : "+{v[96:99]}"(w));
  else if constexpr (I == 1) asm volatile("" : "+{v[100:103]}"(w));
  else if constexpr (I == 2) asm volatile("" : "+{v[104:107]}"(w));
  else if constexpr (I == 3) asm volatile("" : "+{v[108:111]}"(w));
  else asm volatile("" : "+{v[112:115]}"(w));
}
template <int U>
DI void mlp_track(char* lds, const char* __restrict__ XN, const char* __restrict__ W1,
                  const unsigned short* __restrict__ b1, char* H, const char* __restrict__ W2,
                  const unsigned short* __restrict__ b2, const char* __restrict__ Wo, const char* O,
                  const char* __restrict__ G, char* P, char* FL, char* __restrict__ out, int rt, int sp, int j4,
                  int koff, int wave, int lane) {
  constexpr int NBW = 9 - U, U2 = U == 5 ? 2 : 3;
  constexpr int NS1 = U == 5 ? 5 : 6;
  SSD();
  SST(0);
  const bool up = j4 < 2;
  const int unit0 = sp * 18 + (U == 5 ? (up ? 0 : 13) : (up ? 5 : 9));
  const unsigned lbase = lds_addr(lds);
  const lds_p L = (lds_p)lds;
  const int wm = wave & 3, wn = wave >> 2;
  char* FLg = FL + (size_t)(rt * 4 + sp) * 512;
  f32x16 acc[5];
#pragma unroll
  for (int b = 0; b < 5; ++b)
#pragma unroll
    for (int i = 0; i < 16; ++i) acc[b][i] = 0.f;
  bf16x8 w[5];
  {
    constexpr int WS = 2 * U * 1024, XB = NS1 * WS, NP = 4 + 2 * U;
    const int h = lane >> 5;
    const unsigned voffW = (unsigned)((lane >> 3) * 9216 + (((lane & 7) ^ (((lane >> 4) & 1) << 2)) << 4));
    const unsigned voff0 = wave < 4 ? (unsigned)(lane * 16) : voffW;
    const int q0 = wave - 4, q1 = wave + 4;
    const char* wb = W1 + unit0 * 128;
    const char* src0 =
        wave < 4 ? XN + (size_t)(rt * 4 + wave) * (36 * 1024) : wb + (q0 / U) * (8 * 9216) + (q0 % U) * 128;
    const char* src1 = wb + (q1 / U) * (8 * 9216) + (q1 % U) * 128;
    const unsigned st0 = wave < 4 ? 1024u : 147456u;
    const bool two = wave + 8 < NP;
    const unsigned dst0 = lbase + (wave < 4 ? XB + wave * 1024 : q0 * 1024), ds0 = wave < 4 ? 4096u : (unsigned)WS;
    auto issue = [&](int s, int e) {
      int ks = s + koff;
      ks = ks >= 36 ? ks - 36 : ks;
      if (e == 0) glds16s(src0 + (unsigned)ks * st0, voff0, dst0 + (s % NS1) * ds0);
      else if (two) glds16s(src1 + (unsigned)ks * 147456u, voffW, lbase + (s % NS1) * WS + q1 * 1024);
    };
    auto waitn = [&](auto Nc) {
      constexpr int N = decltype(Nc)::v;
      if (two) wait_vm<2 * N>(); else wait_vm<N>();
    };
#pragma unroll
    for (int s = 0; s < NS1; ++s) { issue(s, 0); issue(s, 1); }
    const int q = (lane >> 2) & 3, p = lane & 3, g = (lane >> 4) & 1;
    lds_p xa = L + XB + wm * 1024 + lane * 16;
    lds_p wa[2];
#pragma unroll
    for (int e = 0; e < 2; ++e) {
      int b = wn * U + e;
      wa[e] = L + (h * U + (b >> 1)) * 1024 + q * 128 + (((b & 1) ^ (q >> 1)) << 6) + g * 32 + p * 8;
    }
    asm volatile("" : "+v"(xa), "+v"(wa[0]), "+v"(wa[1]));
    waitn(IC<NS1 - 1>{});
    BARRIER();
    __builtin_amdgcn_s_setprio(MLP_PRIO);
    STAMP(4, 1);
    auto rdx = [&](int sl) { return ld8(xa + sl * 4096); };
    auto rdw = [&](int sl, int m) {
      const lds_p a = wa[m & 1] + sl * WS + (m >> 1) * 1024;
      return cat8(trl(a), trl(a + 512));
    };
    bf16x8 x0, x1;
    x0 = rdx(0);
#pragma unroll
    for (int m = 0; m < U; ++m) w[m] = rdw(0, m);
    unsigned short bb[U];
    unsigned pfd;
    static_for<0, 36>([&](auto Jc) {
      constexpr int j = decltype(Jc)::v;
      if constexpr (j + 1 < 36) {
        constexpr int top = j == 0 ? NS1 - 1 : (j + NS1 - 2 < 35 ? j + NS1 - 2 : 35);
        waitn(IC<top - (j + 1)>{});
        BARRIER();
      }
      STAMP2(j);
      if (j == 12) STAMP(4, 2);
      if (j == 24) STAMP(4, 3);
      SB();
      bf16x8& xc = (j & 1) ? x1 : x0;
      bf16x8& xn = (j & 1) ? x0 : x1;
      static_for<0, U>([&](auto Mc) {
        constexpr int m = decltype(Mc)::v;
        acc[m] = MFMA32(w[m], xc, acc[m]);
        SB();
        if constexpr (j + 1 < 36) {
          if constexpr (m == 0) xn = rdx((j + 1) % NS1);
          w[m] = rdw((j + 1) % NS1, m);
        }
        if constexpr (j >= 1 && j + NS1 - 1 < 36 && (m == 1 || m == 2)) issue(j + NS1 - 1, m - 1);
        if constexpr (j == PF2_AT && m == 3) {
          const unsigned l2 = fresh_lane(), li = l2 < 18 * U2 ? l2 : l2 - 32, row = li / U2;
          const unsigned vo = row * 1152u + (li - row * U2) * 128u;
          const int u2 = U == 5 ? (up ? 0 : 7) : 2 * j4;
          gld4(pfd, W2 + (size_t)(sp * 1152 + (rt & 7) * 144 + wave * 18) * 1152 + u2 * 128, vo);
          if (wave == 7) gld4(pfd, Wo + (sp * 144 + (rt & 7) * 18) * 1152 + u2 * 128, vo);
        }
        if constexpr (j == PF2_AT + NS1 + 1 && m == 0) asm volatile("" ::"v"(pfd));
        if constexpr (j == 34 && m == 0) {
          const int l2 = fresh_lane();
#pragma unroll
          for (int b = 0; b < U; ++b) bb[b] = b1[(unit0 * 2 + wn * U + b) * 32 + (l2 & 31)];
        }
        SB();
      });
    });
    STAMP2(36);
    STAMP(4, 4);
    const bf16x8 xx = {(short)(fresh_lane() >> 5 ? 0 : 0x3F80), 0, 0, 0, 0, 0, 0, 0};
#pragma unroll
    for (int b = 0; b < U; ++b) {
      const bf16x8 wx = {(short)bb[b], 0, 0, 0, 0, 0, 0, 0};
      acc[b] = MFMA32(wx, xx, acc[b]);
    }
    asm volatile("" : "+v"(acc[0]), "+v"(acc[U - 1]));
  }
  BARRIER();
  constexpr int B1 = 2 * U, B2 = B1 + 8, B3 = 26;
  constexpr M2S SC = m2_sched(U);
  const int e0 = 2 * (U == 5 ? (up ? 0 : 26) : (up ? 10 : 18));
  const int e1 = 2 * (U == 5 ? (up ? 10 : 18) : (up ? 18 : 10)) - B1;
  const int e2 = 2 * (U == 5 ? (up ? 18 : 10) : (up ? 26 : 0)) - B2;
  const int e3 = 2 * (U == 5 ? (up ? 26 : 0) : (up ? 0 : 26)) - B3;
  auto kstep = [&](int s, int half) {
    const int uc2 = s < B1 ? 2 * U : (s < B2 ? 8 : (s < B3 ? (U == 5 ? 8 : 10) : 10));
    return (unsigned)(s + (s < B1 ? e0 : (s < B2 ? e1 : (s < B3 ? e2 : e3))) + half * uc2);
  };
  const int kh = wn;
  const int unit2 = U == 5 ? (up ? 0 : 7) : 2 * j4, boff = U == 5 ? 0 : j4 - 1;
  const int ln = fresh_lane();
  const unsigned voffX = ln * 16;
  const unsigned voffW = (unsigned)((ln >> 3) * 1152 + (((ln & 7) ^ (((ln >> 4) & 1) << 2)) << 4));
  const char* xsrc = H + ((size_t)(rt * 4 + wm) * 288 + sp * 72) * 1024;
  const int p0 = wave, p2 = 8 + (wave & 3);
  const int kb0 = p0 / U2, kb2 = p2 / U2;
  const unsigned c0 = (kb0 & 1) * (8 * 1152) + (unit2 + p0 % U2) * 128, c2 = (kb2 & 1) * (8 * 1152) + (unit2 + p2 % U2) * 128;
  const char* w2s = W2 + (size_t)sp * 1152 * 1152;
  const char* wos = Wo + sp * (8 * 18 * 1152);
  constexpr int SL2 = U2 * 4096;
  auto issueW = [&](auto Sc, int e) {
    constexpr int s = decltype(Sc)::v;
    const unsigned dst = lbase + SC.slot[s] * SL2 + (e ? p2 : p0) * 1024;
    if (e == 1 && !(U2 == 3 && (s & 1) == kh)) return;
    const int half = e ? 1 : kb0 >> 1;
    const unsigned c = e ? c2 : c0;
    if constexpr (s < M_IT) glds16s(w2s + (c + kstep(s, half) * (16u * 1152u)), voffW, dst);
    else if constexpr (s < M2_NT - 1) glds16s(wos + (c + (unsigned)(4 * half + s - M_IT) * (18u * 1152u)), voffW, dst);
    else {
      const int l2 = fresh_lane(), row = l2 >> 3;
      const unsigned ve = (unsigned)(((4 * (row >> 2) + ((row & 3) >> 1)) * 18 + (row & 1)) * 1152 + (((l2 & 7) ^ (((l2 >> 4) & 1) << 2)) << 4));
      const int kbl = (e ? kb2 : kb0) & 1;
      glds16s(wos + (c + (unsigned)(kbl * (36 - 8) + 16) * 1152u), ve, dst);
    }
  };
  unsigned b2v[NBW];
  const bool addb = sp == 0 && kh == 0;
#pragma unroll
  for (int b = 0; b < NBW; ++b)
    asm volatile("global_load_ushort %0, %1, %2" : "=v"(b2v[b]) : "v"((ln & 31) * 2), "s"(b2 + (unit2 * 2 + boff + b) * 32) : "memory");
  static_for<0, 4>([&](auto Sc) { issueW(Sc, 0); issueW(Sc, 1); });
  bf16x8 xr[4];
  const unsigned stash = lbase + 4 * SL2 + wave * ((2 * U - 4) * 1024) + voffX;
  {
    const float C0 = -2.0f * 1.4426950408889634f * 0.7978845608028654f;
    const float C1 = C0 * 0.044715f;
    char* hb = H + ((size_t)(rt * 4 + wm) * 288 + (unit0 * 2 + wn * U) * 2) * 1024 + fresh_lane() * 16;
#pragma unroll
    for (int b = 0; b < U; ++b) {
#pragma unroll
      for (int gp = 0; gp < 2; ++gp) {
        u32x4 o4;
#pragma unroll
        for (int i = 0; i < 4; ++i) {
          float o[2];
#pragma unroll
          for (int e = 0; e < 2; ++e) {
            float v = acc[b][8 * gp + 2 * i + e];
            float t = v * __builtin_fmaf(v * v, C1, C0);
            o[e] = v * __builtin_amdgcn_rcpf(1.0f + __builtin_amdgcn_exp2f(t));
          }
          o4[i] = pk2(o[0], o[1]);
        }
        st16(hb + (b * 2 + gp) * 1024, o4);
        if (b < 2) xr[b * 2 + gp] = __builtin_bit_cast(bf16x8, o4);
        else *(__attribute__((address_space(3))) u32x4*)(uintptr_t)(stash + (b * 2 + gp - 4) * 1024) = o4;
      }
    }
  }
  STAMP(4, 5);
  lds_p wa[2];
  {
    const int h = ln >> 5, q = (ln >> 2) & 3, p = ln & 3, g = (ln >> 4) & 1;
#pragma unroll
    for (int e = 0; e < 2; ++e) {
      int b = boff + e;
      wa[e] = L + (2 * kh * U2 + (b >> 1)) * 1024 + (4 * h + q) * 128 + (((b & 1) ^ (q >> 1)) << 6) + g * 32 + p * 8;
    }
  }
  asm volatile("" : "+v"(wa[0]), "+v"(wa[1]));
  wait_vm<2 * U>();
  {
    f32x16 zero;
#pragma unroll
    for (int i = 0; i < 16; ++i) zero[i] = 0.f;
    const bf16x8 xx = {(short)((ln >> 5) || !addb ? 0 : 0x3F80), 0, 0, 0, 0, 0, 0, 0};
#pragma unroll
    for (int b = 0; b < NBW; ++b) {
      asm volatile("" : "+v"(b2v[b]));
      const bf16x8 wx = {(short)b2v[b], 0, 0, 0, 0, 0, 0, 0};
      acc[b] = MFMA32(wx, xx, zero);
    }
  }
  BARRIER();
  STAMP(4, 6);
  STAMP(4, 7);
  STAMP(2, 1);
  SST(1);
  auto rdw = [&](int s, int m) {
    const lds_p a = wa[m & 1] + SC.slot[s] * SL2 + (m >> 1) * 1024;
    return cat8(trl(a), trl(a + U2 * 1024));
  };
#pragma unroll
  for (int m = 0; m < NBW; ++m) w[m] = rdw(0, m);
  unsigned fv = 0;
  auto poll = [&]() {
    unsigned vo = voffX;
    asm volatile("" : "+v"(vo));
    vo = (vo & 48u) << 3;
    gld4c(fv, FLg, vo);
  };
  auto check = [&](unsigned long long mask) {
    mask &= ~(1ull << j4);
    asm volatile("" : "+v"(fv));
    if (__ballot(fv != 1u) & mask) {
#pragma unroll 1
      for (int it = 0; it < (1 << 17); ++it) {
        __builtin_amdgcn_s_sleep(4);
        poll();
        wait_vm<0>();
        asm volatile("" : "+v"(fv));
        if (!(__ballot(fv != 1u) & mask)) break;
      }
    }
  };
  auto waitk = [&](auto N0, auto N1) {
    constexpr int n0 = decltype(N0)::v, n1 = decltype(N1)::v;
    if constexpr (n0 == n1) wait_vm<n0>();
    else { if (kh == 0) wait_vm<n0>(); else wait_vm<n1>(); }
  };
  constexpr int CK1 = m2_ck1(U), CK2 = m2_ck2(U);
  unsigned gv;
  static_for<0, M_IT>([&](auto Jc) {
    constexpr int j = decltype(Jc)::v;
    if constexpr (m2_need(U, j)) waitk(IC<m2_wait(U, 0, j, 0)>{}, IC<m2_wait(U, 1, j, 0)>{});
    if constexpr (j > 0) BARRIER();
    if constexpr (j > 0) pin_acc<NBW>(acc);
    static_for<0, 4>([&](auto Ic) {
      constexpr int i = decltype(Ic)::v;
      if constexpr (j + ((i - j) & 3) < M_IT) pin_x1<i>(xr[i]);
    });
    if constexpr (j == 3) {
      if (wave == 0 && voffX == 0) st4(FLg + j4 * 128, 1u);
    }
    STAMP2(40 + j);
    if (j == 9) STAMP(2, 2);
    if (j == 18) STAMP(2, 3);
    if (j == 18) SST(2);
    if (j == 27) STAMP(2, 4);
    if constexpr (j == CK1 - 2 || j == CK2 - 2) poll();
    SB();
    static_for<0, NBW>([&](auto Mc) {
      constexpr int m = decltype(Mc)::v;
      pin_w<m>(w[m]);
      acc[m] = MFMA32(w[m], xr[j & 3], acc[m]);
      SB();
      w[m] = rdw(j + 1, m);
      static_for<j + 3, (j + 7 < M2_NT ? j + 7 : M2_NT)>([&](auto Sc) {
        constexpr int s = decltype(Sc)::v;
        if constexpr (SC.it[s] == j) {
          constexpr bool second = s > 0 && SC.it[s - 1] == j;
          if constexpr (m == (second ? 2 : 0)) issueW(Sc, 0);
          if constexpr (m == (second ? 3 : 1)) issueW(Sc, 1);
        }
      });
      SB();
    });
    if constexpr (j == CK1 || j == CK2) {
      waitk(IC<m2_wait(U, 0, j, 1)>{}, IC<m2_wait(U, 1, j, 1)>{});
      check(j == CK1 ? 6ull : 15ull);
    }
    if constexpr (j + 4 < B1) xstash<(j & 3), j * 1024>(xr[j & 3], stash);
    else if constexpr (j + 4 < M_IT) xload<(j & 3)>(xr[j & 3], xsrc + kstep(j + 4, kh) * 1024u, voffX);
    if constexpr (j == 32) gld4(gv, G + (rt * 4 + wm) * 128, (voffX >> 2) & 124u);
    SB();
  });
  STAMP2(76);
  STAMP(2, 5);
  SST(3);
  asm volatile("s_waitcnt vmcnt(0)" : "+v"(gv)::"memory");
  pin_acc<NBW>(acc);
  {
    const float og = 1.0f - __uint_as_float(gv);
#pragma unroll
    for (int b = 0; b < NBW; ++b)
#pragma unroll
      for (int i = 0; i < 16; ++i) acc[b][i] *= og;
  }
  pin_acc<NBW>(acc);
  static_for<0, NBW>([&](auto Mc) { pin_w<decltype(Mc)::v>(w[decltype(Mc)::v]); });
  if (wave == 0) {
    const int l2 = fresh_lane();
    spin_flags(FL + FL_O + rt * 512 + sp * 32, (unsigned)(((l2 >> 3) & 3) * 128 + (l2 & 7) * 4));
  }
  BARRIER();
  SST(4);
  {
    bf16x8 xe;
    const char* osrc = O + (size_t)((rt * 4 + wm) * 32 + sp * 8 + kh * 4) * 1024;
    static_for<0, 4>([&](auto Ic) { xload<decltype(Ic)::v>(xr[decltype(Ic)::v], osrc + decltype(Ic)::v * 1024, voffX); });
    gld16c(xe, O + OEX + ((rt * 4 + wm) * 4 + sp) * 1024, voffX);
    static_for<0, 4>([&](auto Tc) {
      constexpr int t = decltype(Tc)::v;
      wait_vm<4 - t>();
      pin_x1<t>(xr[t]);
      pin_acc<NBW>(acc);
      static_for<0, NBW>([&](auto Mc) {
        constexpr int m = decltype(Mc)::v;
        pin_w<m>(w[m]);
        acc[m] = MFMA32(w[m], xr[t], acc[m]);
        SB();
        w[m] = rdw(M_IT + t + 1, m);
        SB();
      });
    });
    wait_vm1<0>(xe);
    pin_acc<NBW>(acc);
    static_for<0, NBW>([&](auto Mc) { pin_w<decltype(Mc)::v>(w[decltype(Mc)::v]); });
    if (kh == 0) {
#pragma unroll
      for (int m = 0; m < NBW; ++m) acc[m] = MFMA32(w[m], xe, acc[m]);
    }
    pin_acc<NBW>(acc);
  }
  BARRIER();
  STAMP(2, 6);
  SST(5);
  constexpr int NA = (NBW + 1) / 2;
  const int l2 = fresh_lane();
  char* ex = lds + wm * (NBW * 4096) + l2 * 16;
  if (kh == 0) {
#pragma unroll
    for (int b = NA; b < NBW; ++b)
#pragma unroll
      for (int c = 0; c < 4; ++c) {
        f32x4 v = {acc[b][4 * c], acc[b][4 * c + 1], acc[b][4 * c + 2], acc[b][4 * c + 3]};
        *(f32x4*)(ex + b * 4096 + c * 1024) = v;
      }
  } else {
#pragma unroll
    for (int b = 0; b < NA; ++b)
#pragma unroll
      for (int c = 0; c < 4; ++c) {
        f32x4 v = {acc[b][4 * c], acc[b][4 * c + 1], acc[b][4 * c + 2], acc[b][4 * c + 3]};
        *(f32x4*)(ex + b * 4096 + c * 1024) = v;
      }
    acc[0] = acc[NA];
    acc[1] = acc[NA + 1];
  }
  auto pin_r = [&]() {
    if constexpr (NA == 3) asm volatile("" : "+{v[0:15]}"(acc[0]), "+{v[16:31]}"(acc[1]), "+{v[32:47]}"(acc[2]));
    else asm volatile("" : "+{v[0:15]}"(acc[0]), "+{v[16:31]}"(acc[1]));
  };
  pin_r();
  __syncthreads();
  const int bb0 = kh ? NA : 0;
  const bool third = kh == 0;
  ex += bb0 * 4096;
#pragma unroll
  for (int b = 0; b < NA; ++b) {
    if (b == 2 && !third) break;
#pragma unroll
    for (int c = 0; c < 4; ++c) {
      f32x4 a = *(const f32x4*)(ex + b * 4096 + c * 1024);
#pragma unroll
      for (int i = 0; i < 4; ++i) acc[b][4 * c + i] += a[i];
    }
  }
  pin_r();
  const char* flp = FL + FL_P + (((rt * 4 + j4) * 4 + wm) * 2 + kh) * 16;
  const size_t pofs = ((size_t)(rt * 4 + wm) * 18 + unit2 * 2 + boff + bb0) * 2048;
  if (wm != sp) {
    char* pb = P + (size_t)sp * (64 * 18 * 2048) + pofs + l2 * 16;
#pragma unroll
    for (int b = 0; b < NA; ++b) {
      if (b == 2 && !third) break;
#pragma unroll
      for (int gp = 0; gp < 2; ++gp) {
        u32x4 v;
#pragma unroll
        for (int i = 0; i < 4; ++i) v[i] = pk2(acc[b][8 * gp + 2 * i], acc[b][8 * gp + 2 * i + 1]);
        st16(pb + b * 2048 + gp * 1024, v);
      }
    }
    wait_vm<0>();
    if (l2 == 0) st4((char*)flp + sp * 4, 1u);
  } else {
    spin_flags(flp, (unsigned)(((sp + 1 + (l2 % 3)) & 3) * 4));
    SST(6);
    bf16x8 pv[NA][3][2];
    static_for<0, NA * 6>([&](auto Ic) {
      constexpr int i = decltype(Ic)::v, b = i / 6, o = (i >> 1) % 3, gp = i & 1;
      const char* pb = P + (size_t)((sp + 1 + o) & 3) * (64 * 18 * 2048) + pofs + (b == 2 && !third ? 1 : b) * 2048 + gp * 1024;
      pload<i>(pv[b][o][gp], pb, l2 * 16);
    });
    char* orow = out + (size_t)((rt * 4 + wm) * 32 + (l2 & 31)) * 1152 + (unit2 * 2 + boff + bb0) * 64 + (l2 >> 5) * 16;
    static_for<0, NA>([&](auto Bc) {
      constexpr int b = decltype(Bc)::v;
      wait_vm<(NA - 1 - b) * 6 + 2 * b>();
      if (b < 2 || third) {
        static_for<0, 6>([&](auto Ic) {
          constexpr int i = decltype(Ic)::v, gp = i & 1;
          ppin<b * 6 + i>(pv[b][i >> 1][gp]);
          const u32x4 v = __builtin_bit_cast(u32x4, pv[b][i >> 1][gp]);
#pragma unroll
          for (int k = 0; k < 4; ++k) {
            acc[b][8 * gp + 2 * k] += __uint_as_float(v[k] << 16);
            acc[b][8 * gp + 2 * k + 1] += __uint_as_float(v[k] & 0xffff0000u);
          }
        });
        u32x2 o2[4];
#pragma unroll
        for (int gq = 0; gq < 4; ++gq) {
          o2[gq][0] = pk2(acc[b][4 * gq], acc[b][4 * gq + 1]);
          o2[gq][1] = pk2(acc[b][4 * gq + 2], acc[b][4 * gq + 3]);
        }
#pragma unroll
        for (int gq = 0; gq < 4; gq += 2) {
          auto r0 = __builtin_amdgcn_permlane32_swap(o2[gq][0], o2[gq + 1][0], false, false);
          auto r1 = __builtin_amdgcn_permlane32_swap(o2[gq][1], o2[gq + 1][1], false, false);
          u32x4 wv = {r0[0], r1[0], r0[1], r1[1]};
          st16p(orow + b * 64 + gq * 16, wv);
        }
      }
    });
  }
  STAMP(2, 7);
  SST(7);
  SSW();
}

extern "C" __global__ __launch_bounds__(512, 4) void kernel1(const char* __restrict__ T,
                                                   char* O, const char* __restrict__ XN,
                                                   const char* __restrict__ W1, const unsigned short* __restrict__ b1,
                                                   char* H, const char* __restrict__ W2,
                                                   const unsigned short* __restrict__ b2, const char* __restrict__ Wo,
                                                   const char* __restrict__ G, char* P, char* FL,
                                                   char* __restrict__ out) {
  __shared__ __attribute__((aligned(16))) char lds[81920];
  const int wave = __builtin_amdgcn_readfirstlane(threadIdx.x >> 6);
  const int lane = threadIdx.x & 63;
  const int bid = blockIdx.x;
  if (bid < 256) {
    STAMP(1, 0);
    STAMP(5, 0);
    __builtin_amdgcn_s_setprio(ATT_PRIO);
    attn_role(lds, T, O, G, FL, W2, Wo, bid, wave, fresh_lane());
  } else {
    STAMP(2, 0);
    STAMP(4, 0);
    const int wg = bid - 256;
    const int xcd = wg & 7, idx = wg >> 3;
    const int sp = xcd >> 1, j4 = idx & 3, r8 = idx >> 2, rt = (xcd & 1) * 8 + r8;
    const int koff = (r8 * 9 / 2 + j4 * 9) % 36;
    if (j4 == 0 || j4 == 3)
      mlp_track<5>(lds, XN, W1, b1, H, W2, b2, Wo, O, G, P, FL, out, rt, sp, j4, koff, wave, lane);
    else
      mlp_track<4>(lds, XN, W1, b1, H, W2, b2, Wo, O, G, P, FL, out, rt, sp, j4, koff, wave, lane);
  }
}

constexpr int Q_SLOT = 40960;
#define QGRID 256
#define PF1 0
DI void qkv_role(char* lds, const char* __restrict__ X, const char* __restrict__ Wqkv, const char* __restrict__ pf, int pfl,
                 char* __restrict__ T, int rt, int ct, int joff, int wave, int lane) {
  f32x16 acc[2];
#pragma unroll
  for (int i = 0; i < 16; ++i) { acc[0][i] = 0.f; acc[1][i] = 0.f; }
  const unsigned lbase = lds_addr(lds);
  unsigned voffX[2];
#pragma unroll
  for (int i = 0; i < 2; ++i) {
    int row = wave * 32 + 8 * i + (lane >> 3);
    int c = (lane & 7) ^ ((row >> 1) & 7);
    voffX[i] = (unsigned)((rt * 256 + row) * 1152 + c * 16);
  }
  const unsigned voffW = (unsigned)((lane >> 3) * 3456 + (((lane & 7) ^ (((lane >> 4) & 1) << 2)) << 4));
  const char* wsrc = Wqkv + (size_t)wave * 8 * 3456 + ct * 128;
  auto issue = [&](int J, int e) {
    unsigned dst = lbase + (J & 3) * Q_SLOT;
    unsigned Jr = J + joff;
    Jr = Jr >= 9 ? Jr - 9 : Jr;
    if (e == 0) glds16s(wsrc + Jr * (64u * 3456u), voffW, dst + 32768 + wave * 1024);
    else glds16s(X + Jr * 128u + ((e - 1) >> 1) * (16 * 1152), voffX[(e - 1) & 1], dst + (wave * 4 + e - 1) * 1024);
  };
  STAMP(0, 0);
#pragma unroll
  for (int J = 0; J < 3; ++J)
#pragma unroll
    for (int e = 0; e < 5; ++e) issue(J, e);
  const int r = lane & 31, h = lane >> 5;
  const int q = (lane >> 2) & 3, p = lane & 3, g = (lane >> 4) & 1;
  const lds_p L = (lds_p)lds;
  lds_p xa[4], wa[2][2];
#pragma unroll
  for (int ks = 0; ks < 4; ++ks) xa[ks] = L + (wave * 32 + r) * 128 + (((2 * ks + h) ^ ((r >> 1) & 7)) << 4);
#pragma unroll
  for (int b = 0; b < 2; ++b) {
    wa[0][b] = L + 32768 + h * 1024 + q * 128 + ((b ^ (q >> 1)) << 6) + g * 32 + p * 8;
    wa[1][b] = wa[0][b] + 2 * Q_SLOT;
  }
  lds_p xb[4];
#pragma unroll
  for (int ks = 0; ks < 4; ++ks) xb[ks] = xa[ks] + 2 * Q_SLOT;
  asm volatile("" : "+v"(xb[0]), "+v"(xb[1]), "+v"(xb[2]), "+v"(xb[3]), "+v"(wa[1][0]), "+v"(wa[1][1]));
  wait_vm<10>();
  BARRIER();
  STAMP(0, 1);
  bf16x8 fx[2], fw[2][2];
  auto rd = [&](int buf, int J, int ks) {
    const int sl = J & 3;
    fx[buf] = ld8((sl < 2 ? xa[ks] : xb[ks]) + (sl & 1) * Q_SLOT);
#pragma unroll
    for (int b = 0; b < 2; ++b) {
      const lds_p a = wa[sl >> 1][b] + (sl & 1) * Q_SLOT + ks * 2048;
      fw[buf][b] = cat8(trl(a), trl(a + 512));
    }
  };
  rd(0, 0, 0);
  unsigned pfd = 0;
  static_for<0, 9>([&](auto Jc) {
    constexpr int J = decltype(Jc)::v;
    if constexpr (J + 1 < 9) {
      wait_vm<((J + 2 < 9) ? 5 : 0) + (J >= 6 ? PF1 : 0)>();
      BARRIER();
    }
    if (J == 3) STAMP(0, 2);
    if (J == 6) STAMP(0, 3);
    SB();
    static_for<0, 4>([&](auto Kc) {
      constexpr int ks = decltype(Kc)::v, cur = ks & 1;
      acc[0] = MFMA32(fx[cur], fw[cur][0], acc[0]);
      SB();
      if constexpr (ks < 3) rd(cur ^ 1, J, ks + 1);
      else if constexpr (J + 1 < 9) rd(cur ^ 1, J + 1, 0);
      SB();
      acc[1] = MFMA32(fx[cur], fw[cur][1], acc[1]);
      SB();
      if constexpr (J + 3 < 9) {
        if constexpr (ks == 0) { issue(J + 3, 0); issue(J + 3, 1); }
        else issue(J + 3, ks + 1);
      }
      SB();
    });
  });
  STAMP(0, 5);
  const float QSCALE = 0.23570226039551584f * 1.4426950408889634f;
  const float sc = ct < 9 ? QSCALE : 1.0f;
#pragma unroll
  for (int nb = 0; nb < 2; ++nb) {
    char* base = T + (size_t)h * TP + (ct * 64 + nb * 32 + r) * 16;
#pragma unroll
    for (int s = 0; s < 2; ++s) {
      int step = rt * 16 + wave * 2 + s;
      u32x4 v;
#pragma unroll
      for (int i = 0; i < 4; ++i) v[i] = pk2(acc[nb][8 * s + 2 * i] * sc, acc[nb][8 * s + 2 * i + 1] * sc);
      st16(base + (size_t)step * (2 * TP), v);
    }
  }
  if (ct == 26 && wave < 4) {
    int idx = wave * 64 + lane;
    unsigned c = (idx & 7) == 0 ? 0x3F803F80u : 0u;
    u32x4 v = {c, c, c, c};
    st16(T + (size_t)(rt * 32 + (idx >> 3)) * TP + (1728 + (idx & 7)) * 16, v);
  }
  asm volatile("s_waitcnt vmcnt(0)" : "+v"(pfd)::"memory");
}

DI void ln_role(char* lds, const char* __restrict__ X, const char* __restrict__ gw, char* __restrict__ XN,
                float* __restrict__ G, char* __restrict__ FL, int id, int wave, int lane) {
  const int r = lane & 31, h = lane >> 5;
  const int rb = wave >> 2, kq = wave & 3;
  const int row = id * 64 + rb * 32 + r;
  u32x4 xv[9], gv[9];
#pragma unroll
  for (int i = 0; i < 9; ++i) xv[i] = *(const u32x4*)(X + (size_t)row * 1152 + ((kq * 9 + i) * 2 + h) * 16);
#pragma unroll
  for (int i = 0; i < 9; ++i) gv[i] = *(const u32x4*)(gw + ((kq * 9 + i) * 2 + h) * 16);
  if (wave < 2) {
    u32x4 z = {0u, 0u, 0u, 0u};
    st16(FL + id * 2048 + wave * 1024 + lane * 16, z);
  }
  float* red = (float*)lds;
  float s = 0.f, d = 0.f;
#pragma unroll
  for (int i = 0; i < 9; ++i)
#pragma unroll
    for (int j = 0; j < 4; ++j) {
      float a = __uint_as_float(xv[i][j] << 16), b = __uint_as_float(xv[i][j] & 0xffff0000u);
      s += a + b;
      d = __builtin_fmaf(a, __uint_as_float(gv[i][j] << 16), d);
      d = __builtin_fmaf(b, __uint_as_float(gv[i][j] & 0xffff0000u), d);
    }
  s += xor32(s);
  d += xor32(d);
  if (h == 0) { red[wave * 32 + r] = s; red[256 + wave * 32 + r] = d; }
  __syncthreads();
  const float* rs = red + rb * 128 + r;
  const float mean = (rs[0] + rs[32] + rs[64] + rs[96]) * (1.0f / 576.0f);
  if (kq == 0 && h == 0) {
    float v = rs[256] + rs[288] + rs[320] + rs[352];
    G[row] = 1.0f / (1.0f + __builtin_amdgcn_exp2f(-1.4426950408889634f * v));
  }
  float q2 = 0.f;
#pragma unroll
  for (int i = 0; i < 9; ++i)
#pragma unroll
    for (int j = 0; j < 4; ++j) {
      float a = __uint_as_float(xv[i][j] << 16) - mean, b = __uint_as_float(xv[i][j] & 0xffff0000u) - mean;
      q2 = __builtin_fmaf(a, a, q2);
      q2 = __builtin_fmaf(b, b, q2);
    }
  q2 += xor32(q2);
  if (h == 0) red[512 + wave * 32 + r] = q2;
  __syncthreads();
  const float var = (rs[512] + rs[544] + rs[576] + rs[608]) * (1.0f / 576.0f);
  const float rstd = 1.0f / __builtin_sqrtf(var + 1e-5f);
  char* dst = XN + ((size_t)(id * 2 + rb) * 36 + kq * 9) * 1024 + lane * 16;
#pragma unroll
  for (int i = 0; i < 9; ++i) {
    u32x4 v;
#pragma unroll
    for (int j = 0; j < 4; ++j) {
      float a = __uint_as_float(xv[i][j] << 16) - mean, b = __uint_as_float(xv[i][j] & 0xffff0000u) - mean;
      v[j] = pk2(a * rstd, b * rstd);
    }
    st16(dst + i * 1024, v);
  }
}

__global__ __launch_bounds__(512, 2) void kernelQ(const char* __restrict__ X, const char* __restrict__ Wqkv,
                                                   const char* __restrict__ gw, const char* __restrict__ W1,
                                                   char* __restrict__ T,
                                                   char* __restrict__ XN, float* __restrict__ G,
                                                   char* __restrict__ FL) {
  __shared__ __attribute__((aligned(16))) char lds[163840];
  const int wg = blockIdx.x;
  const int wave = __builtin_amdgcn_readfirstlane(threadIdx.x >> 6);
  const int lane = threadIdx.x & 63;
  if (wg < 216) {
    const int xcd = wg & 7, t = (xcd >> 2) * 27 + (wg >> 3);
    const int joff = ((t >> 1) * 3 + (t & 1) * 4) % 9;
    qkv_role(lds, X, Wqkv, W1 + (xcd >> 1) * 2304, (wg >> 3) * 384, T, (xcd & 3) * 2 + (t & 1), t >> 1, joff, wave, lane);
  } else if (wg < 248) {
    STAMP(0, 0);
    ln_role(lds, X, gw, XN, G, FL, wg - 216, wave, lane);
  } else {
    const char* pc;
    unsigned span;
    asm volatile("s_getpc_b64 s[20:21]\n\ts_mov_b32 %1, kernel1@rel32@lo+4\n\ts_add_u32 s20, s20, %1\n\ts_addc_u32 s21, s21, kernel1@rel32@hi+16"
                 : "={s[20:21]}"(pc), "=s"(span)::"scc");
    span = 0u - span;
    if (span < (1u << 18)) {
      unsigned d0 = 0;
#pragma unroll 1
      for (unsigned o = threadIdx.x * 128u; o < span; o += 65536u) gld4(d0, pc, o);
      asm volatile("s_waitcnt vmcnt(0)" : "+v"(d0)::"memory");
    }
  }
  STAMP(0, 6);
  STAMP(0, 7);
}

extern "C" void kernel_launch(void* const* d_in, const int* in_sizes, int n_in, void* d_out, int out_size, void* d_ws,
                              size_t ws_size, hipStream_t stream) {
  const char* X = (const char*)d_in[0];
  const char* Wqkv = (const char*)d_in[1];
  const char* Wo = (const char*)d_in[2];
  const char* W1 = (const char*)d_in[4];
  const unsigned short* b1 = (const unsigned short*)d_in[5];
  const char* W2 = (const char*)d_in[6];
  const unsigned short* b2 = (const unsigned short*)d_in[7];
  const char* gw = (const char*)d_in[8];
  char* ws = (char*)d_ws;
  char* T = ws + WS_T;
  char* H = ws + WS_H;
  char* O = ws + WS_O;
  char* P = ws + WS_P;
  char* XN = ws + WS_XN;
  char* FL = ws + WS_FL;
  float* G = (float*)(ws + WS_G);
  kernelQ<<<QGRID, 512, 0, stream>>>(X, Wqkv, gw, W1, T, XN, G, FL);
  kernel1<<<512, 512, 0, stream>>>(T, O, XN, W1, b1, H, W2, b2, Wo, (const char*)G, P, FL, (char*)d_out);
}
```

```cpp
#include <hip/hip_runtime.h>
#include <stdint.h>

typedef __attribute__((ext_vector_type(8))) short bf16x8;
typedef __attribute__((ext_vector_type(4))) short s16x4;
typedef __attribute__((ext_vector_type(2))) __bf16 bf16x2_t;
typedef __attribute__((ext_vector_type(2))) float f32x2;
typedef __attribute__((ext_vector_type(4))) float f32x4;
typedef __attribute__((ext_vector_type(16))) float f32x16;
typedef __attribute__((ext_vector_type(4))) unsigned u32x4;
typedef __attribute__((ext_vector_type(2))) unsigned u32x2;
typedef __attribute__((address_space(3))) s16x4* lds_s16x4_p;
typedef __attribute__((address_space(3))) char* lds_p;
typedef __attribute__((address_space(3))) const bf16x8* lds_bf16x8_p;

#define STAMP(k, i)
#define STAMP2(i)
#define SST(i)
#define SSD()
#define SSW()
#define DI __device__ __forceinline__
#define MFMA32(a, b, c) __builtin_amdgcn_mfma_f32_32x32x16_bf16((a), (b), (c), 0, 0, 0)
#define MFMA32K8(a, b, c) __builtin_amdgcn_mfma_f32_32x32x8bf16_1k((a), (b), (c), 0, 0, 0)
#define WAIT_VM(n) asm volatile("s_waitcnt vmcnt(" #n ")" ::: "memory")
#define BARRIER() __builtin_amdgcn_s_barrier()
#define SB() __builtin_amdgcn_sched_barrier(0)

constexpr int S_ = 2048, D_ = 576, F_ = 4608, NQKV = 1728;
constexpr int TSTEP = 640;
constexpr int TP = 1736 * 16;
constexpr int OEX = 64 * 32 * 1024;
constexpr size_t WS_T = 0;
constexpr size_t WS_H = 8u << 20;
constexpr size_t WS_O = 28u << 20;
constexpr size_t WS_G = 31u << 20;
constexpr size_t WS_FL = (31u << 20) + (64u << 10);
constexpr size_t WS_P = 32u << 20;
constexpr size_t WS_XN = 44u << 20;
constexpr size_t WS_XF = 47u << 20;
constexpr int FL_Q = 32768, FL_O = 36864, FL_P = 45056;

DI unsigned pk2(float a, float b) {
  f32x2 v = {a, b};
  bf16x2_t r = __builtin_convertvector(v, bf16x2_t);
  return __builtin_bit_cast(unsigned, r);
}
DI float bf2f(unsigned short u) { return __uint_as_float(((unsigned)u) << 16); }
DI s16x4 tr_read(const char* p) {
  return __builtin_amdgcn_ds_read_tr16_b64_v4i16((lds_s16x4_p)(p));
}
DI s16x4 trl(lds_p p) { return __builtin_amdgcn_ds_read_tr16_b64_v4i16((lds_s16x4_p)p); }
DI bf16x8 ld8(lds_p p) { return *(lds_bf16x8_p)p; }
DI float xor32(float v) { return __shfl_xor(v, 32, 64); }

DI void glds16s(const char* sbase, unsigned voff, unsigned lds_dst) {
  unsigned keep;
  asm volatile("s_mov_b32 %0, m0\n\ts_mov_b32 m0, %3\n\ts_nop 0\n\tglobal_load_lds_dwordx4 %1, %2\n\ts_mov_b32 m0, %0"
               : "=&s"(keep)
               : "v"(voff), "s"(sbase), "s"(lds_dst)
               : "memory");
}
DI void glds16c(const char* sbase, unsigned voff, unsigned lds_dst) {
  unsigned keep;
  asm volatile("s_mov_b32 %0, m0\n\ts_mov_b32 m0, %3\n\ts_nop 0\n\tglobal_load_lds_dwordx4 %1, %2 sc1\n\ts_mov_b32 m0, %0"
               : "=&s"(keep)
               : "v"(voff), "s"(sbase), "s"(lds_dst)
               : "memory");
}
DI void touch(const char* sbase, unsigned voff, unsigned lds_dst) {
  unsigned keep;
  asm volatile("s_mov_b32 %0, m0\n\ts_mov_b32 m0, %3\n\ts_nop 0\n\tglobal_load_lds_dword %1, %2\n\ts_mov_b32 m0, %0"
               : "=&s"(keep)
               : "v"(voff), "s"(sbase), "s"(lds_dst)
               : "memory");
}
DI void gld16(bf16x8& d, const char* sbase, unsigned voff) {
  asm volatile("global_load_dwordx4 %0, %1, %2" : "=v"(d) : "v"(voff), "s"(sbase) : "memory");
}
DI void gld16c(bf16x8& d, const char* sbase, unsigned voff) {
  asm volatile("global_load_dwordx4 %0, %1, %2 sc1" : "=v"(d) : "v"(voff), "s"(sbase) : "memory");
}
DI void gld4c(unsigned& d, const char* sbase, unsigned voff) {
  asm volatile("global_load_dword %0, %1, %2 sc1" : "=v"(d) : "v"(voff), "s"(sbase) : "memory");
}
DI void gld4(unsigned& d, const char* sbase, unsigned voff) {
  asm volatile("global_load_dword %0, %1, %2" : "=v"(d) : "v"(voff), "s"(sbase) : "memory");
}
DI void gld4k(unsigned& d, const char* sbase, unsigned voff) {
  asm volatile("global_load_dword %0, %1, %2" : "+v"(d) : "v"(voff), "s"(sbase) : "memory");
}
DI unsigned lds_addr(const void* p) { return (unsigned)(uintptr_t)(lds_p)p; }

DI void st16(void* p, u32x4 v) { asm volatile("global_store_dwordx4 %0, %1, off sc1\n\ts_nop 1" ::"v"(p), "v"(v) : "memory"); }
DI void st16p(void* p, u32x4 v) { asm volatile("global_store_dwordx4 %0, %1, off\n\ts_nop 1" ::"v"(p), "v"(v) : "memory"); }
DI void st4(void* p, unsigned v) { asm volatile("global_store_dword %0, %1, off sc1" ::"v"(p), "v"(v) : "memory"); }

DI int fresh_lane() {
  int l;
  asm volatile("v_mbcnt_lo_u32_b32 %0, -1, 0\n\tv_mbcnt_hi_u32_b32 %0, -1, %0" : "=v"(l));
  return l;
}
template <int I>
struct IC { static constexpr int v = I; };
template <int I, int N, class F>
DI void static_for(F&& f) {
  if constexpr (I < N) {
    f(IC<I>{});
    static_for<I + 1, N>(f);
  }
}
template <int N>
DI void wait_vm() { asm volatile("s_waitcnt vmcnt(%0)" ::"i"(N) : "memory"); }
template <int N>
DI void wait_vm1(bf16x8& a) { asm volatile("s_waitcnt vmcnt(%1)" : "+v"(a) : "i"(N) : "memory"); }
DI void spin_flags(const char* base, unsigned voff) {
#pragma unroll 1
  for (int it = 0; it < (1 << 17); ++it) {
    unsigned f;
    gld4c(f, base, voff);
    asm volatile("s_waitcnt vmcnt(0)" : "+v"(f)::"memory");
    if (!__ballot(f != 1u)) break;
    __builtin_amdgcn_s_sleep(4);
  }
}
DI bf16x8 cat8(s16x4 lo, s16x4 hi) { return __builtin_shufflevector(lo, hi, 0, 1, 2, 3, 4, 5, 6, 7); }

DI f32x16 qk_tile(lds_p kb, lds_p ke, bf16x8 qf, s16x4 qe, f32x16 c) {
  s16x4 lo = trl(kb);
  s16x4 hi = trl(kb + 64);
  s16x4 ex = trl(ke);
  bf16x8 kf = __builtin_shufflevector(lo, hi, 0, 1, 2, 3, 4, 5, 6, 7);
  f32x16 s = MFMA32(kf, qf, c);
  s = MFMA32K8(ex, qe, s);
  return s;
}
DI f32x16 pv_tile(lds_p vb, const f32x16& pe, f32x16 o) {
  bf16x8 v0 = ld8(vb);
  bf16x8 v1 = ld8(vb + TSTEP);
  u32x4 p0, p1;
#pragma unroll
  for (int i = 0; i < 4; ++i) {
    p0[i] = pk2(pe[2 * i], pe[2 * i + 1]);
    p1[i] = pk2(pe[8 + 2 * i], pe[8 + 2 * i + 1]);
  }
  o = MFMA32(v0, __builtin_bit_cast(bf16x8, p0), o);
  o = MFMA32(v1, __builtin_bit_cast(bf16x8, p1), o);
  return o;
}
struct KFrag { bf16x8 f; s16x4 e; };
struct VFrag { bf16x8 v0, v1; };
DI KFrag load_k(lds_p kb, lds_p ke) {
  KFrag k;
  s16x4 lo = trl(kb);
  s16x4 hi = trl(kb + 64);
  k.e = trl(ke);
  k.f = __builtin_shufflevector(lo, hi, 0, 1, 2, 3, 4, 5, 6, 7);
  return k;
}
DI VFrag load_v(lds_p vb) {
  VFrag v;
  v.v0 = ld8(vb);
  v.v1 = ld8(vb + TSTEP);
  return v;
}
#define EXP2(x) __builtin_amdgcn_exp2f(x)
#define APM 2
#define APMASK 0x77
#define PHI(i) if constexpr ((PM >> (i)) & 1) __builtin_amdgcn_s_setprio(APM)
#define PLO(i) if constexpr ((PM >> (i)) & 1) __builtin_amdgcn_s_setprio(0)
template <int PM>
DI void tile_step(const f32x16& s, f32x16& sn, const KFrag& k, const VFrag& v, bf16x8 qf, s16x4 qe,
                  f32x16& o) {
  f32x16 negm;
#pragma unroll
  for (int i = 0; i < 16; ++i) negm[i] = 0.f;
  u32x4 p0, p1;
  p0[0] = pk2(EXP2(s[0]), EXP2(s[1]));
  p0[1] = pk2(EXP2(s[2]), EXP2(s[3]));
  SB();
  PHI(0);
  SB();
  f32x16 t = MFMA32(k.f, qf, negm);
  SB();
  PLO(0);
  SB();
  p0[2] = pk2(EXP2(s[4]), EXP2(s[5]));
  p0[3] = pk2(EXP2(s[6]), EXP2(s[7]));
  SB();
  PHI(1);
  SB();
  o = MFMA32(v.v0, __builtin_bit_cast(bf16x8, p0), o);
  SB();
  PLO(1);
  SB();
  p1[0] = pk2(EXP2(s[8]), EXP2(s[9]));
  p1[1] = pk2(EXP2(s[10]), EXP2(s[11]));
  SB();
  PHI(2);
  SB();
  t = MFMA32K8(k.e, qe, t);
  SB();
  PLO(2);
  SB();
  p1[2] = pk2(EXP2(s[12]), EXP2(s[13]));
  p1[3] = pk2(EXP2(s[14]), EXP2(s[15]));
  SB();
  PHI(3);
  SB();
  o = MFMA32(v.v1, __builtin_bit_cast(bf16x8, p1), o);
  SB();
  PLO(3);
  SB();
  sn = t;
}
DI float rowmax16(const f32x16& s) {
  float m = __builtin_fmaxf(s[0], s[1]);
#pragma unroll
  for (int i = 2; i < 16; ++i) m = __builtin_fmaxf(m, s[i]);
  return __builtin_fmaxf(m, xor32(m));
}

constexpr int AT_SLOT = 20480;
constexpr int at_cnt(int c, int grp) { return 2 + (((c & 1) == grp) ? 1 : 0); }
constexpr int at_off(int tt) { return ((tt >> 3) & 1) * AT_SLOT + (tt & 7) * 1280; }

#define TOUCH_AT 9
DI void attn_role(char* lds, const char* T, char* O, const char* __restrict__ G, char* FL, const char* __restrict__ W2,
                  const char* __restrict__ Wo, int wg, int wave, int lane) {
  const int head = (wg & 7) * 4 + ((wg >> 3) & 3);
  const int qt = wg >> 5;
  const int r = lane & 31, h = lane >> 5;
  const int grp = wave >> 2;
  const unsigned lbase = lds_addr(lds);
  const int p1 = wave + 8, p2 = 16 + (wave & 3);
  auto mkoff = [&](int pc, int first) {
    int isv = pc >= 10;
    int sl = (pc - 10 * isv) * 64 + lane;
    int seg = sl / 20, row = sl - seg * 20;
    int slot = row < 18 ? first + 576 * isv + head * 18 + row : 1728 + row - 18;
    return (unsigned)(seg * TP + slot * 16);
  };
  const unsigned v0 = mkoff(wave, 576), v1 = mkoff(p1, 576), v2 = mkoff(p2, 576);
  auto issue = [&](int c) {
    unsigned dst = lbase + (c & 3) * AT_SLOT;
    const char* src = T + (size_t)c * (32 * TP);
    glds16s(src, v0, dst + wave * 1024);
    glds16s(src, v1, dst + p1 * 1024);
    if ((c & 1) == grp) glds16s(src, v2, dst + p2 * 1024);
  };
  STAMP(1, 1);
  unsigned gbits;
  gld4(gbits, G + (qt * 256 + wave * 32) * 4, r * 4);
  {
    const char* src = T + (size_t)qt * (32 * TP);
    glds16s(src, mkoff(wave, 0), lbase + 3 * AT_SLOT + wave * 1024);
    if (wave < 2) glds16s(src, mkoff(p1, 0), lbase + 3 * AT_SLOT + p1 * 1024);
  }
  issue(0);
  issue(1);
  issue(2);
  const int q = (lane >> 2) & 3, p = lane & 3, g = (lane >> 4) & 1;
  const lds_p L = (lds_p)lds;
  const lds_p kb = L + g * TSTEP + (p & 1) * (TSTEP / 2) + (8 * h + q) * 16 + (p >> 1) * 8;
  const lds_p ke = L + g * TSTEP + (p & 1) * (TSTEP / 2) + (16 + q) * 16 + (p >> 1) * 8;
  const lds_p vb = L + 10240 + h * (TSTEP / 2) + (r < 19 ? r : 19) * 16;
  lds_p kb2 = kb + 2 * AT_SLOT, ke2 = ke + 2 * AT_SLOT, vb2 = vb + 2 * AT_SLOT;
  asm volatile("" : "+v"(kb2), "+v"(ke2), "+v"(vb2));
  asm volatile("s_waitcnt vmcnt(5)" : "+v"(gbits)::"memory");
  BARRIER();
  STAMP(1, 2);
  bf16x8 qf;
  s16x4 qe;
  {
    KFrag qq = load_k(kb2 + AT_SLOT + wave * 1280, ke2 + AT_SLOT + wave * 1280);
    qf = qq.f;
    qe[0] = h ? (short)0 : qq.e[0];
    qe[1] = h ? (short)0 : qq.e[1];
    qe[2] = 0;
    qe[3] = 0;
  }

  const float gate = __uint_as_float(gbits);
  f32x16 zero;
#pragma unroll
  for (int i = 0; i < 16; ++i) zero[i] = 0.f;
  f32x16 o = zero;
  f32x16 sA = qk_tile(kb, ke, qf, qe, zero);
  {
    const float nx = -rowmax16(sA);
    const unsigned mb = (__float_as_uint(nx) + (nx > 0.f ? 0xffffu : 0u)) >> 16;
    const float nm = __uint_as_float(mb << 16);
#pragma unroll
    for (int i = 0; i < 16; ++i) sA[i] += nm;
    qe[2] = h ? (short)0 : (short)mb;
  }
  KFrag kA, kB;
  VFrag vA, vB;
  kB = load_k(kb + 1280, ke + 1280);
  vA = load_v(vb);
  f32x16 sB;
#pragma unroll 1
  for (int cc = 0; cc < 8; cc += 4) {
    static_for<0, 32>([&](auto Tc) {
      constexpr int tt = decltype(Tc)::v, ci = tt >> 3;
      if constexpr ((tt & 7) == 6) {
        if (cc == 0 || ci < 2) {
          if (grp == 0) wait_vm<at_cnt(ci + 2, 0)>(); else wait_vm<at_cnt(ci + 2, 1)>();
        } else if (ci == 2) wait_vm<0>();
        if (cc == 0 || ci < 3) BARRIER();
        if constexpr (ci == TOUCH_AT) {
          if (cc == 0) {
            const int l2 = fresh_lane();
            const char* src = W2 + (size_t)((wg & 7) >> 1) * (1152 * 1152) + (wg >> 3) * 41472;
            const unsigned dst = lbase + ((ci + 3) & 3) * AT_SLOT + wave * 1024;
            touch(src, (wave * 41 + l2 - (l2 > 40 ? 24 : 0) - (wave >> 2) * 4) * 128u, dst);
            if (wave == 0) touch(Wo + ((wg & 7) >> 1) * (144 * 1152) + (wg >> 3) * 5184, (l2 > 39 ? ((wg & 8) ? 39 : 40) : l2) * 128u + ((wg & 8) ? 64u : 0u), dst);
          }
        }
        if (cc == 0 || ci == 0) issue(cc + ci + 3);
      }
      constexpr int t1 = (tt + 1) & 31, t2 = (tt + 2) & 31;
      const lds_p kp = (t2 < 16 ? kb : kb2) + at_off(t2);
      const lds_p ep = (t2 < 16 ? ke : ke2) + at_off(t2);
      const lds_p vp = (t1 < 16 ? vb : vb2) + at_off(t1);
      if constexpr ((tt & 1) == 0) {
        kA = load_k(kp, ep);
        vB = load_v(vp);
        SB();
        tile_step<APMASK & 15>(sA, sB, kB, vA, qf, qe, o);
      } else {
        kB = load_k(kp, ep);
        vA = load_v(vp);
        SB();
        tile_step<(APMASK >> 4) & 15>(sB, sA, kA, vB, qf, qe, o);
      }
    });
    if (cc == 0) STAMP(1, 3);
  }
  STAMP(1, 4);
  STAMP(1, 5);
  float l = xor32(o[10]);
  l = h ? l : o[10];
  bool bad = !(l < 1.0e30f);
#pragma unroll
  for (int i = 0; i < 10; ++i) bad = bad || !(__builtin_fabsf(o[i]) < 1.0e37f);
  const int ob = (qt * 8 + wave) * 32 + head;
  auto publish = [&]() {
    const float inv = gate / l;
    u32x4 ov;
#pragma unroll
    for (int i = 0; i < 4; ++i) ov[i] = pk2(o[2 * i] * inv, o[2 * i + 1] * inv);
    st16(O + (size_t)ob * 1024 + lane * 16, ov);
    if (h == 0) st4(O + OEX + (ob >> 3) * 1024 + ((head >> 2) & 1) * 512 + r * 16 + (head & 3) * 4, pk2(o[8] * inv, o[9] * inv));
    STAMP(1, 6);
    wait_vm<0>();
    if (lane == 0) st4(FL + FL_O + ob * 4, 1u);
  };
  const bool mybad = __any(bad);
  if (!mybad) publish();
  {
    int* fl = (int*)(lds + AT_SLOT);
    if (lane == 0) fl[wave] = mybad ? 1 : 0;
    __syncthreads();
    int v = fl[lane & 7];
    bad = __any(v != 0);
  }
  if (bad) {
    float m = -3.0e38f;
    o = zero;
    qe[2] = 0;
#pragma unroll 1
    for (int c = 0; c < 8; ++c) {
      __syncthreads();
      {
        const char* src = T + (size_t)c * (32 * TP);
        glds16s(src, v0, lbase + wave * 1024);
        glds16s(src, v1, lbase + p1 * 1024);
        if (grp == 0) glds16s(src, v2, lbase + p2 * 1024);
      }
      wait_vm<0>();
      __syncthreads();
#pragma unroll 1
      for (int t = 0; t < 8; ++t) {
        f32x16 s = qk_tile(kb + t * 1280, ke + t * 1280, qf, qe, zero);
        float mn = __builtin_fmaxf(m, rowmax16(s));
        float alpha = __builtin_amdgcn_exp2f(m - mn);
        m = mn;
        f32x16 pe;
#pragma unroll
        for (int i = 0; i < 16; ++i) { pe[i] = __builtin_amdgcn_exp2f(s[i] - mn); o[i] *= alpha; }
        o = pv_tile(vb + t * 1280, pe, o);
      }
    }
    l = xor32(o[10]);
    l = h ? l : o[10];
    if (mybad) publish();
  }
  STAMP(1, 7);
}

#define QKV_PRIO 2
#define ATT_PRIO 0
#define MLP_PRIO 1
#define PF2_AT 20
constexpr int M_IT = 36;
constexpr int M2_NT = 41;
struct M2S { int slot[M2_NT]; int it[M2_NT]; };
constexpr M2S m2_sched(int U) {
  M2S r{};
  const int E = 2 * U - 2;
  int next = 0;
  for (; next < 4; ++next) { r.slot[next] = next; r.it[next] = -1; }
  for (int j = 1; next < M2_NT; ++j) {
    r.slot[next] = r.slot[j - 1];
    r.it[next] = j;
    ++next;
    if (next < M2_NT && (j == E || j == E + 1)) { r.slot[next] = 4 + (j - E); r.it[next] = j; ++next; }
  }
  return r;
}
constexpr int m2_ck1(int U) { return 2 * U - 4; }
constexpr int m2_ck2(int U) { return (U == 5 ? 26 : 2 * U + 8) - 4; }
constexpr int m2_wait(int U, int kh, int j, int mode) {
  const M2S sc = m2_sched(U);
  const int U2 = U == 5 ? 2 : 3;
  int n = 0, last = -1;
  for (int i = -1; i < j + mode; ++i) {
    if (i == m2_ck1(U) - 2 || i == m2_ck2(U) - 2) { if (mode == 1 && i == j - 2) last = n; ++n; }
    for (int s = 0; s < M2_NT; ++s)
      if (sc.it[s] == i) {
        const int c = (U2 == 3 && (s & 1) == kh) ? 2 : 1;
        n += c;
        if (mode == 0 && s <= j + 1) last = n - 1;
      }
    if (i == -1) n += 2 * U;
    if (i >= 0 && i < j && i + 4 >= 2 * U && i + 4 < M_IT) { if (mode == 0 && i + 4 <= j) last = n; ++n; }
  }
  return n - 1 - last;
}
constexpr bool m2_need(int U, int j) { return j >= 3; }

template <int N>
DI void pin_acc(f32x16* a) {
  if constexpr (N == 5)
    asm volatile("" : "+{v[0:15]}"(a[0]), "+{v[16:31]}"(a[1]), "+{v[32:47]}"(a[2]), "+{v[48:63]}"(a[3]), "+{v[64:79]}"(a[4]));
  else
    asm volatile("" : "+{v[0:15]}"(a[0]), "+{v[16:31]}"(a[1]), "+{v[32:47]}"(a[2]), "+{v[48:63]}"(a[3]));
}
#define XLD(R) asm volatile("global_load_dwordx4 %0, %1, %2 sc1" : "={" R "}"(d) : "v"(voff), "s"(sbase) : "memory")
template <int I>
DI void xload(bf16x8& d, const char* sbase, unsigned voff) {
  if constexpr (I == 0) XLD("v[80:83]");
  else if constexpr (I == 1) XLD("v[84:87]");
  else if constexpr (I == 2) XLD("v[88:91]");
  else XLD("v[92:95]");
}
template <int I>
DI void pload(bf16x8& d, const char* sbase, unsigned voff) {
  if constexpr (I == 0) XLD("v[48:51]");
  else if constexpr (I == 1) XLD("v[52:55]");
  else if constexpr (I == 2) XLD("v[56:59]");
  else if constexpr (I == 3) XLD("v[60:63]");
  else if constexpr (I == 4) XLD("v[64:67]");
  else if constexpr (I == 5) XLD("v[68:71]");
  else if constexpr (I == 6) XLD("v[72:75]");
  else if constexpr (I == 7) XLD("v[76:79]");
  else if constexpr (I == 8) XLD("v[80:83]");
  else if constexpr (I == 9) XLD("v[84:87]");
  else if constexpr (I == 10) XLD("v[88:91]");
  else if constexpr (I == 11) XLD("v[92:95]");
  else if constexpr (I == 12) XLD("v[96:99]");
  else if constexpr (I == 13) XLD("v[100:103]");
  else if constexpr (I == 14) XLD("v[104:107]");
  else if constexpr (I == 15) XLD("v[108:111]");
  else if constexpr (I == 16) XLD("v[112:115]");
  else if constexpr (I == 17) XLD("v[116:119]");
}
template <int I>
DI void ppin(bf16x8& d) {
  if constexpr (I == 0) asm volatile("" : "+{v[48:51]}"(d));
  else if constexpr (I == 1) asm volatile("" : "+{v[52:55]}"(d));
  else if constexpr (I == 2) asm volatile("" : "+{v[56:59]}"(d));
  else if constexpr (I == 3) asm volatile("" : "+{v[60:63]}"(d));
  else if constexpr (I == 4) asm volatile("" : "+{v[64:67]}"(d));
  else if constexpr (I == 5) asm volatile("" : "+{v[68:71]}"(d));
  else if constexpr (I == 6) asm volatile("" : "+{v[72:75]}"(d));
  else if constexpr (I == 7) asm volatile("" : "+{v[76:79]}"(d));
  else if constexpr (I == 8) asm volatile("" : "+{v[80:83]}"(d));
  else if constexpr (I == 9) asm volatile("" : "+{v[84:87]}"(d));
  else if constexpr (I == 10) asm volatile("" : "+{v[88:91]}"(d));
  else if constexpr (I == 11) asm volatile("" : "+{v[92:95]}"(d));
  else if constexpr (I == 12) asm volatile("" : "+{v[96:99]}"(d));
  else if constexpr (I == 13) asm volatile("" : "+{v[100:103]}"(d));
  else if constexpr (I == 14) asm volatile("" : "+{v[104:107]}"(d));
  else if constexpr (I == 15) asm volatile("" : "+{v[108:111]}"(d));
  else if constexpr (I == 16) asm volatile("" : "+{v[112:115]}"(d));
  else if constexpr (I == 17) asm volatile("" : "+{v[116:119]}"(d));
}
#define XDS(R) asm volatile("ds_read_b128 %0, %1 offset:%2" : "={" R "}"(d) : "v"(a), "i"(OFF) : "memory")
template <int I, int OFF>
DI void xstash(bf16x8& d, unsigned a) {
  if constexpr (I == 0) XDS("v[80:83]");
  else if constexpr (I == 1) XDS("v[84:87]");
  else if constexpr (I == 2) XDS("v[88:91]");
  else XDS("v[92:95]");
}
template <int I>
DI void pin_x1(bf16x8& x) {
  if constexpr (I == 0) asm volatile("" : "+{v[80:83]}"(x));
  else if constexpr (I == 1) asm volatile("" : "+{v[84:87]}"(x));
  else if constexpr (I == 2) asm volatile("" : "+{v[88:91]}"(x));
  else asm volatile("" : "+{v[92:95]}"(x));
}
template <int I>
DI void pin_w(bf16x8& w) {
  if constexpr (I == 0) asm volatile("" : "+{v[96:99]}"(w));
  else if constexpr (I == 1) asm volatile("" : "+{v[100:103]}"(w));
  else if constexpr (I == 2) asm volatile("" : "+{v[104:107]}"(w));
  else if constexpr (I == 3) asm volatile("" : "+{v[108:111]}"(w));
  else asm volatile("" : "+{v[112:115]}"(w));
}
template <int U>
DI void mlp_track(char* lds, const char* __restrict__ XN, const char* __restrict__ W1,
                  const unsigned short* __restrict__ b1, char* H, const char* __restrict__ W2,
                  const unsigned short* __restrict__ b2, const char* __restrict__ Wo, const char* O,
                  const char* __restrict__ G, char* P, char* FL, char* __restrict__ out, int rt, int sp, int j4,
                  int koff, int wave, int lane) {
  constexpr int NBW = 9 - U, U2 = U == 5 ? 2 : 3;
  constexpr int NS1 = U == 5 ? 5 : 6;
  SSD();
  SST(0);
  const bool up = j4 < 2;
  const int unit0 = sp * 18 + (U == 5 ? (up ? 0 : 13) : (up ? 5 : 9));
  const unsigned lbase = lds_addr(lds);
  const lds_p L = (lds_p)lds;
  const int wm = wave & 3, wn = wave >> 2;
  char* FLg = FL + (size_t)(rt * 4 + sp) * 512;
  f32x16 acc[5];
#pragma unroll
  for (int b = 0; b < 5; ++b)
#pragma unroll
    for (int i = 0; i < 16; ++i) acc[b][i] = 0.f;
  bf16x8 w[5];
  {
    constexpr int WS = 2 * U * 1024, XB = NS1 * WS, NP = 4 + 2 * U;
    const int h = lane >> 5;
    const unsigned voffW = (unsigned)((lane >> 3) * 9216 + (((lane & 7) ^ (((lane >> 4) & 1) << 2)) << 4));
    const unsigned voff0 = wave < 4 ? (unsigned)(lane * 16) : voffW;
    const int q0 = wave - 4, q1 = wave + 4;
    const char* wb = W1 + unit0 * 128;
    const char* src0 =
        wave < 4 ? XN + (size_t)(rt * 4 + wave) * (36 * 1024) : wb + (q0 / U) * (8 * 9216) + (q0 % U) * 128;
    const char* src1 = wb + (q1 / U) * (8 * 9216) + (q1 % U) * 128;
    const unsigned st0 = wave < 4 ? 1024u : 147456u;
    const bool two = wave + 8 < NP;
    const unsigned dst0 = lbase + (wave < 4 ? XB + wave * 1024 : q0 * 1024), ds0 = wave < 4 ? 4096u : (unsigned)WS;
    auto issue = [&](int s, int e) {
      int ks = s + koff;
      ks = ks >= 36 ? ks - 36 : ks;
      if (e == 0) glds16s(src0 + (unsigned)ks * st0, voff0, dst0 + (s % NS1) * ds0);
      else if (two) glds16s(src1 + (unsigned)ks * 147456u, voffW, lbase + (s % NS1) * WS + q1 * 1024);
    };
    auto waitn = [&](auto Nc) {
      constexpr int N = decltype(Nc)::v;
      if (two) wait_vm<2 * N>(); else wait_vm<N>();
    };
#pragma unroll
    for (int s = 0; s < NS1; ++s) { issue(s, 0); issue(s, 1); }
    const int q = (lane >> 2) & 3, p = lane & 3, g = (lane >> 4) & 1;
    lds_p xa = L + XB + wm * 1024 + lane * 16;
    lds_p wa[2];
#pragma unroll
    for (int e = 0; e < 2; ++e) {
      int b = wn * U + e;
      wa[e] = L + (h * U + (b >> 1)) * 1024 + q * 128 + (((b & 1) ^ (q >> 1)) << 6) + g * 32 + p * 8;
    }
    asm volatile("" : "+v"(xa), "+v"(wa[0]), "+v"(wa[1]));
    waitn(IC<NS1 - 1>{});
    BARRIER();
    __builtin_amdgcn_s_setprio(MLP_PRIO);
    STAMP(4, 1);
    auto rdx = [&](int sl) { return ld8(xa + sl * 4096); };
    auto rdw = [&](int sl, int m) {
      const lds_p a = wa[m & 1] + sl * WS + (m >> 1) * 1024;
      return cat8(trl(a), trl(a + 512));
    };
    bf16x8 x0, x1;
    x0 = rdx(0);
#pragma unroll
    for (int m = 0; m < U; ++m) w[m] = rdw(0, m);
    unsigned short bb[U];
    unsigned pfd;
    static_for<0, 36>([&](auto Jc) {
      constexpr int j = decltype(Jc)::v;
      if constexpr (j + 1 < 36) {
        constexpr int top = j == 0 ? NS1 - 1 : (j + NS1 - 2 < 35 ? j + NS1 - 2 : 35);
        waitn(IC<top - (j + 1)>{});
        BARRIER();
      }
      STAMP2(j);
      if (j == 12) STAMP(4, 2);
      if (j == 24) STAMP(4, 3);
      SB();
      bf16x8& xc = (j & 1) ? x1 : x0;
      bf16x8& xn = (j & 1) ? x0 : x1;
      static_for<0, U>([&](auto Mc) {
        constexpr int m = decltype(Mc)::v;
        acc[m] = MFMA32(w[m], xc, acc[m]);
        SB();
        if constexpr (j + 1 < 36) {
          if constexpr (m == 0) xn = rdx((j + 1) % NS1);
          w[m] = rdw((j + 1) % NS1, m);
        }
        if constexpr (j >= 1 && j + NS1 - 1 < 36 && (m == 1 || m == 2)) issue(j + NS1 - 1, m - 1);
        if constexpr (j == PF2_AT && m == 3) {
          const unsigned l2 = fresh_lane(), li = l2 < 18 * U2 ? l2 : l2 - 32, row = li / U2;
          const unsigned vo = row * 1152u + (li - row * U2) * 128u;
          const int u2 = U == 5 ? (up ? 0 : 7) : 2 * j4;
          gld4(pfd, W2 + (size_t)(sp * 1152 + (rt & 7) * 144 + wave * 18) * 1152 + u2 * 128, vo);
          if (wave == 7) gld4k(pfd, Wo + (sp * 144 + (rt & 7) * 18) * 1152 + u2 * 128, vo);
        }
        if constexpr (j == PF2_AT + NS1 + 1 && m == 0) asm volatile("" ::"v"(pfd));
        if constexpr (j == 34 && m == 0) {
          const int l2 = fresh_lane();
#pragma unroll
          for (int b = 0; b < U; ++b) bb[b] = b1[(unit0 * 2 + wn * U + b) * 32 + (l2 & 31)];
        }
        SB();
      });
    });
    STAMP2(36);
    STAMP(4, 4);
    const bf16x8 xx = {(short)(fresh_lane() >> 5 ? 0 : 0x3F80), 0, 0, 0, 0, 0, 0, 0};
#pragma unroll
    for (int b = 0; b < U; ++b) {
      const bf16x8 wx = {(short)bb[b], 0, 0, 0, 0, 0, 0, 0};
      acc[b] = MFMA32(wx, xx, acc[b]);
    }
    asm volatile("" : "+v"(acc[0]), "+v"(acc[U - 1]));
  }
  BARRIER();
  constexpr int B1 = 2 * U, B2 = B1 + 8, B3 = 26;
  constexpr M2S SC = m2_sched(U);
  const int e0 = 2 * (U == 5 ? (up ? 0 : 26) : (up ? 10 : 18));
  const int e1 = 2 * (U == 5 ? (up ? 10 : 18) : (up ? 18 : 10)) - B1;
  const int e2 = 2 * (U == 5 ? (up ? 18 : 10) : (up ? 26 : 0)) - B2;
  const int e3 = 2 * (U == 5 ? (up ? 26 : 0) : (up ? 0 : 26)) - B3;
  auto kstep = [&](int s, int half) {
    const int uc2 = s < B1 ? 2 * U : (s < B2 ? 8 : (s < B3 ? (U == 5 ? 8 : 10) : 10));
    return (unsigned)(s + (s < B1 ? e0 : (s < B2 ? e1 : (s < B3 ? e2 : e3))) + half * uc2);
  };
  const int kh = wn;
  const int unit2 = U == 5 ? (up ? 0 : 7) : 2 * j4, boff = U == 5 ? 0 : j4 - 1;
  const int ln = fresh_lane();
  const unsigned voffX = ln * 16;
  const unsigned voffW = (unsigned)((ln >> 3) * 1152 + (((ln & 7) ^ (((ln >> 4) & 1) << 2)) << 4));
  const char* xsrc = H + ((size_t)(rt * 4 + wm) * 288 + sp * 72) * 1024;
  const int p0 = wave, p2 = 8 + (wave & 3);
  const int kb0 = p0 / U2, kb2 = p2 / U2;
  const unsigned c0 = (kb0 & 1) * (8 * 1152) + (unit2 + p0 % U2) * 128, c2 = (kb2 & 1) * (8 * 1152) + (unit2 + p2 % U2) * 128;
  const char* w2s = W2 + (size_t)sp * 1152 * 1152;
  const char* wos = Wo + sp * (8 * 18 * 1152);
  constexpr int SL2 = U2 * 4096;
  auto issueW = [&](auto Sc, int e) {
    constexpr int s = decltype(Sc)::v;
    const unsigned dst = lbase + SC.slot[s] * SL2 + (e ? p2 : p0) * 1024;
    if (e == 1 && !(U2 == 3 && (s & 1) == kh)) return;
    const int half = e ? 1 : kb0 >> 1;
    const unsigned c = e ? c2 : c0;
    if constexpr (s < M_IT) glds16s(w2s + (c + kstep(s, half) * (16u * 1152u)), voffW, dst);
    else if constexpr (s < M2_NT - 1) glds16s(wos + (c + (unsigned)(4 * half + s - M_IT) * (18u * 1152u)), voffW, dst);
    else {
      const int l2 = fresh_lane(), row = l2 >> 3;
      const unsigned ve = (unsigned)(((4 * (row >> 2) + ((row & 3) >> 1)) * 18 + (row & 1)) * 1152 + (((l2 & 7) ^ (((l2 >> 4) & 1) << 2)) << 4));
      const int kbl = (e ? kb2 : kb0) & 1;
      glds16s(wos + (c + (unsigned)(kbl * (36 - 8) + 16) * 1152u), ve, dst);
    }
  };
  unsigned b2v[NBW];
  const bool addb = sp == 0 && kh == 0;
#pragma unroll
  for (int b = 0; b < NBW; ++b)
    asm volatile("global_load_ushort %0, %1, %2" : "=v"(b2v[b]) : "v"((ln & 31) * 2), "s"(b2 + (unit2 * 2 + boff + b) * 32) : "memory");
  static_for<0, 4>([&](auto Sc) { issueW(Sc, 0); issueW(Sc, 1); });
  bf16x8 xr[4];
  const unsigned stash = lbase + 4 * SL2 + wave * ((2 * U - 4) * 1024) + voffX;
  {
    const float C0 = -2.0f * 1.4426950408889634f * 0.7978845608028654f;
    const float C1 = C0 * 0.044715f;
    char* hb = H + ((size_t)(rt * 4 + wm) * 288 + (unit0 * 2 + wn * U) * 2) * 1024 + fresh_lane() * 16;
#pragma unroll
    for (int b = 0; b < U; ++b) {
#pragma unroll
      for (int gp = 0; gp < 2; ++gp) {
        u32x4 o4;
#pragma unroll
        for (int i = 0; i < 4; ++i) {
          float o[2];
#pragma unroll
          for (int e = 0; e < 2; ++e) {
            float v = acc[b][8 * gp + 2 * i + e];
            float t = v * __builtin_fmaf(v * v, C1, C0);
            o[e] = v * __builtin_amdgcn_rcpf(1.0f + __builtin_amdgcn_exp2f(t));
          }
          o4[i] = pk2(o[0], o[1]);
        }
        st16(hb + (b * 2 + gp) * 1024, o4);
        if (b < 2) xr[b * 2 + gp] = __builtin_bit_cast(bf16x8, o4);
        else *(__attribute__((address_space(3))) u32x4*)(uintptr_t)(stash + (b * 2 + gp - 4) * 1024) = o4;
      }
    }
  }
  STAMP(4, 5);
  lds_p wa[2];
  {
    const int h = ln >> 5, q = (ln >> 2) & 3, p = ln & 3, g = (ln >> 4) & 1;
#pragma unroll
    for (int e = 0; e < 2; ++e) {
      int b = boff + e;
      wa[e] = L + (2 * kh * U2 + (b >> 1)) * 1024 + (4 * h + q) * 128 + (((b & 1) ^ (q >> 1)) << 6) + g * 32 + p * 8;
    }
  }
  asm volatile("" : "+v"(wa[0]), "+v"(wa[1]));
  wait_vm<2 * U>();
  {
    f32x16 zero;
#pragma unroll
    for (int i = 0; i < 16; ++i) zero[i] = 0.f;
    const bf16x8 xx = {(short)((ln >> 5) || !addb ? 0 : 0x3F80), 0, 0, 0, 0, 0, 0, 0};
#pragma unroll
    for (int b = 0; b < NBW; ++b) {
      asm volatile("" : "+v"(b2v[b]));
      const bf16x8 wx = {(short)b2v[b], 0, 0, 0, 0, 0, 0, 0};
      acc[b] = MFMA32(wx, xx, zero);
    }
  }
  BARRIER();
  STAMP(4, 6);
  STAMP(4, 7);
  STAMP(2, 1);
  SST(1);
  auto rdw = [&](int s, int m) {
    const lds_p a = wa[m & 1] + SC.slot[s] * SL2 + (m >> 1) * 1024;
    return cat8(trl(a), trl(a + U2 * 1024));
  };
#pragma unroll
  for (int m = 0; m < NBW; ++m) w[m] = rdw(0, m);
  unsigned fv = 0;
  auto poll = [&]() {
    unsigned vo = voffX;
    asm volatile("" : "+v"(vo));
    vo = (vo & 48u) << 3;
    gld4c(fv, FLg, vo);
  };
  auto check = [&](unsigned long long mask) {
    mask &= ~(1ull << j4);
    asm volatile("" : "+v"(fv));
    if (__ballot(fv != 1u) & mask) {
#pragma unroll 1
      for (int it = 0; it < (1 << 17); ++it) {
        __builtin_amdgcn_s_sleep(4);
        poll();
        wait_vm<0>();
        asm volatile("" : "+v"(fv));
        if (!(__ballot(fv != 1u) & mask)) break;
      }
    }
  };
  auto waitk = [&](auto N0, auto N1) {
    constexpr int n0 = decltype(N0)::v, n1 = decltype(N1)::v;
    if constexpr (n0 == n1) wait_vm<n0>();
    else { if (kh == 0) wait_vm<n0>(); else wait_vm<n1>(); }
  };
  constexpr int CK1 = m2_ck1(U), CK2 = m2_ck2(U);
  unsigned gv;
  static_for<0, M_IT>([&](auto Jc) {
    constexpr int j = decltype(Jc)::v;
    if constexpr (m2_need(U, j)) waitk(IC<m2_wait(U, 0, j, 0)>{}, IC<m2_wait(U, 1, j, 0)>{});
    if constexpr (j > 0) BARRIER();
    if constexpr (j > 0) pin_acc<NBW>(acc);
    static_for<0, 4>([&](auto Ic) {
      constexpr int i = decltype(Ic)::v;
      if constexpr (j + ((i - j) & 3) < M_IT) pin_x1<i>(xr[i]);
    });
    if constexpr (j == 3) {
      if (wave == 0 && voffX == 0) st4(FLg + j4 * 128, 1u);
    }
    STAMP2(40 + j);
    if (j == 9) STAMP(2, 2);
    if (j == 18) STAMP(2, 3);
    if (j == 18) SST(2);
    if (j == 27) STAMP(2, 4);
    if constexpr (j == CK1 - 2 || j == CK2 - 2) poll();
    SB();
    static_for<0, NBW>([&](auto Mc) {
      constexpr int m = decltype(Mc)::v;
      pin_w<m>(w[m]);
      acc[m] = MFMA32(w[m], xr[j & 3], acc[m]);
      SB();
      w[m] = rdw(j + 1, m);
      static_for<j + 3, (j + 7 < M2_NT ? j + 7 : M2_NT)>([&](auto Sc) {
        constexpr int s = decltype(Sc)::v;
        if constexpr (SC.it[s] == j) {
          constexpr bool second = s > 0 && SC.it[s - 1] == j;
          if constexpr (m == (second ? 2 : 0)) issueW(Sc, 0);
          if constexpr (m == (second ? 3 : 1)) issueW(Sc, 1);
        }
      });
      SB();
    });
    if constexpr (j == CK1 || j == CK2) {
      waitk(IC<m2_wait(U, 0, j, 1)>{}, IC<m2_wait(U, 1, j, 1)>{});
      check(j == CK1 ? 6ull : 15ull);
    }
    if constexpr (j + 4 < B1) xstash<(j & 3), j * 1024>(xr[j & 3], stash);
    else if constexpr (j + 4 < M_IT) xload<(j & 3)>(xr[j & 3], xsrc + kstep(j + 4, kh) * 1024u, voffX);
    if constexpr (j == 32) gld4(gv, G + (rt * 4 + wm) * 128, (voffX >> 2) & 124u);
    SB();
  });
  STAMP2(76);
  STAMP(2, 5);
  SST(3);
  asm volatile("s_waitcnt vmcnt(0)" : "+v"(gv)::"memory");
  pin_acc<NBW>(acc);
  {
    const float og = 1.0f - __uint_as_float(gv);
#pragma unroll
    for (int b = 0; b < NBW; ++b)
#pragma unroll
      for (int i = 0; i < 16; ++i) acc[b][i] *= og;
  }
  pin_acc<NBW>(acc);
  static_for<0, NBW>([&](auto Mc) { pin_w<decltype(Mc)::v>(w[decltype(Mc)::v]); });
  if (wave == 0) {
    const int l2 = fresh_lane();
    spin_flags(FL + FL_O + rt * 512 + sp * 32, (unsigned)(((l2 >> 3) & 3) * 128 + (l2 & 7) * 4));
  }
  BARRIER();
  SST(4);
  {
    bf16x8 xe;
    const char* osrc = O + (size_t)((rt * 4 + wm) * 32 + sp * 8 + kh * 4) * 1024;
    static_for<0, 4>([&](auto Ic) { xload<decltype(Ic)::v>(xr[decltype(Ic)::v], osrc + decltype(Ic)::v * 1024, voffX); });
    gld16c(xe, O + OEX + ((rt * 4 + wm) * 4 + sp) * 1024, voffX);
    static_for<0, 4>([&](auto Tc) {
      constexpr int t = decltype(Tc)::v;
      wait_vm<4 - t>();
      pin_x1<t>(xr[t]);
      pin_acc<NBW>(acc);
      static_for<0, NBW>([&](auto Mc) {
        constexpr int m = decltype(Mc)::v;
        pin_w<m>(w[m]);
        acc[m] = MFMA32(w[m], xr[t], acc[m]);
        SB();
        w[m] = rdw(M_IT + t + 1, m);
        SB();
      });
    });
    wait_vm1<0>(xe);
    pin_acc<NBW>(acc);
    static_for<0, NBW>([&](auto Mc) { pin_w<decltype(Mc)::v>(w[decltype(Mc)::v]); });
    if (kh == 0) {
#pragma unroll
      for (int m = 0; m < NBW; ++m) acc[m] = MFMA32(w[m], xe, acc[m]);
    }
    pin_acc<NBW>(acc);
  }
  BARRIER();
  STAMP(2, 6);
  SST(5);
  constexpr int NA = (NBW + 1) / 2;
  const int l2 = fresh_lane();
  char* ex = lds + wm * (NBW * 4096) + l2 * 16;
  if (kh == 0) {
#pragma unroll
    for (int b = NA; b < NBW; ++b)
#pragma unroll
      for (int c = 0; c < 4; ++c) {
        f32x4 v = {acc[b][4 * c], acc[b][4 * c + 1], acc[b][4 * c + 2], acc[b][4 * c + 3]};
        *(f32x4*)(ex + b * 4096 + c * 1024) = v;
      }
  } else {
#pragma unroll
    for (int b = 0; b < NA; ++b)
#pragma unroll
      for (int c = 0; c < 4; ++c) {
        f32x4 v = {acc[b][4 * c], acc[b][4 * c + 1], acc[b][4 * c + 2], acc[b][4 * c + 3]};
        *(f32x4*)(ex + b * 4096 + c * 1024) = v;
      }
    acc[0] = acc[NA];
    acc[1] = acc[NA + 1];
  }
  auto pin_r = [&]() {
    if constexpr (NA == 3) asm volatile("" : "+{v[0:15]}"(acc[0]), "+{v[16:31]}"(acc[1]), "+{v[32:47]}"(acc[2]));
    else asm volatile("" : "+{v[0:15]}"(acc[0]), "+{v[16:31]}"(acc[1]));
  };
  pin_r();
  __syncthreads();
  const int bb0 = kh ? NA : 0;
  const bool third = kh == 0;
  ex += bb0 * 4096;
#pragma unroll
  for (int b = 0; b < NA; ++b) {
    if (b == 2 && !third) break;
#pragma unroll
    for (int c = 0; c < 4; ++c) {
      f32x4 a = *(const f32x4*)(ex + b * 4096 + c * 1024);
#pragma unroll
      for (int i = 0; i < 4; ++i) acc[b][4 * c + i] += a[i];
    }
  }
  pin_r();
  const char* flp = FL + FL_P + (((rt * 4 + j4) * 4 + wm) * 2 + kh) * 16;
  const size_t pofs = ((size_t)(rt * 4 + wm) * 18 + unit2 * 2 + boff + bb0) * 2048;
  if (wm != sp) {
    char* pb = P + (size_t)sp * (64 * 18 * 2048) + pofs + l2 * 16;
#pragma unroll
    for (int b = 0; b < NA; ++b) {
      if (b == 2 && !third) break;
#pragma unroll
      for (int gp = 0; gp < 2; ++gp) {
        u32x4 v;
#pragma unroll
        for (int i = 0; i < 4; ++i) v[i] = pk2(acc[b][8 * gp + 2 * i], acc[b][8 * gp + 2 * i + 1]);
        st16(pb + b * 2048 + gp * 1024, v);
      }
    }
    wait_vm<0>();
    if (l2 == 0) st4((char*)flp + sp * 4, 1u);
  } else {
    spin_flags(flp, (unsigned)(((sp + 1 + (l2 % 3)) & 3) * 4));
    SST(6);
    bf16x8 pv[NA][3][2];
    static_for<0, NA * 6>([&](auto Ic) {
      constexpr int i = decltype(Ic)::v, b = i / 6, o = (i >> 1) % 3, gp = i & 1;
      const char* pb = P + (size_t)((sp + 1 + o) & 3) * (64 * 18 * 2048) + pofs + (b == 2 && !third ? 1 : b) * 2048 + gp * 1024;
      pload<i>(pv[b][o][gp], pb, l2 * 16);
    });
    char* orow = out + (size_t)((rt * 4 + wm) * 32 + (l2 & 31)) * 1152 + (unit2 * 2 + boff + bb0) * 64 + (l2 >> 5) * 16;
    static_for<0, NA>([&](auto Bc) {
      constexpr int b = decltype(Bc)::v;
      wait_vm<(NA - 1 - b) * 6 + 2 * b>();
      if (b < 2 || third) {
        static_for<0, 6>([&](auto Ic) {
          constexpr int i = decltype(Ic)::v, gp = i & 1;
          ppin<b * 6 + i>(pv[b][i >> 1][gp]);
          const u32x4 v = __builtin_bit_cast(u32x4, pv[b][i >> 1][gp]);
#pragma unroll
          for (int k = 0; k < 4; ++k) {
            acc[b][8 * gp + 2 * k] += __uint_as_float(v[k] << 16);
            acc[b][8 * gp + 2 * k + 1] += __uint_as_float(v[k] & 0xffff0000u);
          }
        });
        u32x2 o2[4];
#pragma unroll
        for (int gq = 0; gq < 4; ++gq) {
          o2[gq][0] = pk2(acc[b][4 * gq], acc[b][4 * gq + 1]);
          o2[gq][1] = pk2(acc[b][4 * gq + 2], acc[b][4 * gq + 3]);
        }
#pragma unroll
        for (int gq = 0; gq < 4; gq += 2) {
          auto r0 = __builtin_amdgcn_permlane32_swap(o2[gq][0], o2[gq + 1][0], false, false);
          auto r1 = __builtin_amdgcn_permlane32_swap(o2[gq][1], o2[gq + 1][1], false, false);
          u32x4 wv = {r0[0], r1[0], r0[1], r1[1]};
          st16p(orow + b * 64 + gq * 16, wv);
        }
      }
    });
  }
  STAMP(2, 7);
  SST(7);
  SSW();
}

extern "C" __global__ __launch_bounds__(512, 4) void kernel1(const char* __restrict__ T,
                                                   char* O, const char* __restrict__ XN,
                                                   const char* __restrict__ W1, const unsigned short* __restrict__ b1,
                                                   char* H, const char* __restrict__ W2,
                                                   const unsigned short* __restrict__ b2, const char* __restrict__ Wo,
                                                   const char* __restrict__ G, char* P, char* FL,
                                                   char* __restrict__ out) {
  __shared__ __attribute__((aligned(16))) char lds[81920];
  const int wave = __builtin_amdgcn_readfirstlane(threadIdx.x >> 6);
  const int lane = threadIdx.x & 63;
  const int bid = blockIdx.x;
  if (bid < 256) {
    STAMP(1, 0);
    STAMP(5, 0);
    __builtin_amdgcn_s_setprio(ATT_PRIO);
    attn_role(lds, T, O, G, FL, W2, Wo, bid, wave, fresh_lane());
  } else {
    STAMP(2, 0);
    STAMP(4, 0);
    const int wg = bid - 256;
    const int xcd = wg & 7, idx = wg >> 3;
    const int sp = xcd >> 1, j4 = idx & 3, r8 = idx >> 2, rt = (xcd & 1) * 8 + r8;
    const int koff = (r8 * 9 / 2 + j4 * 9) % 36;
    if (j4 == 0 || j4 == 3)
      mlp_track<5>(lds, XN, W1, b1, H, W2, b2, Wo, O, G, P, FL, out, rt, sp, j4, koff, wave, lane);
    else
      mlp_track<4>(lds, XN, W1, b1, H, W2, b2, Wo, O, G, P, FL, out, rt, sp, j4, koff, wave, lane);
  }
}

constexpr int Q_SLOT = 40960;
#define QGRID 256
#define PFW 0
#define PF1 0
DI void qkv_role(char* lds, const char* __restrict__ X, const char* __restrict__ Wqkv, const char* __restrict__ pf, int pfl,
                 char* __restrict__ T, int rt, int ct, int joff, int wave, int lane) {
  f32x16 acc[2];
#pragma unroll
  for (int i = 0; i < 16; ++i) { acc[0][i] = 0.f; acc[1][i] = 0.f; }
  const unsigned lbase = lds_addr(lds);
  unsigned voffX[2];
#pragma unroll
  for (int i = 0; i < 2; ++i) {
    int row = wave * 32 + 8 * i + (lane >> 3);
    int c = (lane & 7) ^ ((row >> 1) & 7);
    voffX[i] = (unsigned)((rt * 256 + row) * 1152 + c * 16);
  }
  const unsigned voffW = (unsigned)((lane >> 3) * 3456 + (((lane & 7) ^ (((lane >> 4) & 1) << 2)) << 4));
  const char* wsrc = Wqkv + (size_t)wave * 8 * 3456 + ct * 128;
  auto issue = [&](int J, int e) {
    unsigned dst = lbase + (J & 3) * Q_SLOT;
    unsigned Jr = J + joff;
    Jr = Jr >= 9 ? Jr - 9 : Jr;
    if (e == 0) glds16s(wsrc + Jr * (64u * 3456u), voffW, dst + 32768 + wave * 1024);
    else glds16s(X + Jr * 128u + ((e - 1) >> 1) * (16 * 1152), voffX[(e - 1) & 1], dst + (wave * 4 + e - 1) * 1024);
  };
  STAMP(0, 0);
#pragma unroll
  for (int J = 0; J < 3; ++J)
#pragma unroll
    for (int e = 0; e < 5; ++e) issue(J, e);
  const int r = lane & 31, h = lane >> 5;
  const int q = (lane >> 2) & 3, p = lane & 3, g = (lane >> 4) & 1;
  const lds_p L = (lds_p)lds;
  lds_p xa[4], wa[2][2];
#pragma unroll
  for (int ks = 0; ks < 4; ++ks) xa[ks] = L + (wave * 32 + r) * 128 + (((2 * ks + h) ^ ((r >> 1) & 7)) << 4);
#pragma unroll
  for (int b = 0; b < 2; ++b) {
    wa[0][b] = L + 32768 + h * 1024 + q * 128 + ((b ^ (q >> 1)) << 6) + g * 32 + p * 8;
    wa[1][b] = wa[0][b] + 2 * Q_SLOT;
  }
  lds_p xb[4];
#pragma unroll
  for (int ks = 0; ks < 4; ++ks) xb[ks] = xa[ks] + 2 * Q_SLOT;
  asm volatile("" : "+v"(xb[0]), "+v"(xb[1]), "+v"(xb[2]), "+v"(xb[3]), "+v"(wa[1][0]), "+v"(wa[1][1]));
  wait_vm<10>();
  BARRIER();
  STAMP(0, 1);
  bf16x8 fx[2], fw[2][2];
  auto rd = [&](int buf, int J, int ks) {
    const int sl = J & 3;
    fx[buf] = ld8((sl < 2 ? xa[ks] : xb[ks]) + (sl & 1) * Q_SLOT);
#pragma unroll
    for (int b = 0; b < 2; ++b) {
      const lds_p a = wa[sl >> 1][b] + (sl & 1) * Q_SLOT + ks * 2048;
      fw[buf][b] = cat8(trl(a), trl(a + 512));
    }
  };
  rd(0, 0, 0);
  unsigned pfd = 0;
  static_for<0, 9>([&](auto Jc) {
    constexpr int J = decltype(Jc)::v;
    if constexpr (J + 1 < 9) {
      wait_vm<((J + 2 < 9) ? 5 : 0) + (J >= 6 ? PF1 : 0)>();
      BARRIER();
    }
    if (J == 3) STAMP(0, 2);
    if (J == 6) STAMP(0, 3);
    SB();
    static_for<0, 4>([&](auto Kc) {
      constexpr int ks = decltype(Kc)::v, cur = ks & 1;
      acc[0] = MFMA32(fx[cur], fw[cur][0], acc[0]);
      SB();
      if constexpr (ks < 3) rd(cur ^ 1, J, ks + 1);
      else if constexpr (J + 1 < 9) rd(cur ^ 1, J + 1, 0);
      SB();
      acc[1] = MFMA32(fx[cur], fw[cur][1], acc[1]);
      SB();
      if constexpr (J + 3 < 9) {
        if constexpr (ks == 0) { issue(J + 3, 0); issue(J + 3, 1); }
        else issue(J + 3, ks + 1);
      }
      SB();
    });
  });
  STAMP(0, 5);
  const float QSCALE = 0.23570226039551584f * 1.4426950408889634f;
  const float sc = ct < 9 ? QSCALE : 1.0f;
#pragma unroll
  for (int nb = 0; nb < 2; ++nb) {
    char* base = T + (size_t)h * TP + (ct * 64 + nb * 32 + r) * 16;
#pragma unroll
    for (int s = 0; s < 2; ++s) {
      int step = rt * 16 + wave * 2 + s;
      u32x4 v;
#pragma unroll
      for (int i = 0; i < 4; ++i) v[i] = pk2(acc[nb][8 * s + 2 * i] * sc, acc[nb][8 * s + 2 * i + 1] * sc);
      st16(base + (size_t)step * (2 * TP), v);
    }
  }
  if (ct == 26 && wave < 4) {
    int idx = wave * 64 + lane;
    unsigned c = (idx & 7) == 0 ? 0x3F803F80u : 0u;
    u32x4 v = {c, c, c, c};
    st16(T + (size_t)(rt * 32 + (idx >> 3)) * TP + (1728 + (idx & 7)) * 16, v);
  }
  asm volatile("s_waitcnt vmcnt(0)" : "+v"(pfd)::"memory");
}

DI void ln_role(char* lds, const char* __restrict__ X, const char* __restrict__ gw, char* __restrict__ XN,
                float* __restrict__ G, char* __restrict__ FL, int id, int wave, int lane) {
  const int r = lane & 31, h = lane >> 5;
  const int rb = wave >> 2, kq = wave & 3;
  const int row = id * 64 + rb * 32 + r;
  u32x4 xv[9], gv[9];
#pragma unroll
  for (int i = 0; i < 9; ++i) xv[i] = *(const u32x4*)(X + (size_t)row * 1152 + ((kq * 9 + i) * 2 + h) * 16);
#pragma unroll
  for (int i = 0; i < 9; ++i) gv[i] = *(const u32x4*)(gw + ((kq * 9 + i) * 2 + h) * 16);
  if (wave < 2) {
    u32x4 z = {0u, 0u, 0u, 0u};
    st16(FL + id * 2048 + wave * 1024 + lane * 16, z);
  }
  float* red = (float*)lds;
  float s = 0.f, d = 0.f;
#pragma unroll
  for (int i = 0; i < 9; ++i)
#pragma unroll
    for (int j = 0; j < 4; ++j) {
      float a = __uint_as_float(xv[i][j] << 16), b = __uint_as_float(xv[i][j] & 0xffff0000u);
      s += a + b;
      d = __builtin_fmaf(a, __uint_as_float(gv[i][j] << 16), d);
      d = __builtin_fmaf(b, __uint_as_float(gv[i][j] & 0xffff0000u), d);
    }
  s += xor32(s);
  d += xor32(d);
  if (h == 0) { red[wave * 32 + r] = s; red[256 + wave * 32 + r] = d; }
  __syncthreads();
  const float* rs = red + rb * 128 + r;
  const float mean = (rs[0] + rs[32] + rs[64] + rs[96]) * (1.0f / 576.0f);
  if (kq == 0 && h == 0) {
    float v = rs[256] + rs[288] + rs[320] + rs[352];
    G[row] = 1.0f / (1.0f + __builtin_amdgcn_exp2f(-1.4426950408889634f * v));
  }
  float q2 = 0.f;
#pragma unroll
  for (int i = 0; i < 9; ++i)
#pragma unroll
    for (int j = 0; j < 4; ++j) {
      float a = __uint_as_float(xv[i][j] << 16) - mean, b = __uint_as_float(xv[i][j] & 0xffff0000u) - mean;
      q2 = __builtin_fmaf(a, a, q2);
      q2 = __builtin_fmaf(b, b, q2);
    }
  q2 += xor32(q2);
  if (h == 0) red[512 + wave * 32 + r] = q2;
  __syncthreads();
  const float var = (rs[512] + rs[544] + rs[576] + rs[608]) * (1.0f / 576.0f);
  const float rstd = 1.0f / __builtin_sqrtf(var + 1e-5f);
  char* dst = XN + ((size_t)(id * 2 + rb) * 36 + kq * 9) * 1024 + lane * 16;
#pragma unroll
  for (int i = 0; i < 9; ++i) {
    u32x4 v;
#pragma unroll
    for (int j = 0; j < 4; ++j) {
      float a = __uint_as_float(xv[i][j] << 16) - mean, b = __uint_as_float(xv[i][j] & 0xffff0000u) - mean;
      v[j] = pk2(a * rstd, b * rstd);
    }
    st16(dst + i * 1024, v);
  }
}

__global__ __launch_bounds__(512, 2) void kernelQ(const char* __restrict__ X, const char* __restrict__ Wqkv,
                                                   const char* __restrict__ gw, const char* __restrict__ W1, const char* __restrict__ W2,
                                                   char* __restrict__ T,
                                                   char* __restrict__ XN, float* __restrict__ G,
                                                   char* __restrict__ FL) {
  __shared__ __attribute__((aligned(16))) char lds[163840];
  const int wg = blockIdx.x;
  const int wave = __builtin_amdgcn_readfirstlane(threadIdx.x >> 6);
  const int lane = threadIdx.x & 63;
  if (wg < 216) {
    const int xcd = wg & 7, t = (xcd >> 2) * 27 + (wg >> 3);
    const int joff = ((t >> 1) * 3 + (t & 1) * 4) % 9;
    qkv_role(lds, X, Wqkv, W1 + (xcd >> 1) * 2304, (wg >> 3) * 384, T, (xcd & 3) * 2 + (t & 1), t >> 1, joff, wave, lane);
  } else if (wg < 248) {
    STAMP(0, 0);
    ln_role(lds, X, gw, XN, G, FL, wg - 216, wave, lane);
  } else {
    const char* pc;
    unsigned span;
    asm volatile("s_getpc_b64 s[20:21]\n\ts_mov_b32 %1, kernel1@rel32@lo+4\n\ts_add_u32 s20, s20, %1\n\ts_addc_u32 s21, s21, kernel1@rel32@hi+16"
                 : "={s[20:21]}"(pc), "=s"(span)::"scc");
    span = 0u - span;
    if (span < (1u << 18)) {
      unsigned d0 = 0;
#pragma unroll 1
      for (unsigned o = threadIdx.x * 128u; o < span; o += 65536u) gld4k(d0, pc, o);
      asm volatile("s_waitcnt vmcnt(0)" : "+v"(d0)::"memory");
    }
  }
  STAMP(0, 6);
  STAMP(0, 7);
}

extern "C" void kernel_launch(void* const* d_in, const int* in_sizes, int n_in, void* d_out, int out_size, void* d_ws,
                              size_t ws_size, hipStream_t stream) {
  const char* X = (const char*)d_in[0];
  const char* Wqkv = (const char*)d_in[1];
  const char* Wo = (const char*)d_in[2];
  const char* W1 = (const char*)d_in[4];
  const unsigned short* b1 = (const unsigned short*)d_in[5];
  const char* W2 = (const char*)d_in[6];
  const unsigned short* b2 = (const unsigned short*)d_in[7];
  const char* gw = (const char*)d_in[8];
  char* ws = (char*)d_ws;
  char* T = ws + WS_T;
  char* H = ws + WS_H;
  char* O = ws + WS_O;
  char* P = ws + WS_P;
  char* XN = ws + WS_XN;
  char* FL = ws + WS_FL;
  float* G = (float*)(ws + WS_G);
  kernelQ<<<QGRID, 512, 0, stream>>>(X, Wqkv, gw, W1, W2, T, XN, G, FL);
  kernel1<<<512, 512, 0, stream>>>(T, O, XN, W1, b1, H, W2, b2, Wo, (const char*)G, P, FL, (char*)d_out);
}
```

```cpp
#include <hip/hip_runtime.h>
#include <stdint.h>

typedef __attribute__((ext_vector_type(8))) short bf16x8;
typedef __attribute__((ext_vector_type(4))) short s16x4;
typedef __attribute__((ext_vector_type(2))) __bf16 bf16x2_t;
typedef __attribute__((ext_vector_type(2))) float f32x2;
typedef __attribute__((ext_vector_type(4))) float f32x4;
typedef __attribute__((ext_vector_type(16))) float f32x16;
typedef __attribute__((ext_vector_type(4))) unsigned u32x4;
typedef __attribute__((ext_vector_type(2))) unsigned u32x2;
typedef __attribute__((address_space(3))) s16x4* lds_s16x4_p;
typedef __attribute__((address_space(3))) char* lds_p;
typedef __attribute__((address_space(3))) const bf16x8* lds_bf16x8_p;

#define STAMP(k, i)
#define STAMP2(i)
#define SST(i)
#define SS8(i)
#define SSD()
#define SSW()
#define DI __device__ __forceinline__
#define MFMA32(a, b, c) __builtin_amdgcn_mfma_f32_32x32x16_bf16((a), (b), (c), 0, 0, 0)
#define MFMA32K8(a, b, c) __builtin_amdgcn_mfma_f32_32x32x8bf16_1k((a), (b), (c), 0, 0, 0)
#define WAIT_VM(n) asm volatile("s_waitcnt vmcnt(" #n ")" ::: "memory")
#define BARRIER() __builtin_amdgcn_s_barrier()
#define SB() __builtin_amdgcn_sched_barrier(0)

constexpr int S_ = 2048, D_ = 576, F_ = 4608, NQKV = 1728;
constexpr int TSTEP = 640;
constexpr int TP = 1736 * 16;
constexpr int OEX = 64 * 32 * 1024;
constexpr size_t WS_T = 0;
constexpr size_t WS_H = 8u << 20;
constexpr size_t WS_O = 28u << 20;
constexpr size_t WS_G = 31u << 20;
constexpr size_t WS_FL = (31u << 20) + (64u << 10);
constexpr size_t WS_P = 32u << 20;
constexpr size_t WS_XN = 44u << 20;
constexpr size_t WS_XF = 47u << 20;
constexpr int FL_Q = 32768, FL_O = 36864, FL_P = 45056;

DI unsigned pk2(float a, float b) {
  f32x2 v = {a, b};
  bf16x2_t r = __builtin_convertvector(v, bf16x2_t);
  return __builtin_bit_cast(unsigned, r);
}
DI float bf2f(unsigned short u) { return __uint_as_float(((unsigned)u) << 16); }
DI s16x4 tr_read(const char* p) {
  return __builtin_amdgcn_ds_read_tr16_b64_v4i16((lds_s16x4_p)(p));
}
DI s16x4 trl(lds_p p) { return __builtin_amdgcn_ds_read_tr16_b64_v4i16((lds_s16x4_p)p); }
DI bf16x8 ld8(lds_p p) { return *(lds_bf16x8_p)p; }
DI float xor32(float v) { return __shfl_xor(v, 32, 64); }

DI void glds16s(const char* sbase, unsigned voff, unsigned lds_dst) {
  unsigned keep;
  asm volatile("s_mov_b32 %0, m0\n\ts_mov_b32 m0, %3\n\ts_nop 0\n\tglobal_load_lds_dwordx4 %1, %2\n\ts_mov_b32 m0, %0"
               : "=&s"(keep)
               : "v"(voff), "s"(sbase), "s"(lds_dst)
               : "memory");
}
DI void glds16c(const char* sbase, unsigned voff, unsigned lds_dst) {
  unsigned keep;
  asm volatile("s_mov_b32 %0, m0\n\ts_mov_b32 m0, %3\n\ts_nop 0\n\tglobal_load_lds_dwordx4 %1, %2 sc1\n\ts_mov_b32 m0, %0"
               : "=&s"(keep)
               : "v"(voff), "s"(sbase), "s"(lds_dst)
               : "memory");
}
DI void touch(const char* sbase, unsigned voff, unsigned lds_dst) {
  unsigned keep;
  asm volatile("s_mov_b32 %0, m0\n\ts_mov_b32 m0, %3\n\ts_nop 0\n\tglobal_load_lds_dword %1, %2\n\ts_mov_b32 m0, %0"
               : "=&s"(keep)
               : "v"(voff), "s"(sbase), "s"(lds_dst)
               : "memory");
}
DI void gld16(bf16x8& d, const char* sbase, unsigned voff) {
  asm volatile("global_load_dwordx4 %0, %1, %2" : "=v"(d) : "v"(voff), "s"(sbase) : "memory");
}
DI void gld16c(bf16x8& d, const char* sbase, unsigned voff) {
  asm volatile("global_load_dwordx4 %0, %1, %2 sc1" : "=v"(d) : "v"(voff), "s"(sbase) : "memory");
}
DI void gld4c(unsigned& d, const char* sbase, unsigned voff) {
  asm volatile("global_load_dword %0, %1, %2 sc1" : "=v"(d) : "v"(voff), "s"(sbase) : "memory");
}
DI void gld4(unsigned& d, const char* sbase, unsigned voff) {
  asm volatile("global_load_dword %0, %1, %2" : "=v"(d) : "v"(voff), "s"(sbase) : "memory");
}
DI void gld4k(unsigned& d, const char* sbase, unsigned voff) {
  asm volatile("global_load_dword %0, %1, %2" : "+v"(d) : "v"(voff), "s"(sbase) : "memory");
}
DI unsigned lds_addr(const void* p) { return (unsigned)(uintptr_t)(lds_p)p; }

DI void st16(void* p, u32x4 v) { asm volatile("global_store_dwordx4 %0, %1, off sc1\n\ts_nop 1" ::"v"(p), "v"(v) : "memory"); }
DI void st16p(void* p, u32x4 v) { asm volatile("global_store_dwordx4 %0, %1, off\n\ts_nop 1" ::"v"(p), "v"(v) : "memory"); }
DI void st4(void* p, unsigned v) { asm volatile("global_store_dword %0, %1, off sc1" ::"v"(p), "v"(v) : "memory"); }

DI int fresh_lane() {
  int l;
  asm volatile("v_mbcnt_lo_u32_b32 %0, -1, 0\n\tv_mbcnt_hi_u32_b32 %0, -1, %0" : "=v"(l));
  return l;
}
template <int I>
struct IC { static constexpr int v = I; };
template <int I, int N, class F>
DI void static_for(F&& f) {
  if constexpr (I < N) {
    f(IC<I>{});
    static_for<I + 1, N>(f);
  }
}
template <int N>
DI void wait_vm() { asm volatile("s_waitcnt vmcnt(%0)" ::"i"(N) : "memory"); }
template <int N>
DI void wait_vm1(bf16x8& a) { asm volatile("s_waitcnt vmcnt(%1)" : "+v"(a) : "i"(N) : "memory"); }
DI void spin_flags(const char* base, unsigned voff) {
#pragma unroll 1
  for (int it = 0; it < (1 << 17); ++it) {
    unsigned f;
    gld4c(f, base, voff);
    asm volatile("s_waitcnt vmcnt(0)" : "+v"(f)::"memory");
    if (!__ballot(f != 1u)) break;
    __builtin_amdgcn_s_sleep(4);
  }
}
DI bf16x8 cat8(s16x4 lo, s16x4 hi) { return __builtin_shufflevector(lo, hi, 0, 1, 2, 3, 4, 5, 6, 7); }

DI f32x16 qk_tile(lds_p kb, lds_p ke, bf16x8 qf, s16x4 qe, f32x16 c) {
  s16x4 lo = trl(kb);
  s16x4 hi = trl(kb + 64);
  s16x4 ex = trl(ke);
  bf16x8 kf = __builtin_shufflevector(lo, hi, 0, 1, 2, 3, 4, 5, 6, 7);
  f32x16 s = MFMA32(kf, qf, c);
  s = MFMA32K8(ex, qe, s);
  return s;
}
DI f32x16 pv_tile(lds_p vb, const f32x16& pe, f32x16 o) {
  bf16x8 v0 = ld8(vb);
  bf16x8 v1 = ld8(vb + TSTEP);
  u32x4 p0, p1;
#pragma unroll
  for (int i = 0; i < 4; ++i) {
    p0[i] = pk2(pe[2 * i], pe[2 * i + 1]);
    p1[i] = pk2(pe[8 + 2 * i], pe[8 + 2 * i + 1]);
  }
  o = MFMA32(v0, __builtin_bit_cast(bf16x8, p0), o);
  o = MFMA32(v1, __builtin_bit_cast(bf16x8, p1), o);
  return o;
}
struct KFrag { bf16x8 f; s16x4 e; };
struct VFrag { bf16x8 v0, v1; };
DI KFrag load_k(lds_p kb, lds_p ke) {
  KFrag k;
  s16x4 lo = trl(kb);
  s16x4 hi = trl(kb + 64);
  k.e = trl(ke);
  k.f = __builtin_shufflevector(lo, hi, 0, 1, 2, 3, 4, 5, 6, 7);
  return k;
}
DI VFrag load_v(lds_p vb) {
  VFrag v;
  v.v0 = ld8(vb);
  v.v1 = ld8(vb + TSTEP);
  return v;
}
#define EXP2(x) __builtin_amdgcn_exp2f(x)
#define PTRUNC 1
DI unsigned pkp(float a, float b) {
  return __builtin_amdgcn_perm(__float_as_uint(b), __float_as_uint(a), 0x07060302u);
}
#define APM 2
#define APMASK 0x77
#define PHI(i) if constexpr ((PM >> (i)) & 1) __builtin_amdgcn_s_setprio(APM)
#define PLO(i) if constexpr ((PM >> (i)) & 1) __builtin_amdgcn_s_setprio(0)
template <int PM>
DI void tile_step(const f32x16& s, f32x16& sn, const KFrag& k, const VFrag& v, bf16x8 qf, s16x4 qe,
                  f32x16& o) {
  f32x16 negm;
#pragma unroll
  for (int i = 0; i < 16; ++i) negm[i] = 0.f;
  u32x4 p0, p1;
  p0[0] = pkp(EXP2(s[0]), EXP2(s[1]));
  p0[1] = pkp(EXP2(s[2]), EXP2(s[3]));
  SB();
  PHI(0);
  SB();
  f32x16 t = MFMA32(k.f, qf, negm);
  SB();
  PLO(0);
  SB();
  p0[2] = pkp(EXP2(s[4]), EXP2(s[5]));
  p0[3] = pkp(EXP2(s[6]), EXP2(s[7]));
  SB();
  PHI(1);
  SB();
  o = MFMA32(v.v0, __builtin_bit_cast(bf16x8, p0), o);
  SB();
  PLO(1);
  SB();
  p1[0] = pkp(EXP2(s[8]), EXP2(s[9]));
  p1[1] = pkp(EXP2(s[10]), EXP2(s[11]));
  SB();
  PHI(2);
  SB();
  t = MFMA32K8(k.e, qe, t);
  SB();
  PLO(2);
  SB();
  p1[2] = pkp(EXP2(s[12]), EXP2(s[13]));
  p1[3] = pkp(EXP2(s[14]), EXP2(s[15]));
  SB();
  PHI(3);
  SB();
  o = MFMA32(v.v1, __builtin_bit_cast(bf16x8, p1), o);
  SB();
  PLO(3);
  SB();
  sn = t;
}
DI float rowmax16(const f32x16& s) {
  float m = __builtin_fmaxf(s[0], s[1]);
#pragma unroll
  for (int i = 2; i < 16; ++i) m = __builtin_fmaxf(m, s[i]);
  return __builtin_fmaxf(m, xor32(m));
}

constexpr int AT_SLOT = 20480;
constexpr int at_cnt(int c, int grp) { return 2 + (((c & 1) == grp) ? 1 : 0); }
constexpr int at_off(int tt) { return ((tt >> 3) & 1) * AT_SLOT + (tt & 7) * 1280; }

#define TOUCH_AT 9
DI void attn_role(char* lds, const char* T, char* O, const char* __restrict__ G, char* FL, const char* __restrict__ W2,
                  const char* __restrict__ Wo, int wg, int wave, int lane) {
  const int head = (wg & 7) * 4 + ((wg >> 3) & 3);
  const int qt = wg >> 5;
  const int r = lane & 31, h = lane >> 5;
  const int grp = wave >> 2;
  const unsigned lbase = lds_addr(lds);
  const int p1 = wave + 8, p2 = 16 + (wave & 3);
  auto mkoff = [&](int pc, int first) {
    int isv = pc >= 10;
    int sl = (pc - 10 * isv) * 64 + lane;
    int seg = sl / 20, row = sl - seg * 20;
    int slot = row < 18 ? first + 576 * isv + head * 18 + row : 1728 + row - 18;
    return (unsigned)(seg * TP + slot * 16);
  };
  const unsigned v0 = mkoff(wave, 576), v1 = mkoff(p1, 576), v2 = mkoff(p2, 576);
  auto issue = [&](int c) {
    unsigned dst = lbase + (c & 3) * AT_SLOT;
    const char* src = T + (size_t)c * (32 * TP);
    glds16s(src, v0, dst + wave * 1024);
    glds16s(src, v1, dst + p1 * 1024);
    if ((c & 1) == grp) glds16s(src, v2, dst + p2 * 1024);
  };
  STAMP(1, 1);
  unsigned gbits;
  gld4(gbits, G + (qt * 256 + wave * 32) * 4, r * 4);
  {
    const char* src = T + (size_t)qt * (32 * TP);
    glds16s(src, mkoff(wave, 0), lbase + 3 * AT_SLOT + wave * 1024);
    if (wave < 2) glds16s(src, mkoff(p1, 0), lbase + 3 * AT_SLOT + p1 * 1024);
  }
  issue(0);
  issue(1);
  issue(2);
  const int q = (lane >> 2) & 3, p = lane & 3, g = (lane >> 4) & 1;
  const lds_p L = (lds_p)lds;
  const lds_p kb = L + g * TSTEP + (p & 1) * (TSTEP / 2) + (8 * h + q) * 16 + (p >> 1) * 8;
  const lds_p ke = L + g * TSTEP + (p & 1) * (TSTEP / 2) + (16 + q) * 16 + (p >> 1) * 8;
  const lds_p vb = L + 10240 + h * (TSTEP / 2) + (r < 19 ? r : 19) * 16;
  lds_p kb2 = kb + 2 * AT_SLOT, ke2 = ke + 2 * AT_SLOT, vb2 = vb + 2 * AT_SLOT;
  asm volatile("" : "+v"(kb2), "+v"(ke2), "+v"(vb2));
  asm volatile("s_waitcnt vmcnt(5)" : "+v"(gbits)::"memory");
  BARRIER();
  STAMP(1, 2);
  bf16x8 qf;
  s16x4 qe;
  {
    KFrag qq = load_k(kb2 + AT_SLOT + wave * 1280, ke2 + AT_SLOT + wave * 1280);
    qf = qq.f;
    qe[0] = h ? (short)0 : qq.e[0];
    qe[1] = h ? (short)0 : qq.e[1];
    qe[2] = 0;
    qe[3] = 0;
  }

  const float gate = __uint_as_float(gbits);
  f32x16 zero;
#pragma unroll
  for (int i = 0; i < 16; ++i) zero[i] = 0.f;
  f32x16 o = zero;
  f32x16 sA = qk_tile(kb, ke, qf, qe, zero);
  {
    const float nx = -rowmax16(sA);
    const unsigned mb = (__float_as_uint(nx) + (nx > 0.f ? 0xffffu : 0u)) >> 16;
    const float nm = __uint_as_float(mb << 16);
#pragma unroll
    for (int i = 0; i < 16; ++i) sA[i] += nm;
    qe[2] = h ? (short)0 : (short)mb;
  }
  KFrag kA, kB;
  VFrag vA, vB;
  kB = load_k(kb + 1280, ke + 1280);
  vA = load_v(vb);
  f32x16 sB;
#pragma unroll 1
  for (int cc = 0; cc < 8; cc += 4) {
    static_for<0, 32>([&](auto Tc) {
      constexpr int tt = decltype(Tc)::v, ci = tt >> 3;
      if constexpr ((tt & 7) == 6) {
        if (cc == 0 || ci < 2) {
          if (grp == 0) wait_vm<at_cnt(ci + 2, 0)>(); else wait_vm<at_cnt(ci + 2, 1)>();
        } else if (ci == 2) wait_vm<0>();
        if (cc == 0 || ci < 3) BARRIER();
        if constexpr (ci == TOUCH_AT) {
          if (cc == 0) {
            const int l2 = fresh_lane();
            const char* src = W2 + (size_t)((wg & 7) >> 1) * (1152 * 1152) + (wg >> 3) * 41472;
            const unsigned dst = lbase + ((ci + 3) & 3) * AT_SLOT + wave * 1024;
            touch(src, (wave * 41 + l2 - (l2 > 40 ? 24 : 0) - (wave >> 2) * 4) * 128u, dst);
            if (wave == 0) touch(Wo + ((wg & 7) >> 1) * (144 * 1152) + (wg >> 3) * 5184, (l2 > 39 ? ((wg & 8) ? 39 : 40) : l2) * 128u + ((wg & 8) ? 64u : 0u), dst);
          }
        }
        if (cc == 0 || ci == 0) issue(cc + ci + 3);
      }
      constexpr int t1 = (tt + 1) & 31, t2 = (tt + 2) & 31;
      const lds_p kp = (t2 < 16 ? kb : kb2) + at_off(t2);
      const lds_p ep = (t2 < 16 ? ke : ke2) + at_off(t2);
      const lds_p vp = (t1 < 16 ? vb : vb2) + at_off(t1);
      if constexpr ((tt & 1) == 0) {
        kA = load_k(kp, ep);
        vB = load_v(vp);
        SB();
        tile_step<APMASK & 15>(sA, sB, kB, vA, qf, qe, o);
      } else {
        kB = load_k(kp, ep);
        vA = load_v(vp);
        SB();
        tile_step<(APMASK >> 4) & 15>(sB, sA, kA, vB, qf, qe, o);
      }
    });
    if (cc == 0) STAMP(1, 3);
  }
  STAMP(1, 4);
  STAMP(1, 5);
  float l = xor32(o[10]);
  l = h ? l : o[10];
  bool bad = !(l < 1.0e30f);
#pragma unroll
  for (int i = 0; i < 10; ++i) bad = bad || !(__builtin_fabsf(o[i]) < 1.0e37f);
  const int ob = (qt * 8 + wave) * 32 + head;
  auto publish = [&]() {
    const float inv = gate / l;
    u32x4 ov;
#pragma unroll
    for (int i = 0; i < 4; ++i) ov[i] = pk2(o[2 * i] * inv, o[2 * i + 1] * inv);
    st16(O + (size_t)ob * 1024 + lane * 16, ov);
    if (h == 0) st4(O + OEX + (ob >> 3) * 1024 + ((head >> 2) & 1) * 512 + r * 16 + (head & 3) * 4, pk2(o[8] * inv, o[9] * inv));
    STAMP(1, 6);
    wait_vm<0>();
    if (lane == 0) st4(FL + FL_O + ob * 4, 1u);
  };
  const bool mybad = __any(bad);
  if (!mybad) publish();
  {
    int* fl = (int*)(lds + AT_SLOT);
    if (lane == 0) fl[wave] = mybad ? 1 : 0;
    __syncthreads();
    int v = fl[lane & 7];
    bad = __any(v != 0);
  }
  if (bad) {
    float m = -3.0e38f;
    o = zero;
    qe[2] = 0;
#pragma unroll 1
    for (int c = 0; c < 8; ++c) {
      __syncthreads();
      {
        const char* src = T + (size_t)c * (32 * TP);
        glds16s(src, v0, lbase + wave * 1024);
        glds16s(src, v1, lbase + p1 * 1024);
        if (grp == 0) glds16s(src, v2, lbase + p2 * 1024);
      }
      wait_vm<0>();
      __syncthreads();
#pragma unroll 1
      for (int t = 0; t < 8; ++t) {
        f32x16 s = qk_tile(kb + t * 1280, ke + t * 1280, qf, qe, zero);
        float mn = __builtin_fmaxf(m, rowmax16(s));
        float alpha = __builtin_amdgcn_exp2f(m - mn);
        m = mn;
        f32x16 pe;
#pragma unroll
        for (int i = 0; i < 16; ++i) { pe[i] = __builtin_amdgcn_exp2f(s[i] - mn); o[i] *= alpha; }
        o = pv_tile(vb + t * 1280, pe, o);
      }
    }
    l = xor32(o[10]);
    l = h ? l : o[10];
    if (mybad) publish();
  }
  STAMP(1, 7);
}

#define QKV_PRIO 2
#define ATT_PRIO 0
#define MLP_PRIO 1
#define PF2_AT 20
constexpr int M_IT = 36;
constexpr int M2_NT = 41;
struct M2S { int slot[M2_NT]; int it[M2_NT]; };
constexpr M2S m2_sched(int U) {
  M2S r{};
  const int E = 2 * U - 2;
  int next = 0;
  for (; next < 4; ++next) { r.slot[next] = next; r.it[next] = -1; }
  for (int j = 1; next < M2_NT; ++j) {
    r.slot[next] = r.slot[j - 1];
    r.it[next] = j;
    ++next;
    if (next < M2_NT && (j == E || j == E + 1)) { r.slot[next] = 4 + (j - E); r.it[next] = j; ++next; }
  }
  return r;
}
constexpr int m2_ck1(int U) { return 2 * U - 4; }
constexpr int m2_ck2(int U) { return (U == 5 ? 26 : 2 * U + 8) - 4; }
constexpr int m2_wait(int U, int kh, int j, int mode) {
  const M2S sc = m2_sched(U);
  const int U2 = U == 5 ? 2 : 3;
  int n = 0, last = -1;
  for (int i = -1; i < j + mode; ++i) {
    if (i == m2_ck1(U) - 2 || i == m2_ck2(U) - 2) { if (mode == 1 && i == j - 2) last = n; ++n; }
    for (int s = 0; s < M2_NT; ++s)
      if (sc.it[s] == i) {
        const int c = (U2 == 3 && (s & 1) == kh) ? 2 : 1;
        n += c;
        if (mode == 0 && s <= j + 1) last = n - 1;
      }
    if (i == -1) n += 2 * U;
    if (i >= 0 && i < j && i + 4 >= 2 * U && i + 4 < M_IT) { if (mode == 0 && i + 4 <= j) last = n; ++n; }
  }
  return n - 1 - last;
}
constexpr bool m2_need(int U, int j) { return j >= 3; }

template <int N>
DI void pin_acc(f32x16* a) {
  if constexpr (N == 5)
    asm volatile("" : "+{v[0:15]}"(a[0]), "+{v[16:31]}"(a[1]), "+{v[32:47]}"(a[2]), "+{v[48:63]}"(a[3]), "+{v[64:79]}"(a[4]));
  else
    asm volatile("" : "+{v[0:15]}"(a[0]), "+{v[16:31]}"(a[1]), "+{v[32:47]}"(a[2]), "+{v[48:63]}"(a[3]));
}
#define XLD(R) asm volatile("global_load_dwordx4 %0, %1, %2 sc1" : "={" R "}"(d) : "v"(voff), "s"(sbase) : "memory")
#define XLP(R) asm volatile("global_load_dwordx4 %0, %1, %2" : "={" R "}"(d) : "v"(voff), "s"(sbase) : "memory")
template <int I>
DI void xloadp(bf16x8& d, const char* sbase, unsigned voff) {
  if constexpr (I == 0) XLP("v[80:83]");
  else if constexpr (I == 1) XLP("v[84:87]");
  else if constexpr (I == 2) XLP("v[88:91]");
  else XLP("v[92:95]");
}
template <int I>
DI void xload(bf16x8& d, const char* sbase, unsigned voff) {
  if constexpr (I == 0) XLD("v[80:83]");
  else if constexpr (I == 1) XLD("v[84:87]");
  else if constexpr (I == 2) XLD("v[88:91]");
  else XLD("v[92:95]");
}
template <int I>
DI void pload(bf16x8& d, const char* sbase, unsigned voff) {
  if constexpr (I == 0) XLD("v[48:51]");
  else if constexpr (I == 1) XLD("v[52:55]");
  else if constexpr (I == 2) XLD("v[56:59]");
  else if constexpr (I == 3) XLD("v[60:63]");
  else if constexpr (I == 4) XLD("v[64:67]");
  else if constexpr (I == 5) XLD("v[68:71]");
  else if constexpr (I == 6) XLD("v[72:75]");
  else if constexpr (I == 7) XLD("v[76:79]");
  else if constexpr (I == 8) XLD("v[80:83]");
  else if constexpr (I == 9) XLD("v[84:87]");
  else if constexpr (I == 10) XLD("v[88:91]");
  else if constexpr (I == 11) XLD("v[92:95]");
  else if constexpr (I == 12) XLD("v[96:99]");
  else if constexpr (I == 13) XLD("v[100:103]");
  else if constexpr (I == 14) XLD("v[104:107]");
  else if constexpr (I == 15) XLD("v[108:111]");
  else if constexpr (I == 16) XLD("v[112:115]");
  else if constexpr (I == 17) XLD("v[116:119]");
}
template <int I>
DI void ppin(bf16x8& d) {
  if constexpr (I == 0) asm volatile("" : "+{v[48:51]}"(d));
  else if constexpr (I == 1) asm volatile("" : "+{v[52:55]}"(d));
  else if constexpr (I == 2) asm volatile("" : "+{v[56:59]}"(d));
  else if constexpr (I == 3) asm volatile("" : "+{v[60:63]}"(d));
  else if constexpr (I == 4) asm volatile("" : "+{v[64:67]}"(d));
  else if constexpr (I == 5) asm volatile("" : "+{v[68:71]}"(d));
  else if constexpr (I == 6) asm volatile("" : "+{v[72:75]}"(d));
  else if constexpr (I == 7) asm volatile("" : "+{v[76:79]}"(d));
  else if constexpr (I == 8) asm volatile("" : "+{v[80:83]}"(d));
  else if constexpr (I == 9) asm volatile("" : "+{v[84:87]}"(d));
  else if constexpr (I == 10) asm volatile("" : "+{v[88:91]}"(d));
  else if constexpr (I == 11) asm volatile("" : "+{v[92:95]}"(d));
  else if constexpr (I == 12) asm volatile("" : "+{v[96:99]}"(d));
  else if constexpr (I == 13) asm volatile("" : "+{v[100:103]}"(d));
  else if constexpr (I == 14) asm volatile("" : "+{v[104:107]}"(d));
  else if constexpr (I == 15) asm volatile("" : "+{v[108:111]}"(d));
  else if constexpr (I == 16) asm volatile("" : "+{v[112:115]}"(d));
  else if constexpr (I == 17) asm volatile("" : "+{v[116:119]}"(d));
}
#define XDS(R) asm volatile("ds_read_b128 %0, %1 offset:%2" : "={" R "}"(d) : "v"(a), "i"(OFF) : "memory")
template <int I, int OFF>
DI void xstash(bf16x8& d, unsigned a) {
  if constexpr (I == 0) XDS("v[80:83]");
  else if constexpr (I == 1) XDS("v[84:87]");
  else if constexpr (I == 2) XDS("v[88:91]");
  else XDS("v[92:95]");
}
template <int I>
DI void pin_x1(bf16x8& x) {
  if constexpr (I == 0) asm volatile("" : "+{v[80:83]}"(x));
  else if constexpr (I == 1) asm volatile("" : "+{v[84:87]}"(x));
  else if constexpr (I == 2) asm volatile("" : "+{v[88:91]}"(x));
  else asm volatile("" : "+{v[92:95]}"(x));
}
template <int I>
DI void pin_w(bf16x8& w) {
  if constexpr (I == 0) asm volatile("" : "+{v[96:99]}"(w));
  else if constexpr (I == 1) asm volatile("" : "+{v[100:103]}"(w));
  else if constexpr (I == 2) asm volatile("" : "+{v[104:107]}"(w));
  else if constexpr (I == 3) asm volatile("" : "+{v[108:111]}"(w));
  else asm volatile("" : "+{v[112:115]}"(w));
}
template <int U>
DI void mlp_track(char* lds, const char* __restrict__ XN, const char* __restrict__ W1,
                  const unsigned short* __restrict__ b1, char* H, const char* __restrict__ W2,
                  const unsigned short* __restrict__ b2, const char* __restrict__ Wo, const char* O,
                  const char* __restrict__ G, char* P, char* FL, char* __restrict__ out, int rt, int sp, int j4,
                  int koff, int wave, int lane) {
  constexpr int NBW = 9 - U, U2 = U == 5 ? 2 : 3;
  constexpr int NS1 = U == 5 ? 5 : 6;
  SSD();
  SST(0);
  SS8(0);
  const bool up = j4 < 2;
  const int unit0 = sp * 18 + (U == 5 ? (up ? 0 : 13) : (up ? 5 : 9));
  const unsigned lbase = lds_addr(lds);
  const lds_p L = (lds_p)lds;
  const int wm = wave & 3, wn = wave >> 2;
  char* FLg = FL + (size_t)(rt * 4 + sp) * 512;
  f32x16 acc[5];
  bf16x8 w[5];
  float iog;
  {
    constexpr int WS = 2 * U * 1024, XB = NS1 * WS, NP = 4 + 2 * U;
    const int h = lane >> 5;
    const unsigned voffW = (unsigned)((lane >> 3) * 9216 + (((lane & 7) ^ (((lane >> 4) & 1) << 2)) << 4));
    const unsigned voff0 = wave < 4 ? (unsigned)(lane * 16) : voffW;
    const int q0 = wave - 4, q1 = wave + 4;
    const char* wb = W1 + unit0 * 128;
    const char* src0 =
        wave < 4 ? XN + (size_t)(rt * 4 + wave) * (36 * 1024) : wb + (q0 / U) * (8 * 9216) + (q0 % U) * 128;
    const char* src1 = wb + (q1 / U) * (8 * 9216) + (q1 % U) * 128;
    const unsigned st0 = wave < 4 ? 1024u : 147456u;
    const bool two = wave + 8 < NP;
    const unsigned dst0 = lbase + (wave < 4 ? XB + wave * 1024 : q0 * 1024), ds0 = wave < 4 ? 4096u : (unsigned)WS;
    auto issue = [&](int s, int e) {
      int ks = s + koff;
      ks = ks >= 36 ? ks - 36 : ks;
      if (e == 0) glds16s(src0 + (unsigned)ks * st0, voff0, dst0 + (s % NS1) * ds0);
      else if (two) glds16s(src1 + (unsigned)ks * 147456u, voffW, lbase + (s % NS1) * WS + q1 * 1024);
    };
    auto waitn = [&](auto Nc) {
      constexpr int N = decltype(Nc)::v;
      if (two) wait_vm<2 * N>(); else wait_vm<N>();
    };
    unsigned bb[U];
#pragma unroll
    for (int b = 0; b < U; ++b)
      asm volatile("global_load_ushort %0, %1, %2" : "=v"(bb[b]) : "v"((lane & 31) * 2), "s"(b1 + (unit0 * 2 + wn * U + b) * 32) : "memory");
    asm volatile("global_load_dword %0, %1, %2" : "=v"(iog) : "v"((lane & 31) * 4), "s"(G + (2048 + (rt * 4 + wm) * 32) * 4) : "memory");
#pragma unroll
    for (int s = 0; s < NS1; ++s) { issue(s, 0); issue(s, 1); }
    const int q = (lane >> 2) & 3, p = lane & 3, g = (lane >> 4) & 1;
    lds_p xa = L + XB + wm * 1024 + lane * 16;
    lds_p wa[2];
#pragma unroll
    for (int e = 0; e < 2; ++e) {
      int b = wn * U + e;
      wa[e] = L + (h * U + (b >> 1)) * 1024 + q * 128 + (((b & 1) ^ (q >> 1)) << 6) + g * 32 + p * 8;
    }
    asm volatile("" : "+v"(xa), "+v"(wa[0]), "+v"(wa[1]));
    waitn(IC<NS1 - 1>{});
    {
      f32x16 zero;
#pragma unroll
      for (int i = 0; i < 16; ++i) zero[i] = 0.f;
      asm volatile("" : "+v"(iog));
      const u32x4 xx = {h ? 0u : 0x3F80u, 0u, 0u, 0u};
#pragma unroll
      for (int b = 0; b < U; ++b) {
        asm volatile("" : "+v"(bb[b]));
        const u32x4 wx = {bb[b], 0u, 0u, 0u};
        acc[b] = MFMA32(__builtin_bit_cast(bf16x8, wx), __builtin_bit_cast(bf16x8, xx), zero);
      }
    }
    BARRIER();
    __builtin_amdgcn_s_setprio(MLP_PRIO);
    STAMP(4, 1);
    auto rdx = [&](int sl) { return ld8(xa + sl * 4096); };
    auto rdw = [&](int sl, int m) {
      const lds_p a = wa[m & 1] + sl * WS + (m >> 1) * 1024;
      return cat8(trl(a), trl(a + 512));
    };
    bf16x8 x0, x1;
    x0 = rdx(0);
#pragma unroll
    for (int m = 0; m < U; ++m) w[m] = rdw(0, m);
    unsigned pfd;
    static_for<0, 36>([&](auto Jc) {
      constexpr int j = decltype(Jc)::v;
      if constexpr (j + 1 < 36) {
        constexpr int top = j == 0 ? NS1 - 1 : (j + NS1 - 2 < 35 ? j + NS1 - 2 : 35);
        waitn(IC<top - (j + 1)>{});
        BARRIER();
      }
      STAMP2(j);
      if (j == 12) STAMP(4, 2);
      if (j == 24) STAMP(4, 3);
      SB();
      bf16x8& xc = (j & 1) ? x1 : x0;
      bf16x8& xn = (j & 1) ? x0 : x1;
      static_for<0, U>([&](auto Mc) {
        constexpr int m = decltype(Mc)::v;
        acc[m] = MFMA32(w[m], xc, acc[m]);
        SB();
        if constexpr (j + 1 < 36) {
          if constexpr (m == 0) xn = rdx((j + 1) % NS1);
          w[m] = rdw((j + 1) % NS1, m);
        }
        if constexpr (j >= 1 && j + NS1 - 1 < 36 && (m == 1 || m == 2)) issue(j + NS1 - 1, m - 1);
        if constexpr (j == PF2_AT && m == 3) {
          const unsigned l2 = fresh_lane(), li = l2 < 18 * U2 ? l2 : l2 - 32, row = li / U2;
          const unsigned vo = row * 1152u + (li - row * U2) * 128u;
          const int u2 = U == 5 ? (up ? 0 : 7) : 2 * j4;
          gld4(pfd, W2 + (size_t)(sp * 1152 + (rt & 7) * 144 + wave * 18) * 1152 + u2 * 128, vo);
          if (wave == 7) gld4k(pfd, Wo + (sp * 144 + (rt & 7) * 18) * 1152 + u2 * 128, vo);
        }
        if constexpr (j == PF2_AT + NS1 + 1 && m == 0) asm volatile("" ::"v"(pfd));
        SB();
      });
    });
    STAMP2(36);
    STAMP(4, 4);
    asm volatile("" : "+v"(acc[0]), "+v"(acc[U - 1]), "+v"(iog));
  }
  SS8(1);
  BARRIER();
  SS8(2);
  constexpr int B1 = 2 * U, B2 = B1 + 8, B3 = 26;
  constexpr M2S SC = m2_sched(U);
  const int e0 = 2 * (U == 5 ? (up ? 0 : 26) : (up ? 10 : 18));
  const int e1 = 2 * (U == 5 ? (up ? 10 : 18) : (up ? 18 : 10)) - B1;
  const int e2 = 2 * (U == 5 ? (up ? 18 : 10) : (up ? 26 : 0)) - B2;
  const int e3 = 2 * (U == 5 ? (up ? 26 : 0) : (up ? 0 : 26)) - B3;
  auto kstep = [&](int s, int half) {
    const int uc2 = s < B1 ? 2 * U : (s < B2 ? 8 : (s < B3 ? (U == 5 ? 8 : 10) : 10));
    return (unsigned)(s + (s < B1 ? e0 : (s < B2 ? e1 : (s < B3 ? e2 : e3))) + half * uc2);
  };
  const int kh = wn;
  const int unit2 = U == 5 ? (up ? 0 : 7) : 2 * j4, boff = U == 5 ? 0 : j4 - 1;
  const int ln = fresh_lane();
  const unsigned voffX = ln * 16;
  const unsigned voffW = (unsigned)((ln >> 3) * 1152 + (((ln & 7) ^ (((ln >> 4) & 1) << 2)) << 4));
  const char* xsrc = H + ((size_t)(rt * 4 + wm) * 288 + sp * 72) * 1024;
  const int p0 = wave, p2 = 8 + (wave & 3);
  const int kb0 = p0 / U2, kb2 = p2 / U2;
  const unsigned c0 = (kb0 & 1) * (8 * 1152) + (unit2 + p0 % U2) * 128, c2 = (kb2 & 1) * (8 * 1152) + (unit2 + p2 % U2) * 128;
  const char* w2s = W2 + (size_t)sp * 1152 * 1152;
  const char* wos = Wo + sp * (8 * 18 * 1152);
  constexpr int SL2 = U2 * 4096;
  auto issueW = [&](auto Sc, int e) {
    constexpr int s = decltype(Sc)::v;
    const unsigned dst = lbase + SC.slot[s] * SL2 + (e ? p2 : p0) * 1024;
    if (e == 1 && !(U2 == 3 && (s & 1) == kh)) return;
    const int half = e ? 1 : kb0 >> 1;
    const unsigned c = e ? c2 : c0;
    if constexpr (s < M_IT) glds16s(w2s + (c + kstep(s, half) * (16u * 1152u)), voffW, dst);
    else if constexpr (s < M2_NT - 1) glds16s(wos + (c + (unsigned)(4 * half + s - M_IT) * (18u * 1152u)), voffW, dst);
    else {
      const int l2 = fresh_lane(), row = l2 >> 3;
      const unsigned ve = (unsigned)(((4 * (row >> 2) + ((row & 3) >> 1)) * 18 + (row & 1)) * 1152 + (((l2 & 7) ^ (((l2 >> 4) & 1) << 2)) << 4));
      const int kbl = (e ? kb2 : kb0) & 1;
      glds16s(wos + (c + (unsigned)(kbl * (36 - 8) + 16) * 1152u), ve, dst);
    }
  };
  unsigned b2v[NBW];
  const bool addb = sp == 0 && kh == 0;
#pragma unroll
  for (int b = 0; b < NBW; ++b)
    asm volatile("global_load_ushort %0, %1, %2" : "=v"(b2v[b]) : "v"((ln & 31) * 2), "s"(b2 + (unit2 * 2 + boff + b) * 32) : "memory");
  static_for<0, 4>([&](auto Sc) { issueW(Sc, 0); issueW(Sc, 1); });
  SS8(3);
  bf16x8 xr[4];
  const unsigned stash = lbase + 4 * SL2 + wave * ((2 * U - 4) * 1024) + voffX;
  {
    const float C0 = -2.0f * 1.4426950408889634f * 0.7978845608028654f;
    const float C1 = C0 * 0.044715f;
    f32x2 c0 = {C0, C0}, c1 = {C1, C1};
    asm volatile("" : "+s"(c0), "+s"(c1));
    f32x2 ig = {iog, iog};
    asm volatile("" : "+v"(ig));
    char* hb = H + ((size_t)(rt * 4 + wm) * 288 + (unit0 * 2 + wn * U) * 2) * 1024 + fresh_lane() * 16;
#pragma unroll
    for (int b = 0; b < U; ++b) {
#pragma unroll
      for (int gp = 0; gp < 2; ++gp) {
        u32x4 o4;
#pragma unroll
        for (int i = 0; i < 4; ++i) {
          const f32x2 v = {acc[b][8 * gp + 2 * i], acc[b][8 * gp + 2 * i + 1]};
          const f32x2 t = v * __builtin_elementwise_fma(v * v, c1, c0);
          const f32x2 e = {__builtin_amdgcn_exp2f(t[0]), __builtin_amdgcn_exp2f(t[1])};
          const f32x2 d = __builtin_elementwise_fma(e, ig, ig);
          const f32x2 rc = {__builtin_amdgcn_rcpf(d[0]), __builtin_amdgcn_rcpf(d[1])};
          const f32x2 o = v * rc;
          o4[i] = pk2(o[0], o[1]);
        }
        st16(hb + (b * 2 + gp) * 1024, o4);
        if (b < 2) xr[b * 2 + gp] = __builtin_bit_cast(bf16x8, o4);
        else *(__attribute__((address_space(3))) u32x4*)(uintptr_t)(stash + (b * 2 + gp - 4) * 1024) = o4;
      }
      if (b == 1) SS8(4);
    }
  }
  STAMP(4, 5);
  SS8(5);
  lds_p wa[2];
  {
    const int h = ln >> 5, q = (ln >> 2) & 3, p = ln & 3, g = (ln >> 4) & 1;
#pragma unroll
    for (int e = 0; e < 2; ++e) {
      int b = boff + e;
      wa[e] = L + (2 * kh * U2 + (b >> 1)) * 1024 + (4 * h + q) * 128 + (((b & 1) ^ (q >> 1)) << 6) + g * 32 + p * 8;
    }
  }
  asm volatile("" : "+v"(wa[0]), "+v"(wa[1]));
  wait_vm<2 * U>();
  SS8(6);
  {
    f32x16 zero;
#pragma unroll
    for (int i = 0; i < 16; ++i) zero[i] = 0.f;
    const float og = __builtin_amdgcn_rcpf(iog);
    const float ogh = __uint_as_float(__float_as_uint(og) & 0xffff0000u);
    const u32x4 xx = {(ln >> 5) || !addb ? 0u : pk2(ogh, og - ogh), 0u, 0u, 0u};
#pragma unroll
    for (int b = 0; b < NBW; ++b) {
      asm volatile("" : "+v"(b2v[b]));
      const u32x4 wx = {b2v[b] * 0x10001u, 0u, 0u, 0u};
      acc[b] = MFMA32(__builtin_bit_cast(bf16x8, wx), __builtin_bit_cast(bf16x8, xx), zero);
    }
  }
  BARRIER();
  STAMP(4, 6);
  STAMP(4, 7);
  STAMP(2, 1);
  SST(1);
  SS8(7);
  auto rdw = [&](int s, int m) {
    const lds_p a = wa[m & 1] + SC.slot[s] * SL2 + (m >> 1) * 1024;
    return cat8(trl(a), trl(a + U2 * 1024));
  };
#pragma unroll
  for (int m = 0; m < NBW; ++m) w[m] = rdw(0, m);
  unsigned fv = 0;
  auto poll = [&]() {
    unsigned vo = voffX;
    asm volatile("" : "+v"(vo));
    vo = (vo & 48u) << 3;
    gld4c(fv, FLg, vo);
  };
  auto check = [&](unsigned long long mask) {
    mask &= ~(1ull << j4);
    asm volatile("" : "+v"(fv));
    if (__ballot(fv != 1u) & mask) {
#pragma unroll 1
      for (int it = 0; it < (1 << 17); ++it) {
        __builtin_amdgcn_s_sleep(4);
        poll();
        wait_vm<0>();
        asm volatile("" : "+v"(fv));
        if (!(__ballot(fv != 1u) & mask)) break;
      }
    }
  };
  auto waitk = [&](auto N0, auto N1) {
    constexpr int n0 = decltype(N0)::v, n1 = decltype(N1)::v;
    if constexpr (n0 == n1) wait_vm<n0>();
    else { if (kh == 0) wait_vm<n0>(); else wait_vm<n1>(); }
  };
  constexpr int CK1 = m2_ck1(U), CK2 = m2_ck2(U);
  static_for<0, M_IT>([&](auto Jc) {
    constexpr int j = decltype(Jc)::v;
    if constexpr (m2_need(U, j)) waitk(IC<m2_wait(U, 0, j, 0)>{}, IC<m2_wait(U, 1, j, 0)>{});
    if constexpr (j > 0) BARRIER();
    if constexpr (j > 0) pin_acc<NBW>(acc);
    static_for<0, 4>([&](auto Ic) {
      constexpr int i = decltype(Ic)::v;
      if constexpr (j + ((i - j) & 3) < M_IT) pin_x1<i>(xr[i]);
    });
    if constexpr (j == 3) {
      if (wave == 0 && voffX == 0) st4(FLg + j4 * 128, 1u);
    }
    STAMP2(40 + j);
    if (j == 9) STAMP(2, 2);
    if (j == 18) STAMP(2, 3);
    if (j == 18) SST(2);
    if (j == 27) STAMP(2, 4);
    if constexpr (j == CK1 - 2 || j == CK2 - 2) poll();
    SB();
    static_for<0, NBW>([&](auto Mc) {
      constexpr int m = decltype(Mc)::v;
      pin_w<m>(w[m]);
      acc[m] = MFMA32(w[m], xr[j & 3], acc[m]);
      SB();
      w[m] = rdw(j + 1, m);
      static_for<j + 3, (j + 7 < M2_NT ? j + 7 : M2_NT)>([&](auto Sc) {
        constexpr int s = decltype(Sc)::v;
        if constexpr (SC.it[s] == j) {
          constexpr bool second = s > 0 && SC.it[s - 1] == j;
          if constexpr (m == (second ? 2 : 0)) issueW(Sc, 0);
          if constexpr (m == (second ? 3 : 1)) issueW(Sc, 1);
        }
      });
      SB();
    });
    if constexpr (j == CK1 || j == CK2) {
      waitk(IC<m2_wait(U, 0, j, 1)>{}, IC<m2_wait(U, 1, j, 1)>{});
      check(j == CK1 ? 6ull : 15ull);
    }
    if constexpr (j + 4 < B1) xstash<(j & 3), j * 1024>(xr[j & 3], stash);
    else if constexpr (j + 4 < M_IT) xload<(j & 3)>(xr[j & 3], xsrc + kstep(j + 4, kh) * 1024u, voffX);
    SB();
  });
  STAMP2(76);
  STAMP(2, 5);
  SST(3);
  wait_vm<0>();
  pin_acc<NBW>(acc);
  static_for<0, NBW>([&](auto Mc) { pin_w<decltype(Mc)::v>(w[decltype(Mc)::v]); });
  if (wave == 0) {
    const int l2 = fresh_lane();
    spin_flags(FL + FL_O + rt * 512 + sp * 32, (unsigned)(((l2 >> 3) & 3) * 128 + (l2 & 7) * 4));
  }
  BARRIER();
  SST(4);
  {
    bf16x8 xe;
    const char* osrc = O + (size_t)((rt * 4 + wm) * 32 + sp * 8 + kh * 4) * 1024;
    static_for<0, 4>([&](auto Ic) { xload<decltype(Ic)::v>(xr[decltype(Ic)::v], osrc + decltype(Ic)::v * 1024, voffX); });
    gld16c(xe, O + OEX + ((rt * 4 + wm) * 4 + sp) * 1024, voffX);
    static_for<0, 4>([&](auto Tc) {
      constexpr int t = decltype(Tc)::v;
      wait_vm<4 - t>();
      pin_x1<t>(xr[t]);
      pin_acc<NBW>(acc);
      static_for<0, NBW>([&](auto Mc) {
        constexpr int m = decltype(Mc)::v;
        pin_w<m>(w[m]);
        acc[m] = MFMA32(w[m], xr[t], acc[m]);
        SB();
        w[m] = rdw(M_IT + t + 1, m);
        SB();
      });
    });
    wait_vm1<0>(xe);
    pin_acc<NBW>(acc);
    static_for<0, NBW>([&](auto Mc) { pin_w<decltype(Mc)::v>(w[decltype(Mc)::v]); });
    if (kh == 0) {
#pragma unroll
      for (int m = 0; m < NBW; ++m) acc[m] = MFMA32(w[m], xe, acc[m]);
    }
    pin_acc<NBW>(acc);
  }
  BARRIER();
  STAMP(2, 6);
  SST(5);
  constexpr int NA = (NBW + 1) / 2;
  const int l2 = fresh_lane();
  char* ex = lds + wm * (NBW * 4096) + l2 * 16;
  if (kh == 0) {
#pragma unroll
    for (int b = NA; b < NBW; ++b)
#pragma unroll
      for (int c = 0; c < 4; ++c) {
        f32x4 v = {acc[b][4 * c], acc[b][4 * c + 1], acc[b][4 * c + 2], acc[b][4 * c + 3]};
        *(f32x4*)(ex + b * 4096 + c * 1024) = v;
      }
  } else {
#pragma unroll
    for (int b = 0; b < NA; ++b)
#pragma unroll
      for (int c = 0; c < 4; ++c) {
        f32x4 v = {acc[b][4 * c], acc[b][4 * c + 1], acc[b][4 * c + 2], acc[b][4 * c + 3]};
        *(f32x4*)(ex + b * 4096 + c * 1024) = v;
      }
    acc[0] = acc[NA];
    acc[1] = acc[NA + 1];
  }
  auto pin_r = [&]() {
    if constexpr (NA == 3) asm volatile("" : "+{v[0:15]}"(acc[0]), "+{v[16:31]}"(acc[1]), "+{v[32:47]}"(acc[2]));
    else asm volatile("" : "+{v[0:15]}"(acc[0]), "+{v[16:31]}"(acc[1]));
  };
  pin_r();
  __syncthreads();
  const int bb0 = kh ? NA : 0;
  const bool third = kh == 0;
  ex += bb0 * 4096;
#pragma unroll
  for (int b = 0; b < NA; ++b) {
    if (b == 2 && !third) break;
#pragma unroll
    for (int c = 0; c < 4; ++c) {
      f32x4 a = *(const f32x4*)(ex + b * 4096 + c * 1024);
#pragma unroll
      for (int i = 0; i < 4; ++i) acc[b][4 * c + i] += a[i];
    }
  }
  pin_r();
  const char* flp = FL + FL_P + (((rt * 4 + j4) * 4 + wm) * 2 + kh) * 16;
  const size_t pofs = ((size_t)(rt * 4 + wm) * 18 + unit2 * 2 + boff + bb0) * 2048;
  if (wm != sp) {
    char* pb = P + (size_t)sp * (64 * 18 * 2048) + pofs + l2 * 16;
#pragma unroll
    for (int b = 0; b < NA; ++b) {
      if (b == 2 && !third) break;
#pragma unroll
      for (int gp = 0; gp < 2; ++gp) {
        u32x4 v;
#pragma unroll
        for (int i = 0; i < 4; ++i) v[i] = pk2(acc[b][8 * gp + 2 * i], acc[b][8 * gp + 2 * i + 1]);
        st16(pb + b * 2048 + gp * 1024, v);
      }
    }
    wait_vm<0>();
    if (l2 == 0) st4((char*)flp + sp * 4, 1u);
  } else {
    spin_flags(flp, (unsigned)(((sp + 1 + (l2 % 3)) & 3) * 4));
    SST(6);
    bf16x8 pv[NA][3][2];
    static_for<0, NA * 6>([&](auto Ic) {
      constexpr int i = decltype(Ic)::v, b = i / 6, o = (i >> 1) % 3, gp = i & 1;
      const char* pb = P + (size_t)((sp + 1 + o) & 3) * (64 * 18 * 2048) + pofs + (b == 2 && !third ? 1 : b) * 2048 + gp * 1024;
      pload<i>(pv[b][o][gp], pb, l2 * 16);
    });
    char* orow = out + (size_t)((rt * 4 + wm) * 32 + (l2 & 31)) * 1152 + (unit2 * 2 + boff + bb0) * 64 + (l2 >> 5) * 16;
    static_for<0, NA>([&](auto Bc) {
      constexpr int b = decltype(Bc)::v;
      wait_vm<(NA - 1 - b) * 6 + 2 * b>();
      if (b < 2 || third) {
        static_for<0, 6>([&](auto Ic) {
          constexpr int i = decltype(Ic)::v, gp = i & 1;
          ppin<b * 6 + i>(pv[b][i >> 1][gp]);
          const u32x4 v = __builtin_bit_cast(u32x4, pv[b][i >> 1][gp]);
#pragma unroll
          for (int k = 0; k < 4; ++k) {
            acc[b][8 * gp + 2 * k] += __uint_as_float(v[k] << 16);
            acc[b][8 * gp + 2 * k + 1] += __uint_as_float(v[k] & 0xffff0000u);
          }
        });
        u32x2 o2[4];
#pragma unroll
        for (int gq = 0; gq < 4; ++gq) {
          o2[gq][0] = pk2(acc[b][4 * gq], acc[b][4 * gq + 1]);
          o2[gq][1] = pk2(acc[b][4 * gq + 2], acc[b][4 * gq + 3]);
        }
#pragma unroll
        for (int gq = 0; gq < 4; gq += 2) {
          auto r0 = __builtin_amdgcn_permlane32_swap(o2[gq][0], o2[gq + 1][0], false, false);
          auto r1 = __builtin_amdgcn_permlane32_swap(o2[gq][1], o2[gq + 1][1], false, false);
          u32x4 wv = {r0[0], r1[0], r0[1], r1[1]};
          st16p(orow + b * 64 + gq * 16, wv);
        }
      }
    });
  }
  STAMP(2, 7);
  SST(7);
  SSW();
}

extern "C" __global__ __launch_bounds__(512, 4) void kernel1(const char* __restrict__ T,
                                                   char* O, const char* __restrict__ XN,
                                                   const char* __restrict__ W1, const unsigned short* __restrict__ b1,
                                                   char* H, const char* __restrict__ W2,
                                                   const unsigned short* __restrict__ b2, const char* __restrict__ Wo,
                                                   const char* __restrict__ G, char* P, char* FL,
                                                   char* __restrict__ out) {
  __shared__ __attribute__((aligned(16))) char lds[81920];
  const int wave = __builtin_amdgcn_readfirstlane(threadIdx.x >> 6);
  const int lane = threadIdx.x & 63;
  const int bid = blockIdx.x;
  if (bid < 256) {
    STAMP(1, 0);
    STAMP(5, 0);
    __builtin_amdgcn_s_setprio(ATT_PRIO);
    attn_role(lds, T, O, G, FL, W2, Wo, bid, wave, fresh_lane());
  } else {
    STAMP(2, 0);
    STAMP(4, 0);
    const int wg = bid - 256;
    const int xcd = wg & 7, idx = wg >> 3;
    const int sp = xcd >> 1, j4 = idx & 3, r8 = idx >> 2, rt = (xcd & 1) * 8 + r8;
    const int koff = (r8 * 9 / 2 + j4 * 9) % 36;
    if (j4 == 0 || j4 == 3)
      mlp_track<5>(lds, XN, W1, b1, H, W2, b2, Wo, O, G, P, FL, out, rt, sp, j4, koff, wave, lane);
    else
      mlp_track<4>(lds, XN, W1, b1, H, W2, b2, Wo, O, G, P, FL, out, rt, sp, j4, koff, wave, lane);
  }
}

constexpr int Q_SLOT = 40960;
#define QGRID 248
#define PFW 0
#define PF1 0
DI void qkv_role(char* lds, const char* __restrict__ X, const char* __restrict__ Wqkv, const char* __restrict__ pf, int pfl,
                 char* __restrict__ T, int rt, int ct, int joff, int wave, int lane) {
  f32x16 acc[2];
#pragma unroll
  for (int i = 0; i < 16; ++i) { acc[0][i] = 0.f; acc[1][i] = 0.f; }
  const unsigned lbase = lds_addr(lds);
  unsigned voffX[2];
#pragma unroll
  for (int i = 0; i < 2; ++i) {
    int row = wave * 32 + 8 * i + (lane >> 3);
    int c = (lane & 7) ^ ((row >> 1) & 7);
    voffX[i] = (unsigned)((rt * 256 + row) * 1152 + c * 16);
  }
  const unsigned voffW = (unsigned)((lane >> 3) * 3456 + (((lane & 7) ^ (((lane >> 4) & 1) << 2)) << 4));
  const char* wsrc = Wqkv + (size_t)wave * 8 * 3456 + ct * 128;
  auto issue = [&](int J, int e) {
    unsigned dst = lbase + (J & 3) * Q_SLOT;
    unsigned Jr = J + joff;
    Jr = Jr >= 9 ? Jr - 9 : Jr;
    if (e == 0) glds16s(wsrc + Jr * (64u * 3456u), voffW, dst + 32768 + wave * 1024);
    else glds16s(X + Jr * 128u + ((e - 1) >> 1) * (16 * 1152), voffX[(e - 1) & 1], dst + (wave * 4 + e - 1) * 1024);
  };
  STAMP(0, 0);
#pragma unroll
  for (int J = 0; J < 3; ++J)
#pragma unroll
    for (int e = 0; e < 5; ++e) issue(J, e);
  const int r = lane & 31, h = lane >> 5;
  const int q = (lane >> 2) & 3, p = lane & 3, g = (lane >> 4) & 1;
  const lds_p L = (lds_p)lds;
  lds_p xa[4], wa[2][2];
#pragma unroll
  for (int ks = 0; ks < 4; ++ks) xa[ks] = L + (wave * 32 + r) * 128 + (((2 * ks + h) ^ ((r >> 1) & 7)) << 4);
#pragma unroll
  for (int b = 0; b < 2; ++b) {
    wa[0][b] = L + 32768 + h * 1024 + q * 128 + ((b ^ (q >> 1)) << 6) + g * 32 + p * 8;
    wa[1][b] = wa[0][b] + 2 * Q_SLOT;
  }
  lds_p xb[4];
#pragma unroll
  for (int ks = 0; ks < 4; ++ks) xb[ks] = xa[ks] + 2 * Q_SLOT;
  asm volatile("" : "+v"(xb[0]), "+v"(xb[1]), "+v"(xb[2]), "+v"(xb[3]), "+v"(wa[1][0]), "+v"(wa[1][1]));
  wait_vm<10>();
  BARRIER();
  STAMP(0, 1);
  bf16x8 fx[2], fw[2][2];
  auto rd = [&](int buf, int J, int ks) {
    const int sl = J & 3;
    fx[buf] = ld8((sl < 2 ? xa[ks] : xb[ks]) + (sl & 1) * Q_SLOT);
#pragma unroll
    for (int b = 0; b < 2; ++b) {
      const lds_p a = wa[sl >> 1][b] + (sl & 1) * Q_SLOT + ks * 2048;
      fw[buf][b] = cat8(trl(a), trl(a + 512));
    }
  };
  rd(0, 0, 0);
  unsigned pfd = 0;
  static_for<0, 9>([&](auto Jc) {
    constexpr int J = decltype(Jc)::v;
    if constexpr (J + 1 < 9) {
      wait_vm<((J + 2 < 9) ? 5 : 0) + (J >= 6 ? PF1 : 0)>();
      BARRIER();
    }
    if (J == 3) STAMP(0, 2);
    if (J == 6) STAMP(0, 3);
    SB();
    static_for<0, 4>([&](auto Kc) {
      constexpr int ks = decltype(Kc)::v, cur = ks & 1;
      acc[0] = MFMA32(fx[cur], fw[cur][0], acc[0]);
      SB();
      if constexpr (ks < 3) rd(cur ^ 1, J, ks + 1);
      else if constexpr (J + 1 < 9) rd(cur ^ 1, J + 1, 0);
      SB();
      acc[1] = MFMA32(fx[cur], fw[cur][1], acc[1]);
      SB();
      if constexpr (J + 3 < 9) {
        if constexpr (ks == 0) { issue(J + 3, 0); issue(J + 3, 1); }
        else issue(J + 3, ks + 1);
      }
      SB();
    });
  });
  STAMP(0, 5);
  const float QSCALE = 0.23570226039551584f * 1.4426950408889634f;
  const float sc = ct < 9 ? QSCALE : 1.0f;
#pragma unroll
  for (int nb = 0; nb < 2; ++nb) {
    char* base = T + (size_t)h * TP + (ct * 64 + nb * 32 + r) * 16;
#pragma unroll
    for (int s = 0; s < 2; ++s) {
      int step = rt * 16 + wave * 2 + s;
      u32x4 v;
#pragma unroll
      for (int i = 0; i < 4; ++i) v[i] = pk2(acc[nb][8 * s + 2 * i] * sc, acc[nb][8 * s + 2 * i + 1] * sc);
      st16(base + (size_t)step * (2 * TP), v);
    }
  }
  if (ct == 26 && wave < 4) {
    int idx = wave * 64 + lane;
    unsigned c = (idx & 7) == 0 ? 0x3F803F80u : 0u;
    u32x4 v = {c, c, c, c};
    st16(T + (size_t)(rt * 32 + (idx >> 3)) * TP + (1728 + (idx & 7)) * 16, v);
  }
  asm volatile("s_waitcnt vmcnt(0)" : "+v"(pfd)::"memory");
}

DI void ln_role(char* lds, const char* __restrict__ X, const char* __restrict__ gw, char* __restrict__ XN,
                float* __restrict__ G, char* __restrict__ FL, int id, int wave, int lane) {
  const int r = lane & 31, h = lane >> 5;
  const int rb = wave >> 2, kq = wave & 3;
  const int row = id * 64 + rb * 32 + r;
  u32x4 xv[9], gv[9];
#pragma unroll
  for (int i = 0; i < 9; ++i) xv[i] = *(const u32x4*)(X + (size_t)row * 1152 + ((kq * 9 + i) * 2 + h) * 16);
#pragma unroll
  for (int i = 0; i < 9; ++i) gv[i] = *(const u32x4*)(gw + ((kq * 9 + i) * 2 + h) * 16);
  if (wave < 2) {
    u32x4 z = {0u, 0u, 0u, 0u};
    st16(FL + id * 2048 + wave * 1024 + lane * 16, z);
  }
  float* red = (float*)lds;
  float s = 0.f, d = 0.f;
#pragma unroll
  for (int i = 0; i < 9; ++i)
#pragma unroll
    for (int j = 0; j < 4; ++j) {
      float a = __uint_as_float(xv[i][j] << 16), b = __uint_as_float(xv[i][j] & 0xffff0000u);
      s += a + b;
      d = __builtin_fmaf(a, __uint_as_float(gv[i][j] << 16), d);
      d = __builtin_fmaf(b, __uint_as_float(gv[i][j] & 0xffff0000u), d);
    }
  s += xor32(s);
  d += xor32(d);
  if (h == 0) { red[wave * 32 + r] = s; red[256 + wave * 32 + r] = d; }
  __syncthreads();
  const float* rs = red + rb * 128 + r;
  const float mean = (rs[0] + rs[32] + rs[64] + rs[96]) * (1.0f / 576.0f);
  if (kq == 0 && h == 0) {
    float v = rs[256] + rs[288] + rs[320] + rs[352];
    G[row] = 1.0f / (1.0f + __builtin_amdgcn_exp2f(-1.4426950408889634f * v));
    G[2048 + row] = __builtin_fminf(1.0f + __builtin_amdgcn_exp2f(1.4426950408889634f * v), 1.0e30f);
  }
  float q2 = 0.f;
#pragma unroll
  for (int i = 0; i < 9; ++i)
#pragma unroll
    for (int j = 0; j < 4; ++j) {
      float a = __uint_as_float(xv[i][j] << 16) - mean, b = __uint_as_float(xv[i][j] & 0xffff0000u) - mean;
      q2 = __builtin_fmaf(a, a, q2);
      q2 = __builtin_fmaf(b, b, q2);
    }
  q2 += xor32(q2);
  if (h == 0) red[512 + wave * 32 + r] = q2;
  __syncthreads();
  const float var = (rs[512] + rs[544] + rs[576] + rs[608]) * (1.0f / 576.0f);
  const float rstd = 1.0f / __builtin_sqrtf(var + 1e-5f);
  char* dst = XN + ((size_t)(id * 2 + rb) * 36 + kq * 9) * 1024 + lane * 16;
#pragma unroll
  for (int i = 0; i < 9; ++i) {
    u32x4 v;
#pragma unroll
    for (int j = 0; j < 4; ++j) {
      float a = __uint_as_float(xv[i][j] << 16) - mean, b = __uint_as_float(xv[i][j] & 0xffff0000u) - mean;
      v[j] = pk2(a * rstd, b * rstd);
    }
    st16(dst + i * 1024, v);
  }
}

__global__ __launch_bounds__(512, 2) void kernelQ(const char* __restrict__ X, const char* __restrict__ Wqkv,
                                                   const char* __restrict__ gw, const char* __restrict__ W1, const char* __restrict__ W2,
                                                   char* __restrict__ T,
                                                   char* __restrict__ XN, float* __restrict__ G,
                                                   char* __restrict__ FL) {
  __shared__ __attribute__((aligned(16))) char lds[163840];
  const int wg = blockIdx.x;
  const int wave = __builtin_amdgcn_readfirstlane(threadIdx.x >> 6);
  const int lane = threadIdx.x & 63;
  if (wg < 216) {
    const int xcd = wg & 7, t = (xcd >> 2) * 27 + (wg >> 3);
    const int joff = ((t >> 1) * 3 + (t & 1) * 4) % 9;
    qkv_role(lds, X, Wqkv, W1 + (xcd >> 1) * 2304, (wg >> 3) * 384, T, (xcd & 3) * 2 + (t & 1), t >> 1, joff, wave, lane);
  } else if (wg < 248) {
    STAMP(0, 0);
    ln_role(lds, X, gw, XN, G, FL, wg - 216, wave, lane);
  } else {
    const char* pc;
    unsigned span;
    asm volatile("s_getpc_b64 s[20:21]\n\ts_mov_b32 %1, kernel1@rel32@lo+4\n\ts_add_u32 s20, s20, %1\n\ts_addc_u32 s21, s21, kernel1@rel32@hi+16"
                 : "={s[20:21]}"(pc), "=s"(span)::"scc");
    span = 0u - span;
    if (span < (1u << 18)) {
      unsigned d0 = 0;
#pragma unroll 1
      for (unsigned o = threadIdx.x * 128u; o < span; o += 65536u) gld4k(d0, pc, o);
      asm volatile("s_waitcnt vmcnt(0)" : "+v"(d0)::"memory");
    }
  }
  STAMP(0, 6);
  STAMP(0, 7);
}

extern "C" void kernel_launch(void* const* d_in, const int* in_sizes, int n_in, void* d_out, int out_size, void* d_ws,
                              size_t ws_size, hipStream_t stream) {
  const char* X = (const char*)d_in[0];
  const char* Wqkv = (const char*)d_in[1];
  const char* Wo = (const char*)d_in[2];
  const char* W1 = (const char*)d_in[4];
  const unsigned short* b1 = (const unsigned short*)d_in[5];
  const char* W2 = (const char*)d_in[6];
  const unsigned short* b2 = (const unsigned short*)d_in[7];
  const char* gw = (const char*)d_in[8];
  char* ws = (char*)d_ws;
  char* T = ws + WS_T;
  char* H = ws + WS_H;
  char* O = ws + WS_O;
  char* P = ws + WS_P;
  char* XN = ws + WS_XN;
  char* FL = ws + WS_FL;
  float* G = (float*)(ws + WS_G);
  kernelQ<<<QGRID, 512, 0, stream>>>(X, Wqkv, gw, W1, W2, T, XN, G, FL);
  kernel1<<<512, 512, 0, stream>>>(T, O, XN, W1, b1, H, W2, b2, Wo, (const char*)G, P, FL, (char*)d_out);
}
```

```cpp
#include <hip/hip_runtime.h>
#include <stdint.h>

typedef __attribute__((ext_vector_type(8))) short bf16x8;
typedef __attribute__((ext_vector_type(4))) short s16x4;
typedef __attribute__((ext_vector_type(2))) __bf16 bf16x2_t;
typedef __attribute__((ext_vector_type(2))) float f32x2;
typedef __attribute__((ext_vector_type(4))) float f32x4;
typedef __attribute__((ext_vector_type(16))) float f32x16;
typedef __attribute__((ext_vector_type(4))) unsigned u32x4;
typedef __attribute__((ext_vector_type(2))) unsigned u32x2;
typedef __attribute__((address_space(3))) s16x4* lds_s16x4_p;
typedef __attribute__((address_space(3))) char* lds_p;
typedef __attribute__((address_space(3))) const bf16x8* lds_bf16x8_p;

#define STAMP(k, i)
#define STAMP2(i)
#define SST(i)
#define SS8(i)
#define SSD()
#define SSW()
#define DI __device__ __forceinline__
#define MFMA32(a, b, c) __builtin_amdgcn_mfma_f32_32x32x16_bf16((a), (b), (c), 0, 0, 0)
#define MFMA32K8(a, b, c) __builtin_amdgcn_mfma_f32_32x32x8bf16_1k((a), (b), (c), 0, 0, 0)
#define WAIT_VM(n) asm volatile("s_waitcnt vmcnt(" #n ")" ::: "memory")
#define BARRIER() __builtin_amdgcn_s_barrier()
#define SB() __builtin_amdgcn_sched_barrier(0)

constexpr int S_ = 2048, D_ = 576, F_ = 4608, NQKV = 1728;
constexpr int TSTEP = 640;
constexpr int TP = 1736 * 16;
constexpr int OEX = 64 * 32 * 1024;
constexpr size_t WS_T = 0;
constexpr size_t WS_H = 8u << 20;
constexpr size_t WS_O = 28u << 20;
constexpr size_t WS_G = 31u << 20;
constexpr size_t WS_FL = (31u << 20) + (64u << 10);
constexpr size_t WS_P = 32u << 20;
constexpr size_t WS_XN = 44u << 20;
constexpr size_t WS_XF = 47u << 20;
constexpr int FL_Q = 32768, FL_O = 36864, FL_P = 45056;

DI unsigned pk2(float a, float b) {
  f32x2 v = {a, b};
  bf16x2_t r = __builtin_convertvector(v, bf16x2_t);
  return __builtin_bit_cast(unsigned, r);
}
DI float bf2f(unsigned short u) { return __uint_as_float(((unsigned)u) << 16); }
DI s16x4 tr_read(const char* p) {
  return __builtin_amdgcn_ds_read_tr16_b64_v4i16((lds_s16x4_p)(p));
}
DI s16x4 trl(lds_p p) { return __builtin_amdgcn_ds_read_tr16_b64_v4i16((lds_s16x4_p)p); }
DI bf16x8 ld8(lds_p p) { return *(lds_bf16x8_p)p; }
DI float xor32(float v) { return __shfl_xor(v, 32, 64); }

DI void glds16s(const char* sbase, unsigned voff, unsigned lds_dst) {
  unsigned keep;
  asm volatile("s_mov_b32 %0, m0\n\ts_mov_b32 m0, %3\n\ts_nop 0\n\tglobal_load_lds_dwordx4 %1, %2\n\ts_mov_b32 m0, %0"
               : "=&s"(keep)
               : "v"(voff), "s"(sbase), "s"(lds_dst)
               : "memory");
}
DI void glds16c(const char* sbase, unsigned voff, unsigned lds_dst) {
  unsigned keep;
  asm volatile("s_mov_b32 %0, m0\n\ts_mov_b32 m0, %3\n\ts_nop 0\n\tglobal_load_lds_dwordx4 %1, %2 sc1\n\ts_mov_b32 m0, %0"
               : "=&s"(keep)
               : "v"(voff), "s"(sbase), "s"(lds_dst)
               : "memory");
}
DI void touch(const char* sbase, unsigned voff, unsigned lds_dst) {
  unsigned keep;
  asm volatile("s_mov_b32 %0, m0\n\ts_mov_b32 m0, %3\n\ts_nop 0\n\tglobal_load_lds_dword %1, %2\n\ts_mov_b32 m0, %0"
               : "=&s"(keep)
               : "v"(voff), "s"(sbase), "s"(lds_dst)
               : "memory");
}
DI void gld16(bf16x8& d, const char* sbase, unsigned voff) {
  asm volatile("global_load_dwordx4 %0, %1, %2" : "=v"(d) : "v"(voff), "s"(sbase) : "memory");
}
DI void gld16c(bf16x8& d, const char* sbase, unsigned voff) {
  asm volatile("global_load_dwordx4 %0, %1, %2 sc1" : "=v"(d) : "v"(voff), "s"(sbase) : "memory");
}
DI void gld4c(unsigned& d, const char* sbase, unsigned voff) {
  asm volatile("global_load_dword %0, %1, %2 sc1" : "=v"(d) : "v"(voff), "s"(sbase) : "memory");
}
DI void gld4(unsigned& d, const char* sbase, unsigned voff) {
  asm volatile("global_load_dword %0, %1, %2" : "=v"(d) : "v"(voff), "s"(sbase) : "memory");
}
DI void gld4k(unsigned& d, const char* sbase, unsigned voff) {
  asm volatile("global_load_dword %0, %1, %2" : "+v"(d) : "v"(voff), "s"(sbase) : "memory");
}
DI unsigned lds_addr(const void* p) { return (unsigned)(uintptr_t)(lds_p)p; }

DI void st16(void* p, u32x4 v) { asm volatile("global_store_dwordx4 %0, %1, off sc1\n\ts_nop 1" ::"v"(p), "v"(v) : "memory"); }
DI void st16p(void* p, u32x4 v) { asm volatile("global_store_dwordx4 %0, %1, off\n\ts_nop 1" ::"v"(p), "v"(v) : "memory"); }
DI void st4(void* p, unsigned v) { asm volatile("global_store_dword %0, %1, off sc1" ::"v"(p), "v"(v) : "memory"); }

DI int fresh_lane() {
  int l;
  asm volatile("v_mbcnt_lo_u32_b32 %0, -1, 0\n\tv_mbcnt_hi_u32_b32 %0, -1, %0" : "=v"(l));
  return l;
}
template <int I>
struct IC { static constexpr int v = I; };
template <int I, int N, class F>
DI void static_for(F&& f) {
  if constexpr (I < N) {
    f(IC<I>{});
    static_for<I + 1, N>(f);
  }
}
template <int N>
DI void wait_vm() { asm volatile("s_waitcnt vmcnt(%0)" ::"i"(N) : "memory"); }
template <int N>
DI void wait_vm1(bf16x8& a) { asm volatile("s_waitcnt vmcnt(%1)" : "+v"(a) : "i"(N) : "memory"); }
DI void spin_flags(const char* base, unsigned voff) {
#pragma unroll 1
  for (int it = 0; it < (1 << 17); ++it) {
    unsigned f;
    gld4c(f, base, voff);
    asm volatile("s_waitcnt vmcnt(0)" : "+v"(f)::"memory");
    if (!__ballot(f != 1u)) break;
    __builtin_amdgcn_s_sleep(4);
  }
}
DI bf16x8 cat8(s16x4 lo, s16x4 hi) { return __builtin_shufflevector(lo, hi, 0, 1, 2, 3, 4, 5, 6, 7); }

#define SENT 0
DI unsigned nsent(unsigned v) { return SENT ? __builtin_elementwise_min(v, 0xfffffffeu) : v; }
DI unsigned umax4(bf16x8 x, unsigned m) {
  const u32x4 v = __builtin_bit_cast(u32x4, x);
  m = __builtin_elementwise_max(__builtin_elementwise_max(v[0], v[1]), m);
  return __builtin_elementwise_max(__builtin_elementwise_max(v[2], v[3]), m);
}
DI f32x16 qk_tile(lds_p kb, lds_p ke, bf16x8 qf, s16x4 qe, f32x16 c) {
  s16x4 lo = trl(kb);
  s16x4 hi = trl(kb + 64);
  s16x4 ex = trl(ke);
  bf16x8 kf = __builtin_shufflevector(lo, hi, 0, 1, 2, 3, 4, 5, 6, 7);
  f32x16 s = MFMA32(kf, qf, c);
  s = MFMA32K8(ex, qe, s);
  return s;
}
DI f32x16 pv_tile(lds_p vb, const f32x16& pe, f32x16 o) {
  bf16x8 v0 = ld8(vb);
  bf16x8 v1 = ld8(vb + TSTEP);
  u32x4 p0, p1;
#pragma unroll
  for (int i = 0; i < 4; ++i) {
    p0[i] = pk2(pe[2 * i], pe[2 * i + 1]);
    p1[i] = pk2(pe[8 + 2 * i], pe[8 + 2 * i + 1]);
  }
  o = MFMA32(v0, __builtin_bit_cast(bf16x8, p0), o);
  o = MFMA32(v1, __builtin_bit_cast(bf16x8, p1), o);
  return o;
}
struct KFrag { bf16x8 f; s16x4 e; };
struct VFrag { bf16x8 v0, v1; };
DI KFrag load_k(lds_p kb, lds_p ke) {
  KFrag k;
  s16x4 lo = trl(kb);
  s16x4 hi = trl(kb + 64);
  k.e = trl(ke);
  k.f = __builtin_shufflevector(lo, hi, 0, 1, 2, 3, 4, 5, 6, 7);
  return k;
}
DI VFrag load_v(lds_p vb) {
  VFrag v;
  v.v0 = ld8(vb);
  v.v1 = ld8(vb + TSTEP);
  return v;
}
#define EXP2(x) __builtin_amdgcn_exp2f(x)
#define PTRUNC 1
DI unsigned pkp(float a, float b) {
  return __builtin_amdgcn_perm(__float_as_uint(b), __float_as_uint(a), 0x07060302u);
}
#define APM 2
#define APMASK 0x77
#define PHI(i) if constexpr ((PM >> (i)) & 1) __builtin_amdgcn_s_setprio(APM)
#define PLO(i) if constexpr ((PM >> (i)) & 1) __builtin_amdgcn_s_setprio(0)
template <int PM>
DI void tile_step(const f32x16& s, f32x16& sn, const KFrag& k, const VFrag& v, bf16x8 qf, s16x4 qe,
                  f32x16& o) {
  f32x16 negm;
#pragma unroll
  for (int i = 0; i < 16; ++i) negm[i] = 0.f;
  u32x4 p0, p1;
  p0[0] = pkp(EXP2(s[0]), EXP2(s[1]));
  p0[1] = pkp(EXP2(s[2]), EXP2(s[3]));
  SB();
  PHI(0);
  SB();
  f32x16 t = MFMA32(k.f, qf, negm);
  SB();
  PLO(0);
  SB();
  p0[2] = pkp(EXP2(s[4]), EXP2(s[5]));
  p0[3] = pkp(EXP2(s[6]), EXP2(s[7]));
  SB();
  PHI(1);
  SB();
  o = MFMA32(v.v0, __builtin_bit_cast(bf16x8, p0), o);
  SB();
  PLO(1);
  SB();
  p1[0] = pkp(EXP2(s[8]), EXP2(s[9]));
  p1[1] = pkp(EXP2(s[10]), EXP2(s[11]));
  SB();
  PHI(2);
  SB();
  t = MFMA32K8(k.e, qe, t);
  SB();
  PLO(2);
  SB();
  p1[2] = pkp(EXP2(s[12]), EXP2(s[13]));
  p1[3] = pkp(EXP2(s[14]), EXP2(s[15]));
  SB();
  PHI(3);
  SB();
  o = MFMA32(v.v1, __builtin_bit_cast(bf16x8, p1), o);
  SB();
  PLO(3);
  SB();
  sn = t;
}
DI float rowmax16(const f32x16& s) {
  float m = __builtin_fmaxf(s[0], s[1]);
#pragma unroll
  for (int i = 2; i < 16; ++i) m = __builtin_fmaxf(m, s[i]);
  return __builtin_fmaxf(m, xor32(m));
}

constexpr int AT_SLOT = 20480;
constexpr int at_cnt(int c, int grp) { return 2 + (((c & 1) == grp) ? 1 : 0); }
constexpr int at_off(int tt) { return ((tt >> 3) & 1) * AT_SLOT + (tt & 7) * 1280; }

#define TOUCH_AT 9
#define ADEF 0
#define PRE_AT 0
DI void attn_role(char* lds, const char* T, char* O, char* P, const char* __restrict__ G, char* FL, const char* __restrict__ W2,
                  const char* __restrict__ Wo, int wg, int wave, int lane) {
  const int head = (wg & 7) * 4 + ((wg >> 3) & 3);
  const int qt = wg >> 5;
  const int r = lane & 31, h = lane >> 5;
  const int grp = wave >> 2;
  const unsigned lbase = lds_addr(lds);
  const int p1 = wave + 8, p2 = 16 + (wave & 3);
  auto mkoff = [&](int pc, int first) {
    int isv = pc >= 10;
    int sl = (pc - 10 * isv) * 64 + lane;
    int seg = sl / 20, row = sl - seg * 20;
    int slot = row < 18 ? first + 576 * isv + head * 18 + row : 1728 + row - 18;
    return (unsigned)(seg * TP + slot * 16);
  };
  const unsigned v0 = mkoff(wave, 576), v1 = mkoff(p1, 576), v2 = mkoff(p2, 576);
  auto issue = [&](int c) {
    unsigned dst = lbase + (c & 3) * AT_SLOT;
    const char* src = T + (size_t)c * (32 * TP);
    glds16s(src, v0, dst + wave * 1024);
    glds16s(src, v1, dst + p1 * 1024);
    if ((c & 1) == grp) glds16s(src, v2, dst + p2 * 1024);
  };
  STAMP(1, 1);
  unsigned gbits;
  gld4(gbits, G + (qt * 256 + wave * 32) * 4, r * 4);
  {
    const char* src = T + (size_t)qt * (32 * TP);
    glds16s(src, mkoff(wave, 0), lbase + 3 * AT_SLOT + wave * 1024);
    if (wave < 2) glds16s(src, mkoff(p1, 0), lbase + 3 * AT_SLOT + p1 * 1024);
  }
  issue(0);
  issue(1);
  issue(2);
  const int q = (lane >> 2) & 3, p = lane & 3, g = (lane >> 4) & 1;
  const lds_p L = (lds_p)lds;
  const lds_p kb = L + g * TSTEP + (p & 1) * (TSTEP / 2) + (8 * h + q) * 16 + (p >> 1) * 8;
  const lds_p ke = L + g * TSTEP + (p & 1) * (TSTEP / 2) + (16 + q) * 16 + (p >> 1) * 8;
  const lds_p vb = L + 10240 + h * (TSTEP / 2) + (r < 19 ? r : 19) * 16;
  lds_p kb2 = kb + 2 * AT_SLOT, ke2 = ke + 2 * AT_SLOT, vb2 = vb + 2 * AT_SLOT;
  asm volatile("" : "+v"(kb2), "+v"(ke2), "+v"(vb2));
  asm volatile("s_waitcnt vmcnt(5)" : "+v"(gbits)::"memory");
  BARRIER();
  STAMP(1, 2);
  bf16x8 qf;
  s16x4 qe;
  {
    KFrag qq = load_k(kb2 + AT_SLOT + wave * 1280, ke2 + AT_SLOT + wave * 1280);
    qf = qq.f;
    qe[0] = h ? (short)0 : qq.e[0];
    qe[1] = h ? (short)0 : qq.e[1];
    qe[2] = 0;
    qe[3] = 0;
  }

  const float gate = __uint_as_float(gbits);
  f32x16 zero;
#pragma unroll
  for (int i = 0; i < 16; ++i) zero[i] = 0.f;
  f32x16 o = zero;
  f32x16 sA = qk_tile(kb, ke, qf, qe, zero);
  {
    const float nx = -rowmax16(sA);
    const unsigned mb = (__float_as_uint(nx) + (nx > 0.f ? 0xffffu : 0u)) >> 16;
    const float nm = __uint_as_float(mb << 16);
#pragma unroll
    for (int i = 0; i < 16; ++i) sA[i] += nm;
    qe[2] = h ? (short)0 : (short)mb;
  }
  KFrag kA, kB;
  VFrag vA, vB;
  kB = load_k(kb + 1280, ke + 1280);
  vA = load_v(vb);
  f32x16 sB;
#pragma unroll 1
  for (int cc = 0; cc < 8; cc += 4) {
    static_for<0, 32>([&](auto Tc) {
      constexpr int tt = decltype(Tc)::v, ci = tt >> 3;
      if constexpr ((tt & 7) == 6) {
        if (cc == 0 || ci < 2) {
          if (grp == 0) wait_vm<at_cnt(ci + 2, 0)>(); else wait_vm<at_cnt(ci + 2, 1)>();
        } else if (ci == 2) wait_vm<0>();
        if (cc == 0 || ci < 3) BARRIER();
        if constexpr (ci == TOUCH_AT) {
          if (cc == 0) {
            const int l2 = fresh_lane();
            const char* src = W2 + (size_t)((wg & 7) >> 1) * (1152 * 1152) + (wg >> 3) * 41472;
            const unsigned dst = lbase + ((ci + 3) & 3) * AT_SLOT + wave * 1024;
            touch(src, (wave * 41 + l2 - (l2 > 40 ? 24 : 0) - (wave >> 2) * 4) * 128u, dst);
            if (wave == 0) touch(Wo + ((wg & 7) >> 1) * (144 * 1152) + (wg >> 3) * 5184, (l2 > 39 ? ((wg & 8) ? 39 : 40) : l2) * 128u + ((wg & 8) ? 64u : 0u), dst);
          }
        }
        if (cc == 0 || ci == 0) issue(cc + ci + 3);
      }
      constexpr int t1 = (tt + 1) & 31, t2 = (tt + 2) & 31;
      const lds_p kp = (t2 < 16 ? kb : kb2) + at_off(t2);
      const lds_p ep = (t2 < 16 ? ke : ke2) + at_off(t2);
      const lds_p vp = (t1 < 16 ? vb : vb2) + at_off(t1);
      if constexpr ((tt & 1) == 0) {
        kA = load_k(kp, ep);
        vB = load_v(vp);
        SB();
        tile_step<APMASK & 15>(sA, sB, kB, vA, qf, qe, o);
      } else {
        kB = load_k(kp, ep);
        vA = load_v(vp);
        SB();
        tile_step<(APMASK >> 4) & 15>(sB, sA, kA, vB, qf, qe, o);
      }
    });
    if (cc == 0) STAMP(1, 3);
  }
  STAMP(1, 4);
  STAMP(1, 5);
  float l = xor32(o[10]);
  l = h ? l : o[10];
  bool bad = !(l < 1.0e30f);
#pragma unroll
  for (int i = 0; i < 10; ++i) bad = bad || !(__builtin_fabsf(o[i]) < 1.0e37f);
  const int ob = (qt * 8 + wave) * 32 + head;
  auto publish = [&]() {
    const float inv = gate / l;
    u32x4 ov;
#pragma unroll
    for (int i = 0; i < 4; ++i) ov[i] = nsent(pk2(o[2 * i] * inv, o[2 * i + 1] * inv));
    st16(O + (size_t)ob * 1024 + lane * 16, ov);
    if (h == 0) st4(O + OEX + (ob >> 3) * 1024 + ((head >> 2) & 1) * 512 + r * 16 + (head & 3) * 4, nsent(pk2(o[8] * inv, o[9] * inv)));
    STAMP(1, 6);
    wait_vm<0>();
    if (lane == 0) st4(FL + FL_O + ob * 4, 1u);
  };
  const bool mybad = __any(bad);
  if (!mybad) publish();
  {
    int* fl = (int*)(lds + AT_SLOT);
    if (lane == 0) fl[wave] = mybad ? 1 : 0;
    __syncthreads();
    int v = fl[lane & 7];
    bad = __any(v != 0);
  }
  if (bad) {
    float m = -3.0e38f;
    o = zero;
    qe[2] = 0;
#pragma unroll 1
    for (int c = 0; c < 8; ++c) {
      __syncthreads();
      {
        const char* src = T + (size_t)c * (32 * TP);
        glds16s(src, v0, lbase + wave * 1024);
        glds16s(src, v1, lbase + p1 * 1024);
        if (grp == 0) glds16s(src, v2, lbase + p2 * 1024);
      }
      wait_vm<0>();
      __syncthreads();
#pragma unroll 1
      for (int t = 0; t < 8; ++t) {
        f32x16 s = qk_tile(kb + t * 1280, ke + t * 1280, qf, qe, zero);
        float mn = __builtin_fmaxf(m, rowmax16(s));
        float alpha = __builtin_amdgcn_exp2f(m - mn);
        m = mn;
        f32x16 pe;
#pragma unroll
        for (int i = 0; i < 16; ++i) { pe[i] = __builtin_amdgcn_exp2f(s[i] - mn); o[i] *= alpha; }
        o = pv_tile(vb + t * 1280, pe, o);
      }
    }
    l = xor32(o[10]);
    l = h ? l : o[10];
    if (mybad) publish();
  }
  STAMP(1, 7);
}

#define QKV_PRIO 2
#define ATT_PRIO 0
#define SKIP1 0
#define MLP_PRIO 1
#define GELU_PRIO MLP_PRIO
#define MLP2_PRIO MLP_PRIO
#define PF2_AT 20
#define PSLP 2
#define XE_AT 33
constexpr int CK3 = 30;
constexpr int M_IT = 36;
constexpr int M2_NT = 41;
struct M2S { int slot[M2_NT]; int it[M2_NT]; };
constexpr M2S m2_sched(int U) {
  M2S r{};
  const int E = 2 * U - 2;
  int next = 0;
  for (; next < 4; ++next) { r.slot[next] = next; r.it[next] = -1; }
  for (int j = 1; next < M2_NT; ++j) {
    r.slot[next] = r.slot[j - 1];
    r.it[next] = j;
    ++next;
    if (next < M2_NT && (j == E || j == E + 1)) { r.slot[next] = 4 + (j - E); r.it[next] = j; ++next; }
  }
  return r;
}
constexpr int m2_ck1(int U) { return 2 * U - 4; }
constexpr int m2_ck2(int U) { return (U == 5 ? 26 : 2 * U + 8) - 4; }
constexpr int m2_wait(int U, int kh, int j, int mode) {
  const M2S sc = m2_sched(U);
  const int U2 = U == 5 ? 2 : 3;
  int n = 0, last = -1;
  for (int i = -1; i < j + mode; ++i) {
    if (i == m2_ck1(U) - 2 || i == m2_ck2(U) - 2 || (SENT == 2 && i == CK3 - 2)) { if (mode == 1 && i == j - 2) last = n; ++n; }
    for (int s = 0; s < M2_NT; ++s)
      if (sc.it[s] == i) {
        const int c = (U2 == 3 && (s & 1) == kh) ? 2 : 1;
        n += c;
        if (mode == 0 && s <= j + 1) last = n - 1;
      }
    if (i == -1) n += 2 * U;
    if (i >= 0 && i < j && i + 4 >= 2 * U && i + 4 < M_IT) { if (mode == 0 && i + 4 <= j) last = n; ++n; }
  }
  return n - 1 - last;
}
constexpr bool m2_need(int U, int j) { return j >= 3; }

template <int N>
DI void pin_acc(f32x16* a) {
  if constexpr (N == 5)
    asm volatile("" : "+{v[0:15]}"(a[0]), "+{v[16:31]}"(a[1]), "+{v[32:47]}"(a[2]), "+{v[48:63]}"(a[3]), "+{v[64:79]}"(a[4]));
  else
    asm volatile("" : "+{v[0:15]}"(a[0]), "+{v[16:31]}"(a[1]), "+{v[32:47]}"(a[2]), "+{v[48:63]}"(a[3]));
}
#define XLD(R) asm volatile("global_load_dwordx4 %0, %1, %2 sc1" : "={" R "}"(d) : "v"(voff), "s"(sbase) : "memory")
#define XLP(R) asm volatile("global_load_dwordx4 %0, %1, %2" : "={" R "}"(d) : "v"(voff), "s"(sbase) : "memory")
template <int I>
DI void xloadp(bf16x8& d, const char* sbase, unsigned voff) {
  if constexpr (I == 0) XLP("v[80:83]");
  else if constexpr (I == 1) XLP("v[84:87]");
  else if constexpr (I == 2) XLP("v[88:91]");
  else XLP("v[92:95]");
}
template <int I>
DI void xload(bf16x8& d, const char* sbase, unsigned voff) {
  if constexpr (I == 0) XLD("v[80:83]");
  else if constexpr (I == 1) XLD("v[84:87]");
  else if constexpr (I == 2) XLD("v[88:91]");
  else XLD("v[92:95]");
}
template <int I>
DI void pload(bf16x8& d, const char* sbase, unsigned voff) {
  if constexpr (I == 0) XLD("v[48:51]");
  else if constexpr (I == 1) XLD("v[52:55]");
  else if constexpr (I == 2) XLD("v[56:59]");
  else if constexpr (I == 3) XLD("v[60:63]");
  else if constexpr (I == 4) XLD("v[64:67]");
  else if constexpr (I == 5) XLD("v[68:71]");
  else if constexpr (I == 6) XLD("v[72:75]");
  else if constexpr (I == 7) XLD("v[76:79]");
  else if constexpr (I == 8) XLD("v[80:83]");
  else if constexpr (I == 9) XLD("v[84:87]");
  else if constexpr (I == 10) XLD("v[88:91]");
  else if constexpr (I == 11) XLD("v[92:95]");
  else if constexpr (I == 12) XLD("v[96:99]");
  else if constexpr (I == 13) XLD("v[100:103]");
  else if constexpr (I == 14) XLD("v[104:107]");
  else if constexpr (I == 15) XLD("v[108:111]");
  else if constexpr (I == 16) XLD("v[112:115]");
  else if constexpr (I == 17) XLD("v[116:119]");
}
template <int I>
DI void ppin(bf16x8& d) {
  if constexpr (I == 0) asm volatile("" : "+{v[48:51]}"(d));
  else if constexpr (I == 1) asm volatile("" : "+{v[52:55]}"(d));
  else if constexpr (I == 2) asm volatile("" : "+{v[56:59]}"(d));
  else if constexpr (I == 3) asm volatile("" : "+{v[60:63]}"(d));
  else if constexpr (I == 4) asm volatile("" : "+{v[64:67]}"(d));
  else if constexpr (I == 5) asm volatile("" : "+{v[68:71]}"(d));
  else if constexpr (I == 6) asm volatile("" : "+{v[72:75]}"(d));
  else if constexpr (I == 7) asm volatile("" : "+{v[76:79]}"(d));
  else if constexpr (I == 8) asm volatile("" : "+{v[80:83]}"(d));
  else if constexpr (I == 9) asm volatile("" : "+{v[84:87]}"(d));
  else if constexpr (I == 10) asm volatile("" : "+{v[88:91]}"(d));
  else if constexpr (I == 11) asm volatile("" : "+{v[92:95]}"(d));
  else if constexpr (I == 12) asm volatile("" : "+{v[96:99]}"(d));
  else if constexpr (I == 13) asm volatile("" : "+{v[100:103]}"(d));
  else if constexpr (I == 14) asm volatile("" : "+{v[104:107]}"(d));
  else if constexpr (I == 15) asm volatile("" : "+{v[108:111]}"(d));
  else if constexpr (I == 16) asm volatile("" : "+{v[112:115]}"(d));
  else if constexpr (I == 17) asm volatile("" : "+{v[116:119]}"(d));
}
#define XDS(R) asm volatile("ds_read_b128 %0, %1 offset:%2" : "={" R "}"(d) : "v"(a), "i"(OFF) : "memory")
template <int I, int OFF>
DI void xstash(bf16x8& d, unsigned a) {
  if constexpr (I == 0) XDS("v[80:83]");
  else if constexpr (I == 1) XDS("v[84:87]");
  else if constexpr (I == 2) XDS("v[88:91]");
  else XDS("v[92:95]");
}
template <int I>
DI void pin_x1(bf16x8& x) {
  if constexpr (I == 0) asm volatile("" : "+{v[80:83]}"(x));
  else if constexpr (I == 1) asm volatile("" : "+{v[84:87]}"(x));
  else if constexpr (I == 2) asm volatile("" : "+{v[88:91]}"(x));
  else asm volatile("" : "+{v[92:95]}"(x));
}
template <int I>
DI void pin_w(bf16x8& w) {
  if constexpr (I == 0) asm volatile("" : "+{v[96:99]}"(w));
  else if constexpr (I == 1) asm volatile("" : "+{v[100:103]}"(w));
  else if constexpr (I == 2) asm volatile("" : "+{v[104:107]}"(w));
  else if constexpr (I == 3) asm volatile("" : "+{v[108:111]}"(w));
  else asm volatile("" : "+{v[112:115]}"(w));
}
template <int U>
DI void mlp_track(char* lds, const char* __restrict__ XN, const char* __restrict__ W1,
                  const unsigned short* __restrict__ b1, char* H, const char* __restrict__ W2,
                  const unsigned short* __restrict__ b2, const char* __restrict__ Wo, const char* O,
                  const char* __restrict__ G, char* P, char* FL, char* __restrict__ out, int rt, int sp, int j4,
                  int koff, int wave, int lane) {
  constexpr int NBW = 9 - U, U2 = U == 5 ? 2 : 3;
  constexpr int NS1 = U == 5 ? 5 : 6;
  SSD();
  SST(0);
  SS8(0);
  const bool up = j4 < 2;
  const int unit0 = sp * 18 + (U == 5 ? (up ? 0 : 13) : (up ? 5 : 9));
  const unsigned lbase = lds_addr(lds);
  const lds_p L = (lds_p)lds;
  const int wm = wave & 3, wn = wave >> 2;
  char* FLg = FL + (size_t)(rt * 4 + sp) * 512;
  f32x16 acc[5];
  bf16x8 w[5];
  float iog;
  {
    constexpr int WS = 2 * U * 1024, XB = NS1 * WS, NP = 4 + 2 * U;
    const int h = lane >> 5;
    const unsigned voffW = (unsigned)((lane >> 3) * 9216 + (((lane & 7) ^ (((lane >> 4) & 1) << 2)) << 4));
    const unsigned voff0 = wave < 4 ? (unsigned)(lane * 16) : voffW;
    const int q0 = wave - 4, q1 = wave + 4;
    const char* wb = W1 + unit0 * 128;
    const char* src0 =
        wave < 4 ? XN + (size_t)(rt * 4 + wave) * (36 * 1024) : wb + (q0 / U) * (8 * 9216) + (q0 % U) * 128;
    const char* src1 = wb + (q1 / U) * (8 * 9216) + (q1 % U) * 128;
    const unsigned st0 = wave < 4 ? 1024u : 147456u;
    const bool two = wave + 8 < NP;
    const unsigned dst0 = lbase + (wave < 4 ? XB + wave * 1024 : q0 * 1024), ds0 = wave < 4 ? 4096u : (unsigned)WS;
    auto issue = [&](int s, int e) {
      int ks = s + koff;
      ks = ks >= 36 ? ks - 36 : ks;
      if (e == 0) glds16s(src0 + (unsigned)ks * st0, voff0, dst0 + (s % NS1) * ds0);
      else if (two) glds16s(src1 + (unsigned)ks * 147456u, voffW, lbase + (s % NS1) * WS + q1 * 1024);
    };
    auto waitn = [&](auto Nc) {
      constexpr int N = decltype(Nc)::v;
      if (two) wait_vm<2 * N>(); else wait_vm<N>();
    };
    unsigned bb[U];
#pragma unroll
    for (int b = 0; b < U; ++b)
      asm volatile("global_load_ushort %0, %1, %2" : "=v"(bb[b]) : "v"((lane & 31) * 2), "s"(b1 + (unit0 * 2 + wn * U + b) * 32) : "memory");
    asm volatile("global_load_dword %0, %1, %2" : "=v"(iog) : "v"((lane & 31) * 4), "s"(G + (2048 + (rt * 4 + wm) * 32) * 4) : "memory");
#pragma unroll
    for (int s = 0; s < (SKIP1 ? 0 : NS1); ++s) { issue(s, 0); issue(s, 1); }
    if (SKIP1) wait_vm<0>();
    const int q = (lane >> 2) & 3, p = lane & 3, g = (lane >> 4) & 1;
    lds_p xa = L + XB + wm * 1024 + lane * 16;
    lds_p wa[2];
#pragma unroll
    for (int e = 0; e < 2; ++e) {
      int b = wn * U + e;
      wa[e] = L + (h * U + (b >> 1)) * 1024 + q * 128 + (((b & 1) ^ (q >> 1)) << 6) + g * 32 + p * 8;
    }
    asm volatile("" : "+v"(xa), "+v"(wa[0]), "+v"(wa[1]));
    waitn(IC<NS1 - 1>{});
    {
      f32x16 zero;
#pragma unroll
      for (int i = 0; i < 16; ++i) zero[i] = 0.f;
      asm volatile("" : "+v"(iog));
      const u32x4 xx = {h ? 0u : 0x3F80u, 0u, 0u, 0u};
#pragma unroll
      for (int b = 0; b < U; ++b) {
        asm volatile("" : "+v"(bb[b]));
        const u32x4 wx = {bb[b], 0u, 0u, 0u};
        acc[b] = MFMA32(__builtin_bit_cast(bf16x8, wx), __builtin_bit_cast(bf16x8, xx), zero);
      }
    }
    BARRIER();
    __builtin_amdgcn_s_setprio(MLP_PRIO);
    STAMP(4, 1);
    auto rdx = [&](int sl) { return ld8(xa + sl * 4096); };
    auto rdw = [&](int sl, int m) {
      const lds_p a = wa[m & 1] + sl * WS + (m >> 1) * 1024;
      return cat8(trl(a), trl(a + 512));
    };
    bf16x8 x0, x1;
    x0 = rdx(0);
#pragma unroll
    for (int m = 0; m < U; ++m) w[m] = rdw(0, m);
    unsigned pfd;
    static_for<0, SKIP1 ? 0 : 36>([&](auto Jc) {
      constexpr int j = decltype(Jc)::v;
      if constexpr (j + 1 < 36) {
        constexpr int top = j == 0 ? NS1 - 1 : (j + NS1 - 2 < 35 ? j + NS1 - 2 : 35);
        waitn(IC<top - (j + 1)>{});
        BARRIER();
      }
      STAMP2(j);
      if (j == 12) STAMP(4, 2);
      if (j == 24) STAMP(4, 3);
      SB();
      bf16x8& xc = (j & 1) ? x1 : x0;
      bf16x8& xn = (j & 1) ? x0 : x1;
      static_for<0, U>([&](auto Mc) {
        constexpr int m = decltype(Mc)::v;
        acc[m] = MFMA32(w[m], xc, acc[m]);
        SB();
        if constexpr (j + 1 < 36) {
          if constexpr (m == 0) xn = rdx((j + 1) % NS1);
          w[m] = rdw((j + 1) % NS1, m);
        }
        if constexpr (j >= 1 && j + NS1 - 1 < 36 && (m == 1 || m == 2)) issue(j + NS1 - 1, m - 1);
        if constexpr (j == PF2_AT && m == 3) {
          const unsigned l2 = fresh_lane(), li = l2 < 18 * U2 ? l2 : l2 - 32, row = li / U2;
          const unsigned vo = row * 1152u + (li - row * U2) * 128u;
          const int u2 = U == 5 ? (up ? 0 : 7) : 2 * j4;
          gld4(pfd, W2 + (size_t)(sp * 1152 + (rt & 7) * 144 + wave * 18) * 1152 + u2 * 128, vo);
          if (wave == 7) gld4k(pfd, Wo + (sp * 144 + (rt & 7) * 18) * 1152 + u2 * 128, vo);
        }
        if constexpr (j == PF2_AT + NS1 + 1 && m == 0) asm volatile("" ::"v"(pfd));
        SB();
      });
    });
    STAMP2(36);
    STAMP(4, 4);
    asm volatile("" : "+v"(acc[0]), "+v"(acc[U - 1]), "+v"(iog));
  }
  SS8(1);
  __builtin_amdgcn_s_setprio(GELU_PRIO);
  BARRIER();
  SS8(2);
  constexpr int B1 = 2 * U, B2 = B1 + 8, B3 = 26;
  constexpr M2S SC = m2_sched(U);
  const int e0 = 2 * (U == 5 ? (up ? 0 : 26) : (up ? 10 : 18));
  const int e1 = 2 * (U == 5 ? (up ? 10 : 18) : (up ? 18 : 10)) - B1;
  const int e2 = 2 * (U == 5 ? (up ? 18 : 10) : (up ? 26 : 0)) - B2;
  const int e3 = 2 * (U == 5 ? (up ? 26 : 0) : (up ? 0 : 26)) - B3;
  auto kstep = [&](int s, int half) {
    const int uc2 = s < B1 ? 2 * U : (s < B2 ? 8 : (s < B3 ? (U == 5 ? 8 : 10) : 10));
    return (unsigned)(s + (s < B1 ? e0 : (s < B2 ? e1 : (s < B3 ? e2 : e3))) + half * uc2);
  };
  const int kh = wn;
  const int unit2 = U == 5 ? (up ? 0 : 7) : 2 * j4, boff = U == 5 ? 0 : j4 - 1;
  const int ln = fresh_lane();
  const unsigned voffX = ln * 16;
  const unsigned voffW = (unsigned)((ln >> 3) * 1152 + (((ln & 7) ^ (((ln >> 4) & 1) << 2)) << 4));
  const char* xsrc = H + ((size_t)(rt * 4 + wm) * 288 + sp * 72) * 1024;
  const int p0 = wave, p2 = 8 + (wave & 3);
  const int kb0 = p0 / U2, kb2 = p2 / U2;
  const unsigned c0 = (kb0 & 1) * (8 * 1152) + (unit2 + p0 % U2) * 128, c2 = (kb2 & 1) * (8 * 1152) + (unit2 + p2 % U2) * 128;
  const char* w2s = W2 + (size_t)sp * 1152 * 1152;
  const char* wos = Wo + sp * (8 * 18 * 1152);
  constexpr int SL2 = U2 * 4096;
  auto issueW = [&](auto Sc, int e) {
    constexpr int s = decltype(Sc)::v;
    const unsigned dst = lbase + SC.slot[s] * SL2 + (e ? p2 : p0) * 1024;
    if (e == 1 && !(U2 == 3 && (s & 1) == kh)) return;
    const int half = e ? 1 : kb0 >> 1;
    const unsigned c = e ? c2 : c0;
    if constexpr (s < M_IT) glds16s(w2s + (c + kstep(s, half) * (16u * 1152u)), voffW, dst);
    else if constexpr (s < M2_NT - 1) glds16s(wos + (c + (unsigned)(4 * half + s - M_IT) * (18u * 1152u)), voffW, dst);
    else {
      const int l2 = fresh_lane(), row = l2 >> 3;
      const unsigned ve = (unsigned)(((4 * (row >> 2) + ((row & 3) >> 1)) * 18 + (row & 1)) * 1152 + (((l2 & 7) ^ (((l2 >> 4) & 1) << 2)) << 4));
      const int kbl = (e ? kb2 : kb0) & 1;
      glds16s(wos + (c + (unsigned)(kbl * (36 - 8) + 16) * 1152u), ve, dst);
    }
  };
  unsigned b2v[NBW];
  const bool addb = sp == 0 && kh == 0;
#pragma unroll
  for (int b = 0; b < NBW; ++b)
    asm volatile("global_load_ushort %0, %1, %2" : "=v"(b2v[b]) : "v"((ln & 31) * 2), "s"(b2 + (unit2 * 2 + boff + b) * 32) : "memory");
  static_for<0, 4>([&](auto Sc) { issueW(Sc, 0); issueW(Sc, 1); });
  SS8(3);
  bf16x8 xr[4];
  const unsigned stash = lbase + 4 * SL2 + wave * ((2 * U - 4) * 1024) + voffX;
  {
    const float C0 = -2.0f * 1.4426950408889634f * 0.7978845608028654f;
    const float C1 = C0 * 0.044715f;
    f32x2 c0 = {C0, C0}, c1 = {C1, C1};
    asm volatile("" : "+s"(c0), "+s"(c1));
    float vc1 = C1;
    asm volatile("" : "+v"(vc1));
    f32x2 ig = {iog, iog};
    asm volatile("" : "+v"(ig));
    char* hb = H + ((size_t)(rt * 4 + wm) * 288 + (unit0 * 2 + wn * U) * 2) * 1024 + fresh_lane() * 16;
#pragma unroll
    for (int b = 0; b < U; ++b) {
#pragma unroll
      for (int gp = 0; gp < 2; ++gp) {
        u32x4 o4;
#pragma unroll
        for (int i = 0; i < 4; ++i) {
          const f32x2 v = {acc[b][8 * gp + 2 * i], acc[b][8 * gp + 2 * i + 1]};
          const f32x2 t = v * __builtin_elementwise_fma(v * v, c1, c0);
          const f32x2 e = {__builtin_amdgcn_exp2f(t[0]), __builtin_amdgcn_exp2f(t[1])};
          const f32x2 d = __builtin_elementwise_fma(e, ig, ig);
          const f32x2 rc = {__builtin_amdgcn_rcpf(d[0]), __builtin_amdgcn_rcpf(d[1])};
          const f32x2 o = v * rc;
          o4[i] = pk2(o[0], o[1]);
        }
        st16(hb + (b * 2 + gp) * 1024, o4);
        if (b < 2) xr[b * 2 + gp] = __builtin_bit_cast(bf16x8, o4);
        else *(__attribute__((address_space(3))) u32x4*)(uintptr_t)(stash + (b * 2 + gp - 4) * 1024) = o4;
      }
      if (b == 1) SS8(4);
    }
  }
  STAMP(4, 5);
  SS8(5);
  lds_p wa[2];
  {
    const int h = ln >> 5, q = (ln >> 2) & 3, p = ln & 3, g = (ln >> 4) & 1;
#pragma unroll
    for (int e = 0; e < 2; ++e) {
      int b = boff + e;
      wa[e] = L + (2 * kh * U2 + (b >> 1)) * 1024 + (4 * h + q) * 128 + (((b & 1) ^ (q >> 1)) << 6) + g * 32 + p * 8;
    }
  }
  asm volatile("" : "+v"(wa[0]), "+v"(wa[1]));
  wait_vm<2 * U>();
  SS8(6);
  {
    f32x16 zero;
#pragma unroll
    for (int i = 0; i < 16; ++i) zero[i] = 0.f;
    const float og = __builtin_amdgcn_rcpf(iog);
    const float ogh = __uint_as_float(__float_as_uint(og) & 0xffff0000u);
    const u32x4 xx = {(ln >> 5) || !addb ? 0u : pk2(ogh, og - ogh), 0u, 0u, 0u};
#pragma unroll
    for (int b = 0; b < NBW; ++b) {
      asm volatile("" : "+v"(b2v[b]));
      const u32x4 wx = {b2v[b] * 0x10001u, 0u, 0u, 0u};
      acc[b] = MFMA32(__builtin_bit_cast(bf16x8, wx), __builtin_bit_cast(bf16x8, xx), zero);
    }
  }
  BARRIER();
  __builtin_amdgcn_s_setprio(MLP2_PRIO);
  STAMP(4, 6);
  STAMP(4, 7);
  STAMP(2, 1);
  SST(1);
  SS8(7);
  auto rdw = [&](int s, int m) {
    const lds_p a = wa[m & 1] + SC.slot[s] * SL2 + (m >> 1) * 1024;
    return cat8(trl(a), trl(a + U2 * 1024));
  };
#pragma unroll
  for (int m = 0; m < NBW; ++m) w[m] = rdw(0, m);
  unsigned fv = 0;
  auto poll = [&]() {
    unsigned vo = voffX;
    asm volatile("" : "+v"(vo));
    vo = (vo & 48u) << 3;
    gld4c(fv, FLg, vo);
  };
  auto poll3 = [&]() {
    const unsigned l2 = fresh_lane(), mb = rt * 4 + wm;
    const unsigned vo = (l2 & 12u) == 8u && (l2 & 3u) != (unsigned)wm ? FL_P + mb * 16u + (l2 & 3u) * 4u : FL_O + mb * 128u + sp * 32u + (l2 & 7u) * 4u;
    gld4c(fv, FL, vo);
  };
  auto check = [&](unsigned long long mask) {
    mask &= ~(1ull << j4);
    asm volatile("" : "+v"(fv));
    if (__ballot(fv != 1u) & mask) {
#pragma unroll 1
      for (int it = 0; it < (1 << 17); ++it) {
        __builtin_amdgcn_s_sleep(4);
        poll();
        wait_vm<0>();
        asm volatile("" : "+v"(fv));
        if (!(__ballot(fv != 1u) & mask)) break;
      }
    }
  };
  auto waitk = [&](auto N0, auto N1) {
    constexpr int n0 = decltype(N0)::v, n1 = decltype(N1)::v;
    if constexpr (n0 == n1) wait_vm<n0>();
    else { if (kh == 0) wait_vm<n0>(); else wait_vm<n1>(); }
  };
  constexpr int CK1 = m2_ck1(U), CK2 = m2_ck2(U);
  bf16x8 xe;
  const char* osrc = O + (size_t)((rt * 4 + wm) * 32 + sp * 8 + kh * 4) * 1024;
  const char* oesrc = O + OEX + ((rt * 4 + wm) * 4 + sp) * 1024;
  static_for<0, M_IT>([&](auto Jc) {
    constexpr int j = decltype(Jc)::v;
    if constexpr (m2_need(U, j)) waitk(IC<m2_wait(U, 0, j, 0)>{}, IC<m2_wait(U, 1, j, 0)>{});
    if constexpr (j > 0) BARRIER();
    if constexpr (j > 0) pin_acc<NBW>(acc);
    static_for<0, 4>([&](auto Ic) {
      constexpr int i = decltype(Ic)::v;
      if constexpr (SENT || j + ((i - j) & 3) < M_IT) pin_x1<i>(xr[i]);
    });
    if constexpr (j == 3) {
      if (wave == 0 && voffX == 0) st4(FLg + j4 * 128, 1u);
    }
    STAMP2(40 + j);
    if (j == 9) STAMP(2, 2);
    if (j == 18) STAMP(2, 3);
    if (j == 18) SST(2);
    if (j == 27) STAMP(2, 4);
    if constexpr (j == CK1 - 2 || j == CK2 - 2) poll();
    SB();
    static_for<0, NBW>([&](auto Mc) {
      constexpr int m = decltype(Mc)::v;
      pin_w<m>(w[m]);
      acc[m] = MFMA32(w[m], xr[j & 3], acc[m]);
      SB();
      w[m] = rdw(j + 1, m);
      static_for<j + 3, (j + 7 < M2_NT ? j + 7 : M2_NT)>([&](auto Sc) {
        constexpr int s = decltype(Sc)::v;
        if constexpr (SC.it[s] == j) {
          constexpr bool second = s > 0 && SC.it[s - 1] == j;
          if constexpr (m == (second ? 2 : 0)) issueW(Sc, 0);
          if constexpr (m == (second ? 3 : 1)) issueW(Sc, 1);
        }
      });
      SB();
    });
    if constexpr (j == CK1 || j == CK2) {
      waitk(IC<m2_wait(U, 0, j, 1)>{}, IC<m2_wait(U, 1, j, 1)>{});
      check(j == CK1 ? 6ull : 15ull);
    }
    if constexpr (j + 4 < B1) xstash<(j & 3), j * 1024>(xr[j & 3], stash);
    else if constexpr (j + 4 < M_IT) xload<(j & 3)>(xr[j & 3], xsrc + kstep(j + 4, kh) * 1024u, voffX);
    SB();
  });
  STAMP2(76);
  STAMP(2, 5);
  SST(3);
  wait_vm<0>();
  pin_acc<NBW>(acc);
  static_for<0, NBW>([&](auto Mc) { pin_w<decltype(Mc)::v>(w[decltype(Mc)::v]); });
  if (wave == 0) {
    const int l2 = fresh_lane();
    spin_flags(FL + FL_O + rt * 512 + sp * 32, (unsigned)(((l2 >> 3) & 3) * 128 + (l2 & 7) * 4));
  }
  BARRIER();
  SST(4);
  {
    static_for<0, 4>([&](auto Ic) { xload<decltype(Ic)::v>(xr[decltype(Ic)::v], osrc + decltype(Ic)::v * 1024, voffX); });
    gld16c(xe, oesrc, voffX);
    static_for<0, 4>([&](auto Tc) {
      constexpr int t = decltype(Tc)::v;
      wait_vm<4 - t>();
      pin_x1<t>(xr[t]);
      pin_acc<NBW>(acc);
      static_for<0, NBW>([&](auto Mc) {
        constexpr int m = decltype(Mc)::v;
        pin_w<m>(w[m]);
        acc[m] = MFMA32(w[m], xr[t], acc[m]);
        SB();
        w[m] = rdw(M_IT + t + 1, m);
        SB();
      });
    });
    wait_vm1<0>(xe);
    pin_acc<NBW>(acc);
    static_for<0, NBW>([&](auto Mc) { pin_w<decltype(Mc)::v>(w[decltype(Mc)::v]); });
    if (kh == 0) {
#pragma unroll
      for (int m = 0; m < NBW; ++m) acc[m] = MFMA32(w[m], xe, acc[m]);
    }
    pin_acc<NBW>(acc);
  }
  BARRIER();
  STAMP(2, 6);
  SST(5);
  constexpr int NA = (NBW + 1) / 2;
  const int l2 = fresh_lane();
  char* ex = lds + wm * (NBW * 4096) + l2 * 16;
  if (kh == 0) {
#pragma unroll
    for (int b = NA; b < NBW; ++b)
#pragma unroll
      for (int c = 0; c < 4; ++c) {
        f32x4 v = {acc[b][4 * c], acc[b][4 * c + 1], acc[b][4 * c + 2], acc[b][4 * c + 3]};
        *(f32x4*)(ex + b * 4096 + c * 1024) = v;
      }
  } else {
#pragma unroll
    for (int b = 0; b < NA; ++b)
#pragma unroll
      for (int c = 0; c < 4; ++c) {
        f32x4 v = {acc[b][4 * c], acc[b][4 * c + 1], acc[b][4 * c + 2], acc[b][4 * c + 3]};
        *(f32x4*)(ex + b * 4096 + c * 1024) = v;
      }
    acc[0] = acc[NA];
    acc[1] = acc[NA + 1];
  }
  auto pin_r = [&]() {
    if constexpr (NA == 3) asm volatile("" : "+{v[0:15]}"(acc[0]), "+{v[16:31]}"(acc[1]), "+{v[32:47]}"(acc[2]));
    else asm volatile("" : "+{v[0:15]}"(acc[0]), "+{v[16:31]}"(acc[1]));
  };
  pin_r();
  __syncthreads();
  const int bb0 = kh ? NA : 0;
  const bool third = kh == 0;
  ex += bb0 * 4096;
#pragma unroll
  for (int b = 0; b < NA; ++b) {
    if (b == 2 && !third) break;
#pragma unroll
    for (int c = 0; c < 4; ++c) {
      f32x4 a = *(const f32x4*)(ex + b * 4096 + c * 1024);
#pragma unroll
      for (int i = 0; i < 4; ++i) acc[b][4 * c + i] += a[i];
    }
  }
  pin_r();
  const char* flp = FL + FL_P + (((rt * 4 + j4) * 4 + wm) * 2 + kh) * 16;
  const size_t pofs = ((size_t)(rt * 4 + wm) * 18 + unit2 * 2 + boff + bb0) * 2048;
  if (wm != sp) {
    char* pb = P + (size_t)sp * (64 * 18 * 2048) + pofs + l2 * 16;
#pragma unroll
    for (int b = 0; b < NA; ++b) {
      if (b == 2 && !third) break;
#pragma unroll
      for (int gp = 0; gp < 2; ++gp) {
        u32x4 v;
#pragma unroll
        for (int i = 0; i < 4; ++i) v[i] = nsent(pk2(acc[b][8 * gp + 2 * i], acc[b][8 * gp + 2 * i + 1]));
        st16(pb + b * 2048 + gp * 1024, v);
      }
    }
    wait_vm<0>();
    if (l2 == 0) st4((char*)flp + sp * 4, 1u);
  } else {
    bf16x8 pv[NA][3][2];
    auto pget = [&](auto Oc) {
      static_for<0, NA * 2>([&](auto Ic) {
        constexpr int o = decltype(Oc)::v, b = decltype(Ic)::v >> 1, gp = decltype(Ic)::v & 1, i = b * 6 + o * 2 + gp;
        const char* pb = P + (size_t)((sp + 1 + o) & 3) * (64 * 18 * 2048) + pofs + (b == 2 && !third ? 1 : b) * 2048 + gp * 1024;
        pload<i>(pv[b][o][gp], pb, l2 * 16);
      });
    };
    spin_flags(flp, (unsigned)(((sp + 1 + (l2 % 3)) & 3) * 4));
    SST(6);
    static_for<0, NA * 6>([&](auto Ic) {
      constexpr int i = decltype(Ic)::v, b = i / 6, o = (i >> 1) % 3, gp = i & 1;
      const char* pb = P + (size_t)((sp + 1 + o) & 3) * (64 * 18 * 2048) + pofs + (b == 2 && !third ? 1 : b) * 2048 + gp * 1024;
      pload<i>(pv[b][o][gp], pb, l2 * 16);
    });
    char* orow = out + (size_t)((rt * 4 + wm) * 32 + (l2 & 31)) * 1152 + (unit2 * 2 + boff + bb0) * 64 + (l2 >> 5) * 16;
    static_for<0, NA>([&](auto Bc) {
      constexpr int b = decltype(Bc)::v;
      wait_vm<(NA - 1 - b) * 6 + 2 * b>();
      if (b < 2 || third) {
        static_for<0, 6>([&](auto Ic) {
          constexpr int i = decltype(Ic)::v, gp = i & 1;
          ppin<b * 6 + i>(pv[b][i >> 1][gp]);
          const u32x4 v = __builtin_bit_cast(u32x4, pv[b][i >> 1][gp]);
#pragma unroll
          for (int k = 0; k < 4; ++k) {
            acc[b][8 * gp + 2 * k] += __uint_as_float(v[k] << 16);
            acc[b][8 * gp + 2 * k + 1] += __uint_as_float(v[k] & 0xffff0000u);
          }
        });
        u32x2 o2[4];
#pragma unroll
        for (int gq = 0; gq < 4; ++gq) {
          o2[gq][0] = pk2(acc[b][4 * gq], acc[b][4 * gq + 1]);
          o2[gq][1] = pk2(acc[b][4 * gq + 2], acc[b][4 * gq + 3]);
        }
#pragma unroll
        for (int gq = 0; gq < 4; gq += 2) {
          auto r0 = __builtin_amdgcn_permlane32_swap(o2[gq][0], o2[gq + 1][0], false, false);
          auto r1 = __builtin_amdgcn_permlane32_swap(o2[gq][1], o2[gq + 1][1], false, false);
          u32x4 wv = {r0[0], r1[0], r0[1], r1[1]};
          st16p(orow + b * 64 + gq * 16, wv);
        }
      }
    });
  }
  STAMP(2, 7);
  SST(7);
  SSW();
}

extern "C" __global__ __launch_bounds__(512, 4) void kernel1(const char* __restrict__ T,
                                                   char* O, const char* __restrict__ XN,
                                                   const char* __restrict__ W1, const unsigned short* __restrict__ b1,
                                                   char* H, const char* __restrict__ W2,
                                                   const unsigned short* __restrict__ b2, const char* __restrict__ Wo,
                                                   const char* __restrict__ G, char* P, char* FL,
                                                   char* __restrict__ out) {
  __shared__ __attribute__((aligned(16))) char lds[81920];
  const int wave = __builtin_amdgcn_readfirstlane(threadIdx.x >> 6);
  const int lane = threadIdx.x & 63;
  const int bid = blockIdx.x;
  if (bid < 256) {
    STAMP(1, 0);
    STAMP(5, 0);
    __builtin_amdgcn_s_setprio(ATT_PRIO);
    attn_role(lds, T, O, P, G, FL, W2, Wo, bid, wave, fresh_lane());
  } else {
    STAMP(2, 0);
    STAMP(4, 0);
    const int wg = bid - 256;
    const int xcd = wg & 7, idx = wg >> 3;
    const int sp = xcd >> 1, j4 = idx & 3, r8 = idx >> 2, rt = (xcd & 1) * 8 + r8;
    const int koff = (r8 * 9 / 2 + j4 * 9) % 36;
    if (j4 == 0 || j4 == 3)
      mlp_track<5>(lds, XN, W1, b1, H, W2, b2, Wo, O, G, P, FL, out, rt, sp, j4, koff, wave, fresh_lane());
    else
      mlp_track<4>(lds, XN, W1, b1, H, W2, b2, Wo, O, G, P, FL, out, rt, sp, j4, koff, wave, fresh_lane());
  }
}

constexpr int Q_SLOT = 32768;
#define NSQ 4
constexpr int QGRID = 256;
DI void qkv_role(char* lds, const char* __restrict__ X, const char* __restrict__ Wqkv, char* __restrict__ T, int rt,
                 int ct, int joff, int wave, int lane) {
  f32x16 acc[2];
#pragma unroll
  for (int i = 0; i < 16; ++i) { acc[0][i] = 0.f; acc[1][i] = 0.f; }
  const unsigned lbase = lds_addr(lds);
  const int wm = wave & 3, wn = wave >> 2;
  const bool half = ct == 13;
  unsigned voffX[2];
#pragma unroll
  for (int i = 0; i < 2; ++i) {
    int row = wave * 16 + 8 * i + (lane >> 3);
    int c = (lane & 7) ^ ((row >> 1) & 7);
    voffX[i] = (unsigned)((rt * 128 + row) * 1152 + c * 16);
  }
  const unsigned voffW = (unsigned)((lane >> 3) * 3456 + (((lane & 7) ^ (((lane >> 4) & 1) << 2)) << 4));
  const char* wsrc = Wqkv + (size_t)wave * 8 * 3456 + ct * 256;
  const unsigned wh = half ? 0u : 128u;
  auto issue = [&](int J, int e) {
    unsigned dst = lbase + (J % NSQ) * Q_SLOT;
    unsigned Jr = J + joff;
    Jr = Jr >= 9 ? Jr - 9 : Jr;
    if (e < 2) glds16s(wsrc + (Jr * (64u * 3456u) + e * wh), voffW, dst + 16384 + e * 8192 + wave * 1024);
    else glds16s(X + Jr * 128u, voffX[e - 2], dst + (wave * 2 + e - 2) * 1024);
  };
  STAMP(0, 0);
#pragma unroll
  for (int J = 0; J < NSQ - 1; ++J)
#pragma unroll
    for (int e = 0; e < 4; ++e) issue(J, e);
  const int r = lane & 31, h = lane >> 5;
  const int q = (lane >> 2) & 3, p = lane & 3, g = (lane >> 4) & 1;
  const lds_p L = (lds_p)lds;
  constexpr int NB = (NSQ + 1) / 2;
  lds_p xa[NB][4], wa[NB][2];
#pragma unroll
  for (int z = 0; z < NB; ++z) {
#pragma unroll
    for (int ks = 0; ks < 4; ++ks) {
      xa[z][ks] = L + z * 2 * Q_SLOT + (wm * 32 + r) * 128 + (((2 * ks + h) ^ ((r >> 1) & 7)) << 4);
      asm volatile("" : "+v"(xa[z][ks]));
    }
#pragma unroll
    for (int b = 0; b < 2; ++b) {
      wa[z][b] = L + z * 2 * Q_SLOT + 16384 + wn * 8192 + h * 1024 + q * 128 + ((b ^ (q >> 1)) << 6) + g * 32 + p * 8;
      asm volatile("" : "+v"(wa[z][b]));
    }
  }
  wait_vm<4 * (NSQ - 2)>();
  BARRIER();
  STAMP(0, 1);
  bf16x8 fx[2], fw[2][2];
  auto rd = [&](int buf, int J, int ks) {
    const int sl = J % NSQ;
    fx[buf] = ld8(xa[sl >> 1][ks] + (sl & 1) * Q_SLOT);
#pragma unroll
    for (int b = 0; b < 2; ++b) {
      const lds_p a = wa[sl >> 1][b] + (sl & 1) * Q_SLOT + ks * 2048;
      fw[buf][b] = cat8(trl(a), trl(a + 512));
    }
  };
  rd(0, 0, 0);
  static_for<0, 9>([&](auto Jc) {
    constexpr int J = decltype(Jc)::v;
    if constexpr (J + 1 < 9) {
      constexpr int top = J + NSQ - 2 < 8 ? J + NSQ - 2 : 8;
      wait_vm<4 * (top - (J + 1))>();
      BARRIER();
    }
    if (J == 3) STAMP(0, 2);
    if (J == 6) STAMP(0, 3);
    SB();
    static_for<0, 4>([&](auto Kc) {
      constexpr int ks = decltype(Kc)::v, cur = ks & 1;
      acc[0] = MFMA32(fx[cur], fw[cur][0], acc[0]);
      SB();
      if constexpr (ks < 3) rd(cur ^ 1, J, ks + 1);
      else if constexpr (J + 1 < 9) rd(cur ^ 1, J + 1, 0);
      SB();
      acc[1] = MFMA32(fx[cur], fw[cur][1], acc[1]);
      SB();
      if constexpr (J + NSQ - 1 < 9) {
        if constexpr (ks == 0) { issue(J + NSQ - 1, 0); issue(J + NSQ - 1, 1); }
        else if constexpr (ks < 3) issue(J + NSQ - 1, ks + 1);
      }
      SB();
    });
  });
  STAMP(0, 5);
  const float QSCALE = 0.23570226039551584f * 1.4426950408889634f;
  const float sc = ct * 2 + wn < 9 ? QSCALE : 1.0f;
  if (!(half && wn)) {
#pragma unroll
    for (int nb = 0; nb < 2; ++nb) {
      char* base = T + (size_t)h * TP + ((ct * 2 + wn) * 64 + nb * 32 + r) * 16;
#pragma unroll
      for (int s = 0; s < 2; ++s) {
        int step = rt * 8 + wm * 2 + s;
        u32x4 v;
#pragma unroll
        for (int i = 0; i < 4; ++i) v[i] = pk2(acc[nb][8 * s + 2 * i] * sc, acc[nb][8 * s + 2 * i + 1] * sc);
        st16(base + (size_t)step * (2 * TP), v);
      }
    }
  } else if (wm < 2) {
    int idx = wm * 64 + lane;
    unsigned c = (idx & 7) == 0 ? 0x3F803F80u : 0u;
    u32x4 v = {c, c, c, c};
    st16(T + (size_t)(rt * 16 + (idx >> 3)) * TP + (1728 + (idx & 7)) * 16, v);
  }
}

DI float dot2(unsigned a, unsigned b, float c) {
  return __builtin_amdgcn_fdot2_f32_bf16(__builtin_bit_cast(bf16x2_t, a), __builtin_bit_cast(bf16x2_t, b), c, false);
}
template <int C>
DI float dpp_add(float v) {
  return v + __builtin_bit_cast(float, __builtin_amdgcn_update_dpp(0, __builtin_bit_cast(int, v), C, 0xf, 0xf, true));
}
DI float sum8(float v) { return dpp_add<0x141>(dpp_add<0x4E>(dpp_add<0xB1>(v))); }
DI void ln_role(const char* __restrict__ X, const char* __restrict__ gw, char* __restrict__ XN, float* __restrict__ G,
                char* __restrict__ FL, int id, int wave, int lane) {
  const int c = lane & 7;
  const int row = id * 64 + wave * 8 + (lane >> 3);
  bf16x8 xv[9], gv[9];
  {
    const unsigned vx = (unsigned)(row * 1152 + c * 16), vg = (unsigned)(c * 16);
#pragma unroll
    for (int i = 0; i < 9; ++i) asm volatile("global_load_dwordx4 %0, %1, %2 offset:%3" : "=v"(xv[i]) : "v"(vx), "s"(X), "i"(i * 128) : "memory");
#pragma unroll
    for (int i = 0; i < 9; ++i) asm volatile("global_load_dwordx4 %0, %1, %2 offset:%3" : "=v"(gv[i]) : "v"(vg), "s"(gw), "i"(i * 128) : "memory");
  }
  st4(FL + id * 2048 + wave * 256 + lane * 4, 0u);
  char* dst = XN + ((size_t)(row >> 5) * 36 + (c >> 1)) * 1024 + ((c & 1) * 32 + (row & 31)) * 16;
  asm volatile("s_waitcnt vmcnt(10)" : "+v"(xv[0]), "+v"(xv[1]), "+v"(xv[2]), "+v"(xv[3]), "+v"(xv[4]), "+v"(xv[5]), "+v"(xv[6]), "+v"(xv[7]), "+v"(xv[8])::"memory");
  float sa[4] = {0.f, 0.f, 0.f, 0.f};
#pragma unroll
  for (int i = 0; i < 9; ++i)
#pragma unroll
    for (int j = 0; j < 4; ++j) sa[j] = dot2(__builtin_bit_cast(u32x4, xv[i])[j], 0x3F803F80u, sa[j]);
  STAMP(0, 1);
  const float mean = sum8((sa[0] + sa[1]) + (sa[2] + sa[3])) * (1.0f / 576.0f);
  const f32x2 m2 = {mean, mean};
  f32x2 xc[9][4];
  f32x2 qa[4] = {{0.f, 0.f}, {0.f, 0.f}, {0.f, 0.f}, {0.f, 0.f}};
#pragma unroll
  for (int i = 0; i < 9; ++i)
#pragma unroll
    for (int j = 0; j < 4; ++j) {
      const unsigned u = __builtin_bit_cast(u32x4, xv[i])[j];
      xc[i][j] = f32x2{__uint_as_float(u << 16), __uint_as_float(u & 0xffff0000u)} - m2;
      qa[j] = __builtin_elementwise_fma(xc[i][j], xc[i][j], qa[j]);
    }
  const f32x2 qq = (qa[0] + qa[1]) + (qa[2] + qa[3]);
  const float var = sum8(qq[0] + qq[1]) * (1.0f / 576.0f);
  STAMP(0, 3);
  const float rstd = 1.0f / __builtin_sqrtf(var + 1e-5f);
  const f32x2 r2 = {rstd, rstd};
#pragma unroll
  for (int i = 0; i < 9; ++i) {
    u32x4 v;
#pragma unroll
    for (int j = 0; j < 4; ++j) {
      const f32x2 y = xc[i][j] * r2;
      v[j] = pk2(y[0], y[1]);
    }
    st16(dst + i * 4096, v);
  }
  asm volatile("s_waitcnt vmcnt(10)" : "+v"(gv[0]), "+v"(gv[1]), "+v"(gv[2]), "+v"(gv[3]), "+v"(gv[4]), "+v"(gv[5]), "+v"(gv[6]), "+v"(gv[7]), "+v"(gv[8])::"memory");
  float da[4] = {0.f, 0.f, 0.f, 0.f};
#pragma unroll
  for (int i = 0; i < 9; ++i)
#pragma unroll
    for (int j = 0; j < 4; ++j) da[j] = dot2(__builtin_bit_cast(u32x4, xv[i])[j], __builtin_bit_cast(u32x4, gv[i])[j], da[j]);
  const float v = sum8((da[0] + da[1]) + (da[2] + da[3])) * 1.4426950408889634f;
  const float e = 1.0f + __builtin_amdgcn_exp2f((c & 1) ? v : -v);
  const float e1 = __builtin_fminf(e, 1.0e30f), e0 = 1.0f / e;
  st4(G + (c & 1) * 2048 + row, __float_as_uint((c & 1) ? e1 : e0));
  STAMP(0, 5);
  unsigned d0;
  {
    const char* pc;
    unsigned span;
    asm volatile("s_getpc_b64 s[20:21]\n\ts_mov_b32 %1, kernel1@rel32@lo+4\n\ts_add_u32 s20, s20, %1\n\ts_addc_u32 s21, s21, kernel1@rel32@hi+16"
                 : "={s[20:21]}"(pc), "=s"(span)::"scc");
    span = 0u - span;
    span = span < (1u << 18) ? span : 0u;
    const unsigned o = (unsigned)(((id >> 3) * 8 + wave) * 64 + lane) * 128u;
    gld4(d0, pc, o < span ? o : 0u);
  }
  asm volatile("" ::"v"(d0) : "memory");
}

__global__ __launch_bounds__(512, 2) void kernelQ(const char* __restrict__ X, const char* __restrict__ Wqkv,
                                                   const char* __restrict__ gw, char* __restrict__ T,
                                                   char* __restrict__ XN, float* __restrict__ G,
                                                   char* __restrict__ FL) {
  __shared__ __attribute__((aligned(16))) char lds[NSQ * Q_SLOT];
  const int wg = blockIdx.x;
  const int wave = __builtin_amdgcn_readfirstlane(threadIdx.x >> 6);
  const int lane = threadIdx.x & 63;
  if (wg < 224) {
    const int xcd = wg & 7, t = wg >> 3, rl = t & 3, cl = t >> 2;
    const int joff = (3 * (cl + rl) + (rl == 3 ? 1 : 0)) % 9;
    qkv_role(lds, X, Wqkv, T, (xcd & 3) * 4 + rl, (xcd >> 2) * 7 + cl, joff, wave, lane);
  } else {
    STAMP(0, 0);
    ln_role(X, gw, XN, G, FL, wg - 224, wave, lane);
  }
  STAMP(0, 6);
  STAMP(0, 7);
}

extern "C" void kernel_launch(void* const* d_in, const int* in_sizes, int n_in, void* d_out, int out_size, void* d_ws,
                              size_t ws_size, hipStream_t stream) {
  const char* X = (const char*)d_in[0];
  const char* Wqkv = (const char*)d_in[1];
  const char* Wo = (const char*)d_in[2];
  const char* W1 = (const char*)d_in[4];
  const unsigned short* b1 = (const unsigned short*)d_in[5];
  const char* W2 = (const char*)d_in[6];
  const unsigned short* b2 = (const unsigned short*)d_in[7];
  const char* gw = (const char*)d_in[8];
  char* ws = (char*)d_ws;
  char* T = ws + WS_T;
  char* H = ws + WS_H;
  char* O = ws + WS_O;
  char* P = ws + WS_P;
  char* XN = ws + WS_XN;
  char* FL = ws + WS_FL;
  float* G = (float*)(ws + WS_G);
  kernelQ<<<QGRID, 512, 0, stream>>>(X, Wqkv, gw, T, XN, G, FL);
  kernel1<<<512, 512, 0, stream>>>(T, O, XN, W1, b1, H, W2, b2, Wo, (const char*)G, P, FL, (char*)d_out);
}
```

```cpp
#include <hip/hip_runtime.h>
#include <stdint.h>

typedef __attribute__((ext_vector_type(8))) short bf16x8;
typedef __attribute__((ext_vector_type(4))) short s16x4;
typedef __attribute__((ext_vector_type(2))) __bf16 bf16x2_t;
typedef __attribute__((ext_vector_type(2))) float f32x2;
typedef __attribute__((ext_vector_type(4))) float f32x4;
typedef __attribute__((ext_vector_type(16))) float f32x16;
typedef __attribute__((ext_vector_type(4))) unsigned u32x4;
typedef __attribute__((ext_vector_type(2))) unsigned u32x2;
typedef __attribute__((address_space(3))) s16x4* lds_s16x4_p;
typedef __attribute__((address_space(3))) char* lds_p;
typedef __attribute__((address_space(3))) const bf16x8* lds_bf16x8_p;

#define STAMP(k, i)
#define STAMP2(i)
#define SST(i)
#define SS8(i)
#define SSD()
#define SSW()
#define DI __device__ __forceinline__
#define MFMA32(a, b, c) __builtin_amdgcn_mfma_f32_32x32x16_bf16((a), (b), (c), 0, 0, 0)
#define MFMA32K8(a, b, c) __builtin_amdgcn_mfma_f32_32x32x8bf16_1k((a), (b), (c), 0, 0, 0)
#define WAIT_VM(n) asm volatile("s_waitcnt vmcnt(" #n ")" ::: "memory")
#define BARRIER() __builtin_amdgcn_s_barrier()
#define SB() __builtin_amdgcn_sched_barrier(0)

constexpr int S_ = 2048, D_ = 576, F_ = 4608, NQKV = 1728;
constexpr int TSTEP = 640;
constexpr int TP = 1736 * 16;
constexpr int OEX = 64 * 32 * 1024;
constexpr size_t WS_T = 0;
constexpr size_t WS_H = 8u << 20;
constexpr size_t WS_O = 28u << 20;
constexpr size_t WS_G = 31u << 20;
constexpr size_t WS_FL = (31u << 20) + (64u << 10);
constexpr size_t WS_P = 32u << 20;
constexpr size_t WS_XN = 44u << 20;
constexpr size_t WS_XF = 47u << 20;
constexpr int FL_Q = 32768, FL_O = 36864, FL_P = 45056, FL_P2 = 65536;

DI unsigned pk2(float a, float b) {
  f32x2 v = {a, b};
  bf16x2_t r = __builtin_convertvector(v, bf16x2_t);
  return __builtin_bit_cast(unsigned, r);
}
DI float bf2f(unsigned short u) { return __uint_as_float(((unsigned)u) << 16); }
DI s16x4 tr_read(const char* p) {
  return __builtin_amdgcn_ds_read_tr16_b64_v4i16((lds_s16x4_p)(p));
}
DI s16x4 trl(lds_p p) { return __builtin_amdgcn_ds_read_tr16_b64_v4i16((lds_s16x4_p)p); }
DI bf16x8 ld8(lds_p p) { return *(lds_bf16x8_p)p; }
DI float xor32(float v) { return __shfl_xor(v, 32, 64); }

DI void glds16s(const char* sbase, unsigned voff, unsigned lds_dst) {
  unsigned keep;
  asm volatile("s_mov_b32 %0, m0\n\ts_mov_b32 m0, %3\n\ts_nop 0\n\tglobal_load_lds_dwordx4 %1, %2\n\ts_mov_b32 m0, %0"
               : "=&s"(keep)
               : "v"(voff), "s"(sbase), "s"(lds_dst)
               : "memory");
}
DI void glds16c(const char* sbase, unsigned voff, unsigned lds_dst) {
  unsigned keep;
  asm volatile("s_mov_b32 %0, m0\n\ts_mov_b32 m0, %3\n\ts_nop 0\n\tglobal_load_lds_dwordx4 %1, %2 sc1\n\ts_mov_b32 m0, %0"
               : "=&s"(keep)
               : "v"(voff), "s"(sbase), "s"(lds_dst)
               : "memory");
}
DI void touch(const char* sbase, unsigned voff, unsigned lds_dst) {
  unsigned keep;
  asm volatile("s_mov_b32 %0, m0\n\ts_mov_b32 m0, %3\n\ts_nop 0\n\tglobal_load_lds_dword %1, %2\n\ts_mov_b32 m0, %0"
               : "=&s"(keep)
               : "v"(voff), "s"(sbase), "s"(lds_dst)
               : "memory");
}
DI void gld16(bf16x8& d, const char* sbase, unsigned voff) {
  asm volatile("global_load_dwordx4 %0, %1, %2" : "=v"(d) : "v"(voff), "s"(sbase) : "memory");
}
DI void gld16c(bf16x8& d, const char* sbase, unsigned voff) {
  asm volatile("global_load_dwordx4 %0, %1, %2 sc1" : "=v"(d) : "v"(voff), "s"(sbase) : "memory");
}
DI void gld4c(unsigned& d, const char* sbase, unsigned voff) {
  asm volatile("global_load_dword %0, %1, %2 sc1" : "=v"(d) : "v"(voff), "s"(sbase) : "memory");
}
DI void gld4(unsigned& d, const char* sbase, unsigned voff) {
  asm volatile("global_load_dword %0, %1, %2" : "=v"(d) : "v"(voff), "s"(sbase) : "memory");
}
DI void gld4k(unsigned& d, const char* sbase, unsigned voff) {
  asm volatile("global_load_dword %0, %1, %2" : "+v"(d) : "v"(voff), "s"(sbase) : "memory");
}
DI unsigned lds_addr(const void* p) { return (unsigned)(uintptr_t)(lds_p)p; }

DI void st16(void* p, u32x4 v) { asm volatile("global_store_dwordx4 %0, %1, off sc1\n\ts_nop 1" ::"v"(p), "v"(v) : "memory"); }
DI void st16p(void* p, u32x4 v) { asm volatile("global_store_dwordx4 %0, %1, off\n\ts_nop 1" ::"v"(p), "v"(v) : "memory"); }
DI void st4(void* p, unsigned v) { asm volatile("global_store_dword %0, %1, off sc1" ::"v"(p), "v"(v) : "memory"); }

DI int fresh_lane() {
  int l;
  asm volatile("v_mbcnt_lo_u32_b32 %0, -1, 0\n\tv_mbcnt_hi_u32_b32 %0, -1, %0" : "=v"(l));
  return l;
}
template <int I>
struct IC { static constexpr int v = I; };
template <int I, int N, class F>
DI void static_for(F&& f) {
  if constexpr (I < N) {
    f(IC<I>{});
    static_for<I + 1, N>(f);
  }
}
template <int N>
DI void wait_vm() { asm volatile("s_waitcnt vmcnt(%0)" ::"i"(N) : "memory"); }
template <int N>
DI void wait_vm1(bf16x8& a) { asm volatile("s_waitcnt vmcnt(%1)" : "+v"(a) : "i"(N) : "memory"); }
DI void spin_flags(const char* base, unsigned voff) {
#pragma unroll 1
  for (int it = 0; it < (1 << 17); ++it) {
    unsigned f;
    gld4c(f, base, voff);
    asm volatile("s_waitcnt vmcnt(0)" : "+v"(f)::"memory");
    if (!__ballot(f != 1u)) break;
    __builtin_amdgcn_s_sleep(4);
  }
}
DI bf16x8 cat8(s16x4 lo, s16x4 hi) { return __builtin_shufflevector(lo, hi, 0, 1, 2, 3, 4, 5, 6, 7); }

#define SENT 0
DI unsigned nsent(unsigned v) { return SENT ? __builtin_elementwise_min(v, 0xfffffffeu) : v; }
DI unsigned umax4(bf16x8 x, unsigned m) {
  const u32x4 v = __builtin_bit_cast(u32x4, x);
  m = __builtin_elementwise_max(__builtin_elementwise_max(v[0], v[1]), m);
  return __builtin_elementwise_max(__builtin_elementwise_max(v[2], v[3]), m);
}
DI f32x16 qk_tile(lds_p kb, lds_p ke, bf16x8 qf, s16x4 qe, f32x16 c) {
  s16x4 lo = trl(kb);
  s16x4 hi = trl(kb + 64);
  s16x4 ex = trl(ke);
  bf16x8 kf = __builtin_shufflevector(lo, hi, 0, 1, 2, 3, 4, 5, 6, 7);
  f32x16 s = MFMA32(kf, qf, c);
  s = MFMA32K8(ex, qe, s);
  return s;
}
DI f32x16 pv_tile(lds_p vb, const f32x16& pe, f32x16 o) {
  bf16x8 v0 = ld8(vb);
  bf16x8 v1 = ld8(vb + TSTEP);
  u32x4 p0, p1;
#pragma unroll
  for (int i = 0; i < 4; ++i) {
    p0[i] = pk2(pe[2 * i], pe[2 * i + 1]);
    p1[i] = pk2(pe[8 + 2 * i], pe[8 + 2 * i + 1]);
  }
  o = MFMA32(v0, __builtin_bit_cast(bf16x8, p0), o);
  o = MFMA32(v1, __builtin_bit_cast(bf16x8, p1), o);
  return o;
}
struct KFrag { bf16x8 f; s16x4 e; };
struct VFrag { bf16x8 v0, v1; };
DI KFrag load_k(lds_p kb, lds_p ke) {
  KFrag k;
  s16x4 lo = trl(kb);
  s16x4 hi = trl(kb + 64);
  k.e = trl(ke);
  k.f = __builtin_shufflevector(lo, hi, 0, 1, 2, 3, 4, 5, 6, 7);
  return k;
}
DI VFrag load_v(lds_p vb) {
  VFrag v;
  v.v0 = ld8(vb);
  v.v1 = ld8(vb + TSTEP);
  return v;
}
#define EXP2(x) __builtin_amdgcn_exp2f(x)
#define PTRUNC 1
DI unsigned pkp(float a, float b) {
  return __builtin_amdgcn_perm(__float_as_uint(b), __float_as_uint(a), 0x07060302u);
}
#define APM 2
#define APMASK 0x77
#define PHI(i) if constexpr ((PM >> (i)) & 1) __builtin_amdgcn_s_setprio(APM)
#define PLO(i) if constexpr ((PM >> (i)) & 1) __builtin_amdgcn_s_setprio(0)
template <int PM>
DI void tile_step(const f32x16& s, f32x16& sn, const KFrag& k, const VFrag& v, bf16x8 qf, s16x4 qe,
                  f32x16& o) {
  f32x16 negm;
#pragma unroll
  for (int i = 0; i < 16; ++i) negm[i] = 0.f;
  u32x4 p0, p1;
  p0[0] = pkp(EXP2(s[0]), EXP2(s[1]));
  p0[1] = pkp(EXP2(s[2]), EXP2(s[3]));
  SB();
  PHI(0);
  SB();
  f32x16 t = MFMA32(k.f, qf, negm);
  SB();
  PLO(0);
  SB();
  p0[2] = pkp(EXP2(s[4]), EXP2(s[5]));
  p0[3] = pkp(EXP2(s[6]), EXP2(s[7]));
  SB();
  PHI(1);
  SB();
  o = MFMA32(v.v0, __builtin_bit_cast(bf16x8, p0), o);
  SB();
  PLO(1);
  SB();
  p1[0] = pkp(EXP2(s[8]), EXP2(s[9]));
  p1[1] = pkp(EXP2(s[10]), EXP2(s[11]));
  SB();
  PHI(2);
  SB();
  t = MFMA32K8(k.e, qe, t);
  SB();
  PLO(2);
  SB();
  p1[2] = pkp(EXP2(s[12]), EXP2(s[13]));
  p1[3] = pkp(EXP2(s[14]), EXP2(s[15]));
  SB();
  PHI(3);
  SB();
  o = MFMA32(v.v1, __builtin_bit_cast(bf16x8, p1), o);
  SB();
  PLO(3);
  SB();
  sn = t;
}
DI float rowmax16(const f32x16& s) {
  float m = __builtin_fmaxf(s[0], s[1]);
#pragma unroll
  for (int i = 2; i < 16; ++i) m = __builtin_fmaxf(m, s[i]);
  return __builtin_fmaxf(m, xor32(m));
}

constexpr int AT_SLOT = 20480;
constexpr int at_cnt(int c, int grp) { return 2 + (((c & 1) == grp) ? 1 : 0); }
constexpr int at_off(int tt) { return ((tt >> 3) & 1) * AT_SLOT + (tt & 7) * 1280; }

#define TOUCH_AT 9
#define ADEF 0
#define OPRIV 0
#define AEP 3
#define PRE_AT 0
DI void attn_role(char* lds, const char* T, char* O, char* P, const char* __restrict__ G, char* FL, const char* __restrict__ W2,
                  const char* __restrict__ Wo, int wg, int wave, int lane) {
  const int head = (wg & 7) * 4 + ((wg >> 3) & 3);
  const int qt = wg >> 5;
  const int r = lane & 31, h = lane >> 5;
  const int grp = wave >> 2;
  const unsigned lbase = lds_addr(lds);
  const int p1 = wave + 8, p2 = 16 + (wave & 3);
  auto mkoff = [&](int pc, int first) {
    int isv = pc >= 10;
    int sl = (pc - 10 * isv) * 64 + lane;
    int seg = sl / 20, row = sl - seg * 20;
    int slot = row < 18 ? first + 576 * isv + head * 18 + row : 1728 + row - 18;
    return (unsigned)(seg * TP + slot * 16);
  };
  const unsigned v0 = mkoff(wave, 576), v1 = mkoff(p1, 576), v2 = mkoff(p2, 576);
  auto issue = [&](int c) {
    unsigned dst = lbase + (c & 3) * AT_SLOT;
    const char* src = T + (size_t)c * (32 * TP);
    glds16s(src, v0, dst + wave * 1024);
    glds16s(src, v1, dst + p1 * 1024);
    if ((c & 1) == grp) glds16s(src, v2, dst + p2 * 1024);
  };
  STAMP(1, 1);
  unsigned gbits;
  gld4(gbits, G + (qt * 256 + wave * 32) * 4, r * 4);
  {
    const char* src = T + (size_t)qt * (32 * TP);
    glds16s(src, mkoff(wave, 0), lbase + 3 * AT_SLOT + wave * 1024);
    if (wave < 2) glds16s(src, mkoff(p1, 0), lbase + 3 * AT_SLOT + p1 * 1024);
  }
  issue(0);
  issue(1);
  issue(2);
  const int q = (lane >> 2) & 3, p = lane & 3, g = (lane >> 4) & 1;
  const lds_p L = (lds_p)lds;
  const lds_p kb = L + g * TSTEP + (p & 1) * (TSTEP / 2) + (8 * h + q) * 16 + (p >> 1) * 8;
  const lds_p ke = L + g * TSTEP + (p & 1) * (TSTEP / 2) + (16 + q) * 16 + (p >> 1) * 8;
  const lds_p vb = L + 10240 + h * (TSTEP / 2) + (r < 19 ? r : 19) * 16;
  lds_p kb2 = kb + 2 * AT_SLOT, ke2 = ke + 2 * AT_SLOT, vb2 = vb + 2 * AT_SLOT;
  asm volatile("" : "+v"(kb2), "+v"(ke2), "+v"(vb2));
  asm volatile("s_waitcnt vmcnt(5)" : "+v"(gbits)::"memory");
  BARRIER();
  STAMP(1, 2);
  bf16x8 qf;
  s16x4 qe;
  {
    KFrag qq = load_k(kb2 + AT_SLOT + wave * 1280, ke2 + AT_SLOT + wave * 1280);
    qf = qq.f;
    qe[0] = h ? (short)0 : qq.e[0];
    qe[1] = h ? (short)0 : qq.e[1];
    qe[2] = 0;
    qe[3] = 0;
  }

  const float gate = __uint_as_float(gbits);
  f32x16 zero;
#pragma unroll
  for (int i = 0; i < 16; ++i) zero[i] = 0.f;
  f32x16 o = zero;
  f32x16 sA = qk_tile(kb, ke, qf, qe, zero);
  {
    const float nx = -rowmax16(sA);
    const unsigned mb = (__float_as_uint(nx) + (nx > 0.f ? 0xffffu : 0u)) >> 16;
    const float nm = __uint_as_float(mb << 16);
#pragma unroll
    for (int i = 0; i < 16; ++i) sA[i] += nm;
    qe[2] = h ? (short)0 : (short)mb;
  }
  KFrag kA, kB;
  VFrag vA, vB;
  kB = load_k(kb + 1280, ke + 1280);
  vA = load_v(vb);
  f32x16 sB;
#pragma unroll 1
  for (int cc = 0; cc < 8; cc += 4) {
    static_for<0, 32>([&](auto Tc) {
      constexpr int tt = decltype(Tc)::v, ci = tt >> 3;
      if constexpr ((tt & 7) == 6) {
        if (cc == 0 || ci < 2) {
          if (grp == 0) wait_vm<at_cnt(ci + 2, 0)>(); else wait_vm<at_cnt(ci + 2, 1)>();
        } else if (ci == 2) wait_vm<0>();
        if (cc == 0 || ci < 3) BARRIER();
        if constexpr (ci == TOUCH_AT) {
          if (cc == 0) {
            const int l2 = fresh_lane();
            const char* src = W2 + (size_t)((wg & 7) >> 1) * (1152 * 1152) + (wg >> 3) * 41472;
            const unsigned dst = lbase + ((ci + 3) & 3) * AT_SLOT + wave * 1024;
            touch(src, (wave * 41 + l2 - (l2 > 40 ? 24 : 0) - (wave >> 2) * 4) * 128u, dst);
            if (wave == 0) touch(Wo + ((wg & 7) >> 1) * (144 * 1152) + (wg >> 3) * 5184, (l2 > 39 ? ((wg & 8) ? 39 : 40) : l2) * 128u + ((wg & 8) ? 64u : 0u), dst);
          }
        }
        if (cc == 0 || ci == 0) issue(cc + ci + 3);
      }
      constexpr int t1 = (tt + 1) & 31, t2 = (tt + 2) & 31;
      const lds_p kp = (t2 < 16 ? kb : kb2) + at_off(t2);
      const lds_p ep = (t2 < 16 ? ke : ke2) + at_off(t2);
      const lds_p vp = (t1 < 16 ? vb : vb2) + at_off(t1);
      if constexpr ((tt & 1) == 0) {
        kA = load_k(kp, ep);
        vB = load_v(vp);
        SB();
        tile_step<APMASK & 15>(sA, sB, kB, vA, qf, qe, o);
      } else {
        kB = load_k(kp, ep);
        vA = load_v(vp);
        SB();
        tile_step<(APMASK >> 4) & 15>(sB, sA, kA, vB, qf, qe, o);
      }
    });
    if (cc == 0) STAMP(1, 3);
  }
  STAMP(1, 4);
  STAMP(1, 5);
  __builtin_amdgcn_s_setprio(AEP);
  auto rowsum = [&]() { return __uint_as_float(__builtin_amdgcn_permlane32_swap(__float_as_uint(o[10]), __float_as_uint(o[10]), false, false)[0]); };
  float l = rowsum();
  auto ab = [&](int i) { return __builtin_fabsf(o[i]) + __builtin_fabsf(o[i + 1]); };
  bool bad = !(((ab(0) + ab(2)) + (ab(4) + ab(6))) + __builtin_fmaf(l, 1.0e7f, ab(8)) < 1.0e37f);
  const int ob = (qt * 8 + wave) * 32 + head;
  auto publish = [&]() {
    const float inv = gate * __builtin_amdgcn_rcpf(l);
    u32x4 ov;
#pragma unroll
    for (int i = 0; i < 4; ++i) ov[i] = pk2(o[2 * i] * inv, o[2 * i + 1] * inv);
    const unsigned vo = lane * 16;
    asm volatile("global_store_dwordx4 %0, %1, %2 sc1\n\ts_nop 1" ::"v"(vo), "v"(ov), "s"(O + ob * 1024) : "memory");
    const unsigned oe = pk2(o[8] * inv, o[9] * inv);
    if (h == 0) asm volatile("global_store_dword %0, %1, %2 sc1" ::"v"(vo), "v"(oe), "s"(O + (OEX + (ob >> 3) * 1024 + ((head >> 2) & 1) * 512 + (head & 3) * 4)) : "memory");
    STAMP(1, 6);
    const unsigned vf = (lane >> 1) * 4096 + (lane & 1) * 512;
    const char* fb = FL + (FL_P2 + (qt * 2 + (wave >> 2)) * 16384 + (wave & 3) * 1024 + (head >> 3) * 128 + 32 + (head & 7) * 4);
    wait_vm<0>();
    if (lane == 0) asm volatile("global_store_dword %0, %1, %2 sc1" ::"v"(vo), "v"(1u), "s"(FL + (FL_O + ob * 4)) : "memory");
  };
  const bool mybad = __any(bad);
  if (!mybad) publish();
  {
    int* fl = (int*)(lds + AT_SLOT);
    if (lane == 0) fl[wave] = mybad ? 1 : 0;
    __syncthreads();
    int v = fl[lane & 7];
    bad = __any(v != 0);
  }
  if (bad) {
    float m = -3.0e38f;
    o = zero;
    qe[2] = 0;
#pragma unroll 1
    for (int c = 0; c < 8; ++c) {
      __syncthreads();
      {
        const char* src = T + (size_t)c * (32 * TP);
        glds16s(src, v0, lbase + wave * 1024);
        glds16s(src, v1, lbase + p1 * 1024);
        if (grp == 0) glds16s(src, v2, lbase + p2 * 1024);
      }
      wait_vm<0>();
      __syncthreads();
#pragma unroll 1
      for (int t = 0; t < 8; ++t) {
        f32x16 s = qk_tile(kb + t * 1280, ke + t * 1280, qf, qe, zero);
        float mn = __builtin_fmaxf(m, rowmax16(s));
        float alpha = __builtin_amdgcn_exp2f(m - mn);
        m = mn;
        f32x16 pe;
#pragma unroll
        for (int i = 0; i < 16; ++i) { pe[i] = __builtin_amdgcn_exp2f(s[i] - mn); o[i] *= alpha; }
        o = pv_tile(vb + t * 1280, pe, o);
      }
    }
    l = rowsum();
    if (mybad) publish();
  }
  STAMP(1, 7);
}

#define QKV_PRIO 2
#define ATT_PRIO 0
#define SKIP1 0
#define MLP_PRIO 1
#define GELU_PRIO MLP_PRIO
#define MLP2_PRIO MLP_PRIO
#define P1HI4 0
#define P1HI5 0
#define P2HI4 0
#define P2HI5 0
#define HI_PRIO 3
#define PF2_AT 20
#define PSLP 2
#define XE_AT 33
constexpr int CK3 = 30;
constexpr int M_IT = 36;
constexpr int M2_NT = 41;
struct M2S { int slot[M2_NT]; int it[M2_NT]; };
constexpr M2S m2_sched(int U) {
  M2S r{};
  const int E = 2 * U - 2;
  int next = 0;
  for (; next < 4; ++next) { r.slot[next] = next; r.it[next] = -1; }
  for (int j = 1; next < M2_NT; ++j) {
    r.slot[next] = r.slot[j - 1];
    r.it[next] = j;
    ++next;
    if (next < M2_NT && (j == E || j == E + 1)) { r.slot[next] = 4 + (j - E); r.it[next] = j; ++next; }
  }
  return r;
}
constexpr int m2_ck1(int U) { return 2 * U - 4; }
constexpr int m2_ck2(int U) { return (U == 5 ? 26 : 2 * U + 8) - 4; }
constexpr int m2_wait(int U, int kh, int j, int mode) {
  const M2S sc = m2_sched(U);
  const int U2 = U == 5 ? 2 : 3;
  int n = 0, last = -1;
  for (int i = -1; i < j + mode; ++i) {
    if (i == m2_ck1(U) - 2 || i == m2_ck2(U) - 2 || (SENT == 2 && i == CK3 - 2)) { if (mode == 1 && i == j - 2) last = n; ++n; }
    for (int s = 0; s < M2_NT; ++s)
      if (sc.it[s] == i) {
        const int c = (U2 == 3 && (s & 1) == kh) ? 2 : 1;
        n += c;
        if (mode == 0 && s <= j + 1) last = n - 1;
      }
    if (i == -1) n += 2 * U;
    if (i >= 0 && i < j && i + 4 >= 2 * U && i + 4 < M_IT) { if (mode == 0 && i + 4 <= j) last = n; ++n; }
  }
  return n - 1 - last;
}
constexpr bool m2_need(int U, int j) { return j >= 3; }

template <int N>
DI void pin_acc(f32x16* a) {
  if constexpr (N == 5)
    asm volatile("" : "+{v[0:15]}"(a[0]), "+{v[16:31]}"(a[1]), "+{v[32:47]}"(a[2]), "+{v[48:63]}"(a[3]), "+{v[64:79]}"(a[4]));
  else
    asm volatile("" : "+{v[0:15]}"(a[0]), "+{v[16:31]}"(a[1]), "+{v[32:47]}"(a[2]), "+{v[48:63]}"(a[3]));
}
#define XLD(R) asm volatile("global_load_dwordx4 %0, %1, %2 sc1" : "={" R "}"(d) : "v"(voff), "s"(sbase) : "memory")
#define XLP(R) asm volatile("global_load_dwordx4 %0, %1, %2" : "={" R "}"(d) : "v"(voff), "s"(sbase) : "memory")
template <int I>
DI void xloadp(bf16x8& d, const char* sbase, unsigned voff) {
  if constexpr (I == 0) XLP("v[80:83]");
  else if constexpr (I == 1) XLP("v[84:87]");
  else if constexpr (I == 2) XLP("v[88:91]");
  else XLP("v[92:95]");
}
template <int I>
DI void xload(bf16x8& d, const char* sbase, unsigned voff) {
  if constexpr (I == 0) XLD("v[80:83]");
  else if constexpr (I == 1) XLD("v[84:87]");
  else if constexpr (I == 2) XLD("v[88:91]");
  else XLD("v[92:95]");
}
template <int I>
DI void pload(bf16x8& d, const char* sbase, unsigned voff) {
  if constexpr (I == 0) XLD("v[48:51]");
  else if constexpr (I == 1) XLD("v[52:55]");
  else if constexpr (I == 2) XLD("v[56:59]");
  else if constexpr (I == 3) XLD("v[60:63]");
  else if constexpr (I == 4) XLD("v[64:67]");
  else if constexpr (I == 5) XLD("v[68:71]");
  else if constexpr (I == 6) XLD("v[72:75]");
  else if constexpr (I == 7) XLD("v[76:79]");
  else if constexpr (I == 8) XLD("v[80:83]");
  else if constexpr (I == 9) XLD("v[84:87]");
  else if constexpr (I == 10) XLD("v[88:91]");
  else if constexpr (I == 11) XLD("v[92:95]");
  else if constexpr (I == 12) XLD("v[96:99]");
  else if constexpr (I == 13) XLD("v[100:103]");
  else if constexpr (I == 14) XLD("v[104:107]");
  else if constexpr (I == 15) XLD("v[108:111]");
  else if constexpr (I == 16) XLD("v[112:115]");
  else if constexpr (I == 17) XLD("v[116:119]");
}
template <int I>
DI void ppin(bf16x8& d) {
  if constexpr (I == 0) asm volatile("" : "+{v[48:51]}"(d));
  else if constexpr (I == 1) asm volatile("" : "+{v[52:55]}"(d));
  else if constexpr (I == 2) asm volatile("" : "+{v[56:59]}"(d));
  else if constexpr (I == 3) asm volatile("" : "+{v[60:63]}"(d));
  else if constexpr (I == 4) asm volatile("" : "+{v[64:67]}"(d));
  else if constexpr (I == 5) asm volatile("" : "+{v[68:71]}"(d));
  else if constexpr (I == 6) asm volatile("" : "+{v[72:75]}"(d));
  else if constexpr (I == 7) asm volatile("" : "+{v[76:79]}"(d));
  else if constexpr (I == 8) asm volatile("" : "+{v[80:83]}"(d));
  else if constexpr (I == 9) asm volatile("" : "+{v[84:87]}"(d));
  else if constexpr (I == 10) asm volatile("" : "+{v[88:91]}"(d));
  else if constexpr (I == 11) asm volatile("" : "+{v[92:95]}"(d));
  else if constexpr (I == 12) asm volatile("" : "+{v[96:99]}"(d));
  else if constexpr (I == 13) asm volatile("" : "+{v[100:103]}"(d));
  else if constexpr (I == 14) asm volatile("" : "+{v[104:107]}"(d));
  else if constexpr (I == 15) asm volatile("" : "+{v[108:111]}"(d));
  else if constexpr (I == 16) asm volatile("" : "+{v[112:115]}"(d));
  else if constexpr (I == 17) asm volatile("" : "+{v[116:119]}"(d));
}
#define XDS(R) asm volatile("ds_read_b128 %0, %1 offset:%2" : "={" R "}"(d) : "v"(a), "i"(OFF) : "memory")
template <int I, int OFF>
DI void xstash(bf16x8& d, unsigned a) {
  if constexpr (I == 0) XDS("v[80:83]");
  else if constexpr (I == 1) XDS("v[84:87]");
  else if constexpr (I == 2) XDS("v[88:91]");
  else XDS("v[92:95]");
}
template <int I>
DI void pin_x1(bf16x8& x) {
  if constexpr (I == 0) asm volatile("" : "+{v[80:83]}"(x));
  else if constexpr (I == 1) asm volatile("" : "+{v[84:87]}"(x));
  else if constexpr (I == 2) asm volatile("" : "+{v[88:91]}"(x));
  else asm volatile("" : "+{v[92:95]}"(x));
}
template <int I>
DI void pin_w(bf16x8& w) {
  if constexpr (I == 0) asm volatile("" : "+{v[96:99]}"(w));
  else if constexpr (I == 1) asm volatile("" : "+{v[100:103]}"(w));
  else if constexpr (I == 2) asm volatile("" : "+{v[104:107]}"(w));
  else if constexpr (I == 3) asm volatile("" : "+{v[108:111]}"(w));
  else asm volatile("" : "+{v[112:115]}"(w));
}
template <int N>
DI void pair_sum(f32x16* a, unsigned exw, unsigned exr, int kh) {
  if constexpr (N == 5)
    asm volatile(
      "s_nop 15\ns_nop 3\ns_cmp_lg_u32 %[kh], 0\ns_cbranch_scc1 XU%=\nv_cvt_pk_bf16_f32 v80, v8, v9\nv_cvt_pk_bf16_f32 v81, v10, v11\n"
      "v_cvt_pk_bf16_f32 v82, v12, v13\nv_cvt_pk_bf16_f32 v83, v14, v15\nds_write_b128 %[w], v[80:83] offset:0\n"
      "v_cvt_pk_bf16_f32 v84, v24, v25\nv_cvt_pk_bf16_f32 v85, v26, v27\nv_cvt_pk_bf16_f32 v86, v28, v29\nv_cvt_pk_bf16_f32 v87, v30, v31\n"
      "ds_write_b128 %[w], v[84:87] offset:1024\nv_cvt_pk_bf16_f32 v88, v40, v41\nv_cvt_pk_bf16_f32 v89, v42, v43\n"
      "v_cvt_pk_bf16_f32 v90, v44, v45\nv_cvt_pk_bf16_f32 v91, v46, v47\nds_write_b128 %[w], v[88:91] offset:2048\n"
      "v_cvt_pk_bf16_f32 v92, v56, v57\nv_cvt_pk_bf16_f32 v93, v58, v59\nv_cvt_pk_bf16_f32 v94, v60, v61\nv_cvt_pk_bf16_f32 v95, v62, v63\n"
      "ds_write_b128 %[w], v[92:95] offset:3072\nv_cvt_pk_bf16_f32 v96, v72, v73\nv_cvt_pk_bf16_f32 v97, v74, v75\n"
      "v_cvt_pk_bf16_f32 v98, v76, v77\nv_cvt_pk_bf16_f32 v99, v78, v79\nds_write_b128 %[w], v[96:99] offset:4096\ns_waitcnt lgkmcnt(0)\n"
      "s_barrier\nds_read_b128 v[80:83], %[r] offset:0\nds_read_b128 v[84:87], %[r] offset:1024\nds_read_b128 v[88:91], %[r] offset:2048\n"
      "ds_read_b128 v[92:95], %[r] offset:3072\nds_read_b128 v[96:99], %[r] offset:4096\ns_waitcnt lgkmcnt(4)\nv_lshlrev_b32 v8, 16, v80\n"
      "v_and_b32 v9, 0xffff0000, v80\nv_lshlrev_b32 v10, 16, v81\nv_and_b32 v11, 0xffff0000, v81\nv_lshlrev_b32 v12, 16, v82\n"
      "v_and_b32 v13, 0xffff0000, v82\nv_lshlrev_b32 v14, 16, v83\nv_and_b32 v15, 0xffff0000, v83\nv_pk_add_f32 v[0:1], v[8:9], v[0:1]\n"
      "v_pk_add_f32 v[2:3], v[10:11], v[2:3]\nv_pk_add_f32 v[4:5], v[12:13], v[4:5]\nv_pk_add_f32 v[6:7], v[14:15], v[6:7]\n"
      "s_waitcnt lgkmcnt(3)\nv_lshlrev_b32 v24, 16, v84\nv_and_b32 v25, 0xffff0000, v84\nv_lshlrev_b32 v26, 16, v85\n"
      "v_and_b32 v27, 0xffff0000, v85\nv_lshlrev_b32 v28, 16, v86\nv_and_b32 v29, 0xffff0000, v86\nv_lshlrev_b32 v30, 16, v87\n"
      "v_and_b32 v31, 0xffff0000, v87\nv_pk_add_f32 v[16:17], v[24:25], v[16:17]\nv_pk_add_f32 v[18:19], v[26:27], v[18:19]\n"
      "v_pk_add_f32 v[20:21], v[28:29], v[20:21]\nv_pk_add_f32 v[22:23], v[30:31], v[22:23]\ns_waitcnt lgkmcnt(2)\n"
      "v_lshlrev_b32 v40, 16, v88\nv_and_b32 v41, 0xffff0000, v88\nv_lshlrev_b32 v42, 16, v89\nv_and_b32 v43, 0xffff0000, v89\n"
      "v_lshlrev_b32 v44, 16, v90\nv_and_b32 v45, 0xffff0000, v90\nv_lshlrev_b32 v46, 16, v91\nv_and_b32 v47, 0xffff0000, v91\n"
      "v_pk_add_f32 v[32:33], v[40:41], v[32:33]\nv_pk_add_f32 v[34:35], v[42:43], v[34:35]\nv_pk_add_f32 v[36:37], v[44:45], v[36:37]\n"
      "v_pk_add_f32 v[38:39], v[46:47], v[38:39]\ns_waitcnt lgkmcnt(1)\nv_lshlrev_b32 v56, 16, v92\nv_and_b32 v57, 0xffff0000, v92\n"
      "v_lshlrev_b32 v58, 16, v93\nv_and_b32 v59, 0xffff0000, v93\nv_lshlrev_b32 v60, 16, v94\nv_and_b32 v61, 0xffff0000, v94\n"
      "v_lshlrev_b32 v62, 16, v95\nv_and_b32 v63, 0xffff0000, v95\nv_pk_add_f32 v[48:49], v[56:57], v[48:49]\n"
      "v_pk_add_f32 v[50:51], v[58:59], v[50:51]\nv_pk_add_f32 v[52:53], v[60:61], v[52:53]\nv_pk_add_f32 v[54:55], v[62:63], v[54:55]\n"
      "s_waitcnt lgkmcnt(0)\nv_lshlrev_b32 v72, 16, v96\nv_and_b32 v73, 0xffff0000, v96\nv_lshlrev_b32 v74, 16, v97\n"
      "v_and_b32 v75, 0xffff0000, v97\nv_lshlrev_b32 v76, 16, v98\nv_and_b32 v77, 0xffff0000, v98\nv_lshlrev_b32 v78, 16, v99\n"
      "v_and_b32 v79, 0xffff0000, v99\nv_pk_add_f32 v[64:65], v[72:73], v[64:65]\nv_pk_add_f32 v[66:67], v[74:75], v[66:67]\n"
      "v_pk_add_f32 v[68:69], v[76:77], v[68:69]\nv_pk_add_f32 v[70:71], v[78:79], v[70:71]\ns_branch XE%=\nXU%=:\n"
      "v_cvt_pk_bf16_f32 v80, v0, v1\nv_cvt_pk_bf16_f32 v81, v2, v3\nv_cvt_pk_bf16_f32 v82, v4, v5\nv_cvt_pk_bf16_f32 v83, v6, v7\n"
      "ds_write_b128 %[w], v[80:83] offset:0\nv_cvt_pk_bf16_f32 v84, v16, v17\nv_cvt_pk_bf16_f32 v85, v18, v19\n"
      "v_cvt_pk_bf16_f32 v86, v20, v21\nv_cvt_pk_bf16_f32 v87, v22, v23\nds_write_b128 %[w], v[84:87] offset:1024\n"
      "v_cvt_pk_bf16_f32 v88, v32, v33\nv_cvt_pk_bf16_f32 v89, v34, v35\nv_cvt_pk_bf16_f32 v90, v36, v37\nv_cvt_pk_bf16_f32 v91, v38, v39\n"
      "ds_write_b128 %[w], v[88:91] offset:2048\nv_cvt_pk_bf16_f32 v92, v48, v49\nv_cvt_pk_bf16_f32 v93, v50, v51\n"
      "v_cvt_pk_bf16_f32 v94, v52, v53\nv_cvt_pk_bf16_f32 v95, v54, v55\nds_write_b128 %[w], v[92:95] offset:3072\n"
      "v_cvt_pk_bf16_f32 v96, v64, v65\nv_cvt_pk_bf16_f32 v97, v66, v67\nv_cvt_pk_bf16_f32 v98, v68, v69\nv_cvt_pk_bf16_f32 v99, v70, v71\n"
      "ds_write_b128 %[w], v[96:99] offset:4096\ns_waitcnt lgkmcnt(0)\ns_barrier\nds_read_b128 v[80:83], %[r] offset:0\n"
      "ds_read_b128 v[84:87], %[r] offset:1024\nds_read_b128 v[88:91], %[r] offset:2048\nds_read_b128 v[92:95], %[r] offset:3072\n"
      "ds_read_b128 v[96:99], %[r] offset:4096\ns_waitcnt lgkmcnt(4)\nv_lshlrev_b32 v0, 16, v80\nv_and_b32 v1, 0xffff0000, v80\n"
      "v_lshlrev_b32 v2, 16, v81\nv_and_b32 v3, 0xffff0000, v81\nv_lshlrev_b32 v4, 16, v82\nv_and_b32 v5, 0xffff0000, v82\n"
      "v_lshlrev_b32 v6, 16, v83\nv_and_b32 v7, 0xffff0000, v83\nv_pk_add_f32 v[0:1], v[0:1], v[8:9]\n"
      "v_pk_add_f32 v[2:3], v[2:3], v[10:11]\nv_pk_add_f32 v[4:5], v[4:5], v[12:13]\nv_pk_add_f32 v[6:7], v[6:7], v[14:15]\n"
      "s_waitcnt lgkmcnt(3)\nv_lshlrev_b32 v16, 16, v84\nv_and_b32 v17, 0xffff0000, v84\nv_lshlrev_b32 v18, 16, v85\n"
      "v_and_b32 v19, 0xffff0000, v85\nv_lshlrev_b32 v20, 16, v86\nv_and_b32 v21, 0xffff0000, v86\nv_lshlrev_b32 v22, 16, v87\n"
      "v_and_b32 v23, 0xffff0000, v87\nv_pk_add_f32 v[16:17], v[16:17], v[24:25]\nv_pk_add_f32 v[18:19], v[18:19], v[26:27]\n"
      "v_pk_add_f32 v[20:21], v[20:21], v[28:29]\nv_pk_add_f32 v[22:23], v[22:23], v[30:31]\ns_waitcnt lgkmcnt(2)\n"
      "v_lshlrev_b32 v32, 16, v88\nv_and_b32 v33, 0xffff0000, v88\nv_lshlrev_b32 v34, 16, v89\nv_and_b32 v35, 0xffff0000, v89\n"
      "v_lshlrev_b32 v36, 16, v90\nv_and_b32 v37, 0xffff0000, v90\nv_lshlrev_b32 v38, 16, v91\nv_and_b32 v39, 0xffff0000, v91\n"
      "v_pk_add_f32 v[32:33], v[32:33], v[40:41]\nv_pk_add_f32 v[34:35], v[34:35], v[42:43]\nv_pk_add_f32 v[36:37], v[36:37], v[44:45]\n"
      "v_pk_add_f32 v[38:39], v[38:39], v[46:47]\ns_waitcnt lgkmcnt(1)\nv_lshlrev_b32 v48, 16, v92\nv_and_b32 v49, 0xffff0000, v92\n"
      "v_lshlrev_b32 v50, 16, v93\nv_and_b32 v51, 0xffff0000, v93\nv_lshlrev_b32 v52, 16, v94\nv_and_b32 v53, 0xffff0000, v94\n"
      "v_lshlrev_b32 v54, 16, v95\nv_and_b32 v55, 0xffff0000, v95\nv_pk_add_f32 v[48:49], v[48:49], v[56:57]\n"
      "v_pk_add_f32 v[50:51], v[50:51], v[58:59]\nv_pk_add_f32 v[52:53], v[52:53], v[60:61]\nv_pk_add_f32 v[54:55], v[54:55], v[62:63]\n"
      "s_waitcnt lgkmcnt(0)\nv_lshlrev_b32 v64, 16, v96\nv_and_b32 v65, 0xffff0000, v96\nv_lshlrev_b32 v66, 16, v97\n"
      "v_and_b32 v67, 0xffff0000, v97\nv_lshlrev_b32 v68, 16, v98\nv_and_b32 v69, 0xffff0000, v98\nv_lshlrev_b32 v70, 16, v99\n"
      "v_and_b32 v71, 0xffff0000, v99\nv_pk_add_f32 v[64:65], v[64:65], v[72:73]\nv_pk_add_f32 v[66:67], v[66:67], v[74:75]\n"
      "v_pk_add_f32 v[68:69], v[68:69], v[76:77]\nv_pk_add_f32 v[70:71], v[70:71], v[78:79]\nXE%=:\n"
      : "+{v[0:15]}"(a[0]), "+{v[16:31]}"(a[1]), "+{v[32:47]}"(a[2]), "+{v[48:63]}"(a[3]), "+{v[64:79]}"(a[4])
      : [w] "v"(exw), [r] "v"(exr), [kh] "s"(kh)
      : "memory", "scc", "v80", "v81", "v82", "v83", "v84", "v85", "v86", "v87", "v88", "v89", "v90", "v91", "v92", "v93", "v94", "v95", "v96", "v97", "v98", "v99");
  else
    asm volatile(
      "s_nop 15\ns_nop 3\ns_cmp_lg_u32 %[kh], 0\ns_cbranch_scc1 XU%=\nv_cvt_pk_bf16_f32 v80, v8, v9\nv_cvt_pk_bf16_f32 v81, v10, v11\n"
      "v_cvt_pk_bf16_f32 v82, v12, v13\nv_cvt_pk_bf16_f32 v83, v14, v15\nds_write_b128 %[w], v[80:83] offset:0\n"
      "v_cvt_pk_bf16_f32 v84, v24, v25\nv_cvt_pk_bf16_f32 v85, v26, v27\nv_cvt_pk_bf16_f32 v86, v28, v29\nv_cvt_pk_bf16_f32 v87, v30, v31\n"
      "ds_write_b128 %[w], v[84:87] offset:1024\nv_cvt_pk_bf16_f32 v88, v40, v41\nv_cvt_pk_bf16_f32 v89, v42, v43\n"
      "v_cvt_pk_bf16_f32 v90, v44, v45\nv_cvt_pk_bf16_f32 v91, v46, v47\nds_write_b128 %[w], v[88:91] offset:2048\n"
      "v_cvt_pk_bf16_f32 v92, v56, v57\nv_cvt_pk_bf16_f32 v93, v58, v59\nv_cvt_pk_bf16_f32 v94, v60, v61\nv_cvt_pk_bf16_f32 v95, v62, v63\n"
      "ds_write_b128 %[w], v[92:95] offset:3072\ns_waitcnt lgkmcnt(0)\ns_barrier\nds_read_b128 v[80:83], %[r] offset:0\n"
      "ds_read_b128 v[84:87], %[r] offset:1024\nds_read_b128 v[88:91], %[r] offset:2048\nds_read_b128 v[92:95], %[r] offset:3072\n"
      "s_waitcnt lgkmcnt(3)\nv_lshlrev_b32 v8, 16, v80\nv_and_b32 v9, 0xffff0000, v80\nv_lshlrev_b32 v10, 16, v81\n"
      "v_and_b32 v11, 0xffff0000, v81\nv_lshlrev_b32 v12, 16, v82\nv_and_b32 v13, 0xffff0000, v82\nv_lshlrev_b32 v14, 16, v83\n"
      "v_and_b32 v15, 0xffff0000, v83\nv_pk_add_f32 v[0:1], v[8:9], v[0:1]\nv_pk_add_f32 v[2:3], v[10:11], v[2:3]\n"
      "v_pk_add_f32 v[4:5], v[12:13], v[4:5]\nv_pk_add_f32 v[6:7], v[14:15], v[6:7]\ns_waitcnt lgkmcnt(2)\nv_lshlrev_b32 v24, 16, v84\n"
      "v_and_b32 v25, 0xffff0000, v84\nv_lshlrev_b32 v26, 16, v85\nv_and_b32 v27, 0xffff0000, v85\nv_lshlrev_b32 v28, 16, v86\n"
      "v_and_b32 v29, 0xffff0000, v86\nv_lshlrev_b32 v30, 16, v87\nv_and_b32 v31, 0xffff0000, v87\n"
      "v_pk_add_f32 v[16:17], v[24:25], v[16:17]\nv_pk_add_f32 v[18:19], v[26:27], v[18:19]\nv_pk_add_f32 v[20:21], v[28:29], v[20:21]\n"
      "v_pk_add_f32 v[22:23], v[30:31], v[22:23]\ns_waitcnt lgkmcnt(1)\nv_lshlrev_b32 v40, 16, v88\nv_and_b32 v41, 0xffff0000, v88\n"
      "v_lshlrev_b32 v42, 16, v89\nv_and_b32 v43, 0xffff0000, v89\nv_lshlrev_b32 v44, 16, v90\nv_and_b32 v45, 0xffff0000, v90\n"
      "v_lshlrev_b32 v46, 16, v91\nv_and_b32 v47, 0xffff0000, v91\nv_pk_add_f32 v[32:33], v[40:41], v[32:33]\n"
      "v_pk_add_f32 v[34:35], v[42:43], v[34:35]\nv_pk_add_f32 v[36:37], v[44:45], v[36:37]\nv_pk_add_f32 v[38:39], v[46:47], v[38:39]\n"
      "s_waitcnt lgkmcnt(0)\nv_lshlrev_b32 v56, 16, v92\nv_and_b32 v57, 0xffff0000, v92\nv_lshlrev_b32 v58, 16, v93\n"
      "v_and_b32 v59, 0xffff0000, v93\nv_lshlrev_b32 v60, 16, v94\nv_and_b32 v61, 0xffff0000, v94\nv_lshlrev_b32 v62, 16, v95\n"
      "v_and_b32 v63, 0xffff0000, v95\nv_pk_add_f32 v[48:49], v[56:57], v[48:49]\nv_pk_add_f32 v[50:51], v[58:59], v[50:51]\n"
      "v_pk_add_f32 v[52:53], v[60:61], v[52:53]\nv_pk_add_f32 v[54:55], v[62:63], v[54:55]\ns_branch XE%=\nXU%=:\n"
      "v_cvt_pk_bf16_f32 v80, v0, v1\nv_cvt_pk_bf16_f32 v81, v2, v3\nv_cvt_pk_bf16_f32 v82, v4, v5\nv_cvt_pk_bf16_f32 v83, v6, v7\n"
      "ds_write_b128 %[w], v[80:83] offset:0\nv_cvt_pk_bf16_f32 v84, v16, v17\nv_cvt_pk_bf16_f32 v85, v18, v19\n"
      "v_cvt_pk_bf16_f32 v86, v20, v21\nv_cvt_pk_bf16_f32 v87, v22, v23\nds_write_b128 %[w], v[84:87] offset:1024\n"
      "v_cvt_pk_bf16_f32 v88, v32, v33\nv_cvt_pk_bf16_f32 v89, v34, v35\nv_cvt_pk_bf16_f32 v90, v36, v37\nv_cvt_pk_bf16_f32 v91, v38, v39\n"
      "ds_write_b128 %[w], v[88:91] offset:2048\nv_cvt_pk_bf16_f32 v92, v48, v49\nv_cvt_pk_bf16_f32 v93, v50, v51\n"
      "v_cvt_pk_bf16_f32 v94, v52, v53\nv_cvt_pk_bf16_f32 v95, v54, v55\nds_write_b128 %[w], v[92:95] offset:3072\ns_waitcnt lgkmcnt(0)\n"
      "s_barrier\nds_read_b128 v[80:83], %[r] offset:0\nds_read_b128 v[84:87], %[r] offset:1024\nds_read_b128 v[88:91], %[r] offset:2048\n"
      "ds_read_b128 v[92:95], %[r] offset:3072\ns_waitcnt lgkmcnt(3)\nv_lshlrev_b32 v0, 16, v80\nv_and_b32 v1, 0xffff0000, v80\n"
      "v_lshlrev_b32 v2, 16, v81\nv_and_b32 v3, 0xffff0000, v81\nv_lshlrev_b32 v4, 16, v82\nv_and_b32 v5, 0xffff0000, v82\n"
      "v_lshlrev_b32 v6, 16, v83\nv_and_b32 v7, 0xffff0000, v83\nv_pk_add_f32 v[0:1], v[0:1], v[8:9]\n"
      "v_pk_add_f32 v[2:3], v[2:3], v[10:11]\nv_pk_add_f32 v[4:5], v[4:5], v[12:13]\nv_pk_add_f32 v[6:7], v[6:7], v[14:15]\n"
      "s_waitcnt lgkmcnt(2)\nv_lshlrev_b32 v16, 16, v84\nv_and_b32 v17, 0xffff0000, v84\nv_lshlrev_b32 v18, 16, v85\n"
      "v_and_b32 v19, 0xffff0000, v85\nv_lshlrev_b32 v20, 16, v86\nv_and_b32 v21, 0xffff0000, v86\nv_lshlrev_b32 v22, 16, v87\n"
      "v_and_b32 v23, 0xffff0000, v87\nv_pk_add_f32 v[16:17], v[16:17], v[24:25]\nv_pk_add_f32 v[18:19], v[18:19], v[26:27]\n"
      "v_pk_add_f32 v[20:21], v[20:21], v[28:29]\nv_pk_add_f32 v[22:23], v[22:23], v[30:31]\ns_waitcnt lgkmcnt(1)\n"
      "v_lshlrev_b32 v32, 16, v88\nv_and_b32 v33, 0xffff0000, v88\nv_lshlrev_b32 v34, 16, v89\nv_and_b32 v35, 0xffff0000, v89\n"
      "v_lshlrev_b32 v36, 16, v90\nv_and_b32 v37, 0xffff0000, v90\nv_lshlrev_b32 v38, 16, v91\nv_and_b32 v39, 0xffff0000, v91\n"
      "v_pk_add_f32 v[32:33], v[32:33], v[40:41]\nv_pk_add_f32 v[34:35], v[34:35], v[42:43]\nv_pk_add_f32 v[36:37], v[36:37], v[44:45]\n"
      "v_pk_add_f32 v[38:39], v[38:39], v[46:47]\ns_waitcnt lgkmcnt(0)\nv_lshlrev_b32 v48, 16, v92\nv_and_b32 v49, 0xffff0000, v92\n"
      "v_lshlrev_b32 v50, 16, v93\nv_and_b32 v51, 0xffff0000, v93\nv_lshlrev_b32 v52, 16, v94\nv_and_b32 v53, 0xffff0000, v94\n"
      "v_lshlrev_b32 v54, 16, v95\nv_and_b32 v55, 0xffff0000, v95\nv_pk_add_f32 v[48:49], v[48:49], v[56:57]\n"
      "v_pk_add_f32 v[50:51], v[50:51], v[58:59]\nv_pk_add_f32 v[52:53], v[52:53], v[60:61]\nv_pk_add_f32 v[54:55], v[54:55], v[62:63]\n"
      "XE%=:\n"
      : "+{v[0:15]}"(a[0]), "+{v[16:31]}"(a[1]), "+{v[32:47]}"(a[2]), "+{v[48:63]}"(a[3])
      : [w] "v"(exw), [r] "v"(exr), [kh] "s"(kh)
      : "memory", "scc", "v80", "v81", "v82", "v83", "v84", "v85", "v86", "v87", "v88", "v89", "v90", "v91", "v92", "v93", "v94", "v95");
}
DI void gld4ck(unsigned& d, const char* sbase, unsigned voff) {
  asm volatile("global_load_dword %0, %1, %2 sc1" : "+v"(d) : "v"(voff), "s"(sbase) : "memory");
}
#define PSTG 0
#define OUTSC 0
#define TS(a, b) do { if ((a) >= 0) SST((a) < 0 ? 0 : (a)); } while (0)
template <int U>
DI void mlp_track(char* lds, const char* __restrict__ XN, const char* __restrict__ W1,
                  const unsigned short* __restrict__ b1, char* H, const char* __restrict__ W2,
                  const unsigned short* __restrict__ b2, const char* __restrict__ Wo, const char* O,
                  const char* __restrict__ G, char* P, char* FL, char* __restrict__ out, int rt, int sp, int j4,
                  int koff, int wave, int lane) {
  constexpr int NBW = 9 - U, U2 = U == 5 ? 2 : 3;
  constexpr int NS1 = U == 5 ? 5 : 6;
  SSD();
  TS(0, -1);
  SS8(0);
  const bool up = j4 < 2;
  const int unit0 = sp * 18 + (U == 5 ? (up ? 0 : 13) : (up ? 5 : 9));
  const unsigned lbase = lds_addr(lds);
  const lds_p L = (lds_p)lds;
  const int wm = wave & 3, wn = wave >> 2;
  char* FLg = FL + (size_t)(rt * 4 + sp) * 512;
  f32x16 acc[5];
  bf16x8 w[5];
  float iog;
  {
    constexpr int WS = 2 * U * 1024, XB = NS1 * WS, NP = 4 + 2 * U;
    const int h = lane >> 5;
    const unsigned voffW = (unsigned)((lane >> 3) * 9216 + (((lane & 7) ^ (((lane >> 4) & 1) << 2)) << 4));
    const unsigned voff0 = wave < 4 ? (unsigned)(lane * 16) : voffW;
    const int q0 = wave - 4, q1 = wave + 4;
    const char* wb = W1 + unit0 * 128;
    const char* src0 =
        wave < 4 ? XN + (size_t)(rt * 4 + wave) * (36 * 1024) : wb + (q0 / U) * (8 * 9216) + (q0 % U) * 128;
    const char* src1 = wb + (q1 / U) * (8 * 9216) + (q1 % U) * 128;
    const unsigned st0 = wave < 4 ? 1024u : 147456u;
    const bool two = wave + 8 < NP;
    const unsigned dst0 = lbase + (wave < 4 ? XB + wave * 1024 : q0 * 1024), ds0 = wave < 4 ? 4096u : (unsigned)WS;
    auto issue = [&](int s, int e) {
      int ks = s + koff;
      ks = ks >= 36 ? ks - 36 : ks;
      if (e == 0) glds16s(src0 + (unsigned)ks * st0, voff0, dst0 + (s % NS1) * ds0);
      else if (two) glds16s(src1 + (unsigned)ks * 147456u, voffW, lbase + (s % NS1) * WS + q1 * 1024);
    };
    auto waitn = [&](auto Nc) {
      constexpr int N = decltype(Nc)::v;
      if (two) wait_vm<2 * N>(); else wait_vm<N>();
    };
    unsigned bb[U];
#pragma unroll
    for (int b = 0; b < U; ++b)
      asm volatile("global_load_ushort %0, %1, %2" : "=v"(bb[b]) : "v"((lane & 31) * 2), "s"(b1 + (unit0 * 2 + wn * U + b) * 32) : "memory");
    asm volatile("global_load_dword %0, %1, %2" : "=v"(iog) : "v"((lane & 31) * 4), "s"(G + (2048 + (rt * 4 + wm) * 32) * 4) : "memory");
#pragma unroll
    for (int s = 0; s < (SKIP1 ? 0 : NS1); ++s) { issue(s, 0); issue(s, 1); }
    if (SKIP1) wait_vm<0>();
    const int q = (lane >> 2) & 3, p = lane & 3, g = (lane >> 4) & 1;
    lds_p xa = L + XB + wm * 1024 + lane * 16;
    lds_p wa[2];
#pragma unroll
    for (int e = 0; e < 2; ++e) {
      int b = wn * U + e;
      wa[e] = L + (h * U + (b >> 1)) * 1024 + q * 128 + (((b & 1) ^ (q >> 1)) << 6) + g * 32 + p * 8;
    }
    asm volatile("" : "+v"(xa), "+v"(wa[0]), "+v"(wa[1]));
    waitn(IC<NS1 - 1>{});
    {
      f32x16 zero;
#pragma unroll
      for (int i = 0; i < 16; ++i) zero[i] = 0.f;
      asm volatile("" : "+v"(iog));
      const u32x4 xx = {h ? 0u : 0x3F80u, 0u, 0u, 0u};
#pragma unroll
      for (int b = 0; b < U; ++b) {
        asm volatile("" : "+v"(bb[b]));
        const u32x4 wx = {bb[b], 0u, 0u, 0u};
        acc[b] = MFMA32(__builtin_bit_cast(bf16x8, wx), __builtin_bit_cast(bf16x8, xx), zero);
      }
    }
    BARRIER();
    constexpr int P1HI = U == 4 ? P1HI4 : P1HI5;
    __builtin_amdgcn_s_setprio(P1HI > 0 ? HI_PRIO : MLP_PRIO);
    STAMP(4, 1);
    auto rdx = [&](int sl) { return ld8(xa + sl * 4096); };
    auto rdw = [&](int sl, int m) {
      const lds_p a = wa[m & 1] + sl * WS + (m >> 1) * 1024;
      return cat8(trl(a), trl(a + 512));
    };
    bf16x8 x0, x1;
    x0 = rdx(0);
#pragma unroll
    for (int m = 0; m < U; ++m) w[m] = rdw(0, m);
    unsigned pfd;
    static_for<0, SKIP1 ? 0 : 36>([&](auto Jc) {
      constexpr int j = decltype(Jc)::v;
      if constexpr (j + 1 < 36) {
        constexpr int top = j == 0 ? NS1 - 1 : (j + NS1 - 2 < 35 ? j + NS1 - 2 : 35);
        waitn(IC<top - (j + 1)>{});
        BARRIER();
      }
      if constexpr (P1HI > 0 && j == P1HI) __builtin_amdgcn_s_setprio(MLP_PRIO);
      STAMP2(j);
      if (j == 12) STAMP(4, 2);
      if (j == 24) STAMP(4, 3);
      SB();
      bf16x8& xc = (j & 1) ? x1 : x0;
      bf16x8& xn = (j & 1) ? x0 : x1;
      static_for<0, U>([&](auto Mc) {
        constexpr int m = decltype(Mc)::v;
        acc[m] = MFMA32(w[m], xc, acc[m]);
        SB();
        if constexpr (j + 1 < 36) {
          if constexpr (m == 0) xn = rdx((j + 1) % NS1);
          w[m] = rdw((j + 1) % NS1, m);
        }
        if constexpr (j >= 1 && j + NS1 - 1 < 36 && (m == 1 || m == 2)) issue(j + NS1 - 1, m - 1);
        if constexpr (j == PF2_AT && m == 3) {
          const unsigned l2 = fresh_lane(), li = l2 < 18 * U2 ? l2 : l2 - 32, row = li / U2;
          const unsigned vo = row * 1152u + (li - row * U2) * 128u;
          const int u2 = U == 5 ? (up ? 0 : 7) : 2 * j4;
          gld4(pfd, W2 + (size_t)(sp * 1152 + (rt & 7) * 144 + wave * 18) * 1152 + u2 * 128, vo);
          if (wave == 7) gld4k(pfd, Wo + (sp * 144 + (rt & 7) * 18) * 1152 + u2 * 128, vo);
        }
        if constexpr (j == PF2_AT + NS1 + 1 && m == 0) asm volatile("" ::"v"(pfd));
        SB();
      });
    });
    STAMP2(36);
    STAMP(4, 4);
    asm volatile("" : "+v"(acc[0]), "+v"(acc[U - 1]), "+v"(iog));
  }
  SS8(1);
  __builtin_amdgcn_s_setprio(GELU_PRIO);
  BARRIER();
  SS8(2);
  constexpr int B1 = 2 * U, B2 = B1 + 8, B3 = 26;
  constexpr M2S SC = m2_sched(U);
  const int e0 = 2 * (U == 5 ? (up ? 0 : 26) : (up ? 10 : 18));
  const int e1 = 2 * (U == 5 ? (up ? 10 : 18) : (up ? 18 : 10)) - B1;
  const int e2 = 2 * (U == 5 ? (up ? 18 : 10) : (up ? 26 : 0)) - B2;
  const int e3 = 2 * (U == 5 ? (up ? 26 : 0) : (up ? 0 : 26)) - B3;
  auto kstep = [&](int s, int half) {
    const int uc2 = s < B1 ? 2 * U : (s < B2 ? 8 : (s < B3 ? (U == 5 ? 8 : 10) : 10));
    return (unsigned)(s + (s < B1 ? e0 : (s < B2 ? e1 : (s < B3 ? e2 : e3))) + half * uc2);
  };
  const int kh = wn;
  const int unit2 = U == 5 ? (up ? 0 : 7) : 2 * j4, boff = U == 5 ? 0 : j4 - 1;
  const int ln = fresh_lane();
  const unsigned voffX = ln * 16;
  const unsigned voffW = (unsigned)((ln >> 3) * 1152 + (((ln & 7) ^ (((ln >> 4) & 1) << 2)) << 4));
  const char* xsrc = H + ((size_t)(rt * 4 + wm) * 288 + sp * 72) * 1024;
  const int p0 = wave, p2 = 8 + (wave & 3);
  const int kb0 = p0 / U2, kb2 = p2 / U2;
  const unsigned c0 = (kb0 & 1) * (8 * 1152) + (unit2 + p0 % U2) * 128, c2 = (kb2 & 1) * (8 * 1152) + (unit2 + p2 % U2) * 128;
  const char* w2s = W2 + (size_t)sp * 1152 * 1152;
  const char* wos = Wo + sp * (8 * 18 * 1152);
  constexpr int SL2 = U2 * 4096;
  auto issueW = [&](auto Sc, int e) {
    constexpr int s = decltype(Sc)::v;
    const unsigned dst = lbase + SC.slot[s] * SL2 + (e ? p2 : p0) * 1024;
    if (e == 1 && !(U2 == 3 && (s & 1) == kh)) return;
    const int half = e ? 1 : kb0 >> 1;
    const unsigned c = e ? c2 : c0;
    if constexpr (s < M_IT) glds16s(w2s + (c + kstep(s, half) * (16u * 1152u)), voffW, dst);
    else if constexpr (s < M2_NT - 1) glds16s(wos + (c + (unsigned)(4 * half + s - M_IT) * (18u * 1152u)), voffW, dst);
    else {
      const int l2 = fresh_lane(), row = l2 >> 3;
      const unsigned ve = (unsigned)(((4 * (row >> 2) + ((row & 3) >> 1)) * 18 + (row & 1)) * 1152 + (((l2 & 7) ^ (((l2 >> 4) & 1) << 2)) << 4));
      const int kbl = (e ? kb2 : kb0) & 1;
      glds16s(wos + (c + (unsigned)(kbl * (36 - 8) + 16) * 1152u), ve, dst);
    }
  };
  unsigned b2v[NBW];
  const bool addb = sp == 0 && kh == 0;
#pragma unroll
  for (int b = 0; b < NBW; ++b)
    asm volatile("global_load_ushort %0, %1, %2" : "=v"(b2v[b]) : "v"((ln & 31) * 2), "s"(b2 + (unit2 * 2 + boff + b) * 32) : "memory");
  static_for<0, 4>([&](auto Sc) { issueW(Sc, 0); issueW(Sc, 1); });
  SS8(3);
  bf16x8 xr[4];
  const unsigned stash = lbase + 4 * SL2 + wave * ((2 * U - 4) * 1024) + voffX;
  {
    const float C0 = -2.0f * 1.4426950408889634f * 0.7978845608028654f;
    const float C1 = C0 * 0.044715f;
    f32x2 c0 = {C0, C0}, c1 = {C1, C1};
    asm volatile("" : "+s"(c0), "+s"(c1));
    float vc1 = C1;
    asm volatile("" : "+v"(vc1));
    f32x2 ig = {iog, iog};
    asm volatile("" : "+v"(ig));
    char* hb = H + ((size_t)(rt * 4 + wm) * 288 + (unit0 * 2 + wn * U) * 2) * 1024 + fresh_lane() * 16;
#pragma unroll
    for (int b = 0; b < U; ++b) {
#pragma unroll
      for (int gp = 0; gp < 2; ++gp) {
        u32x4 o4;
#pragma unroll
        for (int i = 0; i < 4; ++i) {
          const f32x2 v = {acc[b][8 * gp + 2 * i], acc[b][8 * gp + 2 * i + 1]};
          const f32x2 t = v * __builtin_elementwise_fma(v * v, c1, c0);
          const f32x2 e = {__builtin_amdgcn_exp2f(t[0]), __builtin_amdgcn_exp2f(t[1])};
          const f32x2 d = __builtin_elementwise_fma(e, ig, ig);
          const f32x2 rc = {__builtin_amdgcn_rcpf(d[0]), __builtin_amdgcn_rcpf(d[1])};
          const f32x2 o = v * rc;
          o4[i] = pk2(o[0], o[1]);
        }
        st16(hb + (b * 2 + gp) * 1024, o4);
        if (b < 2) xr[b * 2 + gp] = __builtin_bit_cast(bf16x8, o4);
        else *(__attribute__((address_space(3))) u32x4*)(uintptr_t)(stash + (b * 2 + gp - 4) * 1024) = o4;
      }
      if (b == 1) SS8(4);
    }
  }
  STAMP(4, 5);
  SS8(5);
  lds_p wa[2];
  {
    const int h = ln >> 5, q = (ln >> 2) & 3, p = ln & 3, g = (ln >> 4) & 1;
#pragma unroll
    for (int e = 0; e < 2; ++e) {
      int b = boff + e;
      wa[e] = L + (2 * kh * U2 + (b >> 1)) * 1024 + (4 * h + q) * 128 + (((b & 1) ^ (q >> 1)) << 6) + g * 32 + p * 8;
    }
  }
  asm volatile("" : "+v"(wa[0]), "+v"(wa[1]));
  wait_vm<2 * U>();
  SS8(6);
  {
    f32x16 zero;
#pragma unroll
    for (int i = 0; i < 16; ++i) zero[i] = 0.f;
    const float og = __builtin_amdgcn_rcpf(iog);
    const float ogh = __uint_as_float(__float_as_uint(og) & 0xffff0000u);
    const u32x4 xx = {(ln >> 5) || !addb ? 0u : pk2(ogh, og - ogh), 0u, 0u, 0u};
#pragma unroll
    for (int b = 0; b < NBW; ++b) {
      asm volatile("" : "+v"(b2v[b]));
      const u32x4 wx = {b2v[b] * 0x10001u, 0u, 0u, 0u};
      acc[b] = MFMA32(__builtin_bit_cast(bf16x8, wx), __builtin_bit_cast(bf16x8, xx), zero);
    }
  }
  BARRIER();
  constexpr int P2HI = U == 4 ? P2HI4 : P2HI5;
  __builtin_amdgcn_s_setprio(P2HI > 0 ? HI_PRIO : MLP2_PRIO);
  STAMP(4, 6);
  STAMP(4, 7);
  STAMP(2, 1);
  TS(1, -1);
  SS8(7);
  auto rdw = [&](int s, int m) {
    const lds_p a = wa[m & 1] + SC.slot[s] * SL2 + (m >> 1) * 1024;
    return cat8(trl(a), trl(a + U2 * 1024));
  };
#pragma unroll
  for (int m = 0; m < NBW; ++m) w[m] = rdw(0, m);
  unsigned fv = 0;
  auto poll = [&]() {
    unsigned vo = voffX;
    asm volatile("" : "+v"(vo));
    vo = (vo & 48u) << 3;
    gld4c(fv, FLg, vo);
  };
  auto poll3 = [&]() {
    const unsigned l2 = fresh_lane(), mb = rt * 4 + wm;
    const unsigned vo = (l2 & 12u) == 8u && (l2 & 3u) != (unsigned)wm ? FL_P + mb * 16u + (l2 & 3u) * 4u : FL_O + mb * 128u + sp * 32u + (l2 & 7u) * 4u;
    gld4c(fv, FL, vo);
  };
  auto check = [&](unsigned long long mask) {
    mask &= ~(1ull << j4);
    asm volatile("" : "+v"(fv));
    if (__ballot(fv != 1u) & mask) {
#pragma unroll 1
      for (int it = 0; it < (1 << 17); ++it) {
        __builtin_amdgcn_s_sleep(4);
        poll();
        wait_vm<0>();
        asm volatile("" : "+v"(fv));
        if (!(__ballot(fv != 1u) & mask)) break;
      }
    }
  };
  auto waitk = [&](auto N0, auto N1) {
    constexpr int n0 = decltype(N0)::v, n1 = decltype(N1)::v;
    if constexpr (n0 == n1) wait_vm<n0>();
    else { if (kh == 0) wait_vm<n0>(); else wait_vm<n1>(); }
  };
  constexpr int CK1 = m2_ck1(U), CK2 = m2_ck2(U);
  bf16x8 xe;
  const char* osrc = O + (size_t)((rt * 4 + wm) * 32 + sp * 8 + kh * 4) * 1024;
  const char* oesrc = O + OEX + ((rt * 4 + wm) * 4 + sp) * 1024;
  static_for<0, M_IT>([&](auto Jc) {
    constexpr int j = decltype(Jc)::v;
    if constexpr (m2_need(U, j)) waitk(IC<m2_wait(U, 0, j, 0)>{}, IC<m2_wait(U, 1, j, 0)>{});
    if constexpr (j > 0) BARRIER();
    if constexpr (j > 0) pin_acc<NBW>(acc);
    static_for<0, 4>([&](auto Ic) {
      constexpr int i = decltype(Ic)::v;
      if constexpr (SENT || j + ((i - j) & 3) < M_IT) pin_x1<i>(xr[i]);
    });
    if constexpr (j == 3) {
      if (wave == 0 && voffX == 0) st4(FLg + j4 * 128, 1u);
    }
    if constexpr (P2HI > 0 && j == P2HI) __builtin_amdgcn_s_setprio(MLP2_PRIO);
    STAMP2(40 + j);
    if (j == 9) STAMP(2, 2);
    if (j == 18) STAMP(2, 3);
    if (j == 18) TS(2, -1);
    if (j == 27) STAMP(2, 4);
    if constexpr (j == CK1 - 2 || j == CK2 - 2) poll();
    SB();
    static_for<0, NBW>([&](auto Mc) {
      constexpr int m = decltype(Mc)::v;
      pin_w<m>(w[m]);
      acc[m] = MFMA32(w[m], xr[j & 3], acc[m]);
      SB();
      w[m] = rdw(j + 1, m);
      static_for<j + 3, (j + 7 < M2_NT ? j + 7 : M2_NT)>([&](auto Sc) {
        constexpr int s = decltype(Sc)::v;
        if constexpr (SC.it[s] == j) {
          constexpr bool second = s > 0 && SC.it[s - 1] == j;
          if constexpr (m == (second ? 2 : 0)) issueW(Sc, 0);
          if constexpr (m == (second ? 3 : 1)) issueW(Sc, 1);
        }
      });
      SB();
    });
    if constexpr (j == CK1 || j == CK2) {
      waitk(IC<m2_wait(U, 0, j, 1)>{}, IC<m2_wait(U, 1, j, 1)>{});
      check(j == CK1 ? 6ull : 15ull);
    }
    if constexpr (j + 4 < B1) xstash<(j & 3), j * 1024>(xr[j & 3], stash);
    else if constexpr (j + 4 < M_IT) xload<(j & 3)>(xr[j & 3], xsrc + kstep(j + 4, kh) * 1024u, voffX);
    SB();
  });
  STAMP2(76);
  STAMP(2, 5);
  TS(3, 0);
  const char* flp = FL + FL_P2 + (((rt * 4 + j4) * 4 + wm) * 2 + kh) * 512;
  wait_vm<0>();
  pin_acc<NBW>(acc);
  static_for<0, NBW>([&](auto Mc) { pin_w<decltype(Mc)::v>(w[decltype(Mc)::v]); });
  if (wave == 0) {
    const int l2 = fresh_lane();
    spin_flags(FL + FL_O + rt * 512 + sp * 32, (unsigned)(((l2 >> 3) & 3) * 128 + (l2 & 7) * 4));
  }
  BARRIER();
  TS(4, 1);
  {
    static_for<0, 4>([&](auto Ic) { xload<decltype(Ic)::v>(xr[decltype(Ic)::v], osrc + decltype(Ic)::v * 1024, voffX); });
    gld16c(xe, oesrc, voffX);
    static_for<0, 4>([&](auto Tc) {
      constexpr int t = decltype(Tc)::v;
      wait_vm<4 - t>();
      pin_x1<t>(xr[t]);
      pin_acc<NBW>(acc);
      static_for<0, NBW>([&](auto Mc) {
        constexpr int m = decltype(Mc)::v;
        pin_w<m>(w[m]);
        acc[m] = MFMA32(w[m], xr[t], acc[m]);
        SB();
        w[m] = rdw(M_IT + t + 1, m);
        SB();
      });
    });
    wait_vm1<0>(xe);
    pin_acc<NBW>(acc);
    static_for<0, NBW>([&](auto Mc) { pin_w<decltype(Mc)::v>(w[decltype(Mc)::v]); });
    if (kh == 0) {
#pragma unroll
      for (int m = 0; m < NBW; ++m) acc[m] = MFMA32(w[m], xe, acc[m]);
    }
    pin_acc<NBW>(acc);
  }
  BARRIER();
  STAMP(2, 6);
  TS(5, 2);
  constexpr int NH = NBW;
  const int l2 = fresh_lane();
  pair_sum<NBW>(acc, lbase + (wm * 2 + kh) * (NH * 1024) + l2 * 16, lbase + (wm * 2 + (kh ^ 1)) * (NH * 1024) + l2 * 16, kh);
  float r[NH][8];
#pragma unroll
  for (int q = 0; q < NH; ++q)
#pragma unroll
    for (int i = 0; i < 8; ++i) r[q][i] = acc[q][i];
  TS(-1, 3);
  constexpr unsigned PSP = 64 * 18 * 2048;
  const int nb0 = unit2 * 2 + boff;
  const char* pt = P + ((size_t)(rt * 4 + wm) * 18 + nb0) * 2048;
  const unsigned h0 = kh * 1024u, h4 = 8192u + h0;
  const unsigned vl = l2 * 16;
  float own[8];
#pragma unroll
  for (int q = 0; q < NH; ++q) {
    if (q < 4 && q == sp) {
#pragma unroll
      for (int i = 0; i < 8; ++i) own[i] = r[q][i];
    } else if (q < 4 || wm != sp) {
      u32x4 v;
#pragma unroll
      for (int i = 0; i < 4; ++i) v[i] = pk2(r[q][2 * i], r[q][2 * i + 1]);
      const char* dst = pt + (sp * PSP + (h0 + q * 2048u));
      asm volatile("global_store_dwordx4 %0, %1, %2 sc1\n\ts_nop 1" ::"v"(vl), "v"(v), "s"(dst) : "memory");
    }
  }
  {
    const unsigned oth = (unsigned)((sp + 1 + (l2 % 3)) & 3), vf = sp * 128u + oth * 4u;
    unsigned f0, f1 = 0;
    gld4c(f0, flp, vf);
    wait_vm<1>();
    TS(-1, 4);
    if (l2 < 3) asm volatile("global_store_dword %0, %1, %2 sc1" ::"v"(oth * 128u + sp * 4u), "v"(1u), "s"(flp) : "memory");
    asm volatile("s_waitcnt vmcnt(1)" : "+v"(f0)::"memory");
    if (__ballot(f0 != 1u)) {
#pragma unroll 1
      for (int it = 0; it < (1 << 17); ++it) {
        gld4ck(f0, flp, vf);
        asm volatile("s_waitcnt vmcnt(0)" : "+v"(f0)::"memory");
        if (!__ballot(f0 != 1u)) break;
        __builtin_amdgcn_s_sleep(PSLP);
      }
    }
    TS(6, 5);
    const char* orow = out + ((size_t)(rt * 4 + wm) * (32 * 1152) + nb0 * 64);
    const unsigned vo = (l2 & 31) * 1152 + (l2 >> 5) * 16;
    auto finish = [&](float* a, bf16x8* p, unsigned hoff) {
#pragma unroll
      for (int o = 0; o < 3; ++o) {
        const u32x4 v = __builtin_bit_cast(u32x4, p[o]);
#pragma unroll
        for (int k = 0; k < 4; ++k) {
          a[2 * k] += __uint_as_float(v[k] << 16);
          a[2 * k + 1] += __uint_as_float(v[k] & 0xffff0000u);
        }
      }
      auto r0 = __builtin_amdgcn_permlane32_swap(pk2(a[0], a[1]), pk2(a[4], a[5]), false, false);
      auto r1 = __builtin_amdgcn_permlane32_swap(pk2(a[2], a[3]), pk2(a[6], a[7]), false, false);
      const u32x4 wv = {r0[0], r1[0], r0[1], r1[1]};
      const char* dst = orow + (hoff >> 5);
      asm volatile("global_store_dwordx4 %0, %1, %2\n\ts_nop 1" ::"v"(vo), "v"(wv), "s"(dst) : "memory");
    };
    bf16x8 pv[3], pw[3];
    const bool two = NH == 5 && wm == sp;
#pragma unroll
    for (int o = 0; o < 3; ++o) gld16c(pv[o], pt + (((sp + 1 + o) & 3) * PSP + h0 + sp * 2048u), vl);
    if constexpr (NH == 5) {
      if (two) {
#pragma unroll
        for (int o = 0; o < 3; ++o) gld16c(pw[o], pt + (((sp + 1 + o) & 3) * PSP + h4), vl);
      }
      asm volatile("s_waitcnt vmcnt(0)" : "+v"(pw[0]), "+v"(pw[1]), "+v"(pw[2]), "+v"(pv[0]), "+v"(pv[1]), "+v"(pv[2]) : "v"(f0), "v"(f1) : "memory");
    } else {
      asm volatile("s_waitcnt vmcnt(0)" : "+v"(pv[0]), "+v"(pv[1]), "+v"(pv[2]) : "v"(f0), "v"(f1) : "memory");
    }
    TS(-1, 6);
    finish(own, pv, h0 + sp * 2048u);
    if (two) finish(r[NH - 1], pw, h4);
  }
  STAMP(2, 7);
  SST(7);
  SSW();
}

extern "C" __global__ __launch_bounds__(512, 4) void kernel1(const char* __restrict__ T,
                                                   char* O, const char* __restrict__ XN,
                                                   const char* __restrict__ W1, const unsigned short* __restrict__ b1,
                                                   char* H, const char* __restrict__ W2,
                                                   const unsigned short* __restrict__ b2, const char* __restrict__ Wo,
                                                   const char* __restrict__ G, char* P, char* FL,
                                                   char* __restrict__ out) {
  __shared__ __attribute__((aligned(16))) char lds[81920];
  const int wave = __builtin_amdgcn_readfirstlane(threadIdx.x >> 6);
  const int lane = threadIdx.x & 63;
  const int bid = blockIdx.x;
  if (bid < 256) {
    STAMP(1, 0);
    STAMP(5, 0);
    __builtin_amdgcn_s_setprio(ATT_PRIO);
    attn_role(lds, T, O, P, G, FL, W2, Wo, bid, wave, fresh_lane());
  } else {
    STAMP(2, 0);
    STAMP(4, 0);
    const int wg = bid - 256;
    const int xcd = wg & 7, idx = wg >> 3;
    const int sp = xcd >> 1, j4 = idx & 3, r8 = idx >> 2, rt = (xcd & 1) * 8 + r8;
    const int koff = (r8 * 9 / 2 + j4 * 9) % 36;
    if (j4 == 0 || j4 == 3)
      mlp_track<5>(lds, XN, W1, b1, H, W2, b2, Wo, O, G, P, FL, out, rt, sp, j4, koff, wave, fresh_lane());
    else
      mlp_track<4>(lds, XN, W1, b1, H, W2, b2, Wo, O, G, P, FL, out, rt, sp, j4, koff, wave, fresh_lane());
  }
}

constexpr int Q_SLOT = 32768;
#define NSQ 4
constexpr int QGRID = 256;
DI void qkv_role(char* lds, const char* __restrict__ X, const char* __restrict__ Wqkv, char* __restrict__ T, int rt,
                 int ct, int joff, int wave, int lane) {
  f32x16 acc[2];
#pragma unroll
  for (int i = 0; i < 16; ++i) { acc[0][i] = 0.f; acc[1][i] = 0.f; }
  const unsigned lbase = lds_addr(lds);
  const int wm = wave & 3, wn = wave >> 2;
  const bool half = ct == 13;
  unsigned voffX[2];
#pragma unroll
  for (int i = 0; i < 2; ++i) {
    int row = wave * 16 + 8 * i + (lane >> 3);
    int c = (lane & 7) ^ ((row >> 1) & 7);
    voffX[i] = (unsigned)((rt * 128 + row) * 1152 + c * 16);
  }
  const unsigned voffW = (unsigned)((lane >> 3) * 3456 + (((lane & 7) ^ (((lane >> 4) & 1) << 2)) << 4));
  const char* wsrc = Wqkv + (size_t)wave * 8 * 3456 + ct * 256;
  const unsigned wh = half ? 0u : 128u;
  auto issue = [&](int J, int e) {
    unsigned dst = lbase + (J % NSQ) * Q_SLOT;
    unsigned Jr = J + joff;
    Jr = Jr >= 9 ? Jr - 9 : Jr;
    if (e < 2) glds16s(wsrc + (Jr * (64u * 3456u) + e * wh), voffW, dst + 16384 + e * 8192 + wave * 1024);
    else glds16s(X + Jr * 128u, voffX[e - 2], dst + (wave * 2 + e - 2) * 1024);
  };
  STAMP(0, 0);
#pragma unroll
  for (int J = 0; J < NSQ - 1; ++J)
#pragma unroll
    for (int e = 0; e < 4; ++e) issue(J, e);
  const int r = lane & 31, h = lane >> 5;
  const int q = (lane >> 2) & 3, p = lane & 3, g = (lane >> 4) & 1;
  const lds_p L = (lds_p)lds;
  constexpr int NB = (NSQ + 1) / 2;
  lds_p xa[NB][4], wa[NB][2];
#pragma unroll
  for (int z = 0; z < NB; ++z) {
#pragma unroll
    for (int ks = 0; ks < 4; ++ks) {
      xa[z][ks] = L + z * 2 * Q_SLOT + (wm * 32 + r) * 128 + (((2 * ks + h) ^ ((r >> 1) & 7)) << 4);
      asm volatile("" : "+v"(xa[z][ks]));
    }
#pragma unroll
    for (int b = 0; b < 2; ++b) {
      wa[z][b] = L + z * 2 * Q_SLOT + 16384 + wn * 8192 + h * 1024 + q * 128 + ((b ^ (q >> 1)) << 6) + g * 32 + p * 8;
      asm volatile("" : "+v"(wa[z][b]));
    }
  }
  wait_vm<4 * (NSQ - 2)>();
  BARRIER();
  STAMP(0, 1);
  bf16x8 fx[2], fw[2][2];
  auto rd = [&](int buf, int J, int ks) {
    const int sl = J % NSQ;
    fx[buf] = ld8(xa[sl >> 1][ks] + (sl & 1) * Q_SLOT);
#pragma unroll
    for (int b = 0; b < 2; ++b) {
      const lds_p a = wa[sl >> 1][b] + (sl & 1) * Q_SLOT + ks * 2048;
      fw[buf][b] = cat8(trl(a), trl(a + 512));
    }
  };
  rd(0, 0, 0);
  static_for<0, 9>([&](auto Jc) {
    constexpr int J = decltype(Jc)::v;
    if constexpr (J + 1 < 9) {
      constexpr int top = J + NSQ - 2 < 8 ? J + NSQ - 2 : 8;
      wait_vm<4 * (top - (J + 1))>();
      BARRIER();
    }
    if (J == 3) STAMP(0, 2);
    if (J == 6) STAMP(0, 3);
    SB();
    static_for<0, 4>([&](auto Kc) {
      constexpr int ks = decltype(Kc)::v, cur = ks & 1;
      acc[0] = MFMA32(fx[cur], fw[cur][0], acc[0]);
      SB();
      if constexpr (ks < 3) rd(cur ^ 1, J, ks + 1);
      else if constexpr (J + 1 < 9) rd(cur ^ 1, J + 1, 0);
      SB();
      acc[1] = MFMA32(fx[cur], fw[cur][1], acc[1]);
      SB();
      if constexpr (J + NSQ - 1 < 9) {
        if constexpr (ks == 0) { issue(J + NSQ - 1, 0); issue(J + NSQ - 1, 1); }
        else if constexpr (ks < 3) issue(J + NSQ - 1, ks + 1);
      }
      SB();
    });
  });
  STAMP(0, 5);
  const float QSCALE = 0.23570226039551584f * 1.4426950408889634f;
  const float sc = ct * 2 + wn < 9 ? QSCALE : 1.0f;
  if (!(half && wn)) {
#pragma unroll
    for (int nb = 0; nb < 2; ++nb) {
      char* base = T + (size_t)h * TP + ((ct * 2 + wn) * 64 + nb * 32 + r) * 16;
#pragma unroll
      for (int s = 0; s < 2; ++s) {
        int step = rt * 8 + wm * 2 + s;
        u32x4 v;
#pragma unroll
        for (int i = 0; i < 4; ++i) v[i] = pk2(acc[nb][8 * s + 2 * i] * sc, acc[nb][8 * s + 2 * i + 1] * sc);
        st16(base + (size_t)step * (2 * TP), v);
      }
    }
  } else if (wm < 2) {
    int idx = wm * 64 + lane;
    unsigned c = (idx & 7) == 0 ? 0x3F803F80u : 0u;
    u32x4 v = {c, c, c, c};
    st16(T + (size_t)(rt * 16 + (idx >> 3)) * TP + (1728 + (idx & 7)) * 16, v);
  }
}

DI float dot2(unsigned a, unsigned b, float c) {
  return __builtin_amdgcn_fdot2_f32_bf16(__builtin_bit_cast(bf16x2_t, a), __builtin_bit_cast(bf16x2_t, b), c, false);
}
template <int C>
DI float dpp_add(float v) {
  return v + __builtin_bit_cast(float, __builtin_amdgcn_update_dpp(0, __builtin_bit_cast(int, v), C, 0xf, 0xf, true));
}
DI float sum8(float v) { return dpp_add<0x141>(dpp_add<0x4E>(dpp_add<0xB1>(v))); }
DI void ln_role(const char* __restrict__ X, const char* __restrict__ gw, char* __restrict__ XN, float* __restrict__ G,
                char* __restrict__ FL, int id, int wave, int lane) {
  const int c = lane & 7;
  const int row = id * 64 + wave * 8 + (lane >> 3);
  bf16x8 xv[9], gv[9];
  {
    const unsigned vx = (unsigned)(row * 1152 + c * 16), vg = (unsigned)(c * 16);
#pragma unroll
    for (int i = 0; i < 9; ++i) asm volatile("global_load_dwordx4 %0, %1, %2 offset:%3" : "=v"(xv[i]) : "v"(vx), "s"(X), "i"(i * 128) : "memory");
#pragma unroll
    for (int i = 0; i < 9; ++i) asm volatile("global_load_dwordx4 %0, %1, %2 offset:%3" : "=v"(gv[i]) : "v"(vg), "s"(gw), "i"(i * 128) : "memory");
  }
  st4(FL + id * 2048 + wave * 256 + lane * 4, 0u);
  asm volatile("global_store_dwordx2 %0, %1, off sc1" ::"v"(FL + FL_P2 + (id * 8 + wave) * 1024 + (lane >> 3) * 128 + c * 8), "v"(0ull) : "memory");
  char* dst = XN + ((size_t)(row >> 5) * 36 + (c >> 1)) * 1024 + ((c & 1) * 32 + (row & 31)) * 16;
  asm volatile("s_waitcnt vmcnt(11)" : "+v"(xv[0]), "+v"(xv[1]), "+v"(xv[2]), "+v"(xv[3]), "+v"(xv[4]), "+v"(xv[5]), "+v"(xv[6]), "+v"(xv[7]), "+v"(xv[8])::"memory");
  float sa[4] = {0.f, 0.f, 0.f, 0.f};
#pragma unroll
  for (int i = 0; i < 9; ++i)
#pragma unroll
    for (int j = 0; j < 4; ++j) sa[j] = dot2(__builtin_bit_cast(u32x4, xv[i])[j], 0x3F803F80u, sa[j]);
  STAMP(0, 1);
  const float mean = sum8((sa[0] + sa[1]) + (sa[2] + sa[3])) * (1.0f / 576.0f);
  const f32x2 m2 = {mean, mean};
  f32x2 xc[9][4];
  f32x2 qa[4] = {{0.f, 0.f}, {0.f, 0.f}, {0.f, 0.f}, {0.f, 0.f}};
#pragma unroll
  for (int i = 0; i < 9; ++i)
#pragma unroll
    for (int j = 0; j < 4; ++j) {
      const unsigned u = __builtin_bit_cast(u32x4, xv[i])[j];
      xc[i][j] = f32x2{__uint_as_float(u << 16), __uint_as_float(u & 0xffff0000u)} - m2;
      qa[j] = __builtin_elementwise_fma(xc[i][j], xc[i][j], qa[j]);
    }
  const f32x2 qq = (qa[0] + qa[1]) + (qa[2] + qa[3]);
  const float var = sum8(qq[0] + qq[1]) * (1.0f / 576.0f);
  STAMP(0, 3);
  const float rstd = 1.0f / __builtin_sqrtf(var + 1e-5f);
  const f32x2 r2 = {rstd, rstd};
#pragma unroll
  for (int i = 0; i < 9; ++i) {
    u32x4 v;
#pragma unroll
    for (int j = 0; j < 4; ++j) {
      const f32x2 y = xc[i][j] * r2;
      v[j] = pk2(y[0], y[1]);
    }
    st16(dst + i * 4096, v);
  }
  asm volatile("s_waitcnt vmcnt(11)" : "+v"(gv[0]), "+v"(gv[1]), "+v"(gv[2]), "+v"(gv[3]), "+v"(gv[4]), "+v"(gv[5]), "+v"(gv[6]), "+v"(gv[7]), "+v"(gv[8])::"memory");
  float da[4] = {0.f, 0.f, 0.f, 0.f};
#pragma unroll
  for (int i = 0; i < 9; ++i)
#pragma unroll
    for (int j = 0; j < 4; ++j) da[j] = dot2(__builtin_bit_cast(u32x4, xv[i])[j], __builtin_bit_cast(u32x4, gv[i])[j], da[j]);
  const float v = sum8((da[0] + da[1]) + (da[2] + da[3])) * 1.4426950408889634f;
  const float e = 1.0f + __builtin_amdgcn_exp2f((c & 1) ? v : -v);
  const float e1 = __builtin_fminf(e, 1.0e30f), e0 = 1.0f / e;
  st4(G + (c & 1) * 2048 + row, __float_as_uint((c & 1) ? e1 : e0));
  STAMP(0, 5);
  unsigned d0;
  {
    const char* pc;
    unsigned span;
    asm volatile("s_getpc_b64 s[20:21]\n\ts_mov_b32 %1, kernel1@rel32@lo+4\n\ts_add_u32 s20, s20, %1\n\ts_addc_u32 s21, s21, kernel1@rel32@hi+16"
                 : "={s[20:21]}"(pc), "=s"(span)::"scc");
    span = 0u - span;
    span = span < (1u << 18) ? span : 0u;
    const unsigned o = (unsigned)(((id >> 3) * 8 + wave) * 64 + lane) * 128u;
    gld4(d0, pc, o < span ? o : 0u);
  }
  asm volatile("" ::"v"(d0) : "memory");
}

__global__ __launch_bounds__(512, 2) void kernelQ(const char* __restrict__ X, const char* __restrict__ Wqkv,
                                                   const char* __restrict__ gw, char* __restrict__ T,
                                                   char* __restrict__ XN, float* __restrict__ G,
                                                   char* __restrict__ FL) {
  __shared__ __attribute__((aligned(16))) char lds[NSQ * Q_SLOT];
  const int wg = blockIdx.x;
  const int wave = __builtin_amdgcn_readfirstlane(threadIdx.x >> 6);
  const int lane = threadIdx.x & 63;
  if (wg < 224) {
    const int xcd = wg & 7, t = wg >> 3, rl = t & 3, cl = t >> 2;
    const int joff = (3 * (cl + rl) + (rl == 3 ? 1 : 0)) % 9;
    qkv_role(lds, X, Wqkv, T, (xcd & 3) * 4 + rl, (xcd >> 2) * 7 + cl, joff, wave, lane);
  } else {
    STAMP(0, 0);
    ln_role(X, gw, XN, G, FL, wg - 224, wave, lane);
  }
  STAMP(0, 6);
  STAMP(0, 7);
}

extern "C" void kernel_launch(void* const* d_in, const int* in_sizes, int n_in, void* d_out, int out_size, void* d_ws,
                              size_t ws_size, hipStream_t stream) {
  const char* X = (const char*)d_in[0];
  const char* Wqkv = (const char*)d_in[1];
  const char* Wo = (const char*)d_in[2];
  const char* W1 = (const char*)d_in[4];
  const unsigned short* b1 = (const unsigned short*)d_in[5];
  const char* W2 = (const char*)d_in[6];
  const unsigned short* b2 = (const unsigned short*)d_in[7];
  const char* gw = (const char*)d_in[8];
  char* ws = (char*)d_ws;
  char* T = ws + WS_T;
  char* H = ws + WS_H;
  char* O = ws + WS_O;
  char* P = ws + WS_P;
  char* XN = ws + WS_XN;
  char* FL = ws + WS_FL;
  float* G = (float*)(ws + WS_G);
  kernelQ<<<QGRID, 512, 0, stream>>>(X, Wqkv, gw, T, XN, G, FL);
  kernel1<<<512, 512, 0, stream>>>(T, O, XN, W1, b1, H, W2, b2, Wo, (const char*)G, P, FL, (char*)d_out);
}
```

```cpp
#include <hip/hip_runtime.h>
#include <stdint.h>

typedef __attribute__((ext_vector_type(8))) short bf16x8;
typedef __attribute__((ext_vector_type(4))) short s16x4;
typedef __attribute__((ext_vector_type(2))) __bf16 bf16x2_t;
typedef __attribute__((ext_vector_type(2))) float f32x2;
typedef __attribute__((ext_vector_type(4))) float f32x4;
typedef __attribute__((ext_vector_type(16))) float f32x16;
typedef __attribute__((ext_vector_type(4))) unsigned u32x4;
typedef __attribute__((ext_vector_type(2))) unsigned u32x2;
typedef __attribute__((address_space(3))) s16x4* lds_s16x4_p;
typedef __attribute__((address_space(3))) char* lds_p;
typedef __attribute__((address_space(3))) const bf16x8* lds_bf16x8_p;

#define STAMP(k, i)
#define STAMP2(i)
#define SST(i)
#define SS8(i)
#define SSD()
#define SSW()
#define DI __device__ __forceinline__
#define MFMA32(a, b, c) __builtin_amdgcn_mfma_f32_32x32x16_bf16((a), (b), (c), 0, 0, 0)
#define MFMA32K8(a, b, c) __builtin_amdgcn_mfma_f32_32x32x8bf16_1k((a), (b), (c), 0, 0, 0)
#define WAIT_VM(n) asm volatile("s_waitcnt vmcnt(" #n ")" ::: "memory")
#define BARRIER() __builtin_amdgcn_s_barrier()
#define SB() __builtin_amdgcn_sched_barrier(0)

constexpr int S_ = 2048, D_ = 576, F_ = 4608, NQKV = 1728;
constexpr int TSTEP = 640;
constexpr int TP = 1736 * 16;
constexpr int OEX = 64 * 32 * 1024;
constexpr size_t WS_T = 0;
constexpr size_t WS_H = 8u << 20;
constexpr size_t WS_O = 28u << 20;
constexpr size_t WS_G = 31u << 20;
constexpr size_t WS_FL = (31u << 20) + (64u << 10);
constexpr size_t WS_P = 32u << 20;
constexpr size_t WS_XN = 44u << 20;
constexpr size_t WS_XF = 47u << 20;
constexpr int FL_Q = 32768, FL_O = 36864, FL_P = 45056;

DI unsigned pk2(float a, float b) {
  f32x2 v = {a, b};
  bf16x2_t r = __builtin_convertvector(v, bf16x2_t);
  return __builtin_bit_cast(unsigned, r);
}
DI float bf2f(unsigned short u) { return __uint_as_float(((unsigned)u) << 16); }
DI s16x4 tr_read(const char* p) {
  return __builtin_amdgcn_ds_read_tr16_b64_v4i16((lds_s16x4_p)(p));
}
DI s16x4 trl(lds_p p) { return __builtin_amdgcn_ds_read_tr16_b64_v4i16((lds_s16x4_p)p); }
DI bf16x8 ld8(lds_p p) { return *(lds_bf16x8_p)p; }
DI float xor32(float v) { return __shfl_xor(v, 32, 64); }

DI void glds16s(const char* sbase, unsigned voff, unsigned lds_dst) {
  unsigned keep;
  asm volatile("s_mov_b32 %0, m0\n\ts_mov_b32 m0, %3\n\ts_nop 0\n\tglobal_load_lds_dwordx4 %1, %2\n\ts_mov_b32 m0, %0"
               : "=&s"(keep)
               : "v"(voff), "s"(sbase), "s"(lds_dst)
               : "memory");
}
DI void glds16c(const char* sbase, unsigned voff, unsigned lds_dst) {
  unsigned keep;
  asm volatile("s_mov_b32 %0, m0\n\ts_mov_b32 m0, %3\n\ts_nop 0\n\tglobal_load_lds_dwordx4 %1, %2 sc1\n\ts_mov_b32 m0, %0"
               : "=&s"(keep)
               : "v"(voff), "s"(sbase), "s"(lds_dst)
               : "memory");
}
DI void touch(const char* sbase, unsigned voff, unsigned lds_dst) {
  unsigned keep;
  asm volatile("s_mov_b32 %0, m0\n\ts_mov_b32 m0, %3\n\ts_nop 0\n\tglobal_load_lds_dword %1, %2\n\ts_mov_b32 m0, %0"
               : "=&s"(keep)
               : "v"(voff), "s"(sbase), "s"(lds_dst)
               : "memory");
}
DI void gld16(bf16x8& d, const char* sbase, unsigned voff) {
  asm volatile("global_load_dwordx4 %0, %1, %2" : "=v"(d) : "v"(voff), "s"(sbase) : "memory");
}
DI void gld16c(bf16x8& d, const char* sbase, unsigned voff) {
  asm volatile("global_load_dwordx4 %0, %1, %2 sc1" : "=v"(d) : "v"(voff), "s"(sbase) : "memory");
}
DI void gld4c(unsigned& d, const char* sbase, unsigned voff) {
  asm volatile("global_load_dword %0, %1, %2 sc1" : "=v"(d) : "v"(voff), "s"(sbase) : "memory");
}
DI void gld4(unsigned& d, const char* sbase, unsigned voff) {
  asm volatile("global_load_dword %0, %1, %2" : "=v"(d) : "v"(voff), "s"(sbase) : "memory");
}
DI void gld4k(unsigned& d, const char* sbase, unsigned voff) {
  asm volatile("global_load_dword %0, %1, %2" : "+v"(d) : "v"(voff), "s"(sbase) : "memory");
}
DI unsigned lds_addr(const void* p) { return (unsigned)(uintptr_t)(lds_p)p; }

DI void st16(void* p, u32x4 v) { asm volatile("global_store_dwordx4 %0, %1, off sc1\n\ts_nop 1" ::"v"(p), "v"(v) : "memory"); }
DI void st16p(void* p, u32x4 v) { asm volatile("global_store_dwordx4 %0, %1, off\n\ts_nop 1" ::"v"(p), "v"(v) : "memory"); }
DI void st4(void* p, unsigned v) { asm volatile("global_store_dword %0, %1, off sc1" ::"v"(p), "v"(v) : "memory"); }

DI int fresh_lane() {
  int l;
  asm volatile("v_mbcnt_lo_u32_b32 %0, -1, 0\n\tv_mbcnt_hi_u32_b32 %0, -1, %0" : "=v"(l));
  return l;
}
template <int I>
struct IC { static constexpr int v = I; };
template <int I, int N, class F>
DI void static_for(F&& f) {
  if constexpr (I < N) {
    f(IC<I>{});
    static_for<I + 1, N>(f);
  }
}
template <int N>
DI void wait_vm() { asm volatile("s_waitcnt vmcnt(%0)" ::"i"(N) : "memory"); }
template <int N>
DI void wait_vm1(bf16x8& a) { asm volatile("s_waitcnt vmcnt(%1)" : "+v"(a) : "i"(N) : "memory"); }
DI void spin_flags(const char* base, unsigned voff) {
#pragma unroll 1
  for (int it = 0; it < (1 << 17); ++it) {
    unsigned f;
    gld4c(f, base, voff);
    asm volatile("s_waitcnt vmcnt(0)" : "+v"(f)::"memory");
    if (!__ballot(f != 1u)) break;
    __builtin_amdgcn_s_sleep(4);
  }
}
DI bf16x8 cat8(s16x4 lo, s16x4 hi) { return __builtin_shufflevector(lo, hi, 0, 1, 2, 3, 4, 5, 6, 7); }

#define SENT 0
DI unsigned nsent(unsigned v) { return SENT ? __builtin_elementwise_min(v, 0xfffffffeu) : v; }
DI unsigned umax4(bf16x8 x, unsigned m) {
  const u32x4 v = __builtin_bit_cast(u32x4, x);
  m = __builtin_elementwise_max(__builtin_elementwise_max(v[0], v[1]), m);
  return __builtin_elementwise_max(__builtin_elementwise_max(v[2], v[3]), m);
}
DI f32x16 qk_tile(lds_p kb, lds_p ke, bf16x8 qf, s16x4 qe, f32x16 c) {
  s16x4 lo = trl(kb);
  s16x4 hi = trl(kb + 64);
  s16x4 ex = trl(ke);
  bf16x8 kf = __builtin_shufflevector(lo, hi, 0, 1, 2, 3, 4, 5, 6, 7);
  f32x16 s = MFMA32(kf, qf, c);
  s = MFMA32K8(ex, qe, s);
  return s;
}
DI f32x16 pv_tile(lds_p vb, const f32x16& pe, f32x16 o) {
  bf16x8 v0 = ld8(vb);
  bf16x8 v1 = ld8(vb + TSTEP);
  u32x4 p0, p1;
#pragma unroll
  for (int i = 0; i < 4; ++i) {
    p0[i] = pk2(pe[2 * i], pe[2 * i + 1]);
    p1[i] = pk2(pe[8 + 2 * i], pe[8 + 2 * i + 1]);
  }
  o = MFMA32(v0, __builtin_bit_cast(bf16x8, p0), o);
  o = MFMA32(v1, __builtin_bit_cast(bf16x8, p1), o);
  return o;
}
struct KFrag { bf16x8 f; s16x4 e; };
struct VFrag { bf16x8 v0, v1; };
DI KFrag load_k(lds_p kb, lds_p ke) {
  KFrag k;
  s16x4 lo = trl(kb);
  s16x4 hi = trl(kb + 64);
  k.e = trl(ke);
  k.f = __builtin_shufflevector(lo, hi, 0, 1, 2, 3, 4, 5, 6, 7);
  return k;
}
DI VFrag load_v(lds_p vb) {
  VFrag v;
  v.v0 = ld8(vb);
  v.v1 = ld8(vb + TSTEP);
  return v;
}
#define EXP2(x) __builtin_amdgcn_exp2f(x)
#define PTRUNC 1
DI unsigned pkp(float a, float b) {
  return __builtin_amdgcn_perm(__float_as_uint(b), __float_as_uint(a), 0x07060302u);
}
#define APM 2
#define APMASK 0x77
#define PHI(i) if constexpr ((PM >> (i)) & 1) __builtin_amdgcn_s_setprio(APM)
#define PLO(i) if constexpr ((PM >> (i)) & 1) __builtin_amdgcn_s_setprio(0)
template <int PM>
DI void tile_step(const f32x16& s, f32x16& sn, const KFrag& k, const VFrag& v, bf16x8 qf, s16x4 qe,
                  f32x16& o) {
  f32x16 negm;
#pragma unroll
  for (int i = 0; i < 16; ++i) negm[i] = 0.f;
  u32x4 p0, p1;
  p0[0] = pkp(EXP2(s[0]), EXP2(s[1]));
  p0[1] = pkp(EXP2(s[2]), EXP2(s[3]));
  SB();
  PHI(0);
  SB();
  f32x16 t = MFMA32(k.f, qf, negm);
  SB();
  PLO(0);
  SB();
  p0[2] = pkp(EXP2(s[4]), EXP2(s[5]));
  p0[3] = pkp(EXP2(s[6]), EXP2(s[7]));
  SB();
  PHI(1);
  SB();
  o = MFMA32(v.v0, __builtin_bit_cast(bf16x8, p0), o);
  SB();
  PLO(1);
  SB();
  p1[0] = pkp(EXP2(s[8]), EXP2(s[9]));
  p1[1] = pkp(EXP2(s[10]), EXP2(s[11]));
  SB();
  PHI(2);
  SB();
  t = MFMA32K8(k.e, qe, t);
  SB();
  PLO(2);
  SB();
  p1[2] = pkp(EXP2(s[12]), EXP2(s[13]));
  p1[3] = pkp(EXP2(s[14]), EXP2(s[15]));
  SB();
  PHI(3);
  SB();
  o = MFMA32(v.v1, __builtin_bit_cast(bf16x8, p1), o);
  SB();
  PLO(3);
  SB();
  sn = t;
}
DI float rowmax16(const f32x16& s) {
  float m = __builtin_fmaxf(s[0], s[1]);
#pragma unroll
  for (int i = 2; i < 16; ++i) m = __builtin_fmaxf(m, s[i]);
  return __builtin_fmaxf(m, xor32(m));
}

constexpr int AT_SLOT = 20480;
constexpr int at_cnt(int c, int grp) { return 2 + (((c & 1) == grp) ? 1 : 0); }
constexpr int at_off(int tt) { return ((tt >> 3) & 1) * AT_SLOT + (tt & 7) * 1280; }

#define TOUCH_AT 9
#define ADEF 0
#define PRE_AT 0
DI void attn_role(char* lds, const char* T, char* O, char* P, const char* __restrict__ G, char* FL, const char* __restrict__ W2,
                  const char* __restrict__ Wo, int wg, int wave, int lane) {
  const int head = (wg & 7) * 4 + ((wg >> 3) & 3);
  const int qt = wg >> 5;
  const int r = lane & 31, h = lane >> 5;
  const int grp = wave >> 2;
  const unsigned lbase = lds_addr(lds);
  const int p1 = wave + 8, p2 = 16 + (wave & 3);
  auto mkoff = [&](int pc, int first) {
    int isv = pc >= 10;
    int sl = (pc - 10 * isv) * 64 + lane;
    int seg = sl / 20, row = sl - seg * 20;
    int slot = row < 18 ? first + 576 * isv + head * 18 + row : 1728 + row - 18;
    return (unsigned)(seg * TP + slot * 16);
  };
  const unsigned v0 = mkoff(wave, 576), v1 = mkoff(p1, 576), v2 = mkoff(p2, 576);
  auto issue = [&](int c) {
    unsigned dst = lbase + (c & 3) * AT_SLOT;
    const char* src = T + (size_t)c * (32 * TP);
    glds16s(src, v0, dst + wave * 1024);
    glds16s(src, v1, dst + p1 * 1024);
    if ((c & 1) == grp) glds16s(src, v2, dst + p2 * 1024);
  };
  STAMP(1, 1);
  unsigned gbits;
  gld4(gbits, G + (qt * 256 + wave * 32) * 4, r * 4);
  {
    const char* src = T + (size_t)qt * (32 * TP);
    glds16s(src, mkoff(wave, 0), lbase + 3 * AT_SLOT + wave * 1024);
    if (wave < 2) glds16s(src, mkoff(p1, 0), lbase + 3 * AT_SLOT + p1 * 1024);
  }
  issue(0);
  issue(1);
  issue(2);
  const int q = (lane >> 2) & 3, p = lane & 3, g = (lane >> 4) & 1;
  const lds_p L = (lds_p)lds;
  const lds_p kb = L + g * TSTEP + (p & 1) * (TSTEP / 2) + (8 * h + q) * 16 + (p >> 1) * 8;
  const lds_p ke = L + g * TSTEP + (p & 1) * (TSTEP / 2) + (16 + q) * 16 + (p >> 1) * 8;
  const lds_p vb = L + 10240 + h * (TSTEP / 2) + (r < 19 ? r : 19) * 16;
  lds_p kb2 = kb + 2 * AT_SLOT, ke2 = ke + 2 * AT_SLOT, vb2 = vb + 2 * AT_SLOT;
  asm volatile("" : "+v"(kb2), "+v"(ke2), "+v"(vb2));
  asm volatile("s_waitcnt vmcnt(5)" : "+v"(gbits)::"memory");
  BARRIER();
  STAMP(1, 2);
  bf16x8 qf;
  s16x4 qe;
  {
    KFrag qq = load_k(kb2 + AT_SLOT + wave * 1280, ke2 + AT_SLOT + wave * 1280);
    qf = qq.f;
    qe[0] = h ? (short)0 : qq.e[0];
    qe[1] = h ? (short)0 : qq.e[1];
    qe[2] = 0;
    qe[3] = 0;
  }

  const float gate = __uint_as_float(gbits);
  f32x16 zero;
#pragma unroll
  for (int i = 0; i < 16; ++i) zero[i] = 0.f;
  f32x16 o = zero;
  f32x16 sA = qk_tile(kb, ke, qf, qe, zero);
  {
    const float nx = -rowmax16(sA);
    const unsigned mb = (__float_as_uint(nx) + (nx > 0.f ? 0xffffu : 0u)) >> 16;
    const float nm = __uint_as_float(mb << 16);
#pragma unroll
    for (int i = 0; i < 16; ++i) sA[i] += nm;
    qe[2] = h ? (short)0 : (short)mb;
  }
  KFrag kA, kB;
  VFrag vA, vB;
  kB = load_k(kb + 1280, ke + 1280);
  vA = load_v(vb);
  f32x16 sB;
#pragma unroll 1
  for (int cc = 0; cc < 8; cc += 4) {
    static_for<0, 32>([&](auto Tc) {
      constexpr int tt = decltype(Tc)::v, ci = tt >> 3;
      if constexpr ((tt & 7) == 6) {
        if (cc == 0 || ci < 2) {
          if (grp == 0) wait_vm<at_cnt(ci + 2, 0)>(); else wait_vm<at_cnt(ci + 2, 1)>();
        } else if (ci == 2) wait_vm<0>();
        if (cc == 0 || ci < 3) BARRIER();
        if constexpr (ci == TOUCH_AT) {
          if (cc == 0) {
            const int l2 = fresh_lane();
            const char* src = W2 + (size_t)((wg & 7) >> 1) * (1152 * 1152) + (wg >> 3) * 41472;
            const unsigned dst = lbase + ((ci + 3) & 3) * AT_SLOT + wave * 1024;
            touch(src, (wave * 41 + l2 - (l2 > 40 ? 24 : 0) - (wave >> 2) * 4) * 128u, dst);
            if (wave == 0) touch(Wo + ((wg & 7) >> 1) * (144 * 1152) + (wg >> 3) * 5184, (l2 > 39 ? ((wg & 8) ? 39 : 40) : l2) * 128u + ((wg & 8) ? 64u : 0u), dst);
          }
        }
        if (cc == 0 || ci == 0) issue(cc + ci + 3);
      }
      constexpr int t1 = (tt + 1) & 31, t2 = (tt + 2) & 31;
      const lds_p kp = (t2 < 16 ? kb : kb2) + at_off(t2);
      const lds_p ep = (t2 < 16 ? ke : ke2) + at_off(t2);
      const lds_p vp = (t1 < 16 ? vb : vb2) + at_off(t1);
      if constexpr ((tt & 1) == 0) {
        kA = load_k(kp, ep);
        vB = load_v(vp);
        SB();
        tile_step<APMASK & 15>(sA, sB, kB, vA, qf, qe, o);
      } else {
        kB = load_k(kp, ep);
        vA = load_v(vp);
        SB();
        tile_step<(APMASK >> 4) & 15>(sB, sA, kA, vB, qf, qe, o);
      }
    });
    if (cc == 0) STAMP(1, 3);
  }
  STAMP(1, 4);
  STAMP(1, 5);
  float l = xor32(o[10]);
  l = h ? l : o[10];
  bool bad = !(l < 1.0e30f);
#pragma unroll
  for (int i = 0; i < 10; ++i) bad = bad || !(__builtin_fabsf(o[i]) < 1.0e37f);
  const int ob = (qt * 8 + wave) * 32 + head;
  auto publish = [&]() {
    const float inv = gate / l;
    u32x4 ov;
#pragma unroll
    for (int i = 0; i < 4; ++i) ov[i] = nsent(pk2(o[2 * i] * inv, o[2 * i + 1] * inv));
    st16(O + (size_t)ob * 1024 + lane * 16, ov);
    if (h == 0) st4(O + OEX + (ob >> 3) * 1024 + ((head >> 2) & 1) * 512 + r * 16 + (head & 3) * 4, nsent(pk2(o[8] * inv, o[9] * inv)));
    STAMP(1, 6);
    wait_vm<0>();
    if (lane == 0) st4(FL + FL_O + ob * 4, 1u);
  };
  const bool mybad = __any(bad);
  if (!mybad) publish();
  {
    int* fl = (int*)(lds + AT_SLOT);
    if (lane == 0) fl[wave] = mybad ? 1 : 0;
    __syncthreads();
    int v = fl[lane & 7];
    bad = __any(v != 0);
  }
  if (bad) {
    float m = -3.0e38f;
    o = zero;
    qe[2] = 0;
#pragma unroll 1
    for (int c = 0; c < 8; ++c) {
      __syncthreads();
      {
        const char* src = T + (size_t)c * (32 * TP);
        glds16s(src, v0, lbase + wave * 1024);
        glds16s(src, v1, lbase + p1 * 1024);
        if (grp == 0) glds16s(src, v2, lbase + p2 * 1024);
      }
      wait_vm<0>();
      __syncthreads();
#pragma unroll 1
      for (int t = 0; t < 8; ++t) {
        f32x16 s = qk_tile(kb + t * 1280, ke + t * 1280, qf, qe, zero);
        float mn = __builtin_fmaxf(m, rowmax16(s));
        float alpha = __builtin_amdgcn_exp2f(m - mn);
        m = mn;
        f32x16 pe;
#pragma unroll
        for (int i = 0; i < 16; ++i) { pe[i] = __builtin_amdgcn_exp2f(s[i] - mn); o[i] *= alpha; }
        o = pv_tile(vb + t * 1280, pe, o);
      }
    }
    l = xor32(o[10]);
    l = h ? l : o[10];
    if (mybad) publish();
  }
  STAMP(1, 7);
}

#define QKV_PRIO 2
#define ATT_PRIO 0
#define SKIP1 0
#define MLP_PRIO 1
#define GELU_PRIO MLP_PRIO
#define MLP2_PRIO MLP_PRIO
#define P1HI4 0
#define P1HI5 0
#define P2HI4 0
#define P2HI5 0
#define HI_PRIO 3
#define PF2_AT 20
#define PSLP 2
#define XE_AT 33
constexpr int FL_P2 = 65536;
constexpr int CK3 = 30;
constexpr int M_IT = 36;
constexpr int M2_NT = 41;
struct M2S { int slot[M2_NT]; int it[M2_NT]; };
constexpr M2S m2_sched(int U) {
  M2S r{};
  const int E = 2 * U - 2;
  int next = 0;
  for (; next < 4; ++next) { r.slot[next] = next; r.it[next] = -1; }
  for (int j = 1; next < M2_NT; ++j) {
    r.slot[next] = r.slot[j - 1];
    r.it[next] = j;
    ++next;
    if (next < M2_NT && (j == E || j == E + 1)) { r.slot[next] = 4 + (j - E); r.it[next] = j; ++next; }
  }
  return r;
}
constexpr int m2_ck1(int U) { return 2 * U - 4; }
constexpr int m2_ck2(int U) { return (U == 5 ? 26 : 2 * U + 8) - 4; }
constexpr int m2_wait(int U, int kh, int j, int mode) {
  const M2S sc = m2_sched(U);
  const int U2 = U == 5 ? 2 : 3;
  int n = 0, last = -1;
  for (int i = -1; i < j + mode; ++i) {
    if (i == m2_ck1(U) - 2 || i == m2_ck2(U) - 2 || (SENT == 2 && i == CK3 - 2)) { if (mode == 1 && i == j - 2) last = n; ++n; }
    for (int s = 0; s < M2_NT; ++s)
      if (sc.it[s] == i) {
        const int c = (U2 == 3 && (s & 1) == kh) ? 2 : 1;
        n += c;
        if (mode == 0 && s <= j + 1) last = n - 1;
      }
    if (i == -1) n += 2 * U;
    if (i >= 0 && i < j && i + 4 >= 2 * U && i + 4 < M_IT) { if (mode == 0 && i + 4 <= j) last = n; ++n; }
  }
  return n - 1 - last;
}
constexpr bool m2_need(int U, int j) { return j >= 3; }

template <int N>
DI void pin_acc(f32x16* a) {
  if constexpr (N == 5)
    asm volatile("" : "+{v[0:15]}"(a[0]), "+{v[16:31]}"(a[1]), "+{v[32:47]}"(a[2]), "+{v[48:63]}"(a[3]), "+{v[64:79]}"(a[4]));
  else
    asm volatile("" : "+{v[0:15]}"(a[0]), "+{v[16:31]}"(a[1]), "+{v[32:47]}"(a[2]), "+{v[48:63]}"(a[3]));
}
#define XLD(R) asm volatile("global_load_dwordx4 %0, %1, %2 sc1" : "={" R "}"(d) : "v"(voff), "s"(sbase) : "memory")
#define XLP(R) asm volatile("global_load_dwordx4 %0, %1, %2" : "={" R "}"(d) : "v"(voff), "s"(sbase) : "memory")
template <int I>
DI void xloadp(bf16x8& d, const char* sbase, unsigned voff) {
  if constexpr (I == 0) XLP("v[80:83]");
  else if constexpr (I == 1) XLP("v[84:87]");
  else if constexpr (I == 2) XLP("v[88:91]");
  else XLP("v[92:95]");
}
template <int I>
DI void xload(bf16x8& d, const char* sbase, unsigned voff) {
  if constexpr (I == 0) XLD("v[80:83]");
  else if constexpr (I == 1) XLD("v[84:87]");
  else if constexpr (I == 2) XLD("v[88:91]");
  else XLD("v[92:95]");
}
template <int I>
DI void pload(bf16x8& d, const char* sbase, unsigned voff) {
  if constexpr (I == 0) XLD("v[48:51]");
  else if constexpr (I == 1) XLD("v[52:55]");
  else if constexpr (I == 2) XLD("v[56:59]");
  else if constexpr (I == 3) XLD("v[60:63]");
  else if constexpr (I == 4) XLD("v[64:67]");
  else if constexpr (I == 5) XLD("v[68:71]");
  else if constexpr (I == 6) XLD("v[72:75]");
  else if constexpr (I == 7) XLD("v[76:79]");
  else if constexpr (I == 8) XLD("v[80:83]");
  else if constexpr (I == 9) XLD("v[84:87]");
  else if constexpr (I == 10) XLD("v[88:91]");
  else if constexpr (I == 11) XLD("v[92:95]");
  else if constexpr (I == 12) XLD("v[96:99]");
  else if constexpr (I == 13) XLD("v[100:103]");
  else if constexpr (I == 14) XLD("v[104:107]");
  else if constexpr (I == 15) XLD("v[108:111]");
  else if constexpr (I == 16) XLD("v[112:115]");
  else if constexpr (I == 17) XLD("v[116:119]");
}
template <int I>
DI void ppin(bf16x8& d) {
  if constexpr (I == 0) asm volatile("" : "+{v[48:51]}"(d));
  else if constexpr (I == 1) asm volatile("" : "+{v[52:55]}"(d));
  else if constexpr (I == 2) asm volatile("" : "+{v[56:59]}"(d));
  else if constexpr (I == 3) asm volatile("" : "+{v[60:63]}"(d));
  else if constexpr (I == 4) asm volatile("" : "+{v[64:67]}"(d));
  else if constexpr (I == 5) asm volatile("" : "+{v[68:71]}"(d));
  else if constexpr (I == 6) asm volatile("" : "+{v[72:75]}"(d));
  else if constexpr (I == 7) asm volatile("" : "+{v[76:79]}"(d));
  else if constexpr (I == 8) asm volatile("" : "+{v[80:83]}"(d));
  else if constexpr (I == 9) asm volatile("" : "+{v[84:87]}"(d));
  else if constexpr (I == 10) asm volatile("" : "+{v[88:91]}"(d));
  else if constexpr (I == 11) asm volatile("" : "+{v[92:95]}"(d));
  else if constexpr (I == 12) asm volatile("" : "+{v[96:99]}"(d));
  else if constexpr (I == 13) asm volatile("" : "+{v[100:103]}"(d));
  else if constexpr (I == 14) asm volatile("" : "+{v[104:107]}"(d));
  else if constexpr (I == 15) asm volatile("" : "+{v[108:111]}"(d));
  else if constexpr (I == 16) asm volatile("" : "+{v[112:115]}"(d));
  else if constexpr (I == 17) asm volatile("" : "+{v[116:119]}"(d));
}
#define XDS(R) asm volatile("ds_read_b128 %0, %1 offset:%2" : "={" R "}"(d) : "v"(a), "i"(OFF) : "memory")
template <int I, int OFF>
DI void xstash(bf16x8& d, unsigned a) {
  if constexpr (I == 0) XDS("v[80:83]");
  else if constexpr (I == 1) XDS("v[84:87]");
  else if constexpr (I == 2) XDS("v[88:91]");
  else XDS("v[92:95]");
}
template <int I>
DI void pin_x1(bf16x8& x) {
  if constexpr (I == 0) asm volatile("" : "+{v[80:83]}"(x));
  else if constexpr (I == 1) asm volatile("" : "+{v[84:87]}"(x));
  else if constexpr (I == 2) asm volatile("" : "+{v[88:91]}"(x));
  else asm volatile("" : "+{v[92:95]}"(x));
}
template <int I>
DI void pin_w(bf16x8& w) {
  if constexpr (I == 0) asm volatile("" : "+{v[96:99]}"(w));
  else if constexpr (I == 1) asm volatile("" : "+{v[100:103]}"(w));
  else if constexpr (I == 2) asm volatile("" : "+{v[104:107]}"(w));
  else if constexpr (I == 3) asm volatile("" : "+{v[108:111]}"(w));
  else asm volatile("" : "+{v[112:115]}"(w));
}
template <int N>
DI void pair_sum(f32x16* a, unsigned exw, unsigned exr, int kh) {
  if constexpr (N == 5)
    asm volatile(
      "s_nop 15\ns_nop 3\ns_cmp_lg_u32 %[kh], 0\ns_cbranch_scc1 XU%=\nv_cvt_pk_bf16_f32 v80, v8, v9\nv_cvt_pk_bf16_f32 v81, v10, v11\n"
      "v_cvt_pk_bf16_f32 v82, v12, v13\nv_cvt_pk_bf16_f32 v83, v14, v15\nds_write_b128 %[w], v[80:83] offset:0\n"
      "v_cvt_pk_bf16_f32 v84, v24, v25\nv_cvt_pk_bf16_f32 v85, v26, v27\nv_cvt_pk_bf16_f32 v86, v28, v29\nv_cvt_pk_bf16_f32 v87, v30, v31\n"
      "ds_write_b128 %[w], v[84:87] offset:1024\nv_cvt_pk_bf16_f32 v88, v40, v41\nv_cvt_pk_bf16_f32 v89, v42, v43\n"
      "v_cvt_pk_bf16_f32 v90, v44, v45\nv_cvt_pk_bf16_f32 v91, v46, v47\nds_write_b128 %[w], v[88:91] offset:2048\n"
      "v_cvt_pk_bf16_f32 v92, v56, v57\nv_cvt_pk_bf16_f32 v93, v58, v59\nv_cvt_pk_bf16_f32 v94, v60, v61\nv_cvt_pk_bf16_f32 v95, v62, v63\n"
      "ds_write_b128 %[w], v[92:95] offset:3072\nv_cvt_pk_bf16_f32 v96, v72, v73\nv_cvt_pk_bf16_f32 v97, v74, v75\n"
      "v_cvt_pk_bf16_f32 v98, v76, v77\nv_cvt_pk_bf16_f32 v99, v78, v79\nds_write_b128 %[w], v[96:99] offset:4096\ns_waitcnt lgkmcnt(0)\n"
      "s_barrier\nds_read_b128 v[80:83], %[r] offset:0\nds_read_b128 v[84:87], %[r] offset:1024\nds_read_b128 v[88:91], %[r] offset:2048\n"
      "ds_read_b128 v[92:95], %[r] offset:3072\nds_read_b128 v[96:99], %[r] offset:4096\ns_waitcnt lgkmcnt(4)\nv_lshlrev_b32 v8, 16, v80\n"
      "v_and_b32 v9, 0xffff0000, v80\nv_lshlrev_b32 v10, 16, v81\nv_and_b32 v11, 0xffff0000, v81\nv_lshlrev_b32 v12, 16, v82\n"
      "v_and_b32 v13, 0xffff0000, v82\nv_lshlrev_b32 v14, 16, v83\nv_and_b32 v15, 0xffff0000, v83\nv_pk_add_f32 v[0:1], v[8:9], v[0:1]\n"
      "v_pk_add_f32 v[2:3], v[10:11], v[2:3]\nv_pk_add_f32 v[4:5], v[12:13], v[4:5]\nv_pk_add_f32 v[6:7], v[14:15], v[6:7]\n"
      "s_waitcnt lgkmcnt(3)\nv_lshlrev_b32 v24, 16, v84\nv_and_b32 v25, 0xffff0000, v84\nv_lshlrev_b32 v26, 16, v85\n"
      "v_and_b32 v27, 0xffff0000, v85\nv_lshlrev_b32 v28, 16, v86\nv_and_b32 v29, 0xffff0000, v86\nv_lshlrev_b32 v30, 16, v87\n"
      "v_and_b32 v31, 0xffff0000, v87\nv_pk_add_f32 v[16:17], v[24:25], v[16:17]\nv_pk_add_f32 v[18:19], v[26:27], v[18:19]\n"
      "v_pk_add_f32 v[20:21], v[28:29], v[20:21]\nv_pk_add_f32 v[22:23], v[30:31], v[22:23]\ns_waitcnt lgkmcnt(2)\n"
      "v_lshlrev_b32 v40, 16, v88\nv_and_b32 v41, 0xffff0000, v88\nv_lshlrev_b32 v42, 16, v89\nv_and_b32 v43, 0xffff0000, v89\n"
      "v_lshlrev_b32 v44, 16, v90\nv_and_b32 v45, 0xffff0000, v90\nv_lshlrev_b32 v46, 16, v91\nv_and_b32 v47, 0xffff0000, v91\n"
      "v_pk_add_f32 v[32:33], v[40:41], v[32:33]\nv_pk_add_f32 v[34:35], v[42:43], v[34:35]\nv_pk_add_f32 v[36:37], v[44:45], v[36:37]\n"
      "v_pk_add_f32 v[38:39], v[46:47], v[38:39]\ns_waitcnt lgkmcnt(1)\nv_lshlrev_b32 v56, 16, v92\nv_and_b32 v57, 0xffff0000, v92\n"
      "v_lshlrev_b32 v58, 16, v93\nv_and_b32 v59, 0xffff0000, v93\nv_lshlrev_b32 v60, 16, v94\nv_and_b32 v61, 0xffff0000, v94\n"
      "v_lshlrev_b32 v62, 16, v95\nv_and_b32 v63, 0xffff0000, v95\nv_pk_add_f32 v[48:49], v[56:57], v[48:49]\n"
      "v_pk_add_f32 v[50:51], v[58:59], v[50:51]\nv_pk_add_f32 v[52:53], v[60:61], v[52:53]\nv_pk_add_f32 v[54:55], v[62:63], v[54:55]\n"
      "s_waitcnt lgkmcnt(0)\nv_lshlrev_b32 v72, 16, v96\nv_and_b32 v73, 0xffff0000, v96\nv_lshlrev_b32 v74, 16, v97\n"
      "v_and_b32 v75, 0xffff0000, v97\nv_lshlrev_b32 v76, 16, v98\nv_and_b32 v77, 0xffff0000, v98\nv_lshlrev_b32 v78, 16, v99\n"
      "v_and_b32 v79, 0xffff0000, v99\nv_pk_add_f32 v[64:65], v[72:73], v[64:65]\nv_pk_add_f32 v[66:67], v[74:75], v[66:67]\n"
      "v_pk_add_f32 v[68:69], v[76:77], v[68:69]\nv_pk_add_f32 v[70:71], v[78:79], v[70:71]\ns_branch XE%=\nXU%=:\n"
      "v_cvt_pk_bf16_f32 v80, v0, v1\nv_cvt_pk_bf16_f32 v81, v2, v3\nv_cvt_pk_bf16_f32 v82, v4, v5\nv_cvt_pk_bf16_f32 v83, v6, v7\n"
      "ds_write_b128 %[w], v[80:83] offset:0\nv_cvt_pk_bf16_f32 v84, v16, v17\nv_cvt_pk_bf16_f32 v85, v18, v19\n"
      "v_cvt_pk_bf16_f32 v86, v20, v21\nv_cvt_pk_bf16_f32 v87, v22, v23\nds_write_b128 %[w], v[84:87] offset:1024\n"
      "v_cvt_pk_bf16_f32 v88, v32, v33\nv_cvt_pk_bf16_f32 v89, v34, v35\nv_cvt_pk_bf16_f32 v90, v36, v37\nv_cvt_pk_bf16_f32 v91, v38, v39\n"
      "ds_write_b128 %[w], v[88:91] offset:2048\nv_cvt_pk_bf16_f32 v92, v48, v49\nv_cvt_pk_bf16_f32 v93, v50, v51\n"
      "v_cvt_pk_bf16_f32 v94, v52, v53\nv_cvt_pk_bf16_f32 v95, v54, v55\nds_write_b128 %[w], v[92:95] offset:3072\n"
      "v_cvt_pk_bf16_f32 v96, v64, v65\nv_cvt_pk_bf16_f32 v97, v66, v67\nv_cvt_pk_bf16_f32 v98, v68, v69\nv_cvt_pk_bf16_f32 v99, v70, v71\n"
      "ds_write_b128 %[w], v[96:99] offset:4096\ns_waitcnt lgkmcnt(0)\ns_barrier\nds_read_b128 v[80:83], %[r] offset:0\n"
      "ds_read_b128 v[84:87], %[r] offset:1024\nds_read_b128 v[88:91], %[r] offset:2048\nds_read_b128 v[92:95], %[r] offset:3072\n"
      "ds_read_b128 v[96:99], %[r] offset:4096\ns_waitcnt lgkmcnt(4)\nv_lshlrev_b32 v0, 16, v80\nv_and_b32 v1, 0xffff0000, v80\n"
      "v_lshlrev_b32 v2, 16, v81\nv_and_b32 v3, 0xffff0000, v81\nv_lshlrev_b32 v4, 16, v82\nv_and_b32 v5, 0xffff0000, v82\n"
      "v_lshlrev_b32 v6, 16, v83\nv_and_b32 v7, 0xffff0000, v83\nv_pk_add_f32 v[0:1], v[0:1], v[8:9]\n"
      "v_pk_add_f32 v[2:3], v[2:3], v[10:11]\nv_pk_add_f32 v[4:5], v[4:5], v[12:13]\nv_pk_add_f32 v[6:7], v[6:7], v[14:15]\n"
      "s_waitcnt lgkmcnt(3)\nv_lshlrev_b32 v16, 16, v84\nv_and_b32 v17, 0xffff0000, v84\nv_lshlrev_b32 v18, 16, v85\n"
      "v_and_b32 v19, 0xffff0000, v85\nv_lshlrev_b32 v20, 16, v86\nv_and_b32 v21, 0xffff0000, v86\nv_lshlrev_b32 v22, 16, v87\n"
      "v_and_b32 v23, 0xffff0000, v87\nv_pk_add_f32 v[16:17], v[16:17], v[24:25]\nv_pk_add_f32 v[18:19], v[18:19], v[26:27]\n"
      "v_pk_add_f32 v[20:21], v[20:21], v[28:29]\nv_pk_add_f32 v[22:23], v[22:23], v[30:31]\ns_waitcnt lgkmcnt(2)\n"
      "v_lshlrev_b32 v32, 16, v88\nv_and_b32 v33, 0xffff0000, v88\nv_lshlrev_b32 v34, 16, v89\nv_and_b32 v35, 0xffff0000, v89\n"
      "v_lshlrev_b32 v36, 16, v90\nv_and_b32 v37, 0xffff0000, v90\nv_lshlrev_b32 v38, 16, v91\nv_and_b32 v39, 0xffff0000, v91\n"
      "v_pk_add_f32 v[32:33], v[32:33], v[40:41]\nv_pk_add_f32 v[34:35], v[34:35], v[42:43]\nv_pk_add_f32 v[36:37], v[36:37], v[44:45]\n"
      "v_pk_add_f32 v[38:39], v[38:39], v[46:47]\ns_waitcnt lgkmcnt(1)\nv_lshlrev_b32 v48, 16, v92\nv_and_b32 v49, 0xffff0000, v92\n"
      "v_lshlrev_b32 v50, 16, v93\nv_and_b32 v51, 0xffff0000, v93\nv_lshlrev_b32 v52, 16, v94\nv_and_b32 v53, 0xffff0000, v94\n"
      "v_lshlrev_b32 v54, 16, v95\nv_and_b32 v55, 0xffff0000, v95\nv_pk_add_f32 v[48:49], v[48:49], v[56:57]\n"
      "v_pk_add_f32 v[50:51], v[50:51], v[58:59]\nv_pk_add_f32 v[52:53], v[52:53], v[60:61]\nv_pk_add_f32 v[54:55], v[54:55], v[62:63]\n"
      "s_waitcnt lgkmcnt(0)\nv_lshlrev_b32 v64, 16, v96\nv_and_b32 v65, 0xffff0000, v96\nv_lshlrev_b32 v66, 16, v97\n"
      "v_and_b32 v67, 0xffff0000, v97\nv_lshlrev_b32 v68, 16, v98\nv_and_b32 v69, 0xffff0000, v98\nv_lshlrev_b32 v70, 16, v99\n"
      "v_and_b32 v71, 0xffff0000, v99\nv_pk_add_f32 v[64:65], v[64:65], v[72:73]\nv_pk_add_f32 v[66:67], v[66:67], v[74:75]\n"
      "v_pk_add_f32 v[68:69], v[68:69], v[76:77]\nv_pk_add_f32 v[70:71], v[70:71], v[78:79]\nXE%=:\n"
      : "+{v[0:15]}"(a[0]), "+{v[16:31]}"(a[1]), "+{v[32:47]}"(a[2]), "+{v[48:63]}"(a[3]), "+{v[64:79]}"(a[4])
      : [w] "v"(exw), [r] "v"(exr), [kh] "s"(kh)
      : "memory", "scc", "v80", "v81", "v82", "v83", "v84", "v85", "v86", "v87", "v88", "v89", "v90", "v91", "v92", "v93", "v94", "v95", "v96", "v97", "v98", "v99");
  else
    asm volatile(
      "s_nop 15\ns_nop 3\ns_cmp_lg_u32 %[kh], 0\ns_cbranch_scc1 XU%=\nv_cvt_pk_bf16_f32 v80, v8, v9\nv_cvt_pk_bf16_f32 v81, v10, v11\n"
      "v_cvt_pk_bf16_f32 v82, v12, v13\nv_cvt_pk_bf16_f32 v83, v14, v15\nds_write_b128 %[w], v[80:83] offset:0\n"
      "v_cvt_pk_bf16_f32 v84, v24, v25\nv_cvt_pk_bf16_f32 v85, v26, v27\nv_cvt_pk_bf16_f32 v86, v28, v29\nv_cvt_pk_bf16_f32 v87, v30, v31\n"
      "ds_write_b128 %[w], v[84:87] offset:1024\nv_cvt_pk_bf16_f32 v88, v40, v41\nv_cvt_pk_bf16_f32 v89, v42, v43\n"
      "v_cvt_pk_bf16_f32 v90, v44, v45\nv_cvt_pk_bf16_f32 v91, v46, v47\nds_write_b128 %[w], v[88:91] offset:2048\n"
      "v_cvt_pk_bf16_f32 v92, v56, v57\nv_cvt_pk_bf16_f32 v93, v58, v59\nv_cvt_pk_bf16_f32 v94, v60, v61\nv_cvt_pk_bf16_f32 v95, v62, v63\n"
      "ds_write_b128 %[w], v[92:95] offset:3072\ns_waitcnt lgkmcnt(0)\ns_barrier\nds_read_b128 v[80:83], %[r] offset:0\n"
      "ds_read_b128 v[84:87], %[r] offset:1024\nds_read_b128 v[88:91], %[r] offset:2048\nds_read_b128 v[92:95], %[r] offset:3072\n"
      "s_waitcnt lgkmcnt(3)\nv_lshlrev_b32 v8, 16, v80\nv_and_b32 v9, 0xffff0000, v80\nv_lshlrev_b32 v10, 16, v81\n"
      "v_and_b32 v11, 0xffff0000, v81\nv_lshlrev_b32 v12, 16, v82\nv_and_b32 v13, 0xffff0000, v82\nv_lshlrev_b32 v14, 16, v83\n"
      "v_and_b32 v15, 0xffff0000, v83\nv_pk_add_f32 v[0:1], v[8:9], v[0:1]\nv_pk_add_f32 v[2:3], v[10:11], v[2:3]\n"
      "v_pk_add_f32 v[4:5], v[12:13], v[4:5]\nv_pk_add_f32 v[6:7], v[14:15], v[6:7]\ns_waitcnt lgkmcnt(2)\nv_lshlrev_b32 v24, 16, v84\n"
      "v_and_b32 v25, 0xffff0000, v84\nv_lshlrev_b32 v26, 16, v85\nv_and_b32 v27, 0xffff0000, v85\nv_lshlrev_b32 v28, 16, v86\n"
      "v_and_b32 v29, 0xffff0000, v86\nv_lshlrev_b32 v30, 16, v87\nv_and_b32 v31, 0xffff0000, v87\n"
      "v_pk_add_f32 v[16:17], v[24:25], v[16:17]\nv_pk_add_f32 v[18:19], v[26:27], v[18:19]\nv_pk_add_f32 v[20:21], v[28:29], v[20:21]\n"
      "v_pk_add_f32 v[22:23], v[30:31], v[22:23]\ns_waitcnt lgkmcnt(1)\nv_lshlrev_b32 v40, 16, v88\nv_and_b32 v41, 0xffff0000, v88\n"
      "v_lshlrev_b32 v42, 16, v89\nv_and_b32 v43, 0xffff0000, v89\nv_lshlrev_b32 v44, 16, v90\nv_and_b32 v45, 0xffff0000, v90\n"
      "v_lshlrev_b32 v46, 16, v91\nv_and_b32 v47, 0xffff0000, v91\nv_pk_add_f32 v[32:33], v[40:41], v[32:33]\n"
      "v_pk_add_f32 v[34:35], v[42:43], v[34:35]\nv_pk_add_f32 v[36:37], v[44:45], v[36:37]\nv_pk_add_f32 v[38:39], v[46:47], v[38:39]\n"
      "s_waitcnt lgkmcnt(0)\nv_lshlrev_b32 v56, 16, v92\nv_and_b32 v57, 0xffff0000, v92\nv_lshlrev_b32 v58, 16, v93\n"
      "v_and_b32 v59, 0xffff0000, v93\nv_lshlrev_b32 v60, 16, v94\nv_and_b32 v61, 0xffff0000, v94\nv_lshlrev_b32 v62, 16, v95\n"
      "v_and_b32 v63, 0xffff0000, v95\nv_pk_add_f32 v[48:49], v[56:57], v[48:49]\nv_pk_add_f32 v[50:51], v[58:59], v[50:51]\n"
      "v_pk_add_f32 v[52:53], v[60:61], v[52:53]\nv_pk_add_f32 v[54:55], v[62:63], v[54:55]\ns_branch XE%=\nXU%=:\n"
      "v_cvt_pk_bf16_f32 v80, v0, v1\nv_cvt_pk_bf16_f32 v81, v2, v3\nv_cvt_pk_bf16_f32 v82, v4, v5\nv_cvt_pk_bf16_f32 v83, v6, v7\n"
      "ds_write_b128 %[w], v[80:83] offset:0\nv_cvt_pk_bf16_f32 v84, v16, v17\nv_cvt_pk_bf16_f32 v85, v18, v19\n"
      "v_cvt_pk_bf16_f32 v86, v20, v21\nv_cvt_pk_bf16_f32 v87, v22, v23\nds_write_b128 %[w], v[84:87] offset:1024\n"
      "v_cvt_pk_bf16_f32 v88, v32, v33\nv_cvt_pk_bf16_f32 v89, v34, v35\nv_cvt_pk_bf16_f32 v90, v36, v37\nv_cvt_pk_bf16_f32 v91, v38, v39\n"
      "ds_write_b128 %[w], v[88:91] offset:2048\nv_cvt_pk_bf16_f32 v92, v48, v49\nv_cvt_pk_bf16_f32 v93, v50, v51\n"
      "v_cvt_pk_bf16_f32 v94, v52, v53\nv_cvt_pk_bf16_f32 v95, v54, v55\nds_write_b128 %[w], v[92:95] offset:3072\ns_waitcnt lgkmcnt(0)\n"
      "s_barrier\nds_read_b128 v[80:83], %[r] offset:0\nds_read_b128 v[84:87], %[r] offset:1024\nds_read_b128 v[88:91], %[r] offset:2048\n"
      "ds_read_b128 v[92:95], %[r] offset:3072\ns_waitcnt lgkmcnt(3)\nv_lshlrev_b32 v0, 16, v80\nv_and_b32 v1, 0xffff0000, v80\n"
      "v_lshlrev_b32 v2, 16, v81\nv_and_b32 v3, 0xffff0000, v81\nv_lshlrev_b32 v4, 16, v82\nv_and_b32 v5, 0xffff0000, v82\n"
      "v_lshlrev_b32 v6, 16, v83\nv_and_b32 v7, 0xffff0000, v83\nv_pk_add_f32 v[0:1], v[0:1], v[8:9]\n"
      "v_pk_add_f32 v[2:3], v[2:3], v[10:11]\nv_pk_add_f32 v[4:5], v[4:5], v[12:13]\nv_pk_add_f32 v[6:7], v[6:7], v[14:15]\n"
      "s_waitcnt lgkmcnt(2)\nv_lshlrev_b32 v16, 16, v84\nv_and_b32 v17, 0xffff0000, v84\nv_lshlrev_b32 v18, 16, v85\n"
      "v_and_b32 v19, 0xffff0000, v85\nv_lshlrev_b32 v20, 16, v86\nv_and_b32 v21, 0xffff0000, v86\nv_lshlrev_b32 v22, 16, v87\n"
      "v_and_b32 v23, 0xffff0000, v87\nv_pk_add_f32 v[16:17], v[16:17], v[24:25]\nv_pk_add_f32 v[18:19], v[18:19], v[26:27]\n"
      "v_pk_add_f32 v[20:21], v[20:21], v[28:29]\nv_pk_add_f32 v[22:23], v[22:23], v[30:31]\ns_waitcnt lgkmcnt(1)\n"
      "v_lshlrev_b32 v32, 16, v88\nv_and_b32 v33, 0xffff0000, v88\nv_lshlrev_b32 v34, 16, v89\nv_and_b32 v35, 0xffff0000, v89\n"
      "v_lshlrev_b32 v36, 16, v90\nv_and_b32 v37, 0xffff0000, v90\nv_lshlrev_b32 v38, 16, v91\nv_and_b32 v39, 0xffff0000, v91\n"
      "v_pk_add_f32 v[32:33], v[32:33], v[40:41]\nv_pk_add_f32 v[34:35], v[34:35], v[42:43]\nv_pk_add_f32 v[36:37], v[36:37], v[44:45]\n"
      "v_pk_add_f32 v[38:39], v[38:39], v[46:47]\ns_waitcnt lgkmcnt(0)\nv_lshlrev_b32 v48, 16, v92\nv_and_b32 v49, 0xffff0000, v92\n"
      "v_lshlrev_b32 v50, 16, v93\nv_and_b32 v51, 0xffff0000, v93\nv_lshlrev_b32 v52, 16, v94\nv_and_b32 v53, 0xffff0000, v94\n"
      "v_lshlrev_b32 v54, 16, v95\nv_and_b32 v55, 0xffff0000, v95\nv_pk_add_f32 v[48:49], v[48:49], v[56:57]\n"
      "v_pk_add_f32 v[50:51], v[50:51], v[58:59]\nv_pk_add_f32 v[52:53], v[52:53], v[60:61]\nv_pk_add_f32 v[54:55], v[54:55], v[62:63]\n"
      "XE%=:\n"
      : "+{v[0:15]}"(a[0]), "+{v[16:31]}"(a[1]), "+{v[32:47]}"(a[2]), "+{v[48:63]}"(a[3])
      : [w] "v"(exw), [r] "v"(exr), [kh] "s"(kh)
      : "memory", "scc", "v80", "v81", "v82", "v83", "v84", "v85", "v86", "v87", "v88", "v89", "v90", "v91", "v92", "v93", "v94", "v95");
}
DI void gld4ck(unsigned& d, const char* sbase, unsigned voff) {
  asm volatile("global_load_dword %0, %1, %2 sc1" : "+v"(d) : "v"(voff), "s"(sbase) : "memory");
}
#define PSTG 0
#define OUTSC 0
#define TS(a, b) do { if ((a) >= 0) SST((a) < 0 ? 0 : (a)); } while (0)
template <int U>
DI void mlp_track(char* lds, const char* __restrict__ XN, const char* __restrict__ W1,
                  const unsigned short* __restrict__ b1, char* H, const char* __restrict__ W2,
                  const unsigned short* __restrict__ b2, const char* __restrict__ Wo, const char* O,
                  const char* __restrict__ G, char* P, char* FL, char* __restrict__ out, int rt, int sp, int j4,
                  int koff, int wave, int lane) {
  constexpr int NBW = 9 - U, U2 = U == 5 ? 2 : 3;
  constexpr int NS1 = U == 5 ? 5 : 6;
  SSD();
  TS(0, -1);
  SS8(0);
  const bool up = j4 < 2;
  const int unit0 = sp * 18 + (U == 5 ? (up ? 0 : 13) : (up ? 5 : 9));
  const unsigned lbase = lds_addr(lds);
  const lds_p L = (lds_p)lds;
  const int wm = wave & 3, wn = wave >> 2;
  char* FLg = FL + (size_t)(rt * 4 + sp) * 512;
  f32x16 acc[5];
  bf16x8 w[5];
  float iog;
  {
    constexpr int WS = 2 * U * 1024, XB = NS1 * WS, NP = 4 + 2 * U;
    const int h = lane >> 5;
    const unsigned voffW = (unsigned)((lane >> 3) * 9216 + (((lane & 7) ^ (((lane >> 4) & 1) << 2)) << 4));
    const unsigned voff0 = wave < 4 ? (unsigned)(lane * 16) : voffW;
    const int q0 = wave - 4, q1 = wave + 4;
    const char* wb = W1 + unit0 * 128;
    const char* src0 =
        wave < 4 ? XN + (size_t)(rt * 4 + wave) * (36 * 1024) : wb + (q0 / U) * (8 * 9216) + (q0 % U) * 128;
    const char* src1 = wb + (q1 / U) * (8 * 9216) + (q1 % U) * 128;
    const unsigned st0 = wave < 4 ? 1024u : 147456u;
    const bool two = wave + 8 < NP;
    const unsigned dst0 = lbase + (wave < 4 ? XB + wave * 1024 : q0 * 1024), ds0 = wave < 4 ? 4096u : (unsigned)WS;
    auto issue = [&](int s, int e) {
      int ks = s + koff;
      ks = ks >= 36 ? ks - 36 : ks;
      if (e == 0) glds16s(src0 + (unsigned)ks * st0, voff0, dst0 + (s % NS1) * ds0);
      else if (two) glds16s(src1 + (unsigned)ks * 147456u, voffW, lbase + (s % NS1) * WS + q1 * 1024);
    };
    auto waitn = [&](auto Nc) {
      constexpr int N = decltype(Nc)::v;
      if (two) wait_vm<2 * N>(); else wait_vm<N>();
    };
    unsigned bb[U];
#pragma unroll
    for (int b = 0; b < U; ++b)
      asm volatile("global_load_ushort %0, %1, %2" : "=v"(bb[b]) : "v"((lane & 31) * 2), "s"(b1 + (unit0 * 2 + wn * U + b) * 32) : "memory");
    asm volatile("global_load_dword %0, %1, %2" : "=v"(iog) : "v"((lane & 31) * 4), "s"(G + (2048 + (rt * 4 + wm) * 32) * 4) : "memory");
#pragma unroll
    for (int s = 0; s < (SKIP1 ? 0 : NS1); ++s) { issue(s, 0); issue(s, 1); }
    if (SKIP1) wait_vm<0>();
    const int q = (lane >> 2) & 3, p = lane & 3, g = (lane >> 4) & 1;
    lds_p xa = L + XB + wm * 1024 + lane * 16;
    lds_p wa[2];
#pragma unroll
    for (int e = 0; e < 2; ++e) {
      int b = wn * U + e;
      wa[e] = L + (h * U + (b >> 1)) * 1024 + q * 128 + (((b & 1) ^ (q >> 1)) << 6) + g * 32 + p * 8;
    }
    asm volatile("" : "+v"(xa), "+v"(wa[0]), "+v"(wa[1]));
    waitn(IC<NS1 - 1>{});
    {
      f32x16 zero;
#pragma unroll
      for (int i = 0; i < 16; ++i) zero[i] = 0.f;
      asm volatile("" : "+v"(iog));
      const u32x4 xx = {h ? 0u : 0x3F80u, 0u, 0u, 0u};
#pragma unroll
      for (int b = 0; b < U; ++b) {
        asm volatile("" : "+v"(bb[b]));
        const u32x4 wx = {bb[b], 0u, 0u, 0u};
        acc[b] = MFMA32(__builtin_bit_cast(bf16x8, wx), __builtin_bit_cast(bf16x8, xx), zero);
      }
    }
    BARRIER();
    constexpr int P1HI = U == 4 ? P1HI4 : P1HI5;
    __builtin_amdgcn_s_setprio(P1HI > 0 ? HI_PRIO : MLP_PRIO);
    STAMP(4, 1);
    auto rdx = [&](int sl) { return ld8(xa + sl * 4096); };
    auto rdw = [&](int sl, int m) {
      const lds_p a = wa[m & 1] + sl * WS + (m >> 1) * 1024;
      return cat8(trl(a), trl(a + 512));
    };
    bf16x8 x0, x1;
    x0 = rdx(0);
#pragma unroll
    for (int m = 0; m < U; ++m) w[m] = rdw(0, m);
    unsigned pfd;
    static_for<0, SKIP1 ? 0 : 36>([&](auto Jc) {
      constexpr int j = decltype(Jc)::v;
      if constexpr (j + 1 < 36) {
        constexpr int top = j == 0 ? NS1 - 1 : (j + NS1 - 2 < 35 ? j + NS1 - 2 : 35);
        waitn(IC<top - (j + 1)>{});
        BARRIER();
      }
      if constexpr (P1HI > 0 && j == P1HI) __builtin_amdgcn_s_setprio(MLP_PRIO);
      STAMP2(j);
      if (j == 12) STAMP(4, 2);
      if (j == 24) STAMP(4, 3);
      SB();
      bf16x8& xc = (j & 1) ? x1 : x0;
      bf16x8& xn = (j & 1) ? x0 : x1;
      static_for<0, U>([&](auto Mc) {
        constexpr int m = decltype(Mc)::v;
        acc[m] = MFMA32(w[m], xc, acc[m]);
        SB();
        if constexpr (j + 1 < 36) {
          if constexpr (m == 0) xn = rdx((j + 1) % NS1);
          w[m] = rdw((j + 1) % NS1, m);
        }
        if constexpr (j >= 1 && j + NS1 - 1 < 36 && (m == 1 || m == 2)) issue(j + NS1 - 1, m - 1);
        if constexpr (j == PF2_AT && m == 3) {
          const unsigned l2 = fresh_lane(), li = l2 < 18 * U2 ? l2 : l2 - 32, row = li / U2;
          const unsigned vo = row * 1152u + (li - row * U2) * 128u;
          const int u2 = U == 5 ? (up ? 0 : 7) : 2 * j4;
          gld4(pfd, W2 + (size_t)(sp * 1152 + (rt & 7) * 144 + wave * 18) * 1152 + u2 * 128, vo);
          if (wave == 7) gld4k(pfd, Wo + (sp * 144 + (rt & 7) * 18) * 1152 + u2 * 128, vo);
        }
        if constexpr (j == PF2_AT + NS1 + 1 && m == 0) asm volatile("" ::"v"(pfd));
        SB();
      });
    });
    STAMP2(36);
    STAMP(4, 4);
    asm volatile("" : "+v"(acc[0]), "+v"(acc[U - 1]), "+v"(iog));
  }
  SS8(1);
  __builtin_amdgcn_s_setprio(GELU_PRIO);
  BARRIER();
  SS8(2);
  constexpr int B1 = 2 * U, B2 = B1 + 8, B3 = 26;
  constexpr M2S SC = m2_sched(U);
  const int e0 = 2 * (U == 5 ? (up ? 0 : 26) : (up ? 10 : 18));
  const int e1 = 2 * (U == 5 ? (up ? 10 : 18) : (up ? 18 : 10)) - B1;
  const int e2 = 2 * (U == 5 ? (up ? 18 : 10) : (up ? 26 : 0)) - B2;
  const int e3 = 2 * (U == 5 ? (up ? 26 : 0) : (up ? 0 : 26)) - B3;
  auto kstep = [&](int s, int half) {
    const int uc2 = s < B1 ? 2 * U : (s < B2 ? 8 : (s < B3 ? (U == 5 ? 8 : 10) : 10));
    return (unsigned)(s + (s < B1 ? e0 : (s < B2 ? e1 : (s < B3 ? e2 : e3))) + half * uc2);
  };
  const int kh = wn;
  const int unit2 = U == 5 ? (up ? 0 : 7) : 2 * j4, boff = U == 5 ? 0 : j4 - 1;
  const int ln = fresh_lane();
  const unsigned voffX = ln * 16;
  const unsigned voffW = (unsigned)((ln >> 3) * 1152 + (((ln & 7) ^ (((ln >> 4) & 1) << 2)) << 4));
  const char* xsrc = H + ((size_t)(rt * 4 + wm) * 288 + sp * 72) * 1024;
  const int p0 = wave, p2 = 8 + (wave & 3);
  const int kb0 = p0 / U2, kb2 = p2 / U2;
  const unsigned c0 = (kb0 & 1) * (8 * 1152) + (unit2 + p0 % U2) * 128, c2 = (kb2 & 1) * (8 * 1152) + (unit2 + p2 % U2) * 128;
  const char* w2s = W2 + (size_t)sp * 1152 * 1152;
  const char* wos = Wo + sp * (8 * 18 * 1152);
  constexpr int SL2 = U2 * 4096;
  auto issueW = [&](auto Sc, int e) {
    constexpr int s = decltype(Sc)::v;
    const unsigned dst = lbase + SC.slot[s] * SL2 + (e ? p2 : p0) * 1024;
    if (e == 1 && !(U2 == 3 && (s & 1) == kh)) return;
    const int half = e ? 1 : kb0 >> 1;
    const unsigned c = e ? c2 : c0;
    if constexpr (s < M_IT) glds16s(w2s + (c + kstep(s, half) * (16u * 1152u)), voffW, dst);
    else if constexpr (s < M2_NT - 1) glds16s(wos + (c + (unsigned)(4 * half + s - M_IT) * (18u * 1152u)), voffW, dst);
    else {
      const int l2 = fresh_lane(), row = l2 >> 3;
      const unsigned ve = (unsigned)(((4 * (row >> 2) + ((row & 3) >> 1)) * 18 + (row & 1)) * 1152 + (((l2 & 7) ^ (((l2 >> 4) & 1) << 2)) << 4));
      const int kbl = (e ? kb2 : kb0) & 1;
      glds16s(wos + (c + (unsigned)(kbl * (36 - 8) + 16) * 1152u), ve, dst);
    }
  };
  unsigned b2v[NBW];
  const bool addb = sp == 0 && kh == 0;
#pragma unroll
  for (int b = 0; b < NBW; ++b)
    asm volatile("global_load_ushort %0, %1, %2" : "=v"(b2v[b]) : "v"((ln & 31) * 2), "s"(b2 + (unit2 * 2 + boff + b) * 32) : "memory");
  static_for<0, 4>([&](auto Sc) { issueW(Sc, 0); issueW(Sc, 1); });
  SS8(3);
  bf16x8 xr[4];
  const unsigned stash = lbase + 4 * SL2 + wave * ((2 * U - 4) * 1024) + voffX;
  {
    const float C0 = -2.0f * 1.4426950408889634f * 0.7978845608028654f;
    const float C1 = C0 * 0.044715f;
    f32x2 c0 = {C0, C0}, c1 = {C1, C1};
    asm volatile("" : "+s"(c0), "+s"(c1));
    float vc1 = C1;
    asm volatile("" : "+v"(vc1));
    f32x2 ig = {iog, iog};
    asm volatile("" : "+v"(ig));
    char* hb = H + ((size_t)(rt * 4 + wm) * 288 + (unit0 * 2 + wn * U) * 2) * 1024 + fresh_lane() * 16;
#pragma unroll
    for (int b = 0; b < U; ++b) {
#pragma unroll
      for (int gp = 0; gp < 2; ++gp) {
        u32x4 o4;
#pragma unroll
        for (int i = 0; i < 4; ++i) {
          const f32x2 v = {acc[b][8 * gp + 2 * i], acc[b][8 * gp + 2 * i + 1]};
          const f32x2 t = v * __builtin_elementwise_fma(v * v, c1, c0);
          const f32x2 e = {__builtin_amdgcn_exp2f(t[0]), __builtin_amdgcn_exp2f(t[1])};
          const f32x2 d = __builtin_elementwise_fma(e, ig, ig);
          const f32x2 rc = {__builtin_amdgcn_rcpf(d[0]), __builtin_amdgcn_rcpf(d[1])};
          const f32x2 o = v * rc;
          o4[i] = pk2(o[0], o[1]);
        }
        st16(hb + (b * 2 + gp) * 1024, o4);
        if (b < 2) xr[b * 2 + gp] = __builtin_bit_cast(bf16x8, o4);
        else *(__attribute__((address_space(3))) u32x4*)(uintptr_t)(stash + (b * 2 + gp - 4) * 1024) = o4;
      }
      if (b == 1) SS8(4);
    }
  }
  STAMP(4, 5);
  SS8(5);
  lds_p wa[2];
  {
    const int h = ln >> 5, q = (ln >> 2) & 3, p = ln & 3, g = (ln >> 4) & 1;
#pragma unroll
    for (int e = 0; e < 2; ++e) {
      int b = boff + e;
      wa[e] = L + (2 * kh * U2 + (b >> 1)) * 1024 + (4 * h + q) * 128 + (((b & 1) ^ (q >> 1)) << 6) + g * 32 + p * 8;
    }
  }
  asm volatile("" : "+v"(wa[0]), "+v"(wa[1]));
  wait_vm<2 * U>();
  SS8(6);
  {
    f32x16 zero;
#pragma unroll
    for (int i = 0; i < 16; ++i) zero[i] = 0.f;
    const float og = __builtin_amdgcn_rcpf(iog);
    const float ogh = __uint_as_float(__float_as_uint(og) & 0xffff0000u);
    const u32x4 xx = {(ln >> 5) || !addb ? 0u : pk2(ogh, og - ogh), 0u, 0u, 0u};
#pragma unroll
    for (int b = 0; b < NBW; ++b) {
      asm volatile("" : "+v"(b2v[b]));
      const u32x4 wx = {b2v[b] * 0x10001u, 0u, 0u, 0u};
      acc[b] = MFMA32(__builtin_bit_cast(bf16x8, wx), __builtin_bit_cast(bf16x8, xx), zero);
    }
  }
  BARRIER();
  constexpr int P2HI = U == 4 ? P2HI4 : P2HI5;
  __builtin_amdgcn_s_setprio(P2HI > 0 ? HI_PRIO : MLP2_PRIO);
  STAMP(4, 6);
  STAMP(4, 7);
  STAMP(2, 1);
  TS(1, -1);
  SS8(7);
  auto rdw = [&](int s, int m) {
    const lds_p a = wa[m & 1] + SC.slot[s] * SL2 + (m >> 1) * 1024;
    return cat8(trl(a), trl(a + U2 * 1024));
  };
#pragma unroll
  for (int m = 0; m < NBW; ++m) w[m] = rdw(0, m);
  unsigned fv = 0;
  auto poll = [&]() {
    unsigned vo = voffX;
    asm volatile("" : "+v"(vo));
    vo = (vo & 48u) << 3;
    gld4c(fv, FLg, vo);
  };
  auto poll3 = [&]() {
    const unsigned l2 = fresh_lane(), mb = rt * 4 + wm;
    const unsigned vo = (l2 & 12u) == 8u && (l2 & 3u) != (unsigned)wm ? FL_P + mb * 16u + (l2 & 3u) * 4u : FL_O + mb * 128u + sp * 32u + (l2 & 7u) * 4u;
    gld4c(fv, FL, vo);
  };
  auto check = [&](unsigned long long mask) {
    mask &= ~(1ull << j4);
    asm volatile("" : "+v"(fv));
    if (__ballot(fv != 1u) & mask) {
#pragma unroll 1
      for (int it = 0; it < (1 << 17); ++it) {
        __builtin_amdgcn_s_sleep(4);
        poll();
        wait_vm<0>();
        asm volatile("" : "+v"(fv));
        if (!(__ballot(fv != 1u) & mask)) break;
      }
    }
  };
  auto waitk = [&](auto N0, auto N1) {
    constexpr int n0 = decltype(N0)::v, n1 = decltype(N1)::v;
    if constexpr (n0 == n1) wait_vm<n0>();
    else { if (kh == 0) wait_vm<n0>(); else wait_vm<n1>(); }
  };
  constexpr int CK1 = m2_ck1(U), CK2 = m2_ck2(U);
  bf16x8 xe;
  const char* osrc = O + (size_t)((rt * 4 + wm) * 32 + sp * 8 + kh * 4) * 1024;
  const char* oesrc = O + OEX + ((rt * 4 + wm) * 4 + sp) * 1024;
  static_for<0, M_IT>([&](auto Jc) {
    constexpr int j = decltype(Jc)::v;
    if constexpr (m2_need(U, j)) waitk(IC<m2_wait(U, 0, j, 0)>{}, IC<m2_wait(U, 1, j, 0)>{});
    if constexpr (j > 0) BARRIER();
    if constexpr (j > 0) pin_acc<NBW>(acc);
    static_for<0, 4>([&](auto Ic) {
      constexpr int i = decltype(Ic)::v;
      if constexpr (SENT || j + ((i - j) & 3) < M_IT) pin_x1<i>(xr[i]);
    });
    if constexpr (j == 3) {
      if (wave == 0 && voffX == 0) st4(FLg + j4 * 128, 1u);
    }
    if constexpr (P2HI > 0 && j == P2HI) __builtin_amdgcn_s_setprio(MLP2_PRIO);
    STAMP2(40 + j);
    if (j == 9) STAMP(2, 2);
    if (j == 18) STAMP(2, 3);
    if (j == 18) TS(2, -1);
    if (j == 27) STAMP(2, 4);
    if constexpr (j == CK1 - 2 || j == CK2 - 2) poll();
    SB();
    static_for<0, NBW>([&](auto Mc) {
      constexpr int m = decltype(Mc)::v;
      pin_w<m>(w[m]);
      acc[m] = MFMA32(w[m], xr[j & 3], acc[m]);
      SB();
      w[m] = rdw(j + 1, m);
      static_for<j + 3, (j + 7 < M2_NT ? j + 7 : M2_NT)>([&](auto Sc) {
        constexpr int s = decltype(Sc)::v;
        if constexpr (SC.it[s] == j) {
          constexpr bool second = s > 0 && SC.it[s - 1] == j;
          if constexpr (m == (second ? 2 : 0)) issueW(Sc, 0);
          if constexpr (m == (second ? 3 : 1)) issueW(Sc, 1);
        }
      });
      SB();
    });
    if constexpr (j == CK1 || j == CK2) {
      waitk(IC<m2_wait(U, 0, j, 1)>{}, IC<m2_wait(U, 1, j, 1)>{});
      check(j == CK1 ? 6ull : 15ull);
    }
    if constexpr (j + 4 < B1) xstash<(j & 3), j * 1024>(xr[j & 3], stash);
    else if constexpr (j + 4 < M_IT) xload<(j & 3)>(xr[j & 3], xsrc + kstep(j + 4, kh) * 1024u, voffX);
    SB();
  });
  STAMP2(76);
  STAMP(2, 5);
  TS(3, 0);
  wait_vm<0>();
  pin_acc<NBW>(acc);
  static_for<0, NBW>([&](auto Mc) { pin_w<decltype(Mc)::v>(w[decltype(Mc)::v]); });
  if (wave == 0) {
    const int l2 = fresh_lane();
    spin_flags(FL + FL_O + rt * 512 + sp * 32, (unsigned)(((l2 >> 3) & 3) * 128 + (l2 & 7) * 4));
  }
  BARRIER();
  TS(4, 1);
  {
    static_for<0, 4>([&](auto Ic) { xload<decltype(Ic)::v>(xr[decltype(Ic)::v], osrc + decltype(Ic)::v * 1024, voffX); });
    gld16c(xe, oesrc, voffX);
    static_for<0, 4>([&](auto Tc) {
      constexpr int t = decltype(Tc)::v;
      wait_vm<4 - t>();
      pin_x1<t>(xr[t]);
      pin_acc<NBW>(acc);
      static_for<0, NBW>([&](auto Mc) {
        constexpr int m = decltype(Mc)::v;
        pin_w<m>(w[m]);
        acc[m] = MFMA32(w[m], xr[t], acc[m]);
        SB();
        w[m] = rdw(M_IT + t + 1, m);
        SB();
      });
    });
    wait_vm1<0>(xe);
    pin_acc<NBW>(acc);
    static_for<0, NBW>([&](auto Mc) { pin_w<decltype(Mc)::v>(w[decltype(Mc)::v]); });
    if (kh == 0) {
#pragma unroll
      for (int m = 0; m < NBW; ++m) acc[m] = MFMA32(w[m], xe, acc[m]);
    }
    pin_acc<NBW>(acc);
  }
  BARRIER();
  STAMP(2, 6);
  TS(5, 2);
  constexpr int NH = NBW;
  const int l2 = fresh_lane();
  pair_sum<NBW>(acc, lbase + (wm * 2 + kh) * (NH * 1024) + l2 * 16, lbase + (wm * 2 + (kh ^ 1)) * (NH * 1024) + l2 * 16, kh);
  float r[NH][8];
#pragma unroll
  for (int q = 0; q < NH; ++q)
#pragma unroll
    for (int i = 0; i < 8; ++i) r[q][i] = acc[q][i];
  TS(-1, 3);
  const char* flp = FL + FL_P2 + (((rt * 4 + j4) * 4 + wm) * 2 + kh) * 512;
  constexpr unsigned PSP = 64 * 18 * 2048;
  const int nb0 = unit2 * 2 + boff;
  const char* pt = P + ((size_t)(rt * 4 + wm) * 18 + nb0) * 2048;
  const unsigned h0 = kh * 1024u, h4 = 8192u + h0;
  const unsigned vl = l2 * 16;
  float own[8];
#pragma unroll
  for (int q = 0; q < NH; ++q) {
    if (q < 4 && q == sp) {
#pragma unroll
      for (int i = 0; i < 8; ++i) own[i] = r[q][i];
    } else if (q < 4 || wm != sp) {
      u32x4 v;
#pragma unroll
      for (int i = 0; i < 4; ++i) v[i] = pk2(r[q][2 * i], r[q][2 * i + 1]);
      const char* dst = pt + (sp * PSP + (h0 + q * 2048u));
      asm volatile("global_store_dwordx4 %0, %1, %2 sc1\n\ts_nop 1" ::"v"(vl), "v"(v), "s"(dst) : "memory");
    }
  }
  {
    const unsigned oth = (unsigned)((sp + 1 + (l2 % 3)) & 3), vf = sp * 128u + oth * 4u;
    unsigned f0, f1 = 0;
    gld4c(f0, flp, vf);
    wait_vm<1>();
    TS(-1, 4);
    if (l2 < 3) asm volatile("global_store_dword %0, %1, %2 sc1" ::"v"(oth * 128u + sp * 4u), "v"(1u), "s"(flp) : "memory");
    asm volatile("s_waitcnt vmcnt(1)" : "+v"(f0)::"memory");
    if (__ballot(f0 != 1u)) {
#pragma unroll 1
      for (int it = 0; it < (1 << 17); ++it) {
        gld4ck(f0, flp, vf);
        asm volatile("s_waitcnt vmcnt(0)" : "+v"(f0)::"memory");
        if (!__ballot(f0 != 1u)) break;
        __builtin_amdgcn_s_sleep(PSLP);
      }
    }
    TS(6, 5);
    const char* orow = out + ((size_t)(rt * 4 + wm) * (32 * 1152) + nb0 * 64);
    const unsigned vo = (l2 & 31) * 1152 + (l2 >> 5) * 16;
    auto finish = [&](float* a, bf16x8* p, unsigned hoff) {
#pragma unroll
      for (int o = 0; o < 3; ++o) {
        const u32x4 v = __builtin_bit_cast(u32x4, p[o]);
#pragma unroll
        for (int k = 0; k < 4; ++k) {
          a[2 * k] += __uint_as_float(v[k] << 16);
          a[2 * k + 1] += __uint_as_float(v[k] & 0xffff0000u);
        }
      }
      auto r0 = __builtin_amdgcn_permlane32_swap(pk2(a[0], a[1]), pk2(a[4], a[5]), false, false);
      auto r1 = __builtin_amdgcn_permlane32_swap(pk2(a[2], a[3]), pk2(a[6], a[7]), false, false);
      const u32x4 wv = {r0[0], r1[0], r0[1], r1[1]};
      const char* dst = orow + (hoff >> 5);
      asm volatile("global_store_dwordx4 %0, %1, %2\n\ts_nop 1" ::"v"(vo), "v"(wv), "s"(dst) : "memory");
    };
    bf16x8 pv[3], pw[3];
    const bool two = NH == 5 && wm == sp;
#pragma unroll
    for (int o = 0; o < 3; ++o) gld16c(pv[o], pt + (((sp + 1 + o) & 3) * PSP + h0 + sp * 2048u), vl);
    if constexpr (NH == 5) {
      if (two) {
#pragma unroll
        for (int o = 0; o < 3; ++o) gld16c(pw[o], pt + (((sp + 1 + o) & 3) * PSP + h4), vl);
      }
      asm volatile("s_waitcnt vmcnt(0)" : "+v"(pw[0]), "+v"(pw[1]), "+v"(pw[2]), "+v"(pv[0]), "+v"(pv[1]), "+v"(pv[2]) : "v"(f0), "v"(f1) : "memory");
    } else {
      asm volatile("s_waitcnt vmcnt(0)" : "+v"(pv[0]), "+v"(pv[1]), "+v"(pv[2]) : "v"(f0), "v"(f1) : "memory");
    }
    TS(-1, 6);
    finish(own, pv, h0 + sp * 2048u);
    if (two) finish(r[NH - 1], pw, h4);
  }
  STAMP(2, 7);
  SST(7);
  SSW();
}

extern "C" __global__ __launch_bounds__(512, 4) void kernel1(const char* __restrict__ T,
                                                   char* O, const char* __restrict__ XN,
                                                   const char* __restrict__ W1, const unsigned short* __restrict__ b1,
                                                   char* H, const char* __restrict__ W2,
                                                   const unsigned short* __restrict__ b2, const char* __restrict__ Wo,
                                                   const char* __restrict__ G, char* P, char* FL,
                                                   char* __restrict__ out) {
  __shared__ __attribute__((aligned(16))) char lds[81920];
  const int wave = __builtin_amdgcn_readfirstlane(threadIdx.x >> 6);
  const int lane = threadIdx.x & 63;
  const int bid = blockIdx.x;
  if (bid < 256) {
    STAMP(1, 0);
    STAMP(5, 0);
    __builtin_amdgcn_s_setprio(ATT_PRIO);
    attn_role(lds, T, O, P, G, FL, W2, Wo, bid, wave, fresh_lane());
  } else {
    STAMP(2, 0);
    STAMP(4, 0);
    const int wg = bid - 256;
    const int xcd = wg & 7, idx = wg >> 3;
    const int sp = xcd >> 1, j4 = idx & 3, r8 = idx >> 2, rt = (xcd & 1) * 8 + r8;
    const int koff = (r8 * 9 / 2 + j4 * 9) % 36;
    if (j4 == 0 || j4 == 3)
      mlp_track<5>(lds, XN, W1, b1, H, W2, b2, Wo, O, G, P, FL, out, rt, sp, j4, koff, wave, fresh_lane());
    else
      mlp_track<4>(lds, XN, W1, b1, H, W2, b2, Wo, O, G, P, FL, out, rt, sp, j4, koff, wave, fresh_lane());
  }
}

constexpr int Q_SLOT = 32768;
#define NSQ 4
constexpr int QGRID = 256;
DI void qkv_role(char* lds, const char* __restrict__ X, const char* __restrict__ Wqkv, char* __restrict__ T, int rt,
                 int ct, int joff, int wave, int lane) {
  f32x16 acc[2];
#pragma unroll
  for (int i = 0; i < 16; ++i) { acc[0][i] = 0.f; acc[1][i] = 0.f; }
  const unsigned lbase = lds_addr(lds);
  const int wm = wave & 3, wn = wave >> 2;
  const bool half = ct == 13;
  unsigned voffX[2];
#pragma unroll
  for (int i = 0; i < 2; ++i) {
    int row = wave * 16 + 8 * i + (lane >> 3);
    int c = (lane & 7) ^ ((row >> 1) & 7);
    voffX[i] = (unsigned)((rt * 128 + row) * 1152 + c * 16);
  }
  const unsigned voffW = (unsigned)((lane >> 3) * 3456 + (((lane & 7) ^ (((lane >> 4) & 1) << 2)) << 4));
  const char* wsrc = Wqkv + (size_t)wave * 8 * 3456 + ct * 256;
  const unsigned wh = half ? 0u : 128u;
  auto issue = [&](int J, int e) {
    unsigned dst = lbase + (J % NSQ) * Q_SLOT;
    unsigned Jr = J + joff;
    Jr = Jr >= 9 ? Jr - 9 : Jr;
    if (e < 2) glds16s(wsrc + (Jr * (64u * 3456u) + e * wh), voffW, dst + 16384 + e * 8192 + wave * 1024);
    else glds16s(X + Jr * 128u, voffX[e - 2], dst + (wave * 2 + e - 2) * 1024);
  };
  STAMP(0, 0);
#pragma unroll
  for (int J = 0; J < NSQ - 1; ++J)
#pragma unroll
    for (int e = 0; e < 4; ++e) issue(J, e);
  const int r = lane & 31, h = lane >> 5;
  const int q = (lane >> 2) & 3, p = lane & 3, g = (lane >> 4) & 1;
  const lds_p L = (lds_p)lds;
  constexpr int NB = (NSQ + 1) / 2;
  lds_p xa[NB][4], wa[NB][2];
#pragma unroll
  for (int z = 0; z < NB; ++z) {
#pragma unroll
    for (int ks = 0; ks < 4; ++ks) {
      xa[z][ks] = L + z * 2 * Q_SLOT + (wm * 32 + r) * 128 + (((2 * ks + h) ^ ((r >> 1) & 7)) << 4);
      asm volatile("" : "+v"(xa[z][ks]));
    }
#pragma unroll
    for (int b = 0; b < 2; ++b) {
      wa[z][b] = L + z * 2 * Q_SLOT + 16384 + wn * 8192 + h * 1024 + q * 128 + ((b ^ (q >> 1)) << 6) + g * 32 + p * 8;
      asm volatile("" : "+v"(wa[z][b]));
    }
  }
  wait_vm<4 * (NSQ - 2)>();
  BARRIER();
  STAMP(0, 1);
  bf16x8 fx[2], fw[2][2];
  auto rd = [&](int buf, int J, int ks) {
    const int sl = J % NSQ;
    fx[buf] = ld8(xa[sl >> 1][ks] + (sl & 1) * Q_SLOT);
#pragma unroll
    for (int b = 0; b < 2; ++b) {
      const lds_p a = wa[sl >> 1][b] + (sl & 1) * Q_SLOT + ks * 2048;
      fw[buf][b] = cat8(trl(a), trl(a + 512));
    }
  };
  rd(0, 0, 0);
  static_for<0, 9>([&](auto Jc) {
    constexpr int J = decltype(Jc)::v;
    if constexpr (J + 1 < 9) {
      constexpr int top = J + NSQ - 2 < 8 ? J + NSQ - 2 : 8;
      wait_vm<4 * (top - (J + 1))>();
      BARRIER();
    }
    if (J == 3) STAMP(0, 2);
    if (J == 6) STAMP(0, 3);
    SB();
    static_for<0, 4>([&](auto Kc) {
      constexpr int ks = decltype(Kc)::v, cur = ks & 1;
      acc[0] = MFMA32(fx[cur], fw[cur][0], acc[0]);
      SB();
      if constexpr (ks < 3) rd(cur ^ 1, J, ks + 1);
      else if constexpr (J + 1 < 9) rd(cur ^ 1, J + 1, 0);
      SB();
      acc[1] = MFMA32(fx[cur], fw[cur][1], acc[1]);
      SB();
      if constexpr (J + NSQ - 1 < 9) {
        if constexpr (ks == 0) { issue(J + NSQ - 1, 0); issue(J + NSQ - 1, 1); }
        else if constexpr (ks < 3) issue(J + NSQ - 1, ks + 1);
      }
      SB();
    });
  });
  STAMP(0, 5);
  const float QSCALE = 0.23570226039551584f * 1.4426950408889634f;
  const float sc = ct * 2 + wn < 9 ? QSCALE : 1.0f;
  if (!(half && wn)) {
#pragma unroll
    for (int nb = 0; nb < 2; ++nb) {
      char* base = T + (size_t)h * TP + ((ct * 2 + wn) * 64 + nb * 32 + r) * 16;
#pragma unroll
      for (int s = 0; s < 2; ++s) {
        int step = rt * 8 + wm * 2 + s;
        u32x4 v;
#pragma unroll
        for (int i = 0; i < 4; ++i) v[i] = pk2(acc[nb][8 * s + 2 * i] * sc, acc[nb][8 * s + 2 * i + 1] * sc);
        st16(base + (size_t)step * (2 * TP), v);
      }
    }
  } else if (wm < 2) {
    int idx = wm * 64 + lane;
    unsigned c = (idx & 7) == 0 ? 0x3F803F80u : 0u;
    u32x4 v = {c, c, c, c};
    st16(T + (size_t)(rt * 16 + (idx >> 3)) * TP + (1728 + (idx & 7)) * 16, v);
  }
}

DI float dot2(unsigned a, unsigned b, float c) {
  return __builtin_amdgcn_fdot2_f32_bf16(__builtin_bit_cast(bf16x2_t, a), __builtin_bit_cast(bf16x2_t, b), c, false);
}
template <int C>
DI float dpp_add(float v) {
  return v + __builtin_bit_cast(float, __builtin_amdgcn_update_dpp(0, __builtin_bit_cast(int, v), C, 0xf, 0xf, true));
}
DI float sum8(float v) { return dpp_add<0x141>(dpp_add<0x4E>(dpp_add<0xB1>(v))); }
DI void ln_role(const char* __restrict__ X, const char* __restrict__ gw, char* __restrict__ XN, float* __restrict__ G,
                char* __restrict__ FL, int id, int wave, int lane) {
  const int c = lane & 7;
  const int row = id * 64 + wave * 8 + (lane >> 3);
  bf16x8 xv[9], gv[9];
  {
    const unsigned vx = (unsigned)(row * 1152 + c * 16), vg = (unsigned)(c * 16);
#pragma unroll
    for (int i = 0; i < 9; ++i) asm volatile("global_load_dwordx4 %0, %1, %2 offset:%3" : "=v"(xv[i]) : "v"(vx), "s"(X), "i"(i * 128) : "memory");
#pragma unroll
    for (int i = 0; i < 9; ++i) asm volatile("global_load_dwordx4 %0, %1, %2 offset:%3" : "=v"(gv[i]) : "v"(vg), "s"(gw), "i"(i * 128) : "memory");
  }
  st4(FL + id * 2048 + wave * 256 + lane * 4, 0u);
  st4(FL + FL_P2 + (id * 8 + wave) * 1024 + (lane >> 3) * 128 + c * 4, 0u);
  char* dst = XN + ((size_t)(row >> 5) * 36 + (c >> 1)) * 1024 + ((c & 1) * 32 + (row & 31)) * 16;
  asm volatile("s_waitcnt vmcnt(11)" : "+v"(xv[0]), "+v"(xv[1]), "+v"(xv[2]), "+v"(xv[3]), "+v"(xv[4]), "+v"(xv[5]), "+v"(xv[6]), "+v"(xv[7]), "+v"(xv[8])::"memory");
  float sa[4] = {0.f, 0.f, 0.f, 0.f};
#pragma unroll
  for (int i = 0; i < 9; ++i)
#pragma unroll
    for (int j = 0; j < 4; ++j) sa[j] = dot2(__builtin_bit_cast(u32x4, xv[i])[j], 0x3F803F80u, sa[j]);
  STAMP(0, 1);
  const float mean = sum8((sa[0] + sa[1]) + (sa[2] + sa[3])) * (1.0f / 576.0f);
  const f32x2 m2 = {mean, mean};
  f32x2 xc[9][4];
  f32x2 qa[4] = {{0.f, 0.f}, {0.f, 0.f}, {0.f, 0.f}, {0.f, 0.f}};
#pragma unroll
  for (int i = 0; i < 9; ++i)
#pragma unroll
    for (int j = 0; j < 4; ++j) {
      const unsigned u = __builtin_bit_cast(u32x4, xv[i])[j];
      xc[i][j] = f32x2{__uint_as_float(u << 16), __uint_as_float(u & 0xffff0000u)} - m2;
      qa[j] = __builtin_elementwise_fma(xc[i][j], xc[i][j], qa[j]);
    }
  const f32x2 qq = (qa[0] + qa[1]) + (qa[2] + qa[3]);
  const float var = sum8(qq[0] + qq[1]) * (1.0f / 576.0f);
  STAMP(0, 3);
  const float rstd = 1.0f / __builtin_sqrtf(var + 1e-5f);
  const f32x2 r2 = {rstd, rstd};
#pragma unroll
  for (int i = 0; i < 9; ++i) {
    u32x4 v;
#pragma unroll
    for (int j = 0; j < 4; ++j) {
      const f32x2 y = xc[i][j] * r2;
      v[j] = pk2(y[0], y[1]);
    }
    st16(dst + i * 4096, v);
  }
  asm volatile("s_waitcnt vmcnt(11)" : "+v"(gv[0]), "+v"(gv[1]), "+v"(gv[2]), "+v"(gv[3]), "+v"(gv[4]), "+v"(gv[5]), "+v"(gv[6]), "+v"(gv[7]), "+v"(gv[8])::"memory");
  float da[4] = {0.f, 0.f, 0.f, 0.f};
#pragma unroll
  for (int i = 0; i < 9; ++i)
#pragma unroll
    for (int j = 0; j < 4; ++j) da[j] = dot2(__builtin_bit_cast(u32x4, xv[i])[j], __builtin_bit_cast(u32x4, gv[i])[j], da[j]);
  const float v = sum8((da[0] + da[1]) + (da[2] + da[3])) * 1.4426950408889634f;
  const float e = 1.0f + __builtin_amdgcn_exp2f((c & 1) ? v : -v);
  const float e1 = __builtin_fminf(e, 1.0e30f), e0 = 1.0f / e;
  st4(G + (c & 1) * 2048 + row, __float_as_uint((c & 1) ? e1 : e0));
  STAMP(0, 5);
  unsigned d0;
  {
    const char* pc;
    unsigned span;
    asm volatile("s_getpc_b64 s[20:21]\n\ts_mov_b32 %1, kernel1@rel32@lo+4\n\ts_add_u32 s20, s20, %1\n\ts_addc_u32 s21, s21, kernel1@rel32@hi+16"
                 : "={s[20:21]}"(pc), "=s"(span)::"scc");
    span = 0u - span;
    span = span < (1u << 18) ? span : 0u;
    const unsigned o = (unsigned)(((id >> 3) * 8 + wave) * 64 + lane) * 128u;
    gld4(d0, pc, o < span ? o : 0u);
  }
  asm volatile("" ::"v"(d0) : "memory");
}

__global__ __launch_bounds__(512, 2) void kernelQ(const char* __restrict__ X, const char* __restrict__ Wqkv,
                                                   const char* __restrict__ gw, char* __restrict__ T,
                                                   char* __restrict__ XN, float* __restrict__ G,
                                                   char* __restrict__ FL) {
  __shared__ __attribute__((aligned(16))) char lds[NSQ * Q_SLOT];
  const int wg = blockIdx.x;
  const int wave = __builtin_amdgcn_readfirstlane(threadIdx.x >> 6);
  const int lane = threadIdx.x & 63;
  if (wg < 224) {
    const int xcd = wg & 7, t = wg >> 3, rl = t & 3, cl = t >> 2;
    const int joff = (3 * (cl + rl) + (rl == 3 ? 1 : 0)) % 9;
    qkv_role(lds, X, Wqkv, T, (xcd & 3) * 4 + rl, (xcd >> 2) * 7 + cl, joff, wave, lane);
  } else {
    STAMP(0, 0);
    ln_role(X, gw, XN, G, FL, wg - 224, wave, lane);
  }
  STAMP(0, 6);
  STAMP(0, 7);
}

extern "C" void kernel_launch(void* const* d_in, const int* in_sizes, int n_in, void* d_out, int out_size, void* d_ws,
                              size_t ws_size, hipStream_t stream) {
  const char* X = (const char*)d_in[0];
  const char* Wqkv = (const char*)d_in[1];
  const char* Wo = (const char*)d_in[2];
  const char* W1 = (const char*)d_in[4];
  const unsigned short* b1 = (const unsigned short*)d_in[5];
  const char* W2 = (const char*)d_in[6];
  const unsigned short* b2 = (const unsigned short*)d_in[7];
  const char* gw = (const char*)d_in[8];
  char* ws = (char*)d_ws;
  char* T = ws + WS_T;
  char* H = ws + WS_H;
  char* O = ws + WS_O;
  char* P = ws + WS_P;
  char* XN = ws + WS_XN;
  char* FL = ws + WS_FL;
  float* G = (float*)(ws + WS_G);
  kernelQ<<<QGRID, 512, 0, stream>>>(X, Wqkv, gw, T, XN, G, FL);
  kernel1<<<512, 512, 0, stream>>>(T, O, XN, W1, b1, H, W2, b2, Wo, (const char*)G, P, FL, (char*)d_out);
}
```

```cpp
#include <hip/hip_runtime.h>
#include <stdint.h>

typedef __attribute__((ext_vector_type(8))) short bf16x8;
typedef __attribute__((ext_vector_type(4))) short s16x4;
typedef __attribute__((ext_vector_type(2))) __bf16 bf16x2_t;
typedef __attribute__((ext_vector_type(2))) float f32x2;
typedef __attribute__((ext_vector_type(4))) float f32x4;
typedef __attribute__((ext_vector_type(16))) float f32x16;
typedef __attribute__((ext_vector_type(4))) unsigned u32x4;
typedef __attribute__((ext_vector_type(2))) unsigned u32x2;
typedef __attribute__((address_space(3))) s16x4* lds_s16x4_p;
typedef __attribute__((address_space(3))) char* lds_p;
typedef __attribute__((address_space(3))) const bf16x8* lds_bf16x8_p;

#define STAMP(k, i)
#define STAMP2(i)
#define SST(i)
#define SS8(i)
#define SSD()
#define SSW()
#define DI __device__ __forceinline__
#define MFMA32(a, b, c) __builtin_amdgcn_mfma_f32_32x32x16_bf16((a), (b), (c), 0, 0, 0)
#define MFMA32K8(a, b, c) __builtin_amdgcn_mfma_f32_32x32x8bf16_1k((a), (b), (c), 0, 0, 0)
#define WAIT_VM(n) asm volatile("s_waitcnt vmcnt(" #n ")" ::: "memory")
#define BARRIER() __builtin_amdgcn_s_barrier()
#define SB() __builtin_amdgcn_sched_barrier(0)

constexpr int S_ = 2048, D_ = 576, F_ = 4608, NQKV = 1728;
constexpr int TSTEP = 640;
constexpr int TP = 1736 * 16;
constexpr int OEX = 64 * 32 * 1024;
constexpr size_t WS_T = 0;
constexpr size_t WS_H = 8u << 20;
constexpr size_t WS_O = 28u << 20;
constexpr size_t WS_G = 31u << 20;
constexpr size_t WS_FL = (31u << 20) + (64u << 10);
constexpr size_t WS_P = 32u << 20;
constexpr size_t WS_XN = 44u << 20;
constexpr size_t WS_XF = 47u << 20;
constexpr int FL_Q = 32768, FL_O = 36864, FL_P = 45056, FL_P2 = 65536;

DI unsigned pk2(float a, float b) {
  f32x2 v = {a, b};
  bf16x2_t r = __builtin_convertvector(v, bf16x2_t);
  return __builtin_bit_cast(unsigned, r);
}
DI float bf2f(unsigned short u) { return __uint_as_float(((unsigned)u) << 16); }
DI s16x4 tr_read(const char* p) {
  return __builtin_amdgcn_ds_read_tr16_b64_v4i16((lds_s16x4_p)(p));
}
DI s16x4 trl(lds_p p) { return __builtin_amdgcn_ds_read_tr16_b64_v4i16((lds_s16x4_p)p); }
DI bf16x8 ld8(lds_p p) { return *(lds_bf16x8_p)p; }
DI float xor32(float v) { return __shfl_xor(v, 32, 64); }

DI void glds16s(const char* sbase, unsigned voff, unsigned lds_dst) {
  unsigned keep;
  asm volatile("s_mov_b32 %0, m0\n\ts_mov_b32 m0, %3\n\ts_nop 0\n\tglobal_load_lds_dwordx4 %1, %2\n\ts_mov_b32 m0, %0"
               : "=&s"(keep)
               : "v"(voff), "s"(sbase), "s"(lds_dst)
               : "memory");
}
DI void glds16c(const char* sbase, unsigned voff, unsigned lds_dst) {
  unsigned keep;
  asm volatile("s_mov_b32 %0, m0\n\ts_mov_b32 m0, %3\n\ts_nop 0\n\tglobal_load_lds_dwordx4 %1, %2 sc1\n\ts_mov_b32 m0, %0"
               : "=&s"(keep)
               : "v"(voff), "s"(sbase), "s"(lds_dst)
               : "memory");
}
DI void touch(const char* sbase, unsigned voff, unsigned lds_dst) {
  unsigned keep;
  asm volatile("s_mov_b32 %0, m0\n\ts_mov_b32 m0, %3\n\ts_nop 0\n\tglobal_load_lds_dword %1, %2\n\ts_mov_b32 m0, %0"
               : "=&s"(keep)
               : "v"(voff), "s"(sbase), "s"(lds_dst)
               : "memory");
}
DI void gld16(bf16x8& d, const char* sbase, unsigned voff) {
  asm volatile("global_load_dwordx4 %0, %1, %2" : "=v"(d) : "v"(voff), "s"(sbase) : "memory");
}
DI void gld16c(bf16x8& d, const char* sbase, unsigned voff) {
  asm volatile("global_load_dwordx4 %0, %1, %2 sc1" : "=v"(d) : "v"(voff), "s"(sbase) : "memory");
}
DI void gld4c(unsigned& d, const char* sbase, unsigned voff) {
  asm volatile("global_load_dword %0, %1, %2 sc1" : "=v"(d) : "v"(voff), "s"(sbase) : "memory");
}
DI void gld4(unsigned& d, const char* sbase, unsigned voff) {
  asm volatile("global_load_dword %0, %1, %2" : "=v"(d) : "v"(voff), "s"(sbase) : "memory");
}
DI void gld4k(unsigned& d, const char* sbase, unsigned voff) {
  asm volatile("global_load_dword %0, %1, %2" : "+v"(d) : "v"(voff), "s"(sbase) : "memory");
}
DI unsigned lds_addr(const void* p) { return (unsigned)(uintptr_t)(lds_p)p; }

DI void st16(void* p, u32x4 v) { asm volatile("global_store_dwordx4 %0, %1, off sc1\n\ts_nop 1" ::"v"(p), "v"(v) : "memory"); }
DI void st16p(void* p, u32x4 v) { asm volatile("global_store_dwordx4 %0, %1, off\n\ts_nop 1" ::"v"(p), "v"(v) : "memory"); }
DI void st4(void* p, unsigned v) { asm volatile("global_store_dword %0, %1, off sc1" ::"v"(p), "v"(v) : "memory"); }

DI int fresh_lane() {
  int l;
  asm volatile("v_mbcnt_lo_u32_b32 %0, -1, 0\n\tv_mbcnt_hi_u32_b32 %0, -1, %0" : "=v"(l));
  return l;
}
template <int I>
struct IC { static constexpr int v = I; };
template <int I, int N, class F>
DI void static_for(F&& f) {
  if constexpr (I < N) {
    f(IC<I>{});
    static_for<I + 1, N>(f);
  }
}
template <int N>
DI void wait_vm() { asm volatile("s_waitcnt vmcnt(%0)" ::"i"(N) : "memory"); }
template <int N>
DI void wait_vm1(bf16x8& a) { asm volatile("s_waitcnt vmcnt(%1)" : "+v"(a) : "i"(N) : "memory"); }
DI void spin_flags(const char* base, unsigned voff) {
#pragma unroll 1
  for (int it = 0; it < (1 << 17); ++it) {
    unsigned f;
    gld4c(f, base, voff);
    asm volatile("s_waitcnt vmcnt(0)" : "+v"(f)::"memory");
    if (!__ballot(f != 1u)) break;
    __builtin_amdgcn_s_sleep(4);
  }
}
DI bf16x8 cat8(s16x4 lo, s16x4 hi) { return __builtin_shufflevector(lo, hi, 0, 1, 2, 3, 4, 5, 6, 7); }

#define SENT 0
DI unsigned nsent(unsigned v) { return SENT ? __builtin_elementwise_min(v, 0xfffffffeu) : v; }
DI unsigned umax4(bf16x8 x, unsigned m) {
  const u32x4 v = __builtin_bit_cast(u32x4, x);
  m = __builtin_elementwise_max(__builtin_elementwise_max(v[0], v[1]), m);
  return __builtin_elementwise_max(__builtin_elementwise_max(v[2], v[3]), m);
}
DI f32x16 qk_tile(lds_p kb, lds_p ke, bf16x8 qf, s16x4 qe, f32x16 c) {
  s16x4 lo = trl(kb);
  s16x4 hi = trl(kb + 64);
  s16x4 ex = trl(ke);
  bf16x8 kf = __builtin_shufflevector(lo, hi, 0, 1, 2, 3, 4, 5, 6, 7);
  f32x16 s = MFMA32(kf, qf, c);
  s = MFMA32K8(ex, qe, s);
  return s;
}
DI f32x16 pv_tile(lds_p vb, const f32x16& pe, f32x16 o) {
  bf16x8 v0 = ld8(vb);
  bf16x8 v1 = ld8(vb + TSTEP);
  u32x4 p0, p1;
#pragma unroll
  for (int i = 0; i < 4; ++i) {
    p0[i] = pk2(pe[2 * i], pe[2 * i + 1]);
    p1[i] = pk2(pe[8 + 2 * i], pe[8 + 2 * i + 1]);
  }
  o = MFMA32(v0, __builtin_bit_cast(bf16x8, p0), o);
  o = MFMA32(v1, __builtin_bit_cast(bf16x8, p1), o);
  return o;
}
struct KFrag { bf16x8 f; s16x4 e; };
struct VFrag { bf16x8 v0, v1; };
DI KFrag load_k(lds_p kb, lds_p ke) {
  KFrag k;
  s16x4 lo = trl(kb);
  s16x4 hi = trl(kb + 64);
  k.e = trl(ke);
  k.f = __builtin_shufflevector(lo, hi, 0, 1, 2, 3, 4, 5, 6, 7);
  return k;
}
DI VFrag load_v(lds_p vb) {
  VFrag v;
  v.v0 = ld8(vb);
  v.v1 = ld8(vb + TSTEP);
  return v;
}
#define EXP2(x) __builtin_amdgcn_exp2f(x)
#define PTRUNC 1
DI unsigned pkp(float a, float b) {
  return __builtin_amdgcn_perm(__float_as_uint(b), __float_as_uint(a), 0x07060302u);
}
#define APM 2
#define APMASK 0x33
#define PHI(i) if constexpr ((PM >> (i)) & 1) __builtin_amdgcn_s_setprio(APM)
#define PLO(i) if constexpr ((PM >> (i)) & 1) __builtin_amdgcn_s_setprio(0)
template <int PM>
DI void tile_step(const f32x16& s, f32x16& sn, const KFrag& k, const VFrag& v, bf16x8 qf, s16x4 qe,
                  f32x16& o) {
  f32x16 negm;
#pragma unroll
  for (int i = 0; i < 16; ++i) negm[i] = 0.f;
  u32x4 p0, p1;
  p0[0] = pkp(EXP2(s[0]), EXP2(s[1]));
  p0[1] = pkp(EXP2(s[2]), EXP2(s[3]));
  SB();
  PHI(0);
  SB();
  f32x16 t = MFMA32(k.f, qf, negm);
  SB();
  PLO(0);
  SB();
  p0[2] = pkp(EXP2(s[4]), EXP2(s[5]));
  p0[3] = pkp(EXP2(s[6]), EXP2(s[7]));
  SB();
  PHI(1);
  SB();
  o = MFMA32(v.v0, __builtin_bit_cast(bf16x8, p0), o);
  SB();
  PLO(1);
  SB();
  p1[0] = pkp(EXP2(s[8]), EXP2(s[9]));
  p1[1] = pkp(EXP2(s[10]), EXP2(s[11]));
  SB();
  PHI(2);
  SB();
  t = MFMA32K8(k.e, qe, t);
  SB();
  PLO(2);
  SB();
  p1[2] = pkp(EXP2(s[12]), EXP2(s[13]));
  p1[3] = pkp(EXP2(s[14]), EXP2(s[15]));
  SB();
  PHI(3);
  SB();
  o = MFMA32(v.v1, __builtin_bit_cast(bf16x8, p1), o);
  SB();
  PLO(3);
  SB();
  sn = t;
}
DI float rowmax16(const f32x16& s) {
  float m = __builtin_fmaxf(s[0], s[1]);
#pragma unroll
  for (int i = 2; i < 16; ++i) m = __builtin_fmaxf(m, s[i]);
  return __builtin_fmaxf(m, xor32(m));
}

constexpr int AT_SLOT = 20480;
constexpr int at_cnt(int c, int grp) { return 2 + (((c & 1) == grp) ? 1 : 0); }
constexpr int at_off(int tt) { return ((tt >> 3) & 1) * AT_SLOT + (tt & 7) * 1280; }

#define TOUCH_AT 9
#define ADEF 0
#define OPRIV 0
#define AEP 3
#define PRE_AT 0
DI void attn_role(char* lds, const char* T, char* O, char* P, const char* __restrict__ G, char* FL, const char* __restrict__ W2,
                  const char* __restrict__ Wo, int wg, int wave, int lane) {
  const int head = (wg & 7) * 4 + ((wg >> 3) & 3);
  const int qt = wg >> 5;
  const int r = lane & 31, h = lane >> 5;
  const int grp = wave >> 2;
  const unsigned lbase = lds_addr(lds);
  const int p1 = wave + 8, p2 = 16 + (wave & 3);
  auto mkoff = [&](int pc, int first) {
    int isv = pc >= 10;
    int sl = (pc - 10 * isv) * 64 + lane;
    int seg = sl / 20, row = sl - seg * 20;
    int slot = row < 18 ? first + 576 * isv + head * 18 + row : 1728 + row - 18;
    return (unsigned)(seg * TP + slot * 16);
  };
  const unsigned v0 = mkoff(wave, 576), v1 = mkoff(p1, 576), v2 = mkoff(p2, 576);
  auto issue = [&](int c) {
    unsigned dst = lbase + (c & 3) * AT_SLOT;
    const char* src = T + (size_t)c * (32 * TP);
    glds16s(src, v0, dst + wave * 1024);
    glds16s(src, v1, dst + p1 * 1024);
    if ((c & 1) == grp) glds16s(src, v2, dst + p2 * 1024);
  };
  STAMP(1, 1);
  unsigned gbits;
  gld4(gbits, G + (qt * 256 + wave * 32) * 4, r * 4);
  {
    const char* src = T + (size_t)qt * (32 * TP);
    glds16s(src, mkoff(wave, 0), lbase + 3 * AT_SLOT + wave * 1024);
    if (wave < 2) glds16s(src, mkoff(p1, 0), lbase + 3 * AT_SLOT + p1 * 1024);
  }
  issue(0);
  issue(1);
  issue(2);
  const int q = (lane >> 2) & 3, p = lane & 3, g = (lane >> 4) & 1;
  const lds_p L = (lds_p)lds;
  const lds_p kb = L + g * TSTEP + (p & 1) * (TSTEP / 2) + (8 * h + q) * 16 + (p >> 1) * 8;
  const lds_p ke = L + g * TSTEP + (p & 1) * (TSTEP / 2) + (16 + q) * 16 + (p >> 1) * 8;
  const lds_p vb = L + 10240 + h * (TSTEP / 2) + (r < 19 ? r : 19) * 16;
  lds_p kb2 = kb + 2 * AT_SLOT, ke2 = ke + 2 * AT_SLOT, vb2 = vb + 2 * AT_SLOT;
  asm volatile("" : "+v"(kb2), "+v"(ke2), "+v"(vb2));
  asm volatile("s_waitcnt vmcnt(5)" : "+v"(gbits)::"memory");
  BARRIER();
  STAMP(1, 2);
  bf16x8 qf;
  s16x4 qe;
  {
    KFrag qq = load_k(kb2 + AT_SLOT + wave * 1280, ke2 + AT_SLOT + wave * 1280);
    qf = qq.f;
    qe[0] = h ? (short)0 : qq.e[0];
    qe[1] = h ? (short)0 : qq.e[1];
    qe[2] = 0;
    qe[3] = 0;
  }

  const float gate = __uint_as_float(gbits);
  f32x16 zero;
#pragma unroll
  for (int i = 0; i < 16; ++i) zero[i] = 0.f;
  f32x16 o = zero;
  f32x16 sA = qk_tile(kb, ke, qf, qe, zero);
  {
    const float nx = -rowmax16(sA);
    const unsigned mb = (__float_as_uint(nx) + (nx > 0.f ? 0xffffu : 0u)) >> 16;
    const float nm = __uint_as_float(mb << 16);
#pragma unroll
    for (int i = 0; i < 16; ++i) sA[i] += nm;
    qe[2] = h ? (short)0 : (short)mb;
  }
  KFrag kA, kB;
  VFrag vA, vB;
  kB = load_k(kb + 1280, ke + 1280);
  vA = load_v(vb);
  f32x16 sB;
#pragma unroll 1
  for (int cc = 0; cc < 8; cc += 4) {
    static_for<0, 32>([&](auto Tc) {
      constexpr int tt = decltype(Tc)::v, ci = tt >> 3;
      if constexpr ((tt & 7) == 6) {
        if (cc == 0 || ci < 2) {
          if (grp == 0) wait_vm<at_cnt(ci + 2, 0)>(); else wait_vm<at_cnt(ci + 2, 1)>();
        } else if (ci == 2) wait_vm<0>();
        if (cc == 0 || ci < 3) BARRIER();
        if constexpr (ci == TOUCH_AT) {
          if (cc == 0) {
            const int l2 = fresh_lane();
            const char* src = W2 + (size_t)((wg & 7) >> 1) * (1152 * 1152) + (wg >> 3) * 41472;
            const unsigned dst = lbase + ((ci + 3) & 3) * AT_SLOT + wave * 1024;
            touch(src, (wave * 41 + l2 - (l2 > 40 ? 24 : 0) - (wave >> 2) * 4) * 128u, dst);
            if (wave == 0) touch(Wo + ((wg & 7) >> 1) * (144 * 1152) + (wg >> 3) * 5184, (l2 > 39 ? ((wg & 8) ? 39 : 40) : l2) * 128u + ((wg & 8) ? 64u : 0u), dst);
          }
        }
        if (cc == 0 || ci == 0) issue(cc + ci + 3);
      }
      constexpr int t1 = (tt + 1) & 31, t2 = (tt + 2) & 31;
      const lds_p kp = (t2 < 16 ? kb : kb2) + at_off(t2);
      const lds_p ep = (t2 < 16 ? ke : ke2) + at_off(t2);
      const lds_p vp = (t1 < 16 ? vb : vb2) + at_off(t1);
      if constexpr ((tt & 1) == 0) {
        kA = load_k(kp, ep);
        vB = load_v(vp);
        SB();
        tile_step<APMASK & 15>(sA, sB, kB, vA, qf, qe, o);
      } else {
        kB = load_k(kp, ep);
        vA = load_v(vp);
        SB();
        tile_step<(APMASK >> 4) & 15>(sB, sA, kA, vB, qf, qe, o);
      }
    });
    if (cc == 0) STAMP(1, 3);
  }
  STAMP(1, 4);
  STAMP(1, 5);
  __builtin_amdgcn_s_setprio(AEP);
  auto rowsum = [&]() { return __uint_as_float(__builtin_amdgcn_permlane32_swap(__float_as_uint(o[10]), __float_as_uint(o[10]), false, false)[0]); };
  float l = rowsum();
  auto ab = [&](int i) { return __builtin_fabsf(o[i]) + __builtin_fabsf(o[i + 1]); };
  bool bad = !(((ab(0) + ab(2)) + (ab(4) + ab(6))) + __builtin_fmaf(l, 1.0e7f, ab(8)) < 1.0e37f);
  const int ob = (qt * 8 + wave) * 32 + head;
  auto publish = [&]() {
    const float inv = gate * __builtin_amdgcn_rcpf(l);
    u32x4 ov;
#pragma unroll
    for (int i = 0; i < 4; ++i) ov[i] = pk2(o[2 * i] * inv, o[2 * i + 1] * inv);
    const unsigned vo = lane * 16;
    asm volatile("global_store_dwordx4 %0, %1, %2 sc1\n\ts_nop 1" ::"v"(vo), "v"(ov), "s"(O + ob * 1024) : "memory");
    const unsigned oe = pk2(o[8] * inv, o[9] * inv);
    if (h == 0) asm volatile("global_store_dword %0, %1, %2 sc1" ::"v"(vo), "v"(oe), "s"(O + (OEX + (ob >> 3) * 1024 + ((head >> 2) & 1) * 512 + (head & 3) * 4)) : "memory");
    STAMP(1, 6);
    const unsigned vf = (lane >> 1) * 4096 + (lane & 1) * 512;
    const char* fb = FL + (FL_P2 + (qt * 2 + (wave >> 2)) * 16384 + (wave & 3) * 1024 + (head >> 3) * 128 + 32 + (head & 7) * 4);
    wait_vm<0>();
    if (lane == 0) asm volatile("global_store_dword %0, %1, %2 sc1" ::"v"(vo), "v"(1u), "s"(FL + (FL_O + ob * 4)) : "memory");
  };
  const bool mybad = __any(bad);
  if (!mybad) publish();
  {
    int* fl = (int*)(lds + AT_SLOT);
    if (lane == 0) fl[wave] = mybad ? 1 : 0;
    __syncthreads();
    int v = fl[lane & 7];
    bad = __any(v != 0);
  }
  if (bad) {
    float m = -3.0e38f;
    o = zero;
    qe[2] = 0;
#pragma unroll 1
    for (int c = 0; c < 8; ++c) {
      __syncthreads();
      {
        const char* src = T + (size_t)c * (32 * TP);
        glds16s(src, v0, lbase + wave * 1024);
        glds16s(src, v1, lbase + p1 * 1024);
        if (grp == 0) glds16s(src, v2, lbase + p2 * 1024);
      }
      wait_vm<0>();
      __syncthreads();
#pragma unroll 1
      for (int t = 0; t < 8; ++t) {
        f32x16 s = qk_tile(kb + t * 1280, ke + t * 1280, qf, qe, zero);
        float mn = __builtin_fmaxf(m, rowmax16(s));
        float alpha = __builtin_amdgcn_exp2f(m - mn);
        m = mn;
        f32x16 pe;
#pragma unroll
        for (int i = 0; i < 16; ++i) { pe[i] = __builtin_amdgcn_exp2f(s[i] - mn); o[i] *= alpha; }
        o = pv_tile(vb + t * 1280, pe, o);
      }
    }
    l = rowsum();
    if (mybad) publish();
  }
  STAMP(1, 7);
}

#define QKV_PRIO 2
#define ATT_PRIO 0
#define SKIP1 0
#define MLP_PRIO 1
#define GELU_PRIO MLP_PRIO
#define MLP2_PRIO MLP_PRIO
#define P1HI4 0
#define P1HI5 0
#define P2HI4 0
#define P2HI5 0
#define HI_PRIO 3
#define PF2_AT 20
#define PSLP 2
#define XE_AT 33
constexpr int CK3 = 30;
constexpr int M_IT = 36;
constexpr int M2_NT = 41;
struct M2S { int slot[M2_NT]; int it[M2_NT]; };
constexpr M2S m2_sched(int U) {
  M2S r{};
  const int E = 2 * U - 2;
  int next = 0;
  for (; next < 4; ++next) { r.slot[next] = next; r.it[next] = -1; }
  for (int j = 1; next < M2_NT; ++j) {
    r.slot[next] = r.slot[j - 1];
    r.it[next] = j;
    ++next;
    if (next < M2_NT && (j == E || j == E + 1)) { r.slot[next] = 4 + (j - E); r.it[next] = j; ++next; }
  }
  return r;
}
constexpr int m2_ck1(int U) { return 2 * U - 3; }
constexpr int m2_ck2(int U) { return (U == 5 ? 26 : 2 * U + 8) - 3; }
constexpr int m2_wait(int U, int kh, int j, int mode) {
  const M2S sc = m2_sched(U);
  const int U2 = U == 5 ? 2 : 3;
  int n = 0, last = -1;
  for (int i = -1; i < j + mode; ++i) {
    if (i == m2_ck1(U) - 2 || i == m2_ck2(U) - 2) { if (mode == 1 && i == j - 2) last = n; ++n; }
    for (int s = 0; s < M2_NT; ++s)
      if (sc.it[s] == i) {
        const int c = (U2 == 3 && (s & 1) == kh) ? 2 : 1;
        n += c;
        if (mode == 0 && s <= j + 1) last = n - 1;
      }
    if (i == -1) n += 2 * U;
    if (i >= 0 && (i & 1) && i < j && i + 3 >= 2 * U && i + 3 < M_IT) { n += 2; if (mode == 0 && i + 3 <= j) last = n - 1; }
  }
  return n - 1 - last;
}
constexpr bool m2_need(int U, int j) { return j >= 3; }

template <int N>
DI void pin_acc(f32x16* a) {
  if constexpr (N == 5)
    asm volatile("" : "+{v[0:15]}"(a[0]), "+{v[16:31]}"(a[1]), "+{v[32:47]}"(a[2]), "+{v[48:63]}"(a[3]), "+{v[64:79]}"(a[4]));
  else
    asm volatile("" : "+{v[0:15]}"(a[0]), "+{v[16:31]}"(a[1]), "+{v[32:47]}"(a[2]), "+{v[48:63]}"(a[3]));
}
#define XLD(R) asm volatile("global_load_dwordx4 %0, %1, %2 sc1" : "={" R "}"(d) : "v"(voff), "s"(sbase) : "memory")
#define XLP(R) asm volatile("global_load_dwordx4 %0, %1, %2" : "={" R "}"(d) : "v"(voff), "s"(sbase) : "memory")
template <int I>
DI void xloadp(bf16x8& d, const char* sbase, unsigned voff) {
  if constexpr (I == 0) XLP("v[80:83]");
  else if constexpr (I == 1) XLP("v[84:87]");
  else if constexpr (I == 2) XLP("v[88:91]");
  else XLP("v[92:95]");
}
template <int I>
DI void xload(bf16x8& d, const char* sbase, unsigned voff) {
  if constexpr (I == 0) XLD("v[80:83]");
  else if constexpr (I == 1) XLD("v[84:87]");
  else if constexpr (I == 2) XLD("v[88:91]");
  else XLD("v[92:95]");
}
#define XDS(R) asm volatile("ds_read_b128 %0, %1 offset:%2" : "={" R "}"(d) : "v"(a), "i"(OFF) : "memory")
template <int I, int OFF>
DI void xstash(bf16x8& d, unsigned a) {
  if constexpr (I == 0) XDS("v[80:83]");
  else if constexpr (I == 1) XDS("v[84:87]");
  else if constexpr (I == 2) XDS("v[88:91]");
  else XDS("v[92:95]");
}
template <int I>
DI void pin_x1(bf16x8& x) {
  if constexpr (I == 0) asm volatile("" : "+{v[80:83]}"(x));
  else if constexpr (I == 1) asm volatile("" : "+{v[84:87]}"(x));
  else if constexpr (I == 2) asm volatile("" : "+{v[88:91]}"(x));
  else asm volatile("" : "+{v[92:95]}"(x));
}
template <int I>
DI void pin_w(bf16x8& w) {
  if constexpr (I == 0) asm volatile("" : "+{v[96:99]}"(w));
  else if constexpr (I == 1) asm volatile("" : "+{v[100:103]}"(w));
  else if constexpr (I == 2) asm volatile("" : "+{v[104:107]}"(w));
  else if constexpr (I == 3) asm volatile("" : "+{v[108:111]}"(w));
  else asm volatile("" : "+{v[112:115]}"(w));
}
template <int N>
DI void pair_sum(f32x16* a, unsigned exw, unsigned exr, int kh) {
  if constexpr (N == 5)
    asm volatile(
      "s_nop 15\ns_nop 3\ns_cmp_lg_u32 %[kh], 0\ns_cbranch_scc1 XU%=\nv_cvt_pk_bf16_f32 v80, v8, v9\nv_cvt_pk_bf16_f32 v81, v10, v11\n"
      "v_cvt_pk_bf16_f32 v82, v12, v13\nv_cvt_pk_bf16_f32 v83, v14, v15\nds_write_b128 %[w], v[80:83] offset:0\n"
      "v_cvt_pk_bf16_f32 v84, v24, v25\nv_cvt_pk_bf16_f32 v85, v26, v27\nv_cvt_pk_bf16_f32 v86, v28, v29\nv_cvt_pk_bf16_f32 v87, v30, v31\n"
      "ds_write_b128 %[w], v[84:87] offset:1024\nv_cvt_pk_bf16_f32 v88, v40, v41\nv_cvt_pk_bf16_f32 v89, v42, v43\n"
      "v_cvt_pk_bf16_f32 v90, v44, v45\nv_cvt_pk_bf16_f32 v91, v46, v47\nds_write_b128 %[w], v[88:91] offset:2048\n"
      "v_cvt_pk_bf16_f32 v92, v56, v57\nv_cvt_pk_bf16_f32 v93, v58, v59\nv_cvt_pk_bf16_f32 v94, v60, v61\nv_cvt_pk_bf16_f32 v95, v62, v63\n"
      "ds_write_b128 %[w], v[92:95] offset:3072\nv_cvt_pk_bf16_f32 v96, v72, v73\nv_cvt_pk_bf16_f32 v97, v74, v75\n"
      "v_cvt_pk_bf16_f32 v98, v76, v77\nv_cvt_pk_bf16_f32 v99, v78, v79\nds_write_b128 %[w], v[96:99] offset:4096\ns_waitcnt lgkmcnt(0)\n"
      "s_barrier\nds_read_b128 v[80:83], %[r] offset:0\nds_read_b128 v[84:87], %[r] offset:1024\nds_read_b128 v[88:91], %[r] offset:2048\n"
      "ds_read_b128 v[92:95], %[r] offset:3072\nds_read_b128 v[96:99], %[r] offset:4096\ns_waitcnt lgkmcnt(4)\nv_lshlrev_b32 v8, 16, v80\n"
      "v_and_b32 v9, 0xffff0000, v80\nv_lshlrev_b32 v10, 16, v81\nv_and_b32 v11, 0xffff0000, v81\nv_lshlrev_b32 v12, 16, v82\n"
      "v_and_b32 v13, 0xffff0000, v82\nv_lshlrev_b32 v14, 16, v83\nv_and_b32 v15, 0xffff0000, v83\nv_pk_add_f32 v[0:1], v[8:9], v[0:1]\n"
      "v_pk_add_f32 v[2:3], v[10:11], v[2:3]\nv_pk_add_f32 v[4:5], v[12:13], v[4:5]\nv_pk_add_f32 v[6:7], v[14:15], v[6:7]\n"
      "s_waitcnt lgkmcnt(3)\nv_lshlrev_b32 v24, 16, v84\nv_and_b32 v25, 0xffff0000, v84\nv_lshlrev_b32 v26, 16, v85\n"
      "v_and_b32 v27, 0xffff0000, v85\nv_lshlrev_b32 v28, 16, v86\nv_and_b32 v29, 0xffff0000, v86\nv_lshlrev_b32 v30, 16, v87\n"
      "v_and_b32 v31, 0xffff0000, v87\nv_pk_add_f32 v[16:17], v[24:25], v[16:17]\nv_pk_add_f32 v[18:19], v[26:27], v[18:19]\n"
      "v_pk_add_f32 v[20:21], v[28:29], v[20:21]\nv_pk_add_f32 v[22:23], v[30:31], v[22:23]\ns_waitcnt lgkmcnt(2)\n"
      "v_lshlrev_b32 v40, 16, v88\nv_and_b32 v41, 0xffff0000, v88\nv_lshlrev_b32 v42, 16, v89\nv_and_b32 v43, 0xffff0000, v89\n"
      "v_lshlrev_b32 v44, 16, v90\nv_and_b32 v45, 0xffff0000, v90\nv_lshlrev_b32 v46, 16, v91\nv_and_b32 v47, 0xffff0000, v91\n"
      "v_pk_add_f32 v[32:33], v[40:41], v[32:33]\nv_pk_add_f32 v[34:35], v[42:43], v[34:35]\nv_pk_add_f32 v[36:37], v[44:45], v[36:37]\n"
      "v_pk_add_f32 v[38:39], v[46:47], v[38:39]\ns_waitcnt lgkmcnt(1)\nv_lshlrev_b32 v56, 16, v92\nv_and_b32 v57, 0xffff0000, v92\n"
      "v_lshlrev_b32 v58, 16, v93\nv_and_b32 v59, 0xffff0000, v93\nv_lshlrev_b32 v60, 16, v94\nv_and_b32 v61, 0xffff0000, v94\n"
      "v_lshlrev_b32 v62, 16, v95\nv_and_b32 v63, 0xffff0000, v95\nv_pk_add_f32 v[48:49], v[56:57], v[48:49]\n"
      "v_pk_add_f32 v[50:51], v[58:59], v[50:51]\nv_pk_add_f32 v[52:53], v[60:61], v[52:53]\nv_pk_add_f32 v[54:55], v[62:63], v[54:55]\n"
      "s_waitcnt lgkmcnt(0)\nv_lshlrev_b32 v72, 16, v96\nv_and_b32 v73, 0xffff0000, v96\nv_lshlrev_b32 v74, 16, v97\n"
      "v_and_b32 v75, 0xffff0000, v97\nv_lshlrev_b32 v76, 16, v98\nv_and_b32 v77, 0xffff0000, v98\nv_lshlrev_b32 v78, 16, v99\n"
      "v_and_b32 v79, 0xffff0000, v99\nv_pk_add_f32 v[64:65], v[72:73], v[64:65]\nv_pk_add_f32 v[66:67], v[74:75], v[66:67]\n"
      "v_pk_add_f32 v[68:69], v[76:77], v[68:69]\nv_pk_add_f32 v[70:71], v[78:79], v[70:71]\ns_branch XE%=\nXU%=:\n"
      "v_cvt_pk_bf16_f32 v80, v0, v1\nv_cvt_pk_bf16_f32 v81, v2, v3\nv_cvt_pk_bf16_f32 v82, v4, v5\nv_cvt_pk_bf16_f32 v83, v6, v7\n"
      "ds_write_b128 %[w], v[80:83] offset:0\nv_cvt_pk_bf16_f32 v84, v16, v17\nv_cvt_pk_bf16_f32 v85, v18, v19\n"
      "v_cvt_pk_bf16_f32 v86, v20, v21\nv_cvt_pk_bf16_f32 v87, v22, v23\nds_write_b128 %[w], v[84:87] offset:1024\n"
      "v_cvt_pk_bf16_f32 v88, v32, v33\nv_cvt_pk_bf16_f32 v89, v34, v35\nv_cvt_pk_bf16_f32 v90, v36, v37\nv_cvt_pk_bf16_f32 v91, v38, v39\n"
      "ds_write_b128 %[w], v[88:91] offset:2048\nv_cvt_pk_bf16_f32 v92, v48, v49\nv_cvt_pk_bf16_f32 v93, v50, v51\n"
      "v_cvt_pk_bf16_f32 v94, v52, v53\nv_cvt_pk_bf16_f32 v95, v54, v55\nds_write_b128 %[w], v[92:95] offset:3072\n"
      "v_cvt_pk_bf16_f32 v96, v64, v65\nv_cvt_pk_bf16_f32 v97, v66, v67\nv_cvt_pk_bf16_f32 v98, v68, v69\nv_cvt_pk_bf16_f32 v99, v70, v71\n"
      "ds_write_b128 %[w], v[96:99] offset:4096\ns_waitcnt lgkmcnt(0)\ns_barrier\nds_read_b128 v[80:83], %[r] offset:0\n"
      "ds_read_b128 v[84:87], %[r] offset:1024\nds_read_b128 v[88:91], %[r] offset:2048\nds_read_b128 v[92:95], %[r] offset:3072\n"
      "ds_read_b128 v[96:99], %[r] offset:4096\ns_waitcnt lgkmcnt(4)\nv_lshlrev_b32 v0, 16, v80\nv_and_b32 v1, 0xffff0000, v80\n"
      "v_lshlrev_b32 v2, 16, v81\nv_and_b32 v3, 0xffff0000, v81\nv_lshlrev_b32 v4, 16, v82\nv_and_b32 v5, 0xffff0000, v82\n"
      "v_lshlrev_b32 v6, 16, v83\nv_and_b32 v7, 0xffff0000, v83\nv_pk_add_f32 v[0:1], v[0:1], v[8:9]\n"
      "v_pk_add_f32 v[2:3], v[2:3], v[10:11]\nv_pk_add_f32 v[4:5], v[4:5], v[12:13]\nv_pk_add_f32 v[6:7], v[6:7], v[14:15]\n"
      "s_waitcnt lgkmcnt(3)\nv_lshlrev_b32 v16, 16, v84\nv_and_b32 v17, 0xffff0000, v84\nv_lshlrev_b32 v18, 16, v85\n"
      "v_and_b32 v19, 0xffff0000, v85\nv_lshlrev_b32 v20, 16, v86\nv_and_b32 v21, 0xffff0000, v86\nv_lshlrev_b32 v22, 16, v87\n"
      "v_and_b32 v23, 0xffff0000, v87\nv_pk_add_f32 v[16:17], v[16:17], v[24:25]\nv_pk_add_f32 v[18:19], v[18:19], v[26:27]\n"
      "v_pk_add_f32 v[20:21], v[20:21], v[28:29]\nv_pk_add_f32 v[22:23], v[22:23], v[30:31]\ns_waitcnt lgkmcnt(2)\n"
      "v_lshlrev_b32 v32, 16, v88\nv_and_b32 v33, 0xffff0000, v88\nv_lshlrev_b32 v34, 16, v89\nv_and_b32 v35, 0xffff0000, v89\n"
      "v_lshlrev_b32 v36, 16, v90\nv_and_b32 v37, 0xffff0000, v90\nv_lshlrev_b32 v38, 16, v91\nv_and_b32 v39, 0xffff0000, v91\n"
      "v_pk_add_f32 v[32:33], v[32:33], v[40:41]\nv_pk_add_f32 v[34:35], v[34:35], v[42:43]\nv_pk_add_f32 v[36:37], v[36:37], v[44:45]\n"
      "v_pk_add_f32 v[38:39], v[38:39], v[46:47]\ns_waitcnt lgkmcnt(1)\nv_lshlrev_b32 v48, 16, v92\nv_and_b32 v49, 0xffff0000, v92\n"
      "v_lshlrev_b32 v50, 16, v93\nv_and_b32 v51, 0xffff0000, v93\nv_lshlrev_b32 v52, 16, v94\nv_and_b32 v53, 0xffff0000, v94\n"
      "v_lshlrev_b32 v54, 16, v95\nv_and_b32 v55, 0xffff0000, v95\nv_pk_add_f32 v[48:49], v[48:49], v[56:57]\n"
      "v_pk_add_f32 v[50:51], v[50:51], v[58:59]\nv_pk_add_f32 v[52:53], v[52:53], v[60:61]\nv_pk_add_f32 v[54:55], v[54:55], v[62:63]\n"
      "s_waitcnt lgkmcnt(0)\nv_lshlrev_b32 v64, 16, v96\nv_and_b32 v65, 0xffff0000, v96\nv_lshlrev_b32 v66, 16, v97\n"
      "v_and_b32 v67, 0xffff0000, v97\nv_lshlrev_b32 v68, 16, v98\nv_and_b32 v69, 0xffff0000, v98\nv_lshlrev_b32 v70, 16, v99\n"
      "v_and_b32 v71, 0xffff0000, v99\nv_pk_add_f32 v[64:65], v[64:65], v[72:73]\nv_pk_add_f32 v[66:67], v[66:67], v[74:75]\n"
      "v_pk_add_f32 v[68:69], v[68:69], v[76:77]\nv_pk_add_f32 v[70:71], v[70:71], v[78:79]\nXE%=:\n"
      : "+{v[0:15]}"(a[0]), "+{v[16:31]}"(a[1]), "+{v[32:47]}"(a[2]), "+{v[48:63]}"(a[3]), "+{v[64:79]}"(a[4])
      : [w] "v"(exw), [r] "v"(exr), [kh] "s"(kh)
      : "memory", "scc", "v80", "v81", "v82", "v83", "v84", "v85", "v86", "v87", "v88", "v89", "v90", "v91", "v92", "v93", "v94", "v95", "v96", "v97", "v98", "v99");
  else
    asm volatile(
      "s_nop 15\ns_nop 3\ns_cmp_lg_u32 %[kh], 0\ns_cbranch_scc1 XU%=\nv_cvt_pk_bf16_f32 v80, v8, v9\nv_cvt_pk_bf16_f32 v81, v10, v11\n"
      "v_cvt_pk_bf16_f32 v82, v12, v13\nv_cvt_pk_bf16_f32 v83, v14, v15\nds_write_b128 %[w], v[80:83] offset:0\n"
      "v_cvt_pk_bf16_f32 v84, v24, v25\nv_cvt_pk_bf16_f32 v85, v26, v27\nv_cvt_pk_bf16_f32 v86, v28, v29\nv_cvt_pk_bf16_f32 v87, v30, v31\n"
      "ds_write_b128 %[w], v[84:87] offset:1024\nv_cvt_pk_bf16_f32 v88, v40, v41\nv_cvt_pk_bf16_f32 v89, v42, v43\n"
      "v_cvt_pk_bf16_f32 v90, v44, v45\nv_cvt_pk_bf16_f32 v91, v46, v47\nds_write_b128 %[w], v[88:91] offset:2048\n"
      "v_cvt_pk_bf16_f32 v92, v56, v57\nv_cvt_pk_bf16_f32 v93, v58, v59\nv_cvt_pk_bf16_f32 v94, v60, v61\nv_cvt_pk_bf16_f32 v95, v62, v63\n"
      "ds_write_b128 %[w], v[92:95] offset:3072\ns_waitcnt lgkmcnt(0)\ns_barrier\nds_read_b128 v[80:83], %[r] offset:0\n"
      "ds_read_b128 v[84:87], %[r] offset:1024\nds_read_b128 v[88:91], %[r] offset:2048\nds_read_b128 v[92:95], %[r] offset:3072\n"
      "s_waitcnt lgkmcnt(3)\nv_lshlrev_b32 v8, 16, v80\nv_and_b32 v9, 0xffff0000, v80\nv_lshlrev_b32 v10, 16, v81\n"
      "v_and_b32 v11, 0xffff0000, v81\nv_lshlrev_b32 v12, 16, v82\nv_and_b32 v13, 0xffff0000, v82\nv_lshlrev_b32 v14, 16, v83\n"
      "v_and_b32 v15, 0xffff0000, v83\nv_pk_add_f32 v[0:1], v[8:9], v[0:1]\nv_pk_add_f32 v[2:3], v[10:11], v[2:3]\n"
      "v_pk_add_f32 v[4:5], v[12:13], v[4:5]\nv_pk_add_f32 v[6:7], v[14:15], v[6:7]\ns_waitcnt lgkmcnt(2)\nv_lshlrev_b32 v24, 16, v84\n"
      "v_and_b32 v25, 0xffff0000, v84\nv_lshlrev_b32 v26, 16, v85\nv_and_b32 v27, 0xffff0000, v85\nv_lshlrev_b32 v28, 16, v86\n"
      "v_and_b32 v29, 0xffff0000, v86\nv_lshlrev_b32 v30, 16, v87\nv_and_b32 v31, 0xffff0000, v87\n"
      "v_pk_add_f32 v[16:17], v[24:25], v[16:17]\nv_pk_add_f32 v[18:19], v[26:27], v[18:19]\nv_pk_add_f32 v[20:21], v[28:29], v[20:21]\n"
      "v_pk_add_f32 v[22:23], v[30:31], v[22:23]\ns_waitcnt lgkmcnt(1)\nv_lshlrev_b32 v40, 16, v88\nv_and_b32 v41, 0xffff0000, v88\n"
      "v_lshlrev_b32 v42, 16, v89\nv_and_b32 v43, 0xffff0000, v89\nv_lshlrev_b32 v44, 16, v90\nv_and_b32 v45, 0xffff0000, v90\n"
      "v_lshlrev_b32 v46, 16, v91\nv_and_b32 v47, 0xffff0000, v91\nv_pk_add_f32 v[32:33], v[40:41], v[32:33]\n"
      "v_pk_add_f32 v[34:35], v[42:43], v[34:35]\nv_pk_add_f32 v[36:37], v[44:45], v[36:37]\nv_pk_add_f32 v[38:39], v[46:47], v[38:39]\n"
      "s_waitcnt lgkmcnt(0)\nv_lshlrev_b32 v56, 16, v92\nv_and_b32 v57, 0xffff0000, v92\nv_lshlrev_b32 v58, 16, v93\n"
      "v_and_b32 v59, 0xffff0000, v93\nv_lshlrev_b32 v60, 16, v94\nv_and_b32 v61, 0xffff0000, v94\nv_lshlrev_b32 v62, 16, v95\n"
      "v_and_b32 v63, 0xffff0000, v95\nv_pk_add_f32 v[48:49], v[56:57], v[48:49]\nv_pk_add_f32 v[50:51], v[58:59], v[50:51]\n"
      "v_pk_add_f32 v[52:53], v[60:61], v[52:53]\nv_pk_add_f32 v[54:55], v[62:63], v[54:55]\ns_branch XE%=\nXU%=:\n"
      "v_cvt_pk_bf16_f32 v80, v0, v1\nv_cvt_pk_bf16_f32 v81, v2, v3\nv_cvt_pk_bf16_f32 v82, v4, v5\nv_cvt_pk_bf16_f32 v83, v6, v7\n"
      "ds_write_b128 %[w], v[80:83] offset:0\nv_cvt_pk_bf16_f32 v84, v16, v17\nv_cvt_pk_bf16_f32 v85, v18, v19\n"
      "v_cvt_pk_bf16_f32 v86, v20, v21\nv_cvt_pk_bf16_f32 v87, v22, v23\nds_write_b128 %[w], v[84:87] offset:1024\n"
      "v_cvt_pk_bf16_f32 v88, v32, v33\nv_cvt_pk_bf16_f32 v89, v34, v35\nv_cvt_pk_bf16_f32 v90, v36, v37\nv_cvt_pk_bf16_f32 v91, v38, v39\n"
      "ds_write_b128 %[w], v[88:91] offset:2048\nv_cvt_pk_bf16_f32 v92, v48, v49\nv_cvt_pk_bf16_f32 v93, v50, v51\n"
      "v_cvt_pk_bf16_f32 v94, v52, v53\nv_cvt_pk_bf16_f32 v95, v54, v55\nds_write_b128 %[w], v[92:95] offset:3072\ns_waitcnt lgkmcnt(0)\n"
      "s_barrier\nds_read_b128 v[80:83], %[r] offset:0\nds_read_b128 v[84:87], %[r] offset:1024\nds_read_b128 v[88:91], %[r] offset:2048\n"
      "ds_read_b128 v[92:95], %[r] offset:3072\ns_waitcnt lgkmcnt(3)\nv_lshlrev_b32 v0, 16, v80\nv_and_b32 v1, 0xffff0000, v80\n"
      "v_lshlrev_b32 v2, 16, v81\nv_and_b32 v3, 0xffff0000, v81\nv_lshlrev_b32 v4, 16, v82\nv_and_b32 v5, 0xffff0000, v82\n"
      "v_lshlrev_b32 v6, 16, v83\nv_and_b32 v7, 0xffff0000, v83\nv_pk_add_f32 v[0:1], v[0:1], v[8:9]\n"
      "v_pk_add_f32 v[2:3], v[2:3], v[10:11]\nv_pk_add_f32 v[4:5], v[4:5], v[12:13]\nv_pk_add_f32 v[6:7], v[6:7], v[14:15]\n"
      "s_waitcnt lgkmcnt(2)\nv_lshlrev_b32 v16, 16, v84\nv_and_b32 v17, 0xffff0000, v84\nv_lshlrev_b32 v18, 16, v85\n"
      "v_and_b32 v19, 0xffff0000, v85\nv_lshlrev_b32 v20, 16, v86\nv_and_b32 v21, 0xffff0000, v86\nv_lshlrev_b32 v22, 16, v87\n"
      "v_and_b32 v23, 0xffff0000, v87\nv_pk_add_f32 v[16:17], v[16:17], v[24:25]\nv_pk_add_f32 v[18:19], v[18:19], v[26:27]\n"
      "v_pk_add_f32 v[20:21], v[20:21], v[28:29]\nv_pk_add_f32 v[22:23], v[22:23], v[30:31]\ns_waitcnt lgkmcnt(1)\n"
      "v_lshlrev_b32 v32, 16, v88\nv_and_b32 v33, 0xffff0000, v88\nv_lshlrev_b32 v34, 16, v89\nv_and_b32 v35, 0xffff0000, v89\n"
      "v_lshlrev_b32 v36, 16, v90\nv_and_b32 v37, 0xffff0000, v90\nv_lshlrev_b32 v38, 16, v91\nv_and_b32 v39, 0xffff0000, v91\n"
      "v_pk_add_f32 v[32:33], v[32:33], v[40:41]\nv_pk_add_f32 v[34:35], v[34:35], v[42:43]\nv_pk_add_f32 v[36:37], v[36:37], v[44:45]\n"
      "v_pk_add_f32 v[38:39], v[38:39], v[46:47]\ns_waitcnt lgkmcnt(0)\nv_lshlrev_b32 v48, 16, v92\nv_and_b32 v49, 0xffff0000, v92\n"
      "v_lshlrev_b32 v50, 16, v93\nv_and_b32 v51, 0xffff0000, v93\nv_lshlrev_b32 v52, 16, v94\nv_and_b32 v53, 0xffff0000, v94\n"
      "v_lshlrev_b32 v54, 16, v95\nv_and_b32 v55, 0xffff0000, v95\nv_pk_add_f32 v[48:49], v[48:49], v[56:57]\n"
      "v_pk_add_f32 v[50:51], v[50:51], v[58:59]\nv_pk_add_f32 v[52:53], v[52:53], v[60:61]\nv_pk_add_f32 v[54:55], v[54:55], v[62:63]\n"
      "XE%=:\n"
      : "+{v[0:15]}"(a[0]), "+{v[16:31]}"(a[1]), "+{v[32:47]}"(a[2]), "+{v[48:63]}"(a[3])
      : [w] "v"(exw), [r] "v"(exr), [kh] "s"(kh)
      : "memory", "scc", "v80", "v81", "v82", "v83", "v84", "v85", "v86", "v87", "v88", "v89", "v90", "v91", "v92", "v93", "v94", "v95");
}
DI void gld4ck(unsigned& d, const char* sbase, unsigned voff) {
  asm volatile("global_load_dword %0, %1, %2 sc1" : "+v"(d) : "v"(voff), "s"(sbase) : "memory");
}
#define PSTG 0
#define OUTSC 0
#define TS(a, b) do { if ((a) >= 0) SST((a) < 0 ? 0 : (a)); } while (0)
#define MFMA16(a, b, c) __builtin_amdgcn_mfma_f32_16x16x32_bf16((a), (b), (c), 0, 0, 0)
template <int Q>
DI f32x4 sub4(const f32x16& a) { return __builtin_shufflevector(a, a, 4 * Q, 4 * Q + 1, 4 * Q + 2, 4 * Q + 3); }
template <int Q>
DI void put4(f32x16& a, f32x4 s) {
#pragma unroll
  for (int i = 0; i < 4; ++i) a[4 * Q + i] = s[i];
}
template <int HF>
DI void mm16(f32x16& a, bf16x8 w, bf16x8 x0, bf16x8 x1) {
  put4<2 * HF>(a, MFMA16(w, x0, sub4<2 * HF>(a)));
  put4<2 * HF + 1>(a, MFMA16(w, x1, sub4<2 * HF + 1>(a)));
}
template <int N>
DI void mm16e(f32x16* a, bf16x8* w, u32x2 x0, u32x2 x1, int kh) {
  if constexpr (N == 5)
    asm volatile(
      "s_nop 11\ns_cmp_lg_u32 %[kh], 0\ns_cbranch_scc1 YU%=\n"
      "v_mfma_f32_16x16x16_bf16 v[0:3], v[96:97], %[x0], v[0:3]\n"
      "v_mfma_f32_16x16x16_bf16 v[4:7], v[96:97], %[x1], v[4:7]\n"
      "v_mfma_f32_16x16x16_bf16 v[16:19], v[100:101], %[x0], v[16:19]\n"
      "v_mfma_f32_16x16x16_bf16 v[20:23], v[100:101], %[x1], v[20:23]\n"
      "v_mfma_f32_16x16x16_bf16 v[32:35], v[104:105], %[x0], v[32:35]\n"
      "v_mfma_f32_16x16x16_bf16 v[36:39], v[104:105], %[x1], v[36:39]\n"
      "v_mfma_f32_16x16x16_bf16 v[48:51], v[108:109], %[x0], v[48:51]\n"
      "v_mfma_f32_16x16x16_bf16 v[52:55], v[108:109], %[x1], v[52:55]\n"
      "v_mfma_f32_16x16x16_bf16 v[64:67], v[112:113], %[x0], v[64:67]\n"
      "v_mfma_f32_16x16x16_bf16 v[68:71], v[112:113], %[x1], v[68:71]\n"
      "s_branch YE%=\nYU%=:\n"
      "v_mfma_f32_16x16x16_bf16 v[8:11], v[96:97], %[x0], v[8:11]\n"
      "v_mfma_f32_16x16x16_bf16 v[12:15], v[96:97], %[x1], v[12:15]\n"
      "v_mfma_f32_16x16x16_bf16 v[24:27], v[100:101], %[x0], v[24:27]\n"
      "v_mfma_f32_16x16x16_bf16 v[28:31], v[100:101], %[x1], v[28:31]\n"
      "v_mfma_f32_16x16x16_bf16 v[40:43], v[104:105], %[x0], v[40:43]\n"
      "v_mfma_f32_16x16x16_bf16 v[44:47], v[104:105], %[x1], v[44:47]\n"
      "v_mfma_f32_16x16x16_bf16 v[56:59], v[108:109], %[x0], v[56:59]\n"
      "v_mfma_f32_16x16x16_bf16 v[60:63], v[108:109], %[x1], v[60:63]\n"
      "v_mfma_f32_16x16x16_bf16 v[72:75], v[112:113], %[x0], v[72:75]\n"
      "v_mfma_f32_16x16x16_bf16 v[76:79], v[112:113], %[x1], v[76:79]\n"
      "YE%=:\n"
      : "+{v[0:15]}"(a[0]), "+{v[16:31]}"(a[1]), "+{v[32:47]}"(a[2]), "+{v[48:63]}"(a[3]), "+{v[64:79]}"(a[4])
      : "{v[96:99]}"(w[0]), "{v[100:103]}"(w[1]), "{v[104:107]}"(w[2]), "{v[108:111]}"(w[3]), "{v[112:115]}"(w[4]), [x0] "v"(x0), [x1] "v"(x1), [kh] "s"(kh)
      : "scc");
  else
    asm volatile(
      "s_nop 11\ns_cmp_lg_u32 %[kh], 0\ns_cbranch_scc1 YU%=\n"
      "v_mfma_f32_16x16x16_bf16 v[0:3], v[96:97], %[x0], v[0:3]\n"
      "v_mfma_f32_16x16x16_bf16 v[4:7], v[96:97], %[x1], v[4:7]\n"
      "v_mfma_f32_16x16x16_bf16 v[16:19], v[100:101], %[x0], v[16:19]\n"
      "v_mfma_f32_16x16x16_bf16 v[20:23], v[100:101], %[x1], v[20:23]\n"
      "v_mfma_f32_16x16x16_bf16 v[32:35], v[104:105], %[x0], v[32:35]\n"
      "v_mfma_f32_16x16x16_bf16 v[36:39], v[104:105], %[x1], v[36:39]\n"
      "v_mfma_f32_16x16x16_bf16 v[48:51], v[108:109], %[x0], v[48:51]\n"
      "v_mfma_f32_16x16x16_bf16 v[52:55], v[108:109], %[x1], v[52:55]\n"
      "s_branch YE%=\nYU%=:\n"
      "v_mfma_f32_16x16x16_bf16 v[8:11], v[96:97], %[x0], v[8:11]\n"
      "v_mfma_f32_16x16x16_bf16 v[12:15], v[96:97], %[x1], v[12:15]\n"
      "v_mfma_f32_16x16x16_bf16 v[24:27], v[100:101], %[x0], v[24:27]\n"
      "v_mfma_f32_16x16x16_bf16 v[28:31], v[100:101], %[x1], v[28:31]\n"
      "v_mfma_f32_16x16x16_bf16 v[40:43], v[104:105], %[x0], v[40:43]\n"
      "v_mfma_f32_16x16x16_bf16 v[44:47], v[104:105], %[x1], v[44:47]\n"
      "v_mfma_f32_16x16x16_bf16 v[56:59], v[108:109], %[x0], v[56:59]\n"
      "v_mfma_f32_16x16x16_bf16 v[60:63], v[108:109], %[x1], v[60:63]\n"
      "YE%=:\n"
      : "+{v[0:15]}"(a[0]), "+{v[16:31]}"(a[1]), "+{v[32:47]}"(a[2]), "+{v[48:63]}"(a[3])
      : "{v[96:99]}"(w[0]), "{v[100:103]}"(w[1]), "{v[104:107]}"(w[2]), "{v[108:111]}"(w[3]), [x0] "v"(x0), [x1] "v"(x1), [kh] "s"(kh)
      : "scc");
}
DI bf16x8 w0frag(unsigned v) { const u32x4 t = {v, 0u, 0u, 0u}; return __builtin_bit_cast(bf16x8, t); }
template <int U>
DI void mlp_track(char* lds, const char* __restrict__ XN, const char* __restrict__ W1,
                  const unsigned short* __restrict__ b1, char* H, const char* __restrict__ W2,
                  const unsigned short* __restrict__ b2, const char* __restrict__ Wo, const char* O,
                  const char* __restrict__ G, char* P, char* FL, char* __restrict__ out, int rt, int sp, int j4,
                  int koff, int wave, int lane) {
  constexpr int NBW = 9 - U, U2 = U == 5 ? 2 : 3;
  constexpr int NS1 = U == 5 ? 5 : 6;
  SSD();
  TS(0, -1);
  SS8(0);
  const bool up = j4 < 2;
  const int unit0 = sp * 18 + (U == 5 ? (up ? 0 : 13) : (up ? 5 : 9));
  const unsigned lbase = lds_addr(lds);
  const lds_p L = (lds_p)lds;
  const int wm = wave & 3, wn = wave >> 2;
  char* FLg = FL + (size_t)(rt * 4 + sp) * 512;
  f32x16 acc[5];
  bf16x8 w[5], xr[4];
  float iog0, iog1;
  const f32x4 zero4 = {0.f, 0.f, 0.f, 0.f};
  {
    constexpr int WS = 2 * U * 1024, XB = NS1 * WS, NP = 4 + 2 * U;
    const int g = lane >> 4, r16 = lane & 15;
    const unsigned voffW = (unsigned)(((lane >> 2) & 7) * 9216 + (lane >> 5) * 64 + (((lane & 3) ^ ((wave & 1) << 1)) << 4));
    const unsigned voffE = (unsigned)((lane >> 2) * 9216 + (((lane & 3) ^ ((lane >> 5) << 1)) << 4));
    const unsigned voffG = (unsigned)(g * 512 + r16 * 16);
    const unsigned voff0 = wave < 4 ? voffG : voffW, voff1 = wave < 4 ? voffW : voffE;
    const char* wb = W1 + unit0 * 128;
    const char* src0 = wave < 4 ? XN + (size_t)(rt * 4 + wave) * (36 * 1024) : wb + wm * (8 * 9216);
    const char* src1 = wave < 4 ? wb + (wm * (8 * 9216) + 128) : wb + (wm * (16 * 9216) + 512);
    const unsigned st0 = wave < 4 ? 2048u : 294912u;
    const unsigned ev0 = wave < 4 ? 256u : 0u, od1 = wave < 4 ? 256u : 64u;
    const int koff1 = koff + (wave < 4 ? 1 : 0);
    const bool two = wave + 8 < NP;
    const unsigned dst0 = lbase + (wave < 4 ? XB + wave * 1024 : wm * 1024), ds0 = wave < 4 ? 4096u : (unsigned)WS;
    auto issue = [&](int s, int e) {
      int ks = (s >> 1) + ((s & 1) && e == 0 ? koff1 : koff);
      ks = ks >= 18 ? ks - 18 : ks;
      if (e == 0) glds16s(src0 + ((unsigned)ks * st0 + ((s & 1) ? 256u - ev0 : ev0)), voff0, dst0 + (s % NS1) * ds0);
      else if (two) glds16s(src1 + ((unsigned)ks * 294912u + ((s & 1) ? od1 : 0u)), voff1, lbase + (s % NS1) * WS + (wave + 4) * 1024);
    };
    auto waitn = [&](auto Nc) {
      constexpr int N = decltype(Nc)::v;
      if (two) wait_vm<2 * N>(); else wait_vm<N>();
    };
    unsigned bb[U];
#pragma unroll
    for (int b = 0; b < U; ++b)
      asm volatile("global_load_ushort %0, %1, %2" : "=v"(bb[b]) : "v"((r16 + (g & 1) * (b < 4 ? 128 : 32)) * 2),
                   "s"(b1 + (unit0 * 64 + (b < 4 ? wn * 64 + b * 16 : 256 + wn * 16))) : "memory");
    asm volatile("global_load_dword %0, %1, %2" : "=v"(iog0) : "v"(r16 * 4), "s"(G + (2048 + (rt * 4 + wm) * 32) * 4) : "memory");
    asm volatile("global_load_dword %0, %1, %2 offset:64" : "=v"(iog1) : "v"(r16 * 4), "s"(G + (2048 + (rt * 4 + wm) * 32) * 4) : "memory");
    xloadp<0>(xr[0], XN + ((size_t)(rt * 4 + wm) * (36 * 1024) + koff * 2048), voffG);
#pragma unroll
    for (int s = 0; s < NS1; ++s) { issue(s, 0); issue(s, 1); }
    const int q = (lane >> 2) & 3, p = lane & 3;
    lds_p xa = L + XB + wm * 1024 + lane * 16;
    lds_p wa[2], wae = L + 8192 + g * 512 + q * 64 + ((wn ^ (g & 1)) << 5) + p * 8;
#pragma unroll
    for (int e = 0; e < 2; ++e) wa[e] = L + (wn * 4 + g) * 1024 + q * 64 + ((e ^ (g & 1)) << 5) + p * 8;
    asm volatile("" : "+v"(xa), "+v"(wa[0]), "+v"(wa[1]));
    if constexpr (U == 5) asm volatile("" : "+v"(wae));
    waitn(IC<NS1 - 1>{});
    {
      pin_x1<0>(xr[0]);
      asm volatile("" : "+v"(iog0), "+v"(iog1));
      const bf16x8 o0 = w0frag(g == 0 ? 0x3F80u : 0u), o1 = w0frag(g == 1 ? 0x3F80u : 0u);
#pragma unroll
      for (int b = 0; b < U; ++b) {
        asm volatile("" : "+v"(bb[b]));
        const bf16x8 wx = w0frag(bb[b]);
        put4<0>(acc[b], MFMA16(wx, o0, zero4));
        put4<2>(acc[b], MFMA16(wx, o1, zero4));
        asm volatile("" : "+v"(bb[b]));
        const bf16x8 wy = w0frag(bb[b]);
        put4<1>(acc[b], MFMA16(wy, o0, zero4));
        put4<3>(acc[b], MFMA16(wy, o1, zero4));
      }
    }
    BARRIER();
    __builtin_amdgcn_s_setprio(MLP_PRIO);
    STAMP(4, 1);
    auto rdx = [&](int sl) { return ld8(xa + sl * 4096); };
    auto rdw = [&](int sl, int m) {
      const lds_p a = m < 4 ? wa[m & 1] + (sl * WS + (m >> 1) * 512) : wae + sl * WS;
      return cat8(trl(a), trl(a + 256));
    };
    xr[1] = rdx(0);
#pragma unroll
    for (int m = 0; m < U; ++m) w[m] = rdw(0, m);
    unsigned pfd;
    static_for<0, 36>([&](auto Jc) {
      constexpr int j = decltype(Jc)::v;
      if constexpr (j + 1 < 36) {
        constexpr int top = j == 0 ? NS1 - 1 : (j + NS1 - 2 < 35 ? j + NS1 - 2 : 35);
        waitn(IC<top - (j + 1)>{});
        BARRIER();
      }
      pin_acc<U>(acc);
      pin_x1<(j & 2)>(xr[j & 2]);
      STAMP2(j);
      if (j == 12) STAMP(4, 2);
      if (j == 24) STAMP(4, 3);
      SB();
      static_for<0, U>([&](auto Mc) {
        constexpr int m = decltype(Mc)::v;
        pin_w<m>(w[m]);
        if constexpr (m == 0) pin_x1<(j & 2) + 1>(xr[(j & 2) + 1]);
        mm16<(j & 1)>(acc[m], w[m], xr[j & 2], xr[(j & 2) + 1]);
        SB();
        if constexpr (j + 1 < 36) {
          if constexpr (m == 0 && j + 2 < 36) xr[(j + 2) & 3] = rdx((j + 1) % NS1);
          w[m] = rdw((j + 1) % NS1, m);
        }
        if constexpr (j >= 1 && j + NS1 - 1 < 36 && (m == 1 || m == 2)) issue(j + NS1 - 1, m - 1);
        if constexpr (j == PF2_AT && m == 3) {
          const unsigned l2 = fresh_lane(), li = l2 < 18 * U2 ? l2 : l2 - 32, row = li / U2;
          const unsigned vo = row * 1152u + (li - row * U2) * 128u;
          const int u2 = U == 5 ? (up ? 0 : 7) : 2 * j4;
          gld4(pfd, W2 + (size_t)(sp * 1152 + (rt & 7) * 144 + wave * 18) * 1152 + u2 * 128, vo);
          if (wave == 7) gld4k(pfd, Wo + (sp * 144 + (rt & 7) * 18) * 1152 + u2 * 128, vo);
        }
        if constexpr (j == PF2_AT + NS1 + 1 && m == 0) asm volatile("" ::"v"(pfd));
        SB();
      });
    });
    STAMP2(36);
    STAMP(4, 4);
    pin_acc<U>(acc);
    asm volatile("" : "+v"(iog0), "+v"(iog1));
  }
  SS8(1);
  __builtin_amdgcn_s_setprio(GELU_PRIO);
  BARRIER();
  SS8(2);
  constexpr int B1 = 2 * U, B2 = B1 + 8, B3 = 26;
  constexpr M2S SC = m2_sched(U);
  const int e0 = 2 * (U == 5 ? (up ? 0 : 26) : (up ? 10 : 18));
  const int e1 = 2 * (U == 5 ? (up ? 10 : 18) : (up ? 18 : 10)) - B1;
  const int e2 = 2 * (U == 5 ? (up ? 18 : 10) : (up ? 26 : 0)) - B2;
  const int e3 = 2 * (U == 5 ? (up ? 26 : 0) : (up ? 0 : 26)) - B3;
  auto kstep = [&](int s, int half) {
    const int uc2 = s < B1 ? 2 * U : (s < B2 ? 8 : (s < B3 ? (U == 5 ? 8 : 10) : 10));
    return (unsigned)(s + (s < B1 ? e0 : (s < B2 ? e1 : (s < B3 ? e2 : e3))) + half * uc2);
  };
  const int kh = wn;
  const unsigned nbyte = U == 5 ? (up ? 0u : 896u) : (up ? 256u : 640u), ebyte = up ? 512u : 576u;
  const int ln = fresh_lane();
  const unsigned voffX = ln * 16;
  auto mkw = [&](int l, unsigned hi) {
    return (unsigned)(((l >> 2) & 7) * 1152 + (l >> 5) * hi + (((l & 3) ^ ((l >> 3) & 2)) << 4));
  };
  const unsigned voffW = mkw(ln, 64u);
  unsigned voffE = mkw(ln, 128u * 1152u);
  if constexpr (U2 == 3) asm volatile("" : "+v"(voffE));
  const char* xsrc = H + ((size_t)(rt * 4 + wm) * 288 + sp * 72) * 1024;
  const int k0 = wave & 1, k1 = (wave >> 1) & 1;
  const unsigned cN = (kh * 64 + k1 * 128 + k0 * 8) * 1152 + nbyte, cE = (k1 * 64 + k0 * 8) * 1152 + ebyte;
  const char* w2s = W2 + (size_t)sp * 1152 * 1152;
  const char* wos = Wo + sp * (8 * 18 * 1152);
  constexpr int SL2 = U2 * 4096;
  const unsigned d0 = lbase + (kh * 2 * U2 + wm) * 1024, d2 = lbase + (k1 * 6 + 4 + k0) * 1024;
  auto issueW = [&](auto Sc, int e) {
    constexpr int s = decltype(Sc)::v;
    constexpr unsigned hf = (s & 1) * 128u;
    const unsigned dst = (e ? d2 : d0) + SC.slot[s] * SL2;
    if (e == 1 && !(U2 == 3 && (s & 1) == kh)) return;
    if constexpr (s < M_IT) {
      constexpr int Bc = s < B1 ? 0 : (s < B2 ? B1 : (s < B3 ? B2 : B3)), m = (s - Bc) >> 1;
      const unsigned cb = kstep(Bc, 0) * (16u * 1152u);
      if constexpr (m < 4) {
        if (e == 0) glds16s(w2s + (cb + cN + (m * 16u * 1152u + hf)), voffW, dst);
        else glds16s(w2s + (cb + cE + m * 16u * 1152u), voffE, dst);
      } else {
        if (e == 0) glds16s(w2s + (cb + (unsigned)(256 + kh * 16 + k1 * 32 + k0 * 8) * 1152u + nbyte + hf), voffW, dst);
        else glds16s(w2s + (cb + (unsigned)(256 + k1 * 16 + k0 * 8) * 1152u + ebyte), mkw(fresh_lane(), 32u * 1152u), dst);
      }
    } else if constexpr (s < M2_NT - 1) {
      constexpr int t2 = (s - M_IT) & ~1;
      if (e == 0) glds16s(wos + ((unsigned)((kh * 4 + t2 + k0) * 18 + k1 * 8) * 1152u + nbyte + hf), voffW, dst);
      else glds16s(wos + ((unsigned)((k1 * 4 + t2 + k0) * 18) * 1152u + ebyte), mkw(fresh_lane(), 8u * 1152u), dst);
    } else {
      const int l2 = fresh_lane();
      const unsigned ve = (unsigned)((((l2 >> 3) & 3) * 18 + ((l2 >> 2) & 1)) * 1152 + (((l2 & 3) ^ ((l2 >> 3) & 2)) << 4));
      if (e == 0) glds16s(wos + ((unsigned)(k0 * 72 + 16) * 1152u + nbyte + kh * 128u), ve + (l2 >> 5) * 64u, dst);
      else glds16s(wos + ((unsigned)(k0 * 72 + 16) * 1152u + ebyte), ve ^ (k1 * 32u), dst);
    }
  };
  unsigned b2v[NBW];
  const bool addb = sp == 0 && kh == 0;
#pragma unroll
  for (int b = 0; b < NBW; ++b)
    asm volatile("global_load_ushort %0, %1, %2" : "=v"(b2v[b]) : "v"((ln & 15) * 2 + ((ln >> 4) & 1) * (b < 4 ? 128 : 32)),
                 "s"((const char*)b2 + (b < 4 ? nbyte + b * 32 : ebyte)) : "memory");
  static_for<0, 4>([&](auto Sc) { issueW(Sc, 0); issueW(Sc, 1); });
  SS8(3);
  const unsigned stash = lbase + 4 * SL2 + wave * ((2 * U - 4) * 1024) + voffX;
  {
    const float C0 = -2.0f * 1.4426950408889634f * 0.7978845608028654f;
    const float C1 = C0 * 0.044715f;
    f32x2 c0 = {C0, C0}, c1 = {C1, C1};
    asm volatile("" : "+s"(c0), "+s"(c1));
    f32x2 ig[2] = {{iog0, iog0}, {iog1, iog1}};
    asm volatile("" : "+v"(ig[0]), "+v"(ig[1]));
    char* hb = H + ((size_t)(rt * 4 + wm) * 288 + (unit0 * 2 + wn * U) * 2) * 1024 + fresh_lane() * 16;
#pragma unroll
    for (int b = 0; b < U; ++b) {
#pragma unroll
      for (int rg = 0; rg < 2; ++rg) {
        u32x4 o4;
#pragma unroll
        for (int i = 0; i < 4; ++i) {
          const int a = 8 * (i >> 1) + 4 * rg + 2 * (i & 1);
          const f32x2 v = {acc[b][a], acc[b][a + 1]};
          const f32x2 t = v * __builtin_elementwise_fma(v * v, c1, c0);
          const f32x2 e = {__builtin_amdgcn_exp2f(t[0]), __builtin_amdgcn_exp2f(t[1])};
          const f32x2 d = __builtin_elementwise_fma(e, ig[rg], ig[rg]);
          const f32x2 rc = {__builtin_amdgcn_rcpf(d[0]), __builtin_amdgcn_rcpf(d[1])};
          const f32x2 o = v * rc;
          o4[i] = pk2(o[0], o[1]);
        }
        st16(hb + (b * 2 + rg) * 1024, o4);
        if (b < 2) xr[b * 2 + rg] = __builtin_bit_cast(bf16x8, o4);
        else *(__attribute__((address_space(3))) u32x4*)(uintptr_t)(stash + (b * 2 + rg - 4) * 1024) = o4;
      }
      if (b == 1) SS8(4);
    }
    iog0 = ig[0][0];
    iog1 = ig[1][0];
  }
  STAMP(4, 5);
  SS8(5);
  lds_p wa[2];
  {
    const int g = ln >> 4, q = (ln >> 2) & 3, p = ln & 3;
#pragma unroll
    for (int e = 0; e < 2; ++e)
      wa[e] = L + (kh * 2 * U2 + (g >> 1)) * 1024 + (4 * (g & 1) + q) * 64 + ((e ^ (g & 1)) << 5) + p * 8;
  }
  asm volatile("" : "+v"(wa[0]), "+v"(wa[1]));
  wait_vm<2 * U>();
  SS8(6);
  {
    const float og0 = __builtin_amdgcn_rcpf(iog0), og1 = __builtin_amdgcn_rcpf(iog1);
    const float oh0 = __uint_as_float(__float_as_uint(og0) & 0xffff0000u), oh1 = __uint_as_float(__float_as_uint(og1) & 0xffff0000u);
    const unsigned s0 = addb ? pk2(oh0, og0 - oh0) : 0u, s1 = addb ? pk2(oh1, og1 - oh1) : 0u;
    const int g = ln >> 4;
    const bf16x8 x00 = w0frag(g == 0 ? s0 : 0u), x01 = w0frag(g == 0 ? s1 : 0u);
    const bf16x8 x10 = w0frag(g == 1 ? s0 : 0u), x11 = w0frag(g == 1 ? s1 : 0u);
#pragma unroll
    for (int b = 0; b < NBW; ++b) {
      asm volatile("" : "+v"(b2v[b]));
      const bf16x8 wx = w0frag(b2v[b] * 0x10001u);
      put4<0>(acc[b], MFMA16(wx, x00, zero4));
      put4<1>(acc[b], MFMA16(wx, x01, zero4));
      put4<2>(acc[b], MFMA16(wx, x10, zero4));
      put4<3>(acc[b], MFMA16(wx, x11, zero4));
    }
  }
  BARRIER();
  __builtin_amdgcn_s_setprio(MLP2_PRIO);
  STAMP(4, 6);
  STAMP(4, 7);
  STAMP(2, 1);
  TS(1, -1);
  SS8(7);
  auto rdw = [&](int s, int m) {
    const lds_p a = m < 4 ? wa[m & 1] + (SC.slot[s] * SL2 + (m >> 1) * 512) : wa[s & 1] + (SC.slot[s] * SL2 + 4096);
    return cat8(trl(a), trl(a + (m < 4 ? 2048 : 512)));
  };
#pragma unroll
  for (int m = 0; m < NBW; ++m) w[m] = rdw(0, m);
  unsigned fv = 0;
  auto poll = [&]() {
    unsigned vo = voffX;
    asm volatile("" : "+v"(vo));
    vo = (vo & 48u) << 3;
    gld4c(fv, FLg, vo);
  };
  auto check = [&](unsigned long long mask) {
    mask &= ~(1ull << j4);
    asm volatile("" : "+v"(fv));
    if (__ballot(fv != 1u) & mask) {
#pragma unroll 1
      for (int it = 0; it < (1 << 17); ++it) {
        __builtin_amdgcn_s_sleep(4);
        poll();
        wait_vm<0>();
        asm volatile("" : "+v"(fv));
        if (!(__ballot(fv != 1u) & mask)) break;
      }
    }
  };
  auto waitk = [&](auto N0, auto N1) {
    constexpr int n0 = decltype(N0)::v, n1 = decltype(N1)::v;
    if constexpr (n0 == n1) wait_vm<n0>();
    else { if (kh == 0) wait_vm<n0>(); else wait_vm<n1>(); }
  };
  constexpr int CK1 = m2_ck1(U), CK2 = m2_ck2(U);
  const char* osrc = O + (size_t)((rt * 4 + wm) * 32 + sp * 8 + kh * 4) * 1024;
  const char* oesrc = O + OEX + ((rt * 4 + wm) * 4 + sp) * 1024;
  static_for<0, M_IT>([&](auto Jc) {
    constexpr int j = decltype(Jc)::v;
    if constexpr (m2_need(U, j)) waitk(IC<m2_wait(U, 0, j, 0)>{}, IC<m2_wait(U, 1, j, 0)>{});
    if constexpr (j > 0) BARRIER();
    pin_acc<NBW>(acc);
    static_for<0, 4>([&](auto Ic) {
      constexpr int i = decltype(Ic)::v;
      if constexpr ((j & ~1) + ((i - (j & ~1)) & 3) < M_IT) pin_x1<i>(xr[i]);
    });
    if constexpr (j == 3) {
      if (wave == 0 && voffX == 0) st4(FLg + j4 * 128, 1u);
    }
    STAMP2(40 + j);
    if (j == 9) STAMP(2, 2);
    if (j == 18) STAMP(2, 3);
    if (j == 18) TS(2, -1);
    if (j == 27) STAMP(2, 4);
    if constexpr (j == CK1 - 2 || j == CK2 - 2) poll();
    SB();
    static_for<0, NBW>([&](auto Mc) {
      constexpr int m = decltype(Mc)::v;
      pin_w<m>(w[m]);
      mm16<(j & 1)>(acc[m], w[m], xr[j & 2], xr[(j & 2) + 1]);
      SB();
      w[m] = rdw(j + 1, m);
      static_for<j + 3, (j + 7 < M2_NT ? j + 7 : M2_NT)>([&](auto Sc) {
        constexpr int s = decltype(Sc)::v;
        if constexpr (SC.it[s] == j) {
          constexpr bool second = s > 0 && SC.it[s - 1] == j;
          if constexpr (m == (second ? 2 : 0)) issueW(Sc, 0);
          if constexpr (m == (second ? 3 : 1)) issueW(Sc, 1);
        }
      });
      SB();
    });
    if constexpr (j == CK1 || j == CK2) {
      waitk(IC<m2_wait(U, 0, j, 1)>{}, IC<m2_wait(U, 1, j, 1)>{});
      check(j == CK1 ? 6ull : 15ull);
    }
    if constexpr (j & 1) {
      if constexpr (j + 3 < B1) {
        xstash<(j & 2), (j - 1) * 1024>(xr[j & 2], stash);
        xstash<(j & 3), j * 1024>(xr[j & 3], stash);
      } else if constexpr (j + 3 < M_IT) {
        xload<(j & 2)>(xr[j & 2], xsrc + kstep(j + 3, kh) * 1024u, voffX);
        xload<(j & 3)>(xr[j & 3], xsrc + kstep(j + 4, kh) * 1024u, voffX);
      }
    }
    SB();
  });
  STAMP2(76);
  STAMP(2, 5);
  TS(3, 0);
  const char* flp = FL + FL_P2 + (((rt * 4 + j4) * 4 + wm) * 2 + kh) * 512;
  wait_vm<0>();
  pin_acc<NBW>(acc);
  static_for<0, NBW>([&](auto Mc) { pin_w<decltype(Mc)::v>(w[decltype(Mc)::v]); });
  if (wave == 0) {
    const int l2 = fresh_lane();
    spin_flags(FL + FL_O + rt * 512 + sp * 32, (unsigned)(((l2 >> 3) & 3) * 128 + (l2 & 7) * 4));
  }
  const unsigned voffO = (voffX & 0xf0u) + ((voffX & 0x300u) << 1);
  const unsigned voffOe = (voffX & 0x2f0u) + ((voffX & 0x100u) >> 5);
  BARRIER();
  TS(4, 1);
  {
    static_for<0, 4>([&](auto Ic) {
      constexpr int i = decltype(Ic)::v;
      xload<i>(xr[i], osrc + ((i >> 1) * 2048 + (i & 1) * 256), voffO);
    });
    u32x2 xe0, xe1;
    asm volatile("global_load_dwordx2 %0, %1, %2 sc1" : "=v"(xe0) : "v"(voffOe), "s"(oesrc) : "memory");
    asm volatile("global_load_dwordx2 %0, %1, %2 offset:256 sc1" : "=v"(xe1) : "v"(voffOe), "s"(oesrc) : "memory");
    static_for<0, 4>([&](auto Tc) {
      constexpr int t = decltype(Tc)::v;
      if constexpr ((t & 1) == 0) wait_vm<4 - t>();
      pin_x1<(t & 2)>(xr[t & 2]);
      pin_x1<(t & 2) + 1>(xr[(t & 2) + 1]);
      pin_acc<NBW>(acc);
      static_for<0, NBW>([&](auto Mc) {
        constexpr int m = decltype(Mc)::v;
        pin_w<m>(w[m]);
        mm16<(t & 1)>(acc[m], w[m], xr[t & 2], xr[(t & 2) + 1]);
        SB();
        w[m] = rdw(M_IT + t + 1, m);
        SB();
      });
    });
    asm volatile("s_waitcnt vmcnt(0)" : "+v"(xe0), "+v"(xe1)::"memory");
    mm16e<NBW>(acc, w, xe0, xe1, kh);
  }
  BARRIER();
  STAMP(2, 6);
  TS(5, 2);
  constexpr int NH = NBW;
  const int l2 = fresh_lane();
  pair_sum<NBW>(acc, lbase + (wm * 2 + kh) * (NH * 1024) + l2 * 16, lbase + (wm * 2 + (kh ^ 1)) * (NH * 1024) + l2 * 16, kh);
  float r[NH][8];
#pragma unroll
  for (int q = 0; q < NH; ++q)
#pragma unroll
    for (int i = 0; i < 8; ++i) r[q][i] = acc[q][i];
  TS(-1, 3);
  constexpr unsigned PSP = 64 * 18 * 2048;
  const int nb0 = U == 5 ? (up ? 0 : 14) : (up ? 4 : 9);
  const char* pt = P + ((size_t)(rt * 4 + wm) * 18 + nb0) * 2048;
  const unsigned h0 = kh * 1024u, h4 = 8192u + h0;
  const unsigned vl = l2 * 16;
  float own[8];
#pragma unroll
  for (int q = 0; q < NH; ++q) {
    if (q < 4 && q == sp) {
#pragma unroll
      for (int i = 0; i < 8; ++i) own[i] = r[q][i];
    } else if (q < 4 || wm != sp) {
      u32x4 v;
#pragma unroll
      for (int i = 0; i < 4; ++i) v[i] = pk2(r[q][2 * i], r[q][2 * i + 1]);
      const char* dst = pt + (sp * PSP + (h0 + q * 2048u));
      asm volatile("global_store_dwordx4 %0, %1, %2 sc1\n\ts_nop 1" ::"v"(vl), "v"(v), "s"(dst) : "memory");
    }
  }
  {
    const unsigned oth = (unsigned)((sp + 1 + (l2 % 3)) & 3), vf = sp * 128u + oth * 4u;
    unsigned f0;
    gld4c(f0, flp, vf);
    wait_vm<1>();
    TS(-1, 4);
    if (l2 < 3) asm volatile("global_store_dword %0, %1, %2 sc1" ::"v"(oth * 128u + sp * 4u), "v"(1u), "s"(flp) : "memory");
    asm volatile("s_waitcnt vmcnt(1)" : "+v"(f0)::"memory");
    if (__ballot(f0 != 1u)) {
#pragma unroll 1
      for (int it = 0; it < (1 << 17); ++it) {
        gld4ck(f0, flp, vf);
        asm volatile("s_waitcnt vmcnt(0)" : "+v"(f0)::"memory");
        if (!__ballot(f0 != 1u)) break;
        __builtin_amdgcn_s_sleep(PSLP);
      }
    }
    TS(6, 5);
    const char* orow = out + (size_t)(rt * 4 + wm) * (32 * 1152);
    const unsigned vo = (l2 & 31) * 1152 + (l2 >> 5) * 16;
    auto finish = [&](float* a, bf16x8* p, unsigned coff) {
#pragma unroll
      for (int o = 0; o < 3; ++o) {
        const u32x4 v = __builtin_bit_cast(u32x4, p[o]);
#pragma unroll
        for (int k = 0; k < 4; ++k) {
          a[2 * k] += __uint_as_float(v[k] << 16);
          a[2 * k + 1] += __uint_as_float(v[k] & 0xffff0000u);
        }
      }
      auto r0 = __builtin_amdgcn_permlane16_swap(pk2(a[0], a[1]), pk2(a[4], a[5]), false, false);
      auto r1 = __builtin_amdgcn_permlane16_swap(pk2(a[2], a[3]), pk2(a[6], a[7]), false, false);
      const u32x4 wv = {r0[0], r1[0], r0[1], r1[1]};
      asm volatile("global_store_dwordx4 %0, %1, %2\n\ts_nop 1" ::"v"(vo), "v"(wv), "s"(orow + coff) : "memory");
    };
    bf16x8 pv[3], pw[3];
    const bool two = NH == 5 && wm == sp;
#pragma unroll
    for (int o = 0; o < 3; ++o) gld16c(pv[o], pt + (((sp + 1 + o) & 3) * PSP + h0 + sp * 2048u), vl);
    if constexpr (NH == 5) {
      if (two) {
#pragma unroll
        for (int o = 0; o < 3; ++o) gld16c(pw[o], pt + (((sp + 1 + o) & 3) * PSP + h4), vl);
      }
      asm volatile("s_waitcnt vmcnt(0)" : "+v"(pw[0]), "+v"(pw[1]), "+v"(pw[2]), "+v"(pv[0]), "+v"(pv[1]), "+v"(pv[2]) : "v"(f0) : "memory");
    } else {
      asm volatile("s_waitcnt vmcnt(0)" : "+v"(pv[0]), "+v"(pv[1]), "+v"(pv[2]) : "v"(f0) : "memory");
    }
    TS(-1, 6);
    finish(own, pv, nbyte + kh * 128u + sp * 32u);
    if (two) finish(r[NH - 1], pw, ebyte + kh * 32u);
  }
  STAMP(2, 7);
  SST(7);
  SSW();
}

extern "C" __global__ __launch_bounds__(512, 4) void kernel1(const char* __restrict__ T,
                                                   char* O, const char* __restrict__ XN,
                                                   const char* __restrict__ W1, const unsigned short* __restrict__ b1,
                                                   char* H, const char* __restrict__ W2,
                                                   const unsigned short* __restrict__ b2, const char* __restrict__ Wo,
                                                   const char* __restrict__ G, char* P, char* FL,
                                                   char* __restrict__ out) {
  __shared__ __attribute__((aligned(16))) char lds[81920];
  const int wave = __builtin_amdgcn_readfirstlane(threadIdx.x >> 6);
  const int lane = threadIdx.x & 63;
  const int bid = blockIdx.x;
  if (bid < 256) {
    STAMP(1, 0);
    STAMP(5, 0);
    __builtin_amdgcn_s_setprio(ATT_PRIO);
    attn_role(lds, T, O, P, G, FL, W2, Wo, bid, wave, fresh_lane());
  } else {
    STAMP(2, 0);
    STAMP(4, 0);
    const int wg = bid - 256;
    const int xcd = wg & 7, idx = wg >> 3;
    const int sp = xcd >> 1, j4 = idx & 3, r8 = idx >> 2, rt = (xcd & 1) * 8 + r8;
    const int koff = ((r8 * 9 / 2 + j4 * 9) % 36) >> 1;
    if (j4 == 0 || j4 == 3)
      mlp_track<5>(lds, XN, W1, b1, H, W2, b2, Wo, O, G, P, FL, out, rt, sp, j4, koff, wave, fresh_lane());
    else
      mlp_track<4>(lds, XN, W1, b1, H, W2, b2, Wo, O, G, P, FL, out, rt, sp, j4, koff, wave, fresh_lane());
  }
}

constexpr int Q_SLOT = 32768;
#define NSQ 4
constexpr int QGRID = 256;
DI void qkv_role(char* lds, const char* __restrict__ X, const char* __restrict__ Wqkv, char* __restrict__ T, int rt,
                 int ct, int joff, int wave, int lane) {
  f32x16 acc[2];
#pragma unroll
  for (int i = 0; i < 16; ++i) { acc[0][i] = 0.f; acc[1][i] = 0.f; }
  const unsigned lbase = lds_addr(lds);
  const int wm = wave & 3, wn = wave >> 2;
  const bool half = ct == 13;
  unsigned voffX[2];
#pragma unroll
  for (int i = 0; i < 2; ++i) {
    int row = wave * 16 + 8 * i + (lane >> 3);
    int c = (lane & 7) ^ ((row >> 1) & 7);
    voffX[i] = (unsigned)((rt * 128 + row) * 1152 + c * 16);
  }
  const unsigned voffW = (unsigned)((lane >> 3) * 3456 + (((lane & 7) ^ (((lane >> 4) & 1) << 2)) << 4));
  const char* wsrc = Wqkv + (size_t)wave * 8 * 3456 + ct * 256;
  const unsigned wh = half ? 0u : 128u;
  auto issue = [&](int J, int e) {
    unsigned dst = lbase + (J % NSQ) * Q_SLOT;
    unsigned Jr = J + joff;
    Jr = Jr >= 9 ? Jr - 9 : Jr;
    if (e < 2) glds16s(wsrc + (Jr * (64u * 3456u) + e * wh), voffW, dst + 16384 + e * 8192 + wave * 1024);
    else glds16s(X + Jr * 128u, voffX[e - 2], dst + (wave * 2 + e - 2) * 1024);
  };
  STAMP(0, 0);
#pragma unroll
  for (int J = 0; J < NSQ - 1; ++J)
#pragma unroll
    for (int e = 0; e < 4; ++e) issue(J, e);
  const int r = lane & 31, h = lane >> 5;
  const int q = (lane >> 2) & 3, p = lane & 3, g = (lane >> 4) & 1;
  const lds_p L = (lds_p)lds;
  constexpr int NB = (NSQ + 1) / 2;
  lds_p xa[NB][4], wa[NB][2];
#pragma unroll
  for (int z = 0; z < NB; ++z) {
#pragma unroll
    for (int ks = 0; ks < 4; ++ks) {
      xa[z][ks] = L + z * 2 * Q_SLOT + (wm * 32 + r) * 128 + (((2 * ks + h) ^ ((r >> 1) & 7)) << 4);
      asm volatile("" : "+v"(xa[z][ks]));
    }
#pragma unroll
    for (int b = 0; b < 2; ++b) {
      wa[z][b] = L + z * 2 * Q_SLOT + 16384 + wn * 8192 + h * 1024 + q * 128 + ((b ^ (q >> 1)) << 6) + g * 32 + p * 8;
      asm volatile("" : "+v"(wa[z][b]));
    }
  }
  wait_vm<4 * (NSQ - 2)>();
  BARRIER();
  STAMP(0, 1);
  bf16x8 fx[2], fw[2][2];
  auto rd = [&](int buf, int J, int ks) {
    const int sl = J % NSQ;
    fx[buf] = ld8(xa[sl >> 1][ks] + (sl & 1) * Q_SLOT);
#pragma unroll
    for (int b = 0; b < 2; ++b) {
      const lds_p a = wa[sl >> 1][b] + (sl & 1) * Q_SLOT + ks * 2048;
      fw[buf][b] = cat8(trl(a), trl(a + 512));
    }
  };
  rd(0, 0, 0);
  static_for<0, 9>([&](auto Jc) {
    constexpr int J = decltype(Jc)::v;
    if constexpr (J + 1 < 9) {
      constexpr int top = J + NSQ - 2 < 8 ? J + NSQ - 2 : 8;
      wait_vm<4 * (top - (J + 1))>();
      BARRIER();
    }
    if (J == 3) STAMP(0, 2);
    if (J == 6) STAMP(0, 3);
    SB();
    static_for<0, 4>([&](auto Kc) {
      constexpr int ks = decltype(Kc)::v, cur = ks & 1;
      acc[0] = MFMA32(fx[cur], fw[cur][0], acc[0]);
      SB();
      if constexpr (ks < 3) rd(cur ^ 1, J, ks + 1);
      else if constexpr (J + 1 < 9) rd(cur ^ 1, J + 1, 0);
      SB();
      acc[1] = MFMA32(fx[cur], fw[cur][1], acc[1]);
      SB();
      if constexpr (J + NSQ - 1 < 9) {
        if constexpr (ks == 0) { issue(J + NSQ - 1, 0); issue(J + NSQ - 1, 1); }
        else if constexpr (ks < 3) issue(J + NSQ - 1, ks + 1);
      }
      SB();
    });
  });
  STAMP(0, 5);
  const float QSCALE = 0.23570226039551584f * 1.4426950408889634f;
  const float sc = ct * 2 + wn < 9 ? QSCALE : 1.0f;
  if (!(half && wn)) {
#pragma unroll
    for (int nb = 0; nb < 2; ++nb) {
      char* base = T + (size_t)h * TP + ((ct * 2 + wn) * 64 + nb * 32 + r) * 16;
#pragma unroll
      for (int s = 0; s < 2; ++s) {
        int step = rt * 8 + wm * 2 + s;
        u32x4 v;
#pragma unroll
        for (int i = 0; i < 4; ++i) v[i] = pk2(acc[nb][8 * s + 2 * i] * sc, acc[nb][8 * s + 2 * i + 1] * sc);
        st16(base + (size_t)step * (2 * TP), v);
      }
    }
  } else if (wm < 2) {
    int idx = wm * 64 + lane;
    unsigned c = (idx & 7) == 0 ? 0x3F803F80u : 0u;
    u32x4 v = {c, c, c, c};
    st16(T + (size_t)(rt * 16 + (idx >> 3)) * TP + (1728 + (idx & 7)) * 16, v);
  }
}

DI float dot2(unsigned a, unsigned b, float c) {
  return __builtin_amdgcn_fdot2_f32_bf16(__builtin_bit_cast(bf16x2_t, a), __builtin_bit_cast(bf16x2_t, b), c, false);
}
template <int C>
DI float dpp_add(float v) {
  return v + __builtin_bit_cast(float, __builtin_amdgcn_update_dpp(0, __builtin_bit_cast(int, v), C, 0xf, 0xf, true));
}
DI float sum8(float v) { return dpp_add<0x141>(dpp_add<0x4E>(dpp_add<0xB1>(v))); }
DI void ln_role(const char* __restrict__ X, const char* __restrict__ gw, char* __restrict__ XN, float* __restrict__ G,
                char* __restrict__ FL, int id, int wave, int lane) {
  const int c = lane & 7;
  const int row = id * 64 + wave * 8 + (lane >> 3);
  bf16x8 xv[9], gv[9];
  {
    const unsigned vx = (unsigned)(row * 1152 + c * 16), vg = (unsigned)(c * 16);
#pragma unroll
    for (int i = 0; i < 9; ++i) asm volatile("global_load_dwordx4 %0, %1, %2 offset:%3" : "=v"(xv[i]) : "v"(vx), "s"(X), "i"(i * 128) : "memory");
#pragma unroll
    for (int i = 0; i < 9; ++i) asm volatile("global_load_dwordx4 %0, %1, %2 offset:%3" : "=v"(gv[i]) : "v"(vg), "s"(gw), "i"(i * 128) : "memory");
  }
  st4(FL + id * 2048 + wave * 256 + lane * 4, 0u);
  asm volatile("global_store_dwordx2 %0, %1, off sc1" ::"v"(FL + FL_P2 + (id * 8 + wave) * 1024 + (lane >> 3) * 128 + c * 8), "v"(0ull) : "memory");
  char* dst = XN + ((size_t)(row >> 5) * 36 + (c >> 1)) * 1024 + ((c & 1) * 32 + (row & 31)) * 16;
  asm volatile("s_waitcnt vmcnt(11)" : "+v"(xv[0]), "+v"(xv[1]), "+v"(xv[2]), "+v"(xv[3]), "+v"(xv[4]), "+v"(xv[5]), "+v"(xv[6]), "+v"(xv[7]), "+v"(xv[8])::"memory");
  float sa[4] = {0.f, 0.f, 0.f, 0.f};
#pragma unroll
  for (int i = 0; i < 9; ++i)
#pragma unroll
    for (int j = 0; j < 4; ++j) sa[j] = dot2(__builtin_bit_cast(u32x4, xv[i])[j], 0x3F803F80u, sa[j]);
  STAMP(0, 1);
  const float mean = sum8((sa[0] + sa[1]) + (sa[2] + sa[3])) * (1.0f / 576.0f);
  const f32x2 m2 = {mean, mean};
  f32x2 xc[9][4];
  f32x2 qa[4] = {{0.f, 0.f}, {0.f, 0.f}, {0.f, 0.f}, {0.f, 0.f}};
#pragma unroll
  for (int i = 0; i < 9; ++i)
#pragma unroll
    for (int j = 0; j < 4; ++j) {
      const unsigned u = __builtin_bit_cast(u32x4, xv[i])[j];
      xc[i][j] = f32x2{__uint_as_float(u << 16), __uint_as_float(u & 0xffff0000u)} - m2;
      qa[j] = __builtin_elementwise_fma(xc[i][j], xc[i][j], qa[j]);
    }
  const f32x2 qq = (qa[0] + qa[1]) + (qa[2] + qa[3]);
  const float var = sum8(qq[0] + qq[1]) * (1.0f / 576.0f);
  STAMP(0, 3);
  const float rstd = 1.0f / __builtin_sqrtf(var + 1e-5f);
  const f32x2 r2 = {rstd, rstd};
#pragma unroll
  for (int i = 0; i < 9; ++i) {
    u32x4 v;
#pragma unroll
    for (int j = 0; j < 4; ++j) {
      const f32x2 y = xc[i][j] * r2;
      v[j] = pk2(y[0], y[1]);
    }
    st16(dst + i * 4096, v);
  }
  asm volatile("s_waitcnt vmcnt(11)" : "+v"(gv[0]), "+v"(gv[1]), "+v"(gv[2]), "+v"(gv[3]), "+v"(gv[4]), "+v"(gv[5]), "+v"(gv[6]), "+v"(gv[7]), "+v"(gv[8])::"memory");
  float da[4] = {0.f, 0.f, 0.f, 0.f};
#pragma unroll
  for (int i = 0; i < 9; ++i)
#pragma unroll
    for (int j = 0; j < 4; ++j) da[j] = dot2(__builtin_bit_cast(u32x4, xv[i])[j], __builtin_bit_cast(u32x4, gv[i])[j], da[j]);
  const float v = sum8((da[0] + da[1]) + (da[2] + da[3])) * 1.4426950408889634f;
  const float e = 1.0f + __builtin_amdgcn_exp2f((c & 1) ? v : -v);
  const float e1 = __builtin_fminf(e, 1.0e30f), e0 = 1.0f / e;
  st4(G + (c & 1) * 2048 + row, __float_as_uint((c & 1) ? e1 : e0));
  STAMP(0, 5);
  unsigned d0;
  {
    const char* pc;
    unsigned span;
    asm volatile("s_getpc_b64 s[20:21]\n\ts_mov_b32 %1, kernel1@rel32@lo+4\n\ts_add_u32 s20, s20, %1\n\ts_addc_u32 s21, s21, kernel1@rel32@hi+16"
                 : "={s[20:21]}"(pc), "=s"(span)::"scc");
    span = 0u - span;
    span = span < (1u << 18) ? span : 0u;
    const unsigned o = (unsigned)(((id >> 3) * 8 + wave) * 64 + lane) * 128u;
    gld4(d0, pc, o < span ? o : 0u);
  }
  asm volatile("" ::"v"(d0) : "memory");
}

__global__ __launch_bounds__(512, 2) void kernelQ(const char* __restrict__ X, const char* __restrict__ Wqkv,
                                                   const char* __restrict__ gw, char* __restrict__ T,
                                                   char* __restrict__ XN, float* __restrict__ G,
                                                   char* __restrict__ FL) {
  __shared__ __attribute__((aligned(16))) char lds[NSQ * Q_SLOT];
  const int wg = blockIdx.x;
  const int wave = __builtin_amdgcn_readfirstlane(threadIdx.x >> 6);
  const int lane = threadIdx.x & 63;
  if (wg < 224) {
    const int xcd = wg & 7, t = wg >> 3, rl = t & 3, cl = t >> 2;
    const int joff = (3 * (cl + rl) + (rl == 3 ? 1 : 0)) % 9;
    qkv_role(lds, X, Wqkv, T, (xcd & 3) * 4 + rl, (xcd >> 2) * 7 + cl, joff, wave, lane);
  } else {
    STAMP(0, 0);
    ln_role(X, gw, XN, G, FL, wg - 224, wave, lane);
  }
  STAMP(0, 6);
  STAMP(0, 7);
}

extern "C" void kernel_launch(void* const* d_in, const int* in_sizes, int n_in, void* d_out, int out_size, void* d_ws,
                              size_t ws_size, hipStream_t stream) {
  const char* X = (const char*)d_in[0];
  const char* Wqkv = (const char*)d_in[1];
  const char* Wo = (const char*)d_in[2];
  const char* W1 = (const char*)d_in[4];
  const unsigned short* b1 = (const unsigned short*)d_in[5];
  const char* W2 = (const char*)d_in[6];
  const unsigned short* b2 = (const unsigned short*)d_in[7];
  const char* gw = (const char*)d_in[8];
  char* ws = (char*)d_ws;
  char* T = ws + WS_T;
  char* H = ws + WS_H;
  char* O = ws + WS_O;
  char* P = ws + WS_P;
  char* XN = ws + WS_XN;
  char* FL = ws + WS_FL;
  float* G = (float*)(ws + WS_G);
  kernelQ<<<QGRID, 512, 0, stream>>>(X, Wqkv, gw, T, XN, G, FL);
  kernel1<<<512, 512, 0, stream>>>(T, O, XN, W1, b1, H, W2, b2, Wo, (const char*)G, P, FL, (char*)d_out);
}
```

```cpp
#include <hip/hip_runtime.h>
#include <stdint.h>

typedef __attribute__((ext_vector_type(8))) short bf16x8;
typedef __attribute__((ext_vector_type(4))) short s16x4;
typedef __attribute__((ext_vector_type(2))) __bf16 bf16x2_t;
typedef __attribute__((ext_vector_type(2))) float f32x2;
typedef __attribute__((ext_vector_type(4))) float f32x4;
typedef __attribute__((ext_vector_type(16))) float f32x16;
typedef __attribute__((ext_vector_type(4))) unsigned u32x4;
typedef __attribute__((ext_vector_type(2))) unsigned u32x2;
typedef __attribute__((address_space(3))) s16x4* lds_s16x4_p;
typedef __attribute__((address_space(3))) char* lds_p;
typedef __attribute__((address_space(3))) const bf16x8* lds_bf16x8_p;

#define STAMP(k, i)
#define STAMP2(i)
#define SST(i)
#define SS8(i)
#define SSD()
#define SSW()
#define DI __device__ __forceinline__
#define MFMA32(a, b, c) __builtin_amdgcn_mfma_f32_32x32x16_bf16((a), (b), (c), 0, 0, 0)
#define MFMA32K8(a, b, c) __builtin_amdgcn_mfma_f32_32x32x8bf16_1k((a), (b), (c), 0, 0, 0)
#define WAIT_VM(n) asm volatile("s_waitcnt vmcnt(" #n ")" ::: "memory")
#define BARRIER() __builtin_amdgcn_s_barrier()
#define SB() __builtin_amdgcn_sched_barrier(0)

constexpr int S_ = 2048, D_ = 576, F_ = 4608, NQKV = 1728;
constexpr int TSTEP = 640;
constexpr int TP = 1736 * 16;
constexpr int OEX = 64 * 32 * 1024;
constexpr size_t WS_T = 0;
constexpr size_t WS_H = 8u << 20;
constexpr size_t WS_O = 28u << 20;
constexpr size_t WS_G = 31u << 20;
constexpr size_t WS_FL = (31u << 20) + (64u << 10);
constexpr size_t WS_P = 32u << 20;
constexpr size_t WS_XN = 44u << 20;
constexpr size_t WS_XF = 47u << 20;
constexpr int FL_Q = 32768, FL_O = 36864, FL_P = 45056, FL_P2 = 65536;

DI unsigned pk2(float a, float b) {
  f32x2 v = {a, b};
  bf16x2_t r = __builtin_convertvector(v, bf16x2_t);
  return __builtin_bit_cast(unsigned, r);
}
DI float bf2f(unsigned short u) { return __uint_as_float(((unsigned)u) << 16); }
DI s16x4 tr_read(const char* p) {
  return __builtin_amdgcn_ds_read_tr16_b64_v4i16((lds_s16x4_p)(p));
}
DI s16x4 trl(lds_p p) { return __builtin_amdgcn_ds_read_tr16_b64_v4i16((lds_s16x4_p)p); }
DI bf16x8 ld8(lds_p p) { return *(lds_bf16x8_p)p; }
DI float xor32(float v) { return __shfl_xor(v, 32, 64); }

DI void glds16s(const char* sbase, unsigned voff, unsigned lds_dst) {
  unsigned keep;
  asm volatile("s_mov_b32 %0, m0\n\ts_mov_b32 m0, %3\n\ts_nop 0\n\tglobal_load_lds_dwordx4 %1, %2\n\ts_mov_b32 m0, %0"
               : "=&s"(keep)
               : "v"(voff), "s"(sbase), "s"(lds_dst)
               : "memory");
}
DI void glds16c(const char* sbase, unsigned voff, unsigned lds_dst) {
  unsigned keep;
  asm volatile("s_mov_b32 %0, m0\n\ts_mov_b32 m0, %3\n\ts_nop 0\n\tglobal_load_lds_dwordx4 %1, %2 sc1\n\ts_mov_b32 m0, %0"
               : "=&s"(keep)
               : "v"(voff), "s"(sbase), "s"(lds_dst)
               : "memory");
}
DI void touch(const char* sbase, unsigned voff, unsigned lds_dst) {
  unsigned keep;
  asm volatile("s_mov_b32 %0, m0\n\ts_mov_b32 m0, %3\n\ts_nop 0\n\tglobal_load_lds_dword %1, %2\n\ts_mov_b32 m0, %0"
               : "=&s"(keep)
               : "v"(voff), "s"(sbase), "s"(lds_dst)
               : "memory");
}
DI void gld16(bf16x8& d, const char* sbase, unsigned voff) {
  asm volatile("global_load_dwordx4 %0, %1, %2" : "=v"(d) : "v"(voff), "s"(sbase) : "memory");
}
DI void gld16c(bf16x8& d, const char* sbase, unsigned voff) {
  asm volatile("global_load_dwordx4 %0, %1, %2 sc1" : "=v"(d) : "v"(voff), "s"(sbase) : "memory");
}
DI void gld4c(unsigned& d, const char* sbase, unsigned voff) {
  asm volatile("global_load_dword %0, %1, %2 sc1" : "=v"(d) : "v"(voff), "s"(sbase) : "memory");
}
DI void gld4(unsigned& d, const char* sbase, unsigned voff) {
  asm volatile("global_load_dword %0, %1, %2" : "=v"(d) : "v"(voff), "s"(sbase) : "memory");
}
DI void gld4k(unsigned& d, const char* sbase, unsigned voff) {
  asm volatile("global_load_dword %0, %1, %2" : "+v"(d) : "v"(voff), "s"(sbase) : "memory");
}
DI unsigned lds_addr(const void* p) { return (unsigned)(uintptr_t)(lds_p)p; }

DI void st16(void* p, u32x4 v) { asm volatile("global_store_dwordx4 %0, %1, off sc1\n\ts_nop 1" ::"v"(p), "v"(v) : "memory"); }
DI void st16p(void* p, u32x4 v) { asm volatile("global_store_dwordx4 %0, %1, off\n\ts_nop 1" ::"v"(p), "v"(v) : "memory"); }
DI void st4(void* p, unsigned v) { asm volatile("global_store_dword %0, %1, off sc1" ::"v"(p), "v"(v) : "memory"); }

DI int fresh_lane() {
  int l;
  asm volatile("v_mbcnt_lo_u32_b32 %0, -1, 0\n\tv_mbcnt_hi_u32_b32 %0, -1, %0" : "=v"(l));
  return l;
}
template <int I>
struct IC { static constexpr int v = I; };
template <int I, int N, class F>
DI void static_for(F&& f) {
  if constexpr (I < N) {
    f(IC<I>{});
    static_for<I + 1, N>(f);
  }
}
template <int N>
DI void wait_vm() { asm volatile("s_waitcnt vmcnt(%0)" ::"i"(N) : "memory"); }
template <int N>
DI void wait_vm1(bf16x8& a) { asm volatile("s_waitcnt vmcnt(%1)" : "+v"(a) : "i"(N) : "memory"); }
DI void spin_flags(const char* base, unsigned voff) {
#pragma unroll 1
  for (int it = 0; it < (1 << 17); ++it) {
    unsigned f;
    gld4c(f, base, voff);
    asm volatile("s_waitcnt vmcnt(0)" : "+v"(f)::"memory");
    if (!__ballot(f != 1u)) break;
    __builtin_amdgcn_s_sleep(4);
  }
}
DI bf16x8 cat8(s16x4 lo, s16x4 hi) { return __builtin_shufflevector(lo, hi, 0, 1, 2, 3, 4, 5, 6, 7); }

#define SENT 0
DI unsigned nsent(unsigned v) { return SENT ? __builtin_elementwise_min(v, 0xfffffffeu) : v; }
DI unsigned umax4(bf16x8 x, unsigned m) {
  const u32x4 v = __builtin_bit_cast(u32x4, x);
  m = __builtin_elementwise_max(__builtin_elementwise_max(v[0], v[1]), m);
  return __builtin_elementwise_max(__builtin_elementwise_max(v[2], v[3]), m);
}
DI f32x16 qk_tile(lds_p kb, lds_p ke, bf16x8 qf, s16x4 qe, f32x16 c) {
  s16x4 lo = trl(kb);
  s16x4 hi = trl(kb + 64);
  s16x4 ex = trl(ke);
  bf16x8 kf = __builtin_shufflevector(lo, hi, 0, 1, 2, 3, 4, 5, 6, 7);
  f32x16 s = MFMA32(kf, qf, c);
  s = MFMA32K8(ex, qe, s);
  return s;
}
DI f32x16 pv_tile(lds_p vb, const f32x16& pe, f32x16 o) {
  bf16x8 v0 = ld8(vb);
  bf16x8 v1 = ld8(vb + TSTEP);
  u32x4 p0, p1;
#pragma unroll
  for (int i = 0; i < 4; ++i) {
    p0[i] = pk2(pe[2 * i], pe[2 * i + 1]);
    p1[i] = pk2(pe[8 + 2 * i], pe[8 + 2 * i + 1]);
  }
  o = MFMA32(v0, __builtin_bit_cast(bf16x8, p0), o);
  o = MFMA32(v1, __builtin_bit_cast(bf16x8, p1), o);
  return o;
}
struct KFrag { bf16x8 f; s16x4 e; };
struct VFrag { bf16x8 v0, v1; };
DI KFrag load_k(lds_p kb, lds_p ke) {
  KFrag k;
  s16x4 lo = trl(kb);
  s16x4 hi = trl(kb + 64);
  k.e = trl(ke);
  k.f = __builtin_shufflevector(lo, hi, 0, 1, 2, 3, 4, 5, 6, 7);
  return k;
}
DI VFrag load_v(lds_p vb) {
  VFrag v;
  v.v0 = ld8(vb);
  v.v1 = ld8(vb + TSTEP);
  return v;
}
#define EXP2(x) __builtin_amdgcn_exp2f(x)
#define PTRUNC 1
DI unsigned pkp(float a, float b) {
  return __builtin_amdgcn_perm(__float_as_uint(b), __float_as_uint(a), 0x07060302u);
}
#define APM 2
#define APMASK 0x33
#define PHI(i) if constexpr ((PM >> (i)) & 1) __builtin_amdgcn_s_setprio(APM)
#define PLO(i) if constexpr ((PM >> (i)) & 1) __builtin_amdgcn_s_setprio(0)
template <int PM>
DI void tile_step(const f32x16& s, f32x16& sn, const KFrag& k, const VFrag& v, bf16x8 qf, s16x4 qe,
                  f32x16& o) {
  f32x16 negm;
#pragma unroll
  for (int i = 0; i < 16; ++i) negm[i] = 0.f;
  u32x4 p0, p1;
  p0[0] = pkp(EXP2(s[0]), EXP2(s[1]));
  p0[1] = pkp(EXP2(s[2]), EXP2(s[3]));
  SB();
  PHI(0);
  SB();
  f32x16 t = MFMA32(k.f, qf, negm);
  SB();
  PLO(0);
  SB();
  p0[2] = pkp(EXP2(s[4]), EXP2(s[5]));
  p0[3] = pkp(EXP2(s[6]), EXP2(s[7]));
  SB();
  PHI(1);
  SB();
  o = MFMA32(v.v0, __builtin_bit_cast(bf16x8, p0), o);
  SB();
  PLO(1);
  SB();
  p1[0] = pkp(EXP2(s[8]), EXP2(s[9]));
  p1[1] = pkp(EXP2(s[10]), EXP2(s[11]));
  SB();
  PHI(2);
  SB();
  t = MFMA32K8(k.e, qe, t);
  SB();
  PLO(2);
  SB();
  p1[2] = pkp(EXP2(s[12]), EXP2(s[13]));
  p1[3] = pkp(EXP2(s[14]), EXP2(s[15]));
  SB();
  PHI(3);
  SB();
  o = MFMA32(v.v1, __builtin_bit_cast(bf16x8, p1), o);
  SB();
  PLO(3);
  SB();
  sn = t;
}
DI float rowmax16(const f32x16& s) {
  float m = __builtin_fmaxf(s[0], s[1]);
#pragma unroll
  for (int i = 2; i < 16; ++i) m = __builtin_fmaxf(m, s[i]);
  return __builtin_fmaxf(m, xor32(m));
}

constexpr int AT_SLOT = 20480;
constexpr int at_cnt(int c, int grp) { return 2 + (((c & 1) == grp) ? 1 : 0); }
constexpr int at_off(int tt) { return ((tt >> 3) & 1) * AT_SLOT + (tt & 7) * 1280; }

#define TOUCH_AT 9
#define ADEF 0
#define OPRIV 0
#define AEP 3
#define PRE_AT 0
DI void attn_role(char* lds, const char* T, char* O, char* P, const char* __restrict__ G, char* FL, const char* __restrict__ W2,
                  const char* __restrict__ Wo, int wg, int wave, int lane) {
  const int head = (wg & 7) * 4 + ((wg >> 3) & 3);
  const int qt = wg >> 5;
  const int r = lane & 31, h = lane >> 5;
  const int grp = wave >> 2;
  const unsigned lbase = lds_addr(lds);
  const int p1 = wave + 8, p2 = 16 + (wave & 3);
  auto mkoff = [&](int pc, int first) {
    int isv = pc >= 10;
    int sl = (pc - 10 * isv) * 64 + lane;
    int seg = sl / 20, row = sl - seg * 20;
    int slot = row < 18 ? first + 576 * isv + head * 18 + row : 1728 + row - 18;
    return (unsigned)(seg * TP + slot * 16);
  };
  const unsigned v0 = mkoff(wave, 576), v1 = mkoff(p1, 576), v2 = mkoff(p2, 576);
  auto issue = [&](int c) {
    unsigned dst = lbase + (c & 3) * AT_SLOT;
    const char* src = T + (size_t)c * (32 * TP);
    glds16s(src, v0, dst + wave * 1024);
    glds16s(src, v1, dst + p1 * 1024);
    if ((c & 1) == grp) glds16s(src, v2, dst + p2 * 1024);
  };
  STAMP(1, 1);
  unsigned gbits;
  gld4(gbits, G + (qt * 256 + wave * 32) * 4, r * 4);
  {
    const char* src = T + (size_t)qt * (32 * TP);
    glds16s(src, mkoff(wave, 0), lbase + 3 * AT_SLOT + wave * 1024);
    if (wave < 2) glds16s(src, mkoff(p1, 0), lbase + 3 * AT_SLOT + p1 * 1024);
  }
  issue(0);
  issue(1);
  issue(2);
  const int q = (lane >> 2) & 3, p = lane & 3, g = (lane >> 4) & 1;
  const lds_p L = (lds_p)lds;
  const lds_p kb = L + g * TSTEP + (p & 1) * (TSTEP / 2) + (8 * h + q) * 16 + (p >> 1) * 8;
  const lds_p ke = L + g * TSTEP + (p & 1) * (TSTEP / 2) + (16 + q) * 16 + (p >> 1) * 8;
  const lds_p vb = L + 10240 + h * (TSTEP / 2) + (r < 19 ? r : 19) * 16;
  lds_p kb2 = kb + 2 * AT_SLOT, ke2 = ke + 2 * AT_SLOT, vb2 = vb + 2 * AT_SLOT;
  asm volatile("" : "+v"(kb2), "+v"(ke2), "+v"(vb2));
  asm volatile("s_waitcnt vmcnt(5)" : "+v"(gbits)::"memory");
  BARRIER();
  STAMP(1, 2);
  bf16x8 qf;
  s16x4 qe;
  {
    KFrag qq = load_k(kb2 + AT_SLOT + wave * 1280, ke2 + AT_SLOT + wave * 1280);
    qf = qq.f;
    qe[0] = h ? (short)0 : qq.e[0];
    qe[1] = h ? (short)0 : qq.e[1];
    qe[2] = 0;
    qe[3] = 0;
  }

  const float gate = __uint_as_float(gbits);
  f32x16 zero;
#pragma unroll
  for (int i = 0; i < 16; ++i) zero[i] = 0.f;
  f32x16 o = zero;
  f32x16 sA = qk_tile(kb, ke, qf, qe, zero);
  {
    const float nx = -rowmax16(sA);
    const unsigned mb = (__float_as_uint(nx) + (nx > 0.f ? 0xffffu : 0u)) >> 16;
    const float nm = __uint_as_float(mb << 16);
#pragma unroll
    for (int i = 0; i < 16; ++i) sA[i] += nm;
    qe[2] = h ? (short)0 : (short)mb;
  }
  KFrag kA, kB;
  VFrag vA, vB;
  kB = load_k(kb + 1280, ke + 1280);
  vA = load_v(vb);
  f32x16 sB;
#pragma unroll 1
  for (int cc = 0; cc < 8; cc += 4) {
    static_for<0, 32>([&](auto Tc) {
      constexpr int tt = decltype(Tc)::v, ci = tt >> 3;
      if constexpr ((tt & 7) == 6) {
        if (cc == 0 || ci < 2) {
          if (grp == 0) wait_vm<at_cnt(ci + 2, 0)>(); else wait_vm<at_cnt(ci + 2, 1)>();
        } else if (ci == 2) wait_vm<0>();
        if (cc == 0 || ci < 3) BARRIER();
        if constexpr (ci == TOUCH_AT) {
          if (cc == 0) {
            const int l2 = fresh_lane();
            const char* src = W2 + (size_t)((wg & 7) >> 1) * (1152 * 1152) + (wg >> 3) * 41472;
            const unsigned dst = lbase + ((ci + 3) & 3) * AT_SLOT + wave * 1024;
            touch(src, (wave * 41 + l2 - (l2 > 40 ? 24 : 0) - (wave >> 2) * 4) * 128u, dst);
            if (wave == 0) touch(Wo + ((wg & 7) >> 1) * (144 * 1152) + (wg >> 3) * 5184, (l2 > 39 ? ((wg & 8) ? 39 : 40) : l2) * 128u + ((wg & 8) ? 64u : 0u), dst);
          }
        }
        if (cc == 0 || ci == 0) issue(cc + ci + 3);
      }
      constexpr int t1 = (tt + 1) & 31, t2 = (tt + 2) & 31;
      const lds_p kp = (t2 < 16 ? kb : kb2) + at_off(t2);
      const lds_p ep = (t2 < 16 ? ke : ke2) + at_off(t2);
      const lds_p vp = (t1 < 16 ? vb : vb2) + at_off(t1);
      if constexpr ((tt & 1) == 0) {
        kA = load_k(kp, ep);
        vB = load_v(vp);
        SB();
        tile_step<APMASK & 15>(sA, sB, kB, vA, qf, qe, o);
      } else {
        kB = load_k(kp, ep);
        vA = load_v(vp);
        SB();
        tile_step<(APMASK >> 4) & 15>(sB, sA, kA, vB, qf, qe, o);
      }
    });
    if (cc == 0) STAMP(1, 3);
  }
  STAMP(1, 4);
  STAMP(1, 5);
  __builtin_amdgcn_s_setprio(AEP);
  auto rowsum = [&]() { return __uint_as_float(__builtin_amdgcn_permlane32_swap(__float_as_uint(o[10]), __float_as_uint(o[10]), false, false)[0]); };
  float l = rowsum();
  auto ab = [&](int i) { return __builtin_fabsf(o[i]) + __builtin_fabsf(o[i + 1]); };
  bool bad = !(((ab(0) + ab(2)) + (ab(4) + ab(6))) + __builtin_fmaf(l, 1.0e7f, ab(8)) < 1.0e37f);
  const int ob = (qt * 8 + wave) * 32 + head;
  auto publish = [&]() {
    const float inv = gate * __builtin_amdgcn_rcpf(l);
    u32x4 ov;
#pragma unroll
    for (int i = 0; i < 4; ++i) ov[i] = pk2(o[2 * i] * inv, o[2 * i + 1] * inv);
    const unsigned vo = lane * 16;
    asm volatile("global_store_dwordx4 %0, %1, %2 sc1\n\ts_nop 1" ::"v"(vo), "v"(ov), "s"(O + ob * 1024) : "memory");
    const unsigned oe = pk2(o[8] * inv, o[9] * inv);
    if (h == 0) asm volatile("global_store_dword %0, %1, %2 sc1" ::"v"(vo), "v"(oe), "s"(O + (OEX + (ob >> 3) * 1024 + ((head >> 2) & 1) * 512 + (head & 3) * 4)) : "memory");
    STAMP(1, 6);
    const unsigned vf = (lane >> 1) * 4096 + (lane & 1) * 512;
    const char* fb = FL + (FL_P2 + (qt * 2 + (wave >> 2)) * 16384 + (wave & 3) * 1024 + (head >> 3) * 128 + 32 + (head & 7) * 4);
    wait_vm<0>();
    if (lane == 0) asm volatile("global_store_dword %0, %1, %2 sc1" ::"v"(vo), "v"(1u), "s"(FL + (FL_O + ob * 4)) : "memory");
  };
  const bool mybad = __any(bad);
  if (!mybad) publish();
  {
    int* fl = (int*)(lds + AT_SLOT);
    if (lane == 0) fl[wave] = mybad ? 1 : 0;
    __syncthreads();
    int v = fl[lane & 7];
    bad = __any(v != 0);
  }
  if (bad) {
    float m = -3.0e38f;
    o = zero;
    qe[2] = 0;
#pragma unroll 1
    for (int c = 0; c < 8; ++c) {
      __syncthreads();
      {
        const char* src = T + (size_t)c * (32 * TP);
        glds16s(src, v0, lbase + wave * 1024);
        glds16s(src, v1, lbase + p1 * 1024);
        if (grp == 0) glds16s(src, v2, lbase + p2 * 1024);
      }
      wait_vm<0>();
      __syncthreads();
#pragma unroll 1
      for (int t = 0; t < 8; ++t) {
        f32x16 s = qk_tile(kb + t * 1280, ke + t * 1280, qf, qe, zero);
        float mn = __builtin_fmaxf(m, rowmax16(s));
        float alpha = __builtin_amdgcn_exp2f(m - mn);
        m = mn;
        f32x16 pe;
#pragma unroll
        for (int i = 0; i < 16; ++i) { pe[i] = __builtin_amdgcn_exp2f(s[i] - mn); o[i] *= alpha; }
        o = pv_tile(vb + t * 1280, pe, o);
      }
    }
    l = rowsum();
    if (mybad) publish();
  }
  STAMP(1, 7);
}

#define QKV_PRIO 2
#define ATT_PRIO 0
#define SKIP1 0
#define MLP_PRIO 1
#define GELU_PRIO MLP_PRIO
#define MLP2_PRIO MLP_PRIO
#define P1HI4 0
#define P1HI5 0
#define P2HI4 0
#define P2HI5 0
#define HI_PRIO 3
#define PF2_AT 20
#define PSLP 2
#define XE_AT 33
constexpr int CK3 = 30;
constexpr int M_IT = 36;
constexpr int M2_NT = 41;
struct M2S { int slot[M2_NT]; int it[M2_NT]; };
constexpr M2S m2_sched(int U) {
  M2S r{};
  const int E = 2 * U - 2;
  int next = 0;
  for (; next < 4; ++next) { r.slot[next] = next; r.it[next] = -1; }
  for (int j = 1; next < M2_NT; ++j) {
    r.slot[next] = r.slot[j - 1];
    r.it[next] = j;
    ++next;
    if (next < M2_NT && (j == E || j == E + 1)) { r.slot[next] = 4 + (j - E); r.it[next] = j; ++next; }
  }
  return r;
}
constexpr int m2_ck1(int U) { return 2 * U - 3; }
constexpr int m2_ck2(int U) { return (U == 5 ? 26 : 2 * U + 8) - 3; }
constexpr int m2_wait(int U, int kh, int j, int mode) {
  const M2S sc = m2_sched(U);
  const int U2 = U == 5 ? 2 : 3;
  int n = 0, last = -1;
  for (int i = -1; i < j + mode; ++i) {
    if (i == m2_ck1(U) - 2 || i == m2_ck2(U) - 2) { if (mode == 1 && i == j - 2) last = n; ++n; }
    for (int s = 0; s < M2_NT; ++s)
      if (sc.it[s] == i) {
        const int c = (U2 == 3 && (s & 1) == kh) ? 2 : 1;
        n += c;
        if (mode == 0 && s <= j + 1) last = n - 1;
      }
    if (i == -1) n += 2 * U;
    if (i >= 0 && (i & 1) && i < j && i + 3 >= 2 * U && i + 3 < M_IT) { n += 2; if (mode == 0 && i + 3 <= j) last = n - 1; }
  }
  return n - 1 - last;
}
constexpr bool m2_need(int U, int j) { return j >= 3; }

template <int N>
DI void pin_acc(f32x16* a) {
  if constexpr (N == 5)
    asm volatile("" : "+{v[0:15]}"(a[0]), "+{v[16:31]}"(a[1]), "+{v[32:47]}"(a[2]), "+{v[48:63]}"(a[3]), "+{v[64:79]}"(a[4]));
  else
    asm volatile("" : "+{v[0:15]}"(a[0]), "+{v[16:31]}"(a[1]), "+{v[32:47]}"(a[2]), "+{v[48:63]}"(a[3]));
}
#define XLD(R) asm volatile("global_load_dwordx4 %0, %1, %2 sc1" : "={" R "}"(d) : "v"(voff), "s"(sbase) : "memory")
#define XLP(R) asm volatile("global_load_dwordx4 %0, %1, %2" : "={" R "}"(d) : "v"(voff), "s"(sbase) : "memory")
template <int I>
DI void xloadp(bf16x8& d, const char* sbase, unsigned voff) {
  if constexpr (I == 0) XLP("v[80:83]");
  else if constexpr (I == 1) XLP("v[84:87]");
  else if constexpr (I == 2) XLP("v[88:91]");
  else XLP("v[92:95]");
}
template <int I>
DI void xload(bf16x8& d, const char* sbase, unsigned voff) {
  if constexpr (I == 0) XLD("v[80:83]");
  else if constexpr (I == 1) XLD("v[84:87]");
  else if constexpr (I == 2) XLD("v[88:91]");
  else XLD("v[92:95]");
}
#define XDS(R) asm volatile("ds_read_b128 %0, %1 offset:%2" : "={" R "}"(d) : "v"(a), "i"(OFF) : "memory")
template <int I, int OFF>
DI void xstash(bf16x8& d, unsigned a) {
  if constexpr (I == 0) XDS("v[80:83]");
  else if constexpr (I == 1) XDS("v[84:87]");
  else if constexpr (I == 2) XDS("v[88:91]");
  else XDS("v[92:95]");
}
template <int I>
DI void pin_x1(bf16x8& x) {
  if constexpr (I == 0) asm volatile("" : "+{v[80:83]}"(x));
  else if constexpr (I == 1) asm volatile("" : "+{v[84:87]}"(x));
  else if constexpr (I == 2) asm volatile("" : "+{v[88:91]}"(x));
  else asm volatile("" : "+{v[92:95]}"(x));
}
template <int I>
DI void pin_w(bf16x8& w) {
  if constexpr (I == 0) asm volatile("" : "+{v[96:99]}"(w));
  else if constexpr (I == 1) asm volatile("" : "+{v[100:103]}"(w));
  else if constexpr (I == 2) asm volatile("" : "+{v[104:107]}"(w));
  else if constexpr (I == 3) asm volatile("" : "+{v[108:111]}"(w));
  else asm volatile("" : "+{v[112:115]}"(w));
}
template <int N>
DI void pair_sum(f32x16* a, unsigned exw, unsigned exr, int kh) {
  if constexpr (N == 5)
    asm volatile(
      "s_nop 15\ns_nop 3\ns_cmp_lg_u32 %[kh], 0\ns_cbranch_scc1 XU%=\nv_cvt_pk_bf16_f32 v80, v8, v9\nv_cvt_pk_bf16_f32 v81, v10, v11\n"
      "v_cvt_pk_bf16_f32 v82, v12, v13\nv_cvt_pk_bf16_f32 v83, v14, v15\nds_write_b128 %[w], v[80:83] offset:0\n"
      "v_cvt_pk_bf16_f32 v84, v24, v25\nv_cvt_pk_bf16_f32 v85, v26, v27\nv_cvt_pk_bf16_f32 v86, v28, v29\nv_cvt_pk_bf16_f32 v87, v30, v31\n"
      "ds_write_b128 %[w], v[84:87] offset:1024\nv_cvt_pk_bf16_f32 v88, v40, v41\nv_cvt_pk_bf16_f32 v89, v42, v43\n"
      "v_cvt_pk_bf16_f32 v90, v44, v45\nv_cvt_pk_bf16_f32 v91, v46, v47\nds_write_b128 %[w], v[88:91] offset:2048\n"
      "v_cvt_pk_bf16_f32 v92, v56, v57\nv_cvt_pk_bf16_f32 v93, v58, v59\nv_cvt_pk_bf16_f32 v94, v60, v61\nv_cvt_pk_bf16_f32 v95, v62, v63\n"
      "ds_write_b128 %[w], v[92:95] offset:3072\nv_cvt_pk_bf16_f32 v96, v72, v73\nv_cvt_pk_bf16_f32 v97, v74, v75\n"
      "v_cvt_pk_bf16_f32 v98, v76, v77\nv_cvt_pk_bf16_f32 v99, v78, v79\nds_write_b128 %[w], v[96:99] offset:4096\ns_waitcnt lgkmcnt(0)\n"
      "s_barrier\nds_read_b128 v[80:83], %[r] offset:0\nds_read_b128 v[84:87], %[r] offset:1024\nds_read_b128 v[88:91], %[r] offset:2048\n"
      "ds_read_b128 v[92:95], %[r] offset:3072\nds_read_b128 v[96:99], %[r] offset:4096\ns_waitcnt lgkmcnt(4)\nv_lshlrev_b32 v8, 16, v80\n"
      "v_and_b32 v9, 0xffff0000, v80\nv_lshlrev_b32 v10, 16, v81\nv_and_b32 v11, 0xffff0000, v81\nv_lshlrev_b32 v12, 16, v82\n"
      "v_and_b32 v13, 0xffff0000, v82\nv_lshlrev_b32 v14, 16, v83\nv_and_b32 v15, 0xffff0000, v83\nv_pk_add_f32 v[0:1], v[8:9], v[0:1]\n"
      "v_pk_add_f32 v[2:3], v[10:11], v[2:3]\nv_pk_add_f32 v[4:5], v[12:13], v[4:5]\nv_pk_add_f32 v[6:7], v[14:15], v[6:7]\n"
      "s_waitcnt lgkmcnt(3)\nv_lshlrev_b32 v24, 16, v84\nv_and_b32 v25, 0xffff0000, v84\nv_lshlrev_b32 v26, 16, v85\n"
      "v_and_b32 v27, 0xffff0000, v85\nv_lshlrev_b32 v28, 16, v86\nv_and_b32 v29, 0xffff0000, v86\nv_lshlrev_b32 v30, 16, v87\n"
      "v_and_b32 v31, 0xffff0000, v87\nv_pk_add_f32 v[16:17], v[24:25], v[16:17]\nv_pk_add_f32 v[18:19], v[26:27], v[18:19]\n"
      "v_pk_add_f32 v[20:21], v[28:29], v[20:21]\nv_pk_add_f32 v[22:23], v[30:31], v[22:23]\ns_waitcnt lgkmcnt(2)\n"
      "v_lshlrev_b32 v40, 16, v88\nv_and_b32 v41, 0xffff0000, v88\nv_lshlrev_b32 v42, 16, v89\nv_and_b32 v43, 0xffff0000, v89\n"
      "v_lshlrev_b32 v44, 16, v90\nv_and_b32 v45, 0xffff0000, v90\nv_lshlrev_b32 v46, 16, v91\nv_and_b32 v47, 0xffff0000, v91\n"
      "v_pk_add_f32 v[32:33], v[40:41], v[32:33]\nv_pk_add_f32 v[34:35], v[42:43], v[34:35]\nv_pk_add_f32 v[36:37], v[44:45], v[36:37]\n"
      "v_pk_add_f32 v[38:39], v[46:47], v[38:39]\ns_waitcnt lgkmcnt(1)\nv_lshlrev_b32 v56, 16, v92\nv_and_b32 v57, 0xffff0000, v92\n"
      "v_lshlrev_b32 v58, 16, v93\nv_and_b32 v59, 0xffff0000, v93\nv_lshlrev_b32 v60, 16, v94\nv_and_b32 v61, 0xffff0000, v94\n"
      "v_lshlrev_b32 v62, 16, v95\nv_and_b32 v63, 0xffff0000, v95\nv_pk_add_f32 v[48:49], v[56:57], v[48:49]\n"
      "v_pk_add_f32 v[50:51], v[58:59], v[50:51]\nv_pk_add_f32 v[52:53], v[60:61], v[52:53]\nv_pk_add_f32 v[54:55], v[62:63], v[54:55]\n"
      "s_waitcnt lgkmcnt(0)\nv_lshlrev_b32 v72, 16, v96\nv_and_b32 v73, 0xffff0000, v96\nv_lshlrev_b32 v74, 16, v97\n"
      "v_and_b32 v75, 0xffff0000, v97\nv_lshlrev_b32 v76, 16, v98\nv_and_b32 v77, 0xffff0000, v98\nv_lshlrev_b32 v78, 16, v99\n"
      "v_and_b32 v79, 0xffff0000, v99\nv_pk_add_f32 v[64:65], v[72:73], v[64:65]\nv_pk_add_f32 v[66:67], v[74:75], v[66:67]\n"
      "v_pk_add_f32 v[68:69], v[76:77], v[68:69]\nv_pk_add_f32 v[70:71], v[78:79], v[70:71]\ns_branch XE%=\nXU%=:\n"
      "v_cvt_pk_bf16_f32 v80, v0, v1\nv_cvt_pk_bf16_f32 v81, v2, v3\nv_cvt_pk_bf16_f32 v82, v4, v5\nv_cvt_pk_bf16_f32 v83, v6, v7\n"
      "ds_write_b128 %[w], v[80:83] offset:0\nv_cvt_pk_bf16_f32 v84, v16, v17\nv_cvt_pk_bf16_f32 v85, v18, v19\n"
      "v_cvt_pk_bf16_f32 v86, v20, v21\nv_cvt_pk_bf16_f32 v87, v22, v23\nds_write_b128 %[w], v[84:87] offset:1024\n"
      "v_cvt_pk_bf16_f32 v88, v32, v33\nv_cvt_pk_bf16_f32 v89, v34, v35\nv_cvt_pk_bf16_f32 v90, v36, v37\nv_cvt_pk_bf16_f32 v91, v38, v39\n"
      "ds_write_b128 %[w], v[88:91] offset:2048\nv_cvt_pk_bf16_f32 v92, v48, v49\nv_cvt_pk_bf16_f32 v93, v50, v51\n"
      "v_cvt_pk_bf16_f32 v94, v52, v53\nv_cvt_pk_bf16_f32 v95, v54, v55\nds_write_b128 %[w], v[92:95] offset:3072\n"
      "v_cvt_pk_bf16_f32 v96, v64, v65\nv_cvt_pk_bf16_f32 v97, v66, v67\nv_cvt_pk_bf16_f32 v98, v68, v69\nv_cvt_pk_bf16_f32 v99, v70, v71\n"
      "ds_write_b128 %[w], v[96:99] offset:4096\ns_waitcnt lgkmcnt(0)\ns_barrier\nds_read_b128 v[80:83], %[r] offset:0\n"
      "ds_read_b128 v[84:87], %[r] offset:1024\nds_read_b128 v[88:91], %[r] offset:2048\nds_read_b128 v[92:95], %[r] offset:3072\n"
      "ds_read_b128 v[96:99], %[r] offset:4096\ns_waitcnt lgkmcnt(4)\nv_lshlrev_b32 v0, 16, v80\nv_and_b32 v1, 0xffff0000, v80\n"
      "v_lshlrev_b32 v2, 16, v81\nv_and_b32 v3, 0xffff0000, v81\nv_lshlrev_b32 v4, 16, v82\nv_and_b32 v5, 0xffff0000, v82\n"
      "v_lshlrev_b32 v6, 16, v83\nv_and_b32 v7, 0xffff0000, v83\nv_pk_add_f32 v[0:1], v[0:1], v[8:9]\n"
      "v_pk_add_f32 v[2:3], v[2:3], v[10:11]\nv_pk_add_f32 v[4:5], v[4:5], v[12:13]\nv_pk_add_f32 v[6:7], v[6:7], v[14:15]\n"
      "s_waitcnt lgkmcnt(3)\nv_lshlrev_b32 v16, 16, v84\nv_and_b32 v17, 0xffff0000, v84\nv_lshlrev_b32 v18, 16, v85\n"
      "v_and_b32 v19, 0xffff0000, v85\nv_lshlrev_b32 v20, 16, v86\nv_and_b32 v21, 0xffff0000, v86\nv_lshlrev_b32 v22, 16, v87\n"
      "v_and_b32 v23, 0xffff0000, v87\nv_pk_add_f32 v[16:17], v[16:17], v[24:25]\nv_pk_add_f32 v[18:19], v[18:19], v[26:27]\n"
      "v_pk_add_f32 v[20:21], v[20:21], v[28:29]\nv_pk_add_f32 v[22:23], v[22:23], v[30:31]\ns_waitcnt lgkmcnt(2)\n"
      "v_lshlrev_b32 v32, 16, v88\nv_and_b32 v33, 0xffff0000, v88\nv_lshlrev_b32 v34, 16, v89\nv_and_b32 v35, 0xffff0000, v89\n"
      "v_lshlrev_b32 v36, 16, v90\nv_and_b32 v37, 0xffff0000, v90\nv_lshlrev_b32 v38, 16, v91\nv_and_b32 v39, 0xffff0000, v91\n"
      "v_pk_add_f32 v[32:33], v[32:33], v[40:41]\nv_pk_add_f32 v[34:35], v[34:35], v[42:43]\nv_pk_add_f32 v[36:37], v[36:37], v[44:45]\n"
      "v_pk_add_f32 v[38:39], v[38:39], v[46:47]\ns_waitcnt lgkmcnt(1)\nv_lshlrev_b32 v48, 16, v92\nv_and_b32 v49, 0xffff0000, v92\n"
      "v_lshlrev_b32 v50, 16, v93\nv_and_b32 v51, 0xffff0000, v93\nv_lshlrev_b32 v52, 16, v94\nv_and_b32 v53, 0xffff0000, v94\n"
      "v_lshlrev_b32 v54, 16, v95\nv_and_b32 v55, 0xffff0000, v95\nv_pk_add_f32 v[48:49], v[48:49], v[56:57]\n"
      "v_pk_add_f32 v[50:51], v[50:51], v[58:59]\nv_pk_add_f32 v[52:53], v[52:53], v[60:61]\nv_pk_add_f32 v[54:55], v[54:55], v[62:63]\n"
      "s_waitcnt lgkmcnt(0)\nv_lshlrev_b32 v64, 16, v96\nv_and_b32 v65, 0xffff0000, v96\nv_lshlrev_b32 v66, 16, v97\n"
      "v_and_b32 v67, 0xffff0000, v97\nv_lshlrev_b32 v68, 16, v98\nv_and_b32 v69, 0xffff0000, v98\nv_lshlrev_b32 v70, 16, v99\n"
      "v_and_b32 v71, 0xffff0000, v99\nv_pk_add_f32 v[64:65], v[64:65], v[72:73]\nv_pk_add_f32 v[66:67], v[66:67], v[74:75]\n"
      "v_pk_add_f32 v[68:69], v[68:69], v[76:77]\nv_pk_add_f32 v[70:71], v[70:71], v[78:79]\nXE%=:\n"
      : "+{v[0:15]}"(a[0]), "+{v[16:31]}"(a[1]), "+{v[32:47]}"(a[2]), "+{v[48:63]}"(a[3]), "+{v[64:79]}"(a[4])
      : [w] "v"(exw), [r] "v"(exr), [kh] "s"(kh)
      : "memory", "scc", "v80", "v81", "v82", "v83", "v84", "v85", "v86", "v87", "v88", "v89", "v90", "v91", "v92", "v93", "v94", "v95", "v96", "v97", "v98", "v99");
  else
    asm volatile(
      "s_nop 15\ns_nop 3\ns_cmp_lg_u32 %[kh], 0\ns_cbranch_scc1 XU%=\nv_cvt_pk_bf16_f32 v80, v8, v9\nv_cvt_pk_bf16_f32 v81, v10, v11\n"
      "v_cvt_pk_bf16_f32 v82, v12, v13\nv_cvt_pk_bf16_f32 v83, v14, v15\nds_write_b128 %[w], v[80:83] offset:0\n"
      "v_cvt_pk_bf16_f32 v84, v24, v25\nv_cvt_pk_bf16_f32 v85, v26, v27\nv_cvt_pk_bf16_f32 v86, v28, v29\nv_cvt_pk_bf16_f32 v87, v30, v31\n"
      "ds_write_b128 %[w], v[84:87] offset:1024\nv_cvt_pk_bf16_f32 v88, v40, v41\nv_cvt_pk_bf16_f32 v89, v42, v43\n"
      "v_cvt_pk_bf16_f32 v90, v44, v45\nv_cvt_pk_bf16_f32 v91, v46, v47\nds_write_b128 %[w], v[88:91] offset:2048\n"
      "v_cvt_pk_bf16_f32 v92, v56, v57\nv_cvt_pk_bf16_f32 v93, v58, v59\nv_cvt_pk_bf16_f32 v94, v60, v61\nv_cvt_pk_bf16_f32 v95, v62, v63\n"
      "ds_write_b128 %[w], v[92:95] offset:3072\ns_waitcnt lgkmcnt(0)\ns_barrier\nds_read_b128 v[80:83], %[r] offset:0\n"
      "ds_read_b128 v[84:87], %[r] offset:1024\nds_read_b128 v[88:91], %[r] offset:2048\nds_read_b128 v[92:95], %[r] offset:3072\n"
      "s_waitcnt lgkmcnt(3)\nv_lshlrev_b32 v8, 16, v80\nv_and_b32 v9, 0xffff0000, v80\nv_lshlrev_b32 v10, 16, v81\n"
      "v_and_b32 v11, 0xffff0000, v81\nv_lshlrev_b32 v12, 16, v82\nv_and_b32 v13, 0xffff0000, v82\nv_lshlrev_b32 v14, 16, v83\n"
      "v_and_b32 v15, 0xffff0000, v83\nv_pk_add_f32 v[0:1], v[8:9], v[0:1]\nv_pk_add_f32 v[2:3], v[10:11], v[2:3]\n"
      "v_pk_add_f32 v[4:5], v[12:13], v[4:5]\nv_pk_add_f32 v[6:7], v[14:15], v[6:7]\ns_waitcnt lgkmcnt(2)\nv_lshlrev_b32 v24, 16, v84\n"
      "v_and_b32 v25, 0xffff0000, v84\nv_lshlrev_b32 v26, 16, v85\nv_and_b32 v27, 0xffff0000, v85\nv_lshlrev_b32 v28, 16, v86\n"
      "v_and_b32 v29, 0xffff0000, v86\nv_lshlrev_b32 v30, 16, v87\nv_and_b32 v31, 0xffff0000, v87\n"
      "v_pk_add_f32 v[16:17], v[24:25], v[16:17]\nv_pk_add_f32 v[18:19], v[26:27], v[18:19]\nv_pk_add_f32 v[20:21], v[28:29], v[20:21]\n"
      "v_pk_add_f32 v[22:23], v[30:31], v[22:23]\ns_waitcnt lgkmcnt(1)\nv_lshlrev_b32 v40, 16, v88\nv_and_b32 v41, 0xffff0000, v88\n"
      "v_lshlrev_b32 v42, 16, v89\nv_and_b32 v43, 0xffff0000, v89\nv_lshlrev_b32 v44, 16, v90\nv_and_b32 v45, 0xffff0000, v90\n"
      "v_lshlrev_b32 v46, 16, v91\nv_and_b32 v47, 0xffff0000, v91\nv_pk_add_f32 v[32:33], v[40:41], v[32:33]\n"
      "v_pk_add_f32 v[34:35], v[42:43], v[34:35]\nv_pk_add_f32 v[36:37], v[44:45], v[36:37]\nv_pk_add_f32 v[38:39], v[46:47], v[38:39]\n"
      "s_waitcnt lgkmcnt(0)\nv_lshlrev_b32 v56, 16, v92\nv_and_b32 v57, 0xffff0000, v92\nv_lshlrev_b32 v58, 16, v93\n"
      "v_and_b32 v59, 0xffff0000, v93\nv_lshlrev_b32 v60, 16, v94\nv_and_b32 v61, 0xffff0000, v94\nv_lshlrev_b32 v62, 16, v95\n"
      "v_and_b32 v63, 0xffff0000, v95\nv_pk_add_f32 v[48:49], v[56:57], v[48:49]\nv_pk_add_f32 v[50:51], v[58:59], v[50:51]\n"
      "v_pk_add_f32 v[52:53], v[60:61], v[52:53]\nv_pk_add_f32 v[54:55], v[62:63], v[54:55]\ns_branch XE%=\nXU%=:\n"
      "v_cvt_pk_bf16_f32 v80, v0, v1\nv_cvt_pk_bf16_f32 v81, v2, v3\nv_cvt_pk_bf16_f32 v82, v4, v5\nv_cvt_pk_bf16_f32 v83, v6, v7\n"
      "ds_write_b128 %[w], v[80:83] offset:0\nv_cvt_pk_bf16_f32 v84, v16, v17\nv_cvt_pk_bf16_f32 v85, v18, v19\n"
      "v_cvt_pk_bf16_f32 v86, v20, v21\nv_cvt_pk_bf16_f32 v87, v22, v23\nds_write_b128 %[w], v[84:87] offset:1024\n"
      "v_cvt_pk_bf16_f32 v88, v32, v33\nv_cvt_pk_bf16_f32 v89, v34, v35\nv_cvt_pk_bf16_f32 v90, v36, v37\nv_cvt_pk_bf16_f32 v91, v38, v39\n"
      "ds_write_b128 %[w], v[88:91] offset:2048\nv_cvt_pk_bf16_f32 v92, v48, v49\nv_cvt_pk_bf16_f32 v93, v50, v51\n"
      "v_cvt_pk_bf16_f32 v94, v52, v53\nv_cvt_pk_bf16_f32 v95, v54, v55\nds_write_b128 %[w], v[92:95] offset:3072\ns_waitcnt lgkmcnt(0)\n"
      "s_barrier\nds_read_b128 v[80:83], %[r] offset:0\nds_read_b128 v[84:87], %[r] offset:1024\nds_read_b128 v[88:91], %[r] offset:2048\n"
      "ds_read_b128 v[92:95], %[r] offset:3072\ns_waitcnt lgkmcnt(3)\nv_lshlrev_b32 v0, 16, v80\nv_and_b32 v1, 0xffff0000, v80\n"
      "v_lshlrev_b32 v2, 16, v81\nv_and_b32 v3, 0xffff0000, v81\nv_lshlrev_b32 v4, 16, v82\nv_and_b32 v5, 0xffff0000, v82\n"
      "v_lshlrev_b32 v6, 16, v83\nv_and_b32 v7, 0xffff0000, v83\nv_pk_add_f32 v[0:1], v[0:1], v[8:9]\n"
      "v_pk_add_f32 v[2:3], v[2:3], v[10:11]\nv_pk_add_f32 v[4:5], v[4:5], v[12:13]\nv_pk_add_f32 v[6:7], v[6:7], v[14:15]\n"
      "s_waitcnt lgkmcnt(2)\nv_lshlrev_b32 v16, 16, v84\nv_and_b32 v17, 0xffff0000, v84\nv_lshlrev_b32 v18, 16, v85\n"
      "v_and_b32 v19, 0xffff0000, v85\nv_lshlrev_b32 v20, 16, v86\nv_and_b32 v21, 0xffff0000, v86\nv_lshlrev_b32 v22, 16, v87\n"
      "v_and_b32 v23, 0xffff0000, v87\nv_pk_add_f32 v[16:17], v[16:17], v[24:25]\nv_pk_add_f32 v[18:19], v[18:19], v[26:27]\n"
      "v_pk_add_f32 v[20:21], v[20:21], v[28:29]\nv_pk_add_f32 v[22:23], v[22:23], v[30:31]\ns_waitcnt lgkmcnt(1)\n"
      "v_lshlrev_b32 v32, 16, v88\nv_and_b32 v33, 0xffff0000, v88\nv_lshlrev_b32 v34, 16, v89\nv_and_b32 v35, 0xffff0000, v89\n"
      "v_lshlrev_b32 v36, 16, v90\nv_and_b32 v37, 0xffff0000, v90\nv_lshlrev_b32 v38, 16, v91\nv_and_b32 v39, 0xffff0000, v91\n"
      "v_pk_add_f32 v[32:33], v[32:33], v[40:41]\nv_pk_add_f32 v[34:35], v[34:35], v[42:43]\nv_pk_add_f32 v[36:37], v[36:37], v[44:45]\n"
      "v_pk_add_f32 v[38:39], v[38:39], v[46:47]\ns_waitcnt lgkmcnt(0)\nv_lshlrev_b32 v48, 16, v92\nv_and_b32 v49, 0xffff0000, v92\n"
      "v_lshlrev_b32 v50, 16, v93\nv_and_b32 v51, 0xffff0000, v93\nv_lshlrev_b32 v52, 16, v94\nv_and_b32 v53, 0xffff0000, v94\n"
      "v_lshlrev_b32 v54, 16, v95\nv_and_b32 v55, 0xffff0000, v95\nv_pk_add_f32 v[48:49], v[48:49], v[56:57]\n"
      "v_pk_add_f32 v[50:51], v[50:51], v[58:59]\nv_pk_add_f32 v[52:53], v[52:53], v[60:61]\nv_pk_add_f32 v[54:55], v[54:55], v[62:63]\n"
      "XE%=:\n"
      : "+{v[0:15]}"(a[0]), "+{v[16:31]}"(a[1]), "+{v[32:47]}"(a[2]), "+{v[48:63]}"(a[3])
      : [w] "v"(exw), [r] "v"(exr), [kh] "s"(kh)
      : "memory", "scc", "v80", "v81", "v82", "v83", "v84", "v85", "v86", "v87", "v88", "v89", "v90", "v91", "v92", "v93", "v94", "v95");
}
DI void gld4ck(unsigned& d, const char* sbase, unsigned voff) {
  asm volatile("global_load_dword %0, %1, %2 sc1" : "+v"(d) : "v"(voff), "s"(sbase) : "memory");
}
#define PSTG 0
#define OUTSC 0
#define TS(a, b) do { if ((a) >= 0) SST((a) < 0 ? 0 : (a)); } while (0)
#define MPRO_PRIO 3
#define MFMA16(a, b, c) __builtin_amdgcn_mfma_f32_16x16x32_bf16((a), (b), (c), 0, 0, 0)
template <int Q>
DI f32x4 sub4(const f32x16& a) { return __builtin_shufflevector(a, a, 4 * Q, 4 * Q + 1, 4 * Q + 2, 4 * Q + 3); }
template <int Q>
DI void put4(f32x16& a, f32x4 s) {
#pragma unroll
  for (int i = 0; i < 4; ++i) a[4 * Q + i] = s[i];
}
template <int HF>
DI void mm16(f32x16& a, bf16x8 w, bf16x8 x0, bf16x8 x1) {
  put4<2 * HF>(a, MFMA16(w, x0, sub4<2 * HF>(a)));
  put4<2 * HF + 1>(a, MFMA16(w, x1, sub4<2 * HF + 1>(a)));
}
template <int HF, int MODE>
DI void mm16s(f32x16& a, bf16x8 w, bf16x8 x0, bf16x8 x1) {
  const f32x4 z = {0.f, 0.f, 0.f, 0.f};
  if constexpr (MODE == 0) mm16<HF>(a, w, x0, x1);
  else {
    const f32x4 c = MODE == 1 ? z : sub4<2 * HF>(a);
    put4<2 * HF + 1>(a, MFMA16(w, x1, c));
    put4<2 * HF>(a, MFMA16(w, x0, c));
  }
}
#define MFMA16K16(a, b, c) __builtin_amdgcn_mfma_f32_16x16x16bf16_1k((a), (b), (c), 0, 0, 0)
DI s16x4 w0half(unsigned v) { const u32x2 t = {v, 0u}; return __builtin_bit_cast(s16x4, t); }
template <int N>
DI void mm16e(f32x16* a, bf16x8* w, u32x4 x0, u32x4 x1, int kh) {
  if constexpr (N == 5)
    asm volatile(
      "s_nop 11\ns_cmp_lg_u32 %[kh], 0\ns_cbranch_scc1 YU%=\n"
      "v_mfma_f32_16x16x32_bf16 v[0:3], v[96:99], %[x0], v[0:3]\n"
      "v_mfma_f32_16x16x32_bf16 v[4:7], v[96:99], %[x1], v[4:7]\n"
      "v_mfma_f32_16x16x32_bf16 v[16:19], v[100:103], %[x0], v[16:19]\n"
      "v_mfma_f32_16x16x32_bf16 v[20:23], v[100:103], %[x1], v[20:23]\n"
      "v_mfma_f32_16x16x32_bf16 v[32:35], v[104:107], %[x0], v[32:35]\n"
      "v_mfma_f32_16x16x32_bf16 v[36:39], v[104:107], %[x1], v[36:39]\n"
      "v_mfma_f32_16x16x32_bf16 v[48:51], v[108:111], %[x0], v[48:51]\n"
      "v_mfma_f32_16x16x32_bf16 v[52:55], v[108:111], %[x1], v[52:55]\n"
      "v_mfma_f32_16x16x32_bf16 v[64:67], v[112:115], %[x0], v[64:67]\n"
      "v_mfma_f32_16x16x32_bf16 v[68:71], v[112:115], %[x1], v[68:71]\n"
      "s_branch YE%=\nYU%=:\n"
      "v_mfma_f32_16x16x32_bf16 v[8:11], v[96:99], %[x0], v[8:11]\n"
      "v_mfma_f32_16x16x32_bf16 v[12:15], v[96:99], %[x1], v[12:15]\n"
      "v_mfma_f32_16x16x32_bf16 v[24:27], v[100:103], %[x0], v[24:27]\n"
      "v_mfma_f32_16x16x32_bf16 v[28:31], v[100:103], %[x1], v[28:31]\n"
      "v_mfma_f32_16x16x32_bf16 v[40:43], v[104:107], %[x0], v[40:43]\n"
      "v_mfma_f32_16x16x32_bf16 v[44:47], v[104:107], %[x1], v[44:47]\n"
      "v_mfma_f32_16x16x32_bf16 v[56:59], v[108:111], %[x0], v[56:59]\n"
      "v_mfma_f32_16x16x32_bf16 v[60:63], v[108:111], %[x1], v[60:63]\n"
      "v_mfma_f32_16x16x32_bf16 v[72:75], v[112:115], %[x0], v[72:75]\n"
      "v_mfma_f32_16x16x32_bf16 v[76:79], v[112:115], %[x1], v[76:79]\n"
      "YE%=:\n"
      : "+{v[0:15]}"(a[0]), "+{v[16:31]}"(a[1]), "+{v[32:47]}"(a[2]), "+{v[48:63]}"(a[3]), "+{v[64:79]}"(a[4])
      : "{v[96:99]}"(w[0]), "{v[100:103]}"(w[1]), "{v[104:107]}"(w[2]), "{v[108:111]}"(w[3]), "{v[112:115]}"(w[4]), [x0] "v"(x0), [x1] "v"(x1), [kh] "s"(kh)
      : "scc");
  else
    asm volatile(
      "s_nop 11\ns_cmp_lg_u32 %[kh], 0\ns_cbranch_scc1 YU%=\n"
      "v_mfma_f32_16x16x32_bf16 v[0:3], v[96:99], %[x0], v[0:3]\n"
      "v_mfma_f32_16x16x32_bf16 v[4:7], v[96:99], %[x1], v[4:7]\n"
      "v_mfma_f32_16x16x32_bf16 v[16:19], v[100:103], %[x0], v[16:19]\n"
      "v_mfma_f32_16x16x32_bf16 v[20:23], v[100:103], %[x1], v[20:23]\n"
      "v_mfma_f32_16x16x32_bf16 v[32:35], v[104:107], %[x0], v[32:35]\n"
      "v_mfma_f32_16x16x32_bf16 v[36:39], v[104:107], %[x1], v[36:39]\n"
      "v_mfma_f32_16x16x32_bf16 v[48:51], v[108:111], %[x0], v[48:51]\n"
      "v_mfma_f32_16x16x32_bf16 v[52:55], v[108:111], %[x1], v[52:55]\n"
      "s_branch YE%=\nYU%=:\n"
      "v_mfma_f32_16x16x32_bf16 v[8:11], v[96:99], %[x0], v[8:11]\n"
      "v_mfma_f32_16x16x32_bf16 v[12:15], v[96:99], %[x1], v[12:15]\n"
      "v_mfma_f32_16x16x32_bf16 v[24:27], v[100:103], %[x0], v[24:27]\n"
      "v_mfma_f32_16x16x32_bf16 v[28:31], v[100:103], %[x1], v[28:31]\n"
      "v_mfma_f32_16x16x32_bf16 v[40:43], v[104:107], %[x0], v[40:43]\n"
      "v_mfma_f32_16x16x32_bf16 v[44:47], v[104:107], %[x1], v[44:47]\n"
      "v_mfma_f32_16x16x32_bf16 v[56:59], v[108:111], %[x0], v[56:59]\n"
      "v_mfma_f32_16x16x32_bf16 v[60:63], v[108:111], %[x1], v[60:63]\n"
      "YE%=:\n"
      : "+{v[0:15]}"(a[0]), "+{v[16:31]}"(a[1]), "+{v[32:47]}"(a[2]), "+{v[48:63]}"(a[3])
      : "{v[96:99]}"(w[0]), "{v[100:103]}"(w[1]), "{v[104:107]}"(w[2]), "{v[108:111]}"(w[3]), [x0] "v"(x0), [x1] "v"(x1), [kh] "s"(kh)
      : "scc");
}
#define WRD(R, A, B) asm volatile("ds_read_b64_tr_b16 " A ", %1 offset:%2\n\tds_read_b64_tr_b16 " B ", %1 offset:%3" : "={" R "}"(d) : "v"(a), "i"(O0), "i"(O1) : "memory")
template <int I, int O0, int O1>
DI void wread(bf16x8& d, unsigned a) {
  if constexpr (O0 > 65535 || O1 > 65535) wread<I, O0 - 4096, O1 - 4096>(d, a + 4096u);
  else if constexpr (I == 0) WRD("v[96:99]", "v[96:97]", "v[98:99]");
  else if constexpr (I == 1) WRD("v[100:103]", "v[100:101]", "v[102:103]");
  else if constexpr (I == 2) WRD("v[104:107]", "v[104:105]", "v[106:107]");
  else if constexpr (I == 3) WRD("v[108:111]", "v[108:109]", "v[110:111]");
  else WRD("v[112:115]", "v[112:113]", "v[114:115]");
}
template <int N>
DI void wait_lgkm() { asm volatile("s_waitcnt lgkmcnt(%0)" ::"i"(N) : "memory"); }
template <int U>
DI void mlp_track(char* lds, const char* __restrict__ XN, const char* __restrict__ W1,
                  const unsigned short* __restrict__ b1, char* H, const char* __restrict__ W2,
                  const unsigned short* __restrict__ b2, const char* __restrict__ Wo, const char* O,
                  const char* __restrict__ G, char* P, char* FL, char* __restrict__ out, int rt, int sp, int j4,
                  int koff, int wave, int lane) {
  constexpr int NBW = 9 - U, U2 = U == 5 ? 2 : 3;
  constexpr int NS1 = U == 5 ? 5 : 6;
  SSD();
  TS(0, -1);
  SS8(0);
  const bool up = j4 < 2;
  const int unit0 = sp * 18 + (U == 5 ? (up ? 0 : 13) : (up ? 5 : 9));
  const unsigned lbase = lds_addr(lds);
  const lds_p L = (lds_p)lds;
  const int wm = wave & 3, wn = wave >> 2;
  char* FLg = FL + (size_t)(rt * 4 + sp) * 512;
  f32x16 acc[5];
  bf16x8 w[5], xr[4];
  float iog0, iog1;
  const f32x4 zero4 = {0.f, 0.f, 0.f, 0.f};
  {
    constexpr int WS = 2 * U * 1024, XB = NS1 * WS, NP = 4 + 2 * U;
    const int g = lane >> 4, r16 = lane & 15;
    const unsigned voffW = (unsigned)((lane >> 2) * 9216 + (((lane & 3) ^ ((lane >> 5) << 1)) << 4));
    const unsigned voffG = (unsigned)(g * 512 + r16 * 16);
    const unsigned voff0 = wave < 4 ? voffG : voffW;
    const char* wb = W1 + (unit0 * 128 + (wave & 1) * (16 * 9216));
    const char* src0 = wave < 4 ? XN + (size_t)(rt * 4 + wave) * (36 * 1024) : wb + (wm >> 1) * 128;
    const char* src1 = wb + ((wave >> 1) + 2) * 128;
    const unsigned st0 = wave < 4 ? 2048u : 294912u;
    const unsigned ev0 = wave < 4 ? 256u : 0u, od0 = wave < 4 ? 0u : 64u;
    const int koff1 = koff + (wave < 4 ? 1 : 0);
    const bool two = wave + 8 < NP;
    const unsigned dst0 = lbase + (wave < 4 ? XB + wave * 1024 : wm * 1024), ds0 = wave < 4 ? 4096u : (unsigned)WS;
    auto issue = [&](int s, int e) {
      int ks = (s >> 1) + ((s & 1) && e == 0 ? koff1 : koff);
      ks = ks >= 18 ? ks - 18 : ks;
      if (e == 0) glds16s(src0 + ((unsigned)ks * st0 + ((s & 1) ? od0 : ev0)), voff0, dst0 + (s % NS1) * ds0);
      else if (two) glds16s(src1 + ((unsigned)ks * 294912u + (s & 1) * 64u), voffW, lbase + (s % NS1) * WS + (wave + 4) * 1024);
    };
    auto waitn = [&](auto Nc) {
      constexpr int N = decltype(Nc)::v;
      if (two) wait_vm<2 * N>(); else wait_vm<N>();
    };
    unsigned bb[U];
#pragma unroll
    for (int b = 0; b < U; ++b)
      asm volatile("global_load_ushort %0, %1, %2" : "=v"(bb[b]) : "v"((r16 + (g & 1) * 32) * 2),
                   "s"(b1 + ((unit0 + b) * 64 + wn * 16)) : "memory");
    asm volatile("global_load_dword %0, %1, %2" : "=v"(iog0) : "v"(r16 * 4), "s"(G + (2048 + (rt * 4 + wm) * 32) * 4) : "memory");
    asm volatile("global_load_dword %0, %1, %2 offset:64" : "=v"(iog1) : "v"(r16 * 4), "s"(G + (2048 + (rt * 4 + wm) * 32) * 4) : "memory");
    xloadp<0>(xr[0], XN + ((size_t)(rt * 4 + wm) * (36 * 1024) + koff * 2048), voffG);
#pragma unroll
    for (int s = 0; s < NS1; ++s) { issue(s, 0); issue(s, 1); }
    __builtin_amdgcn_s_setprio(MPRO_PRIO);
    const int q = (lane >> 2) & 3, p = lane & 3;
    unsigned xa = lbase + XB + wm * 1024 + lane * 16;
    unsigned wa = lbase + g * 512 + q * 64 + ((wn ^ (g & 1)) << 5) + p * 8;
    asm volatile("" : "+v"(xa), "+v"(wa));
    waitn(IC<NS1 - 1>{});
    {
      pin_x1<0>(xr[0]);
      asm volatile("" : "+v"(iog0), "+v"(iog1));
      const s16x4 o0 = w0half(g == 0 ? 0x3F80u : 0u), o1 = w0half(g == 1 ? 0x3F80u : 0u);
#pragma unroll
      for (int b = 0; b < U; ++b) {
        asm volatile("" : "+v"(bb[b]));
        put4<0>(acc[b], MFMA16K16(w0half(bb[b]), o0, zero4));
        put4<2>(acc[b], MFMA16K16(w0half(bb[b]), o1, zero4));
      }
    }
    BARRIER();
    __builtin_amdgcn_s_setprio(MLP_PRIO);
    STAMP(4, 1);
    auto rdw = [&](auto Sl, auto Mc) {
      constexpr int m = decltype(Mc)::v, o = decltype(Sl)::v * WS + m * 2048;
      wread<m, o, o + 256>(w[m], wa);
    };
    pin_acc<U>(acc);
    xstash<1, 0>(xr[1], xa);
    static_for<0, U>([&](auto Mc) { rdw(IC<0>{}, Mc); });
    unsigned pfd;
    static_for<0, 36>([&](auto Jc) {
      constexpr int j = decltype(Jc)::v;
      if constexpr (j + 1 < 36) {
        constexpr int top = j == 0 ? NS1 - 1 : (j + NS1 - 2 < 35 ? j + NS1 - 2 : 35);
        waitn(IC<top - (j + 1)>{});
      }
      pin_acc<U>(acc);
      if constexpr (j + 1 < 36) BARRIER();
      STAMP2(j);
      if (j == 12) STAMP(4, 2);
      if (j == 24) STAMP(4, 3);
      SB();
      static_for<0, U>([&](auto Mc) {
        constexpr int m = decltype(Mc)::v;
        wait_lgkm<(j + 1 < 36 ? 2 * (U - 1) + (m > 0 && j + 2 < 36 ? 1 : 0) : 2 * (U - 1 - m))>();
        SB();
        mm16s<(j & 1), (j < 2 ? 2 : 0)>(acc[m], w[m], xr[j & 2], xr[(j & 2) + 1]);
        SB();
        if constexpr (j + 1 < 36) {
          if constexpr (m == 0 && j + 2 < 36) xstash<((j + 2) & 3), ((j + 1) % NS1) * 4096>(xr[(j + 2) & 3], xa);
          rdw(IC<(j + 1) % NS1>{}, Mc);
        }
        if constexpr (j >= 1 && j + NS1 - 1 < 36 && (m == 1 || m == 2)) issue(j + NS1 - 1, m - 1);
        if constexpr (j == PF2_AT && m == 3) {
          const unsigned l2 = fresh_lane(), li = l2 < 18 * U2 ? l2 : l2 - 32, row = li / U2;
          const unsigned vo = row * 1152u + (li - row * U2) * 128u;
          const int u2 = U == 5 ? (up ? 0 : 7) : 2 * j4;
          gld4(pfd, W2 + (size_t)(sp * 1152 + (rt & 7) * 144 + wave * 18) * 1152 + u2 * 128, vo);
          if (wave == 7) gld4k(pfd, Wo + (sp * 144 + (rt & 7) * 18) * 1152 + u2 * 128, vo);
        }
        if constexpr (j == PF2_AT + NS1 + 1 && m == 0) asm volatile("" ::"v"(pfd));
        SB();
      });
    });
    STAMP2(36);
    STAMP(4, 4);
    pin_acc<U>(acc);
    asm volatile("" : "+v"(iog0), "+v"(iog1));
  }
  SS8(1);
  __builtin_amdgcn_s_setprio(GELU_PRIO);
  BARRIER();
  SS8(2);
  constexpr int B1 = 2 * U, B2 = B1 + 8, B3 = 26;
  constexpr M2S SC = m2_sched(U);
  const int e0 = 2 * (U == 5 ? (up ? 0 : 26) : (up ? 10 : 18));
  const int e1 = 2 * (U == 5 ? (up ? 10 : 18) : (up ? 18 : 10)) - B1;
  const int e2 = 2 * (U == 5 ? (up ? 18 : 10) : (up ? 26 : 0)) - B2;
  const int e3 = 2 * (U == 5 ? (up ? 26 : 0) : (up ? 0 : 26)) - B3;
  auto kstep = [&](int s, int half) {
    const int uc2 = s < B1 ? 2 * U : (s < B2 ? 8 : (s < B3 ? (U == 5 ? 8 : 10) : 10));
    return (unsigned)(s + (s < B1 ? e0 : (s < B2 ? e1 : (s < B3 ? e2 : e3))) + half * uc2);
  };
  const int kh = wn;
  const unsigned nbyte = U == 5 ? (up ? 0u : 896u) : (up ? 256u : 640u), ebyte = up ? 512u : 576u;
  const int ln = fresh_lane();
  const unsigned voffX = ln * 16;
  auto mkw = [&](int l, unsigned hi) {
    return (unsigned)(((l >> 2) & 7) * 1152 + (l >> 5) * hi + (((l & 3) ^ ((l >> 3) & 2)) << 4));
  };
  const unsigned voffW = mkw(ln, 64u);
  unsigned voffE = mkw(ln, 32u * 1152u);
  if constexpr (U2 == 3) asm volatile("" : "+v"(voffE));
  const char* xsrc = H + ((size_t)(rt * 4 + wm) * 288 + sp * 72) * 1024;
  const int k0 = wave & 1, k1 = (wave >> 1) & 1;
  const unsigned cN = (kh * 16 + k1 * 32 + k0 * 8) * 1152 + nbyte, cE = (k1 * 16 + k0 * 8) * 1152 + ebyte;
  const char* w2s = W2 + (size_t)sp * 1152 * 1152;
  const char* wos = Wo + sp * (8 * 18 * 1152);
  constexpr int SL2 = U2 * 4096;
  const unsigned d0 = lbase + (kh * 2 * U2 + wm) * 1024, d2 = lbase + (k1 * 6 + 4 + k0) * 1024;
  auto issueW = [&](auto Sc, int e) {
    constexpr int s = decltype(Sc)::v;
    constexpr unsigned hf = (s & 1) * 128u;
    const unsigned dst = (e ? d2 : d0) + SC.slot[s] * SL2;
    if (e == 1 && !(U2 == 3 && (s & 1) == kh)) return;
    if constexpr (s < M_IT) {
      constexpr int Bc = s < B1 ? 0 : (s < B2 ? B1 : (s < B3 ? B2 : B3)), m = (s - Bc) >> 1;
      const unsigned cb = kstep(Bc, 0) * (16u * 1152u);
      if (e == 0) glds16s(w2s + (cb + cN + (m * 64u * 1152u + hf)), voffW, dst);
      else glds16s(w2s + (cb + cE + m * 64u * 1152u), voffE, dst);
    } else if constexpr (s < M2_NT - 1) {
      constexpr int t2 = (s - M_IT) & ~1;
      if (e == 0) glds16s(wos + ((unsigned)((kh * 4 + t2 + k0) * 18 + k1 * 8) * 1152u + nbyte + hf), voffW, dst);
      else glds16s(wos + ((unsigned)((k1 * 4 + t2 + k0) * 18) * 1152u + ebyte), mkw(fresh_lane(), 8u * 1152u), dst);
    } else {
      const int l2 = fresh_lane();
      const unsigned sw = (unsigned)(((l2 & 3) ^ ((l2 >> 3) & 2)) << 4), hb = (unsigned)(l2 >> 5) * 64u;
      const unsigned ve = (unsigned)((((l2 >> 3) & 3) * 18 + ((l2 >> 2) & 1)) * 1152) + sw;
      if (e == 0) {
        const bool ex = U2 == 3 && k0, bi = k1;
        const char* src = bi ? (const char*)b2 + (ex ? ebyte : nbyte + kh * 128u) : wos + ((unsigned)(k0 * 72 + 16) * 1152u + nbyte + kh * 128u);
        glds16s(src, bi ? (ex ? sw ^ (kh * 32u) : sw + hb) : ve + hb, dst);
      } else glds16s(wos + ((unsigned)(k0 * 72 + 16) * 1152u + ebyte), ve ^ (k1 * 32u), dst);
    }
  };
  static_for<0, 4>([&](auto Sc) { issueW(Sc, 0); issueW(Sc, 1); });
  SS8(3);
  const unsigned stash = lbase + 4 * SL2 + wave * ((2 * U - 4) * 1024) + voffX;
  {
    const float C0 = -2.0f * 1.4426950408889634f * 0.7978845608028654f;
    const float C1 = C0 * 0.044715f;
    f32x2 c0 = {C0, C0}, c1 = {C1, C1};
    asm volatile("" : "+s"(c0), "+s"(c1));
    f32x2 ig[2] = {{iog0, iog0}, {iog1, iog1}};
    asm volatile("" : "+v"(ig[0]), "+v"(ig[1]));
    char* hb = H + ((size_t)(rt * 4 + wm) * 288 + (unit0 * 2 + wn * U) * 2) * 1024 + fresh_lane() * 16;
#pragma unroll
    for (int b = 0; b < U; ++b) {
#pragma unroll
      for (int rg = 0; rg < 2; ++rg) {
        u32x4 o4;
#pragma unroll
        for (int i = 0; i < 4; ++i) {
          const int a = 8 * (i >> 1) + 4 * rg + 2 * (i & 1);
          const f32x2 v = {acc[b][a], acc[b][a + 1]};
          const f32x2 t = v * __builtin_elementwise_fma(v * v, c1, c0);
          const f32x2 e = {__builtin_amdgcn_exp2f(t[0]), __builtin_amdgcn_exp2f(t[1])};
          const f32x2 d = __builtin_elementwise_fma(e, ig[rg], ig[rg]);
          const f32x2 rc = {__builtin_amdgcn_rcpf(d[0]), __builtin_amdgcn_rcpf(d[1])};
          const f32x2 o = v * rc;
          o4[i] = pk2(o[0], o[1]);
        }
        st16(hb + (b * 2 + rg) * 1024, o4);
        if (b < 2) xr[b * 2 + rg] = __builtin_bit_cast(bf16x8, o4);
        else *(__attribute__((address_space(3))) u32x4*)(uintptr_t)(stash + (b * 2 + rg - 4) * 1024) = o4;
      }
      if (b == 1) SS8(4);
    }
  }
  STAMP(4, 5);
  SS8(5);
  unsigned wa[2];
  {
    const int g = ln >> 4, q = (ln >> 2) & 3, p = ln & 3;
#pragma unroll
    for (int e = 0; e < 2; ++e)
      wa[e] = lbase + (kh * 2 * U2 + (g >> 1)) * 1024 + (4 * (g & 1) + q) * 64 + ((e ^ (g & 1)) << 5) + p * 8;
  }
  asm volatile("" : "+v"(wa[0]), "+v"(wa[1]));
  wait_vm<2 * U>();
  SS8(6);
  BARRIER();
  __builtin_amdgcn_s_setprio(MLP2_PRIO);
  STAMP(4, 6);
  STAMP(4, 7);
  STAMP(2, 1);
  TS(1, -1);
  SS8(7);
  auto rdw = [&](auto Sc, auto Mc) {
    constexpr int s = decltype(Sc)::v, m = decltype(Mc)::v, o = SC.slot[s] * SL2 + (m < 4 ? (m >> 1) * 512 : 4096);
    wread<m, o, o + (m < 4 ? 2048 : (s == M2_NT - 1 ? -1024 : 512))>(w[m], wa[(m < 4 ? m : s) & 1]);
  };
  static_for<0, NBW>([&](auto Mc) { rdw(IC<0>{}, Mc); });
  unsigned fv = 0;
  auto poll = [&]() {
    unsigned vo = voffX;
    asm volatile("" : "+v"(vo));
    vo = (vo & 48u) << 3;
    gld4c(fv, FLg, vo);
  };
  auto check = [&](unsigned long long mask) {
    mask &= ~(1ull << j4);
    asm volatile("" : "+v"(fv));
    if (__ballot(fv != 1u) & mask) {
#pragma unroll 1
      for (int it = 0; it < (1 << 17); ++it) {
        __builtin_amdgcn_s_sleep(4);
        poll();
        wait_vm<0>();
        asm volatile("" : "+v"(fv));
        if (!(__ballot(fv != 1u) & mask)) break;
      }
    }
  };
  auto waitk = [&](auto N0, auto N1) {
    constexpr int n0 = decltype(N0)::v, n1 = decltype(N1)::v;
    if constexpr (n0 == n1) wait_vm<n0>();
    else { if (kh == 0) wait_vm<n0>(); else wait_vm<n1>(); }
  };
  constexpr int CK1 = m2_ck1(U), CK2 = m2_ck2(U);
  const char* osrc = O + (size_t)((rt * 4 + wm) * 32 + sp * 8 + kh * 4) * 1024;
  const char* oesrc = O + OEX + ((rt * 4 + wm) * 4 + sp) * 1024;
  static_for<0, M_IT>([&](auto Jc) {
    constexpr int j = decltype(Jc)::v;
    if constexpr (m2_need(U, j)) waitk(IC<m2_wait(U, 0, j, 0)>{}, IC<m2_wait(U, 1, j, 0)>{});
    pin_acc<NBW>(acc);
    static_for<0, 4>([&](auto Ic) {
      constexpr int i = decltype(Ic)::v;
      if constexpr ((j & ~1) + ((i - (j & ~1)) & 3) < M_IT) pin_x1<i>(xr[i]);
    });
    if constexpr (j > 0) BARRIER();
    if constexpr (j == 3) {
      if (wave == 0 && voffX == 0) st4(FLg + j4 * 128, 1u);
    }
    STAMP2(40 + j);
    if (j == 9) STAMP(2, 2);
    if (j == 18) STAMP(2, 3);
    if (j == 18) TS(2, -1);
    if (j == 27) STAMP(2, 4);
    if constexpr (j == CK1 - 2 || j == CK2 - 2) poll();
    SB();
    static_for<0, NBW>([&](auto Mc) {
      constexpr int m = decltype(Mc)::v;
      wait_lgkm<2 * (NBW - 1) + (j > 0 && (j & 1) == 0 && j + 2 < B1 ? 2 : 0)>();
      SB();
      mm16s<(j & 1), (j < 2 ? 1 : 0)>(acc[m], w[m], xr[j & 2], xr[(j & 2) + 1]);
      SB();
      rdw(IC<j + 1>{}, Mc);
      static_for<j + 3, (j + 7 < M2_NT ? j + 7 : M2_NT)>([&](auto Sc) {
        constexpr int s = decltype(Sc)::v;
        if constexpr (SC.it[s] == j) {
          constexpr bool second = s > 0 && SC.it[s - 1] == j;
          if constexpr (m == (second ? 2 : 0)) issueW(Sc, 0);
          if constexpr (m == (second ? 3 : 1)) issueW(Sc, 1);
        }
      });
      SB();
    });
    if constexpr (j == CK1 || j == CK2) {
      waitk(IC<m2_wait(U, 0, j, 1)>{}, IC<m2_wait(U, 1, j, 1)>{});
      check(j == CK1 ? 6ull : 15ull);
    }
    if constexpr (j & 1) {
      if constexpr (j + 3 < B1) {
        xstash<(j & 2), (j - 1) * 1024>(xr[j & 2], stash);
        xstash<(j & 3), j * 1024>(xr[j & 3], stash);
      } else if constexpr (j + 3 < M_IT) {
        xload<(j & 2)>(xr[j & 2], xsrc + kstep(j + 3, kh) * 1024u, voffX);
        xload<(j & 3)>(xr[j & 3], xsrc + kstep(j + 4, kh) * 1024u, voffX);
      }
    }
    SB();
  });
  STAMP2(76);
  STAMP(2, 5);
  TS(3, 0);
  const char* flp = FL + FL_P2 + (((rt * 4 + j4) * 4 + wm) * 2 + kh) * 512;
  wait_vm<0>();
  asm volatile("global_load_dword %0, %1, %2" : "=v"(iog0) : "v"((voffX & 0xf0u) >> 2), "s"(G + (2048 + (rt * 4 + wm) * 32) * 4) : "memory");
  asm volatile("global_load_dword %0, %1, %2 offset:64" : "=v"(iog1) : "v"((voffX & 0xf0u) >> 2), "s"(G + (2048 + (rt * 4 + wm) * 32) * 4) : "memory");
  pin_acc<NBW>(acc);
  static_for<0, NBW>([&](auto Mc) { pin_w<decltype(Mc)::v>(w[decltype(Mc)::v]); });
  if (wave == 0) {
    const int l2 = fresh_lane();
    spin_flags(FL + FL_O + rt * 512 + sp * 32, (unsigned)(((l2 >> 3) & 3) * 128 + (l2 & 7) * 4));
  }
  const unsigned voffO = (voffX & 0xf0u) + ((voffX & 0x300u) << 1);
  const unsigned voffOe = (voffX & 0x2f0u) + ((voffX & 0x100u) >> 5);
  BARRIER();
  TS(4, 1);
  {
    static_for<0, 4>([&](auto Ic) {
      constexpr int i = decltype(Ic)::v;
      xload<i>(xr[i], osrc + ((i >> 1) * 2048 + (i & 1) * 256), voffO);
    });
    u32x2 xe0, xe1;
    u32x4 y0, y1;
    asm volatile("global_load_dwordx2 %0, %1, %2 sc1" : "=v"(xe0) : "v"(voffOe), "s"(oesrc) : "memory");
    asm volatile("global_load_dwordx2 %0, %1, %2 offset:256 sc1" : "=v"(xe1) : "v"(voffOe), "s"(oesrc) : "memory");
    static_for<0, 4>([&](auto Tc) {
      constexpr int t = decltype(Tc)::v;
      if constexpr (t == 0) wait_vm<4>();
      if constexpr (t == 2) {
        asm volatile("s_waitcnt vmcnt(0)" : "+v"(xe0), "+v"(xe1), "+v"(iog0), "+v"(iog1)::"memory");
        const float og0 = __builtin_amdgcn_rcpf(iog0), og1 = __builtin_amdgcn_rcpf(iog1);
        const float oh0 = __uint_as_float(__float_as_uint(og0) & 0xffff0000u), oh1 = __uint_as_float(__float_as_uint(og1) & 0xffff0000u);
        const bool addb = sp == 0 && fresh_lane() < 16;
        y0 = u32x4{xe0[0], xe0[1], addb ? pk2(oh0, og0 - oh0) : 0u, 0u};
        y1 = u32x4{xe1[0], xe1[1], addb ? pk2(oh1, og1 - oh1) : 0u, 0u};
        asm volatile("" : "+v"(y0), "+v"(y1));
      }
      pin_x1<(t & 2)>(xr[t & 2]);
      pin_x1<(t & 2) + 1>(xr[(t & 2) + 1]);
      pin_acc<NBW>(acc);
      static_for<0, NBW>([&](auto Mc) {
        constexpr int m = decltype(Mc)::v;
        wait_lgkm<2 * (NBW - 1)>();
        SB();
        mm16<(t & 1)>(acc[m], w[m], xr[t & 2], xr[(t & 2) + 1]);
        SB();
        rdw(IC<M_IT + t + 1>{}, Mc);
        SB();
      });
    });
    wait_lgkm<0>();
    mm16e<NBW>(acc, w, y0, y1, kh);
  }
  BARRIER();
  STAMP(2, 6);
  TS(5, 2);
  constexpr int NH = NBW;
  const int l2 = fresh_lane();
  pair_sum<NBW>(acc, lbase + (wm * 2 + kh) * (NH * 1024) + l2 * 16, lbase + (wm * 2 + (kh ^ 1)) * (NH * 1024) + l2 * 16, kh);
  float r[NH][8];
#pragma unroll
  for (int q = 0; q < NH; ++q)
#pragma unroll
    for (int i = 0; i < 8; ++i) r[q][i] = acc[q][i];
  TS(-1, 3);
  constexpr unsigned PSP = 64 * 18 * 2048;
  const int nb0 = U == 5 ? (up ? 0 : 14) : (up ? 4 : 9);
  const char* pt = P + ((size_t)(rt * 4 + wm) * 18 + nb0) * 2048;
  const unsigned h0 = kh * 1024u, h4 = 8192u + h0;
  const unsigned vl = l2 * 16;
  float own[8];
#pragma unroll
  for (int q = 0; q < NH; ++q) {
    if (q < 4 && q == sp) {
#pragma unroll
      for (int i = 0; i < 8; ++i) own[i] = r[q][i];
    } else if (q < 4 || wm != sp) {
      u32x4 v;
#pragma unroll
      for (int i = 0; i < 4; ++i) v[i] = pk2(r[q][2 * i], r[q][2 * i + 1]);
      const char* dst = pt + (sp * PSP + (h0 + q * 2048u));
      asm volatile("global_store_dwordx4 %0, %1, %2 sc1\n\ts_nop 1" ::"v"(vl), "v"(v), "s"(dst) : "memory");
    }
  }
  {
    const unsigned oth = (unsigned)((sp + 1 + (l2 % 3)) & 3), vf = sp * 128u + oth * 4u;
    unsigned f0;
    gld4c(f0, flp, vf);
    wait_vm<1>();
    TS(-1, 4);
    if (l2 < 3) asm volatile("global_store_dword %0, %1, %2 sc1" ::"v"(oth * 128u + sp * 4u), "v"(1u), "s"(flp) : "memory");
    asm volatile("s_waitcnt vmcnt(1)" : "+v"(f0)::"memory");
    if (__ballot(f0 != 1u)) {
#pragma unroll 1
      for (int it = 0; it < (1 << 17); ++it) {
        gld4ck(f0, flp, vf);
        asm volatile("s_waitcnt vmcnt(0)" : "+v"(f0)::"memory");
        if (!__ballot(f0 != 1u)) break;
        __builtin_amdgcn_s_sleep(PSLP);
      }
    }
    TS(6, 5);
    const char* orow = out + (size_t)(rt * 4 + wm) * (32 * 1152);
    const unsigned vo = (l2 & 31) * 1152 + (l2 >> 5) * 16;
    auto finish = [&](float* a, bf16x8* p, unsigned coff) {
#pragma unroll
      for (int o = 0; o < 3; ++o) {
        const u32x4 v = __builtin_bit_cast(u32x4, p[o]);
#pragma unroll
        for (int k = 0; k < 4; ++k) {
          a[2 * k] += __uint_as_float(v[k] << 16);
          a[2 * k + 1] += __uint_as_float(v[k] & 0xffff0000u);
        }
      }
      auto r0 = __builtin_amdgcn_permlane16_swap(pk2(a[0], a[1]), pk2(a[4], a[5]), false, false);
      auto r1 = __builtin_amdgcn_permlane16_swap(pk2(a[2], a[3]), pk2(a[6], a[7]), false, false);
      const u32x4 wv = {r0[0], r1[0], r0[1], r1[1]};
      asm volatile("global_store_dwordx4 %0, %1, %2\n\ts_nop 1" ::"v"(vo), "v"(wv), "s"(orow + coff) : "memory");
    };
    bf16x8 pv[3], pw[3];
    const bool two = NH == 5 && wm == sp;
#pragma unroll
    for (int o = 0; o < 3; ++o) gld16c(pv[o], pt + (((sp + 1 + o) & 3) * PSP + h0 + sp * 2048u), vl);
    if constexpr (NH == 5) {
      if (two) {
#pragma unroll
        for (int o = 0; o < 3; ++o) gld16c(pw[o], pt + (((sp + 1 + o) & 3) * PSP + h4), vl);
      }
      asm volatile("s_waitcnt vmcnt(0)" : "+v"(pw[0]), "+v"(pw[1]), "+v"(pw[2]), "+v"(pv[0]), "+v"(pv[1]), "+v"(pv[2]) : "v"(f0) : "memory");
    } else {
      asm volatile("s_waitcnt vmcnt(0)" : "+v"(pv[0]), "+v"(pv[1]), "+v"(pv[2]) : "v"(f0) : "memory");
    }
    TS(-1, 6);
    finish(own, pv, nbyte + kh * 128u + sp * 32u);
    if (two) finish(r[NH - 1], pw, ebyte + kh * 32u);
  }
  STAMP(2, 7);
  SST(7);
  SSW();
}

extern "C" __global__ __launch_bounds__(512, 4) void kernel1(const char* __restrict__ T,
                                                   char* O, const char* __restrict__ XN,
                                                   const char* __restrict__ W1, const unsigned short* __restrict__ b1,
                                                   char* H, const char* __restrict__ W2,
                                                   const unsigned short* __restrict__ b2, const char* __restrict__ Wo,
                                                   const char* __restrict__ G, char* P, char* FL,
                                                   char* __restrict__ out) {
  __shared__ __attribute__((aligned(16))) char lds[81920];
  const int wave = __builtin_amdgcn_readfirstlane(threadIdx.x >> 6);
  const int lane = threadIdx.x & 63;
  const int bid = blockIdx.x;
  if (bid < 256) {
    STAMP(1, 0);
    STAMP(5, 0);
    __builtin_amdgcn_s_setprio(ATT_PRIO);
    attn_role(lds, T, O, P, G, FL, W2, Wo, bid, wave, fresh_lane());
  } else {
    STAMP(2, 0);
    STAMP(4, 0);
    const int wg = bid - 256;
    const int xcd = wg & 7, idx = wg >> 3;
    const int sp = xcd >> 1, j4 = idx & 3, r8 = idx >> 2, rt = (xcd & 1) * 8 + r8;
    const int koff = ((r8 * 9 / 2 + j4 * 9) % 36) >> 1;
    if (j4 == 0 || j4 == 3)
      mlp_track<5>(lds, XN, W1, b1, H, W2, b2, Wo, O, G, P, FL, out, rt, sp, j4, koff, wave, fresh_lane());
    else
      mlp_track<4>(lds, XN, W1, b1, H, W2, b2, Wo, O, G, P, FL, out, rt, sp, j4, koff, wave, fresh_lane());
  }
}

constexpr int Q_SLOT = 32768;
#define NSQ 4
constexpr int QGRID = 256;
DI void qkv_role(char* lds, const char* __restrict__ X, const char* __restrict__ Wqkv, char* __restrict__ T, int rt,
                 int ct, int joff, int wave, int lane) {
  f32x16 acc[2];
#pragma unroll
  for (int i = 0; i < 16; ++i) { acc[0][i] = 0.f; acc[1][i] = 0.f; }
  const unsigned lbase = lds_addr(lds);
  const int wm = wave & 3, wn = wave >> 2;
  const bool half = ct == 13;
  unsigned voffX[2];
#pragma unroll
  for (int i = 0; i < 2; ++i) {
    int row = wave * 16 + 8 * i + (lane >> 3);
    int c = (lane & 7) ^ ((row >> 1) & 7);
    voffX[i] = (unsigned)((rt * 128 + row) * 1152 + c * 16);
  }
  const unsigned voffW = (unsigned)((lane >> 3) * 3456 + (((lane & 7) ^ (((lane >> 4) & 1) << 2)) << 4));
  const char* wsrc = Wqkv + (size_t)wave * 8 * 3456 + ct * 256;
  const unsigned wh = half ? 0u : 128u;
  auto issue = [&](int J, int e) {
    unsigned dst = lbase + (J % NSQ) * Q_SLOT;
    unsigned Jr = J + joff;
    Jr = Jr >= 9 ? Jr - 9 : Jr;
    if (e < 2) glds16s(wsrc + (Jr * (64u * 3456u) + e * wh), voffW, dst + 16384 + e * 8192 + wave * 1024);
    else glds16s(X + Jr * 128u, voffX[e - 2], dst + (wave * 2 + e - 2) * 1024);
  };
  STAMP(0, 0);
#pragma unroll
  for (int J = 0; J < NSQ - 1; ++J)
#pragma unroll
    for (int e = 0; e < 4; ++e) issue(J, e);
  const int r = lane & 31, h = lane >> 5;
  const int q = (lane >> 2) & 3, p = lane & 3, g = (lane >> 4) & 1;
  const lds_p L = (lds_p)lds;
  constexpr int NB = (NSQ + 1) / 2;
  lds_p xa[NB][4], wa[NB][2];
#pragma unroll
  for (int z = 0; z < NB; ++z) {
#pragma unroll
    for (int ks = 0; ks < 4; ++ks) {
      xa[z][ks] = L + z * 2 * Q_SLOT + (wm * 32 + r) * 128 + (((2 * ks + h) ^ ((r >> 1) & 7)) << 4);
      asm volatile("" : "+v"(xa[z][ks]));
    }
#pragma unroll
    for (int b = 0; b < 2; ++b) {
      wa[z][b] = L + z * 2 * Q_SLOT + 16384 + wn * 8192 + h * 1024 + q * 128 + ((b ^ (q >> 1)) << 6) + g * 32 + p * 8;
      asm volatile("" : "+v"(wa[z][b]));
    }
  }
  wait_vm<4 * (NSQ - 2)>();
  BARRIER();
  STAMP(0, 1);
  bf16x8 fx[2], fw[2][2];
  auto rd = [&](int buf, int J, int ks) {
    const int sl = J % NSQ;
    fx[buf] = ld8(xa[sl >> 1][ks] + (sl & 1) * Q_SLOT);
#pragma unroll
    for (int b = 0; b < 2; ++b) {
      const lds_p a = wa[sl >> 1][b] + (sl & 1) * Q_SLOT + ks * 2048;
      fw[buf][b] = cat8(trl(a), trl(a + 512));
    }
  };
  rd(0, 0, 0);
  static_for<0, 9>([&](auto Jc) {
    constexpr int J = decltype(Jc)::v;
    if constexpr (J + 1 < 9) {
      constexpr int top = J + NSQ - 2 < 8 ? J + NSQ - 2 : 8;
      wait_vm<4 * (top - (J + 1))>();
      BARRIER();
    }
    if (J == 3) STAMP(0, 2);
    if (J == 6) STAMP(0, 3);
    SB();
    static_for<0, 4>([&](auto Kc) {
      constexpr int ks = decltype(Kc)::v, cur = ks & 1;
      acc[0] = MFMA32(fx[cur], fw[cur][0], acc[0]);
      SB();
      if constexpr (ks < 3) rd(cur ^ 1, J, ks + 1);
      else if constexpr (J + 1 < 9) rd(cur ^ 1, J + 1, 0);
      SB();
      acc[1] = MFMA32(fx[cur], fw[cur][1], acc[1]);
      SB();
      if constexpr (J + NSQ - 1 < 9) {
        if constexpr (ks == 0) { issue(J + NSQ - 1, 0); issue(J + NSQ - 1, 1); }
        else if constexpr (ks < 3) issue(J + NSQ - 1, ks + 1);
      }
      SB();
    });
  });
  STAMP(0, 5);
  const float QSCALE = 0.23570226039551584f * 1.4426950408889634f;
  const float sc = ct * 2 + wn < 9 ? QSCALE : 1.0f;
  if (!(half && wn)) {
#pragma unroll
    for (int nb = 0; nb < 2; ++nb) {
      char* base = T + (size_t)h * TP + ((ct * 2 + wn) * 64 + nb * 32 + r) * 16;
#pragma unroll
      for (int s = 0; s < 2; ++s) {
        int step = rt * 8 + wm * 2 + s;
        u32x4 v;
#pragma unroll
        for (int i = 0; i < 4; ++i) v[i] = pk2(acc[nb][8 * s + 2 * i] * sc, acc[nb][8 * s + 2 * i + 1] * sc);
        st16(base + (size_t)step * (2 * TP), v);
      }
    }
  } else if (wm < 2) {
    int idx = wm * 64 + lane;
    unsigned c = (idx & 7) == 0 ? 0x3F803F80u : 0u;
    u32x4 v = {c, c, c, c};
    st16(T + (size_t)(rt * 16 + (idx >> 3)) * TP + (1728 + (idx & 7)) * 16, v);
  }
}

DI float dot2(unsigned a, unsigned b, float c) {
  return __builtin_amdgcn_fdot2_f32_bf16(__builtin_bit_cast(bf16x2_t, a), __builtin_bit_cast(bf16x2_t, b), c, false);
}
template <int C>
DI float dpp_add(float v) {
  return v + __builtin_bit_cast(float, __builtin_amdgcn_update_dpp(0, __builtin_bit_cast(int, v), C, 0xf, 0xf, true));
}
DI float sum8(float v) { return dpp_add<0x141>(dpp_add<0x4E>(dpp_add<0xB1>(v))); }
DI void ln_role(const char* __restrict__ X, const char* __restrict__ gw, char* __restrict__ XN, float* __restrict__ G,
                char* __restrict__ FL, int id, int wave, int lane) {
  const int c = lane & 7;
  const int row = id * 64 + wave * 8 + (lane >> 3);
  bf16x8 xv[9], gv[9];
  {
    const unsigned vx = (unsigned)(row * 1152 + c * 16), vg = (unsigned)(c * 16);
#pragma unroll
    for (int i = 0; i < 9; ++i) asm volatile("global_load_dwordx4 %0, %1, %2 offset:%3" : "=v"(xv[i]) : "v"(vx), "s"(X), "i"(i * 128) : "memory");
#pragma unroll
    for (int i = 0; i < 9; ++i) asm volatile("global_load_dwordx4 %0, %1, %2 offset:%3" : "=v"(gv[i]) : "v"(vg), "s"(gw), "i"(i * 128) : "memory");
  }
  st4(FL + id * 2048 + wave * 256 + lane * 4, 0u);
  asm volatile("global_store_dwordx2 %0, %1, off sc1" ::"v"(FL + FL_P2 + (id * 8 + wave) * 1024 + (lane >> 3) * 128 + c * 8), "v"(0ull) : "memory");
  char* dst = XN + ((size_t)(row >> 5) * 36 + (c >> 1)) * 1024 + ((c & 1) * 32 + (row & 31)) * 16;
  asm volatile("s_waitcnt vmcnt(11)" : "+v"(xv[0]), "+v"(xv[1]), "+v"(xv[2]), "+v"(xv[3]), "+v"(xv[4]), "+v"(xv[5]), "+v"(xv[6]), "+v"(xv[7]), "+v"(xv[8])::"memory");
  float sa[4] = {0.f, 0.f, 0.f, 0.f};
#pragma unroll
  for (int i = 0; i < 9; ++i)
#pragma unroll
    for (int j = 0; j < 4; ++j) sa[j] = dot2(__builtin_bit_cast(u32x4, xv[i])[j], 0x3F803F80u, sa[j]);
  STAMP(0, 1);
  const float mean = sum8((sa[0] + sa[1]) + (sa[2] + sa[3])) * (1.0f / 576.0f);
  const f32x2 m2 = {mean, mean};
  f32x2 xc[9][4];
  f32x2 qa[4] = {{0.f, 0.f}, {0.f, 0.f}, {0.f, 0.f}, {0.f, 0.f}};
#pragma unroll
  for (int i = 0; i < 9; ++i)
#pragma unroll
    for (int j = 0; j < 4; ++j) {
      const unsigned u = __builtin_bit_cast(u32x4, xv[i])[j];
      xc[i][j] = f32x2{__uint_as_float(u << 16), __uint_as_float(u & 0xffff0000u)} - m2;
      qa[j] = __builtin_elementwise_fma(xc[i][j], xc[i][j], qa[j]);
    }
  const f32x2 qq = (qa[0] + qa[1]) + (qa[2] + qa[3]);
  const float var = sum8(qq[0] + qq[1]) * (1.0f / 576.0f);
  STAMP(0, 3);
  const float rstd = 1.0f / __builtin_sqrtf(var + 1e-5f);
  const f32x2 r2 = {rstd, rstd};
#pragma unroll
  for (int i = 0; i < 9; ++i) {
    u32x4 v;
#pragma unroll
    for (int j = 0; j < 4; ++j) {
      const f32x2 y = xc[i][j] * r2;
      v[j] = pk2(y[0], y[1]);
    }
    st16(dst + i * 4096, v);
  }
  asm volatile("s_waitcnt vmcnt(11)" : "+v"(gv[0]), "+v"(gv[1]), "+v"(gv[2]), "+v"(gv[3]), "+v"(gv[4]), "+v"(gv[5]), "+v"(gv[6]), "+v"(gv[7]), "+v"(gv[8])::"memory");
  float da[4] = {0.f, 0.f, 0.f, 0.f};
#pragma unroll
  for (int i = 0; i < 9; ++i)
#pragma unroll
    for (int j = 0; j < 4; ++j) da[j] = dot2(__builtin_bit_cast(u32x4, xv[i])[j], __builtin_bit_cast(u32x4, gv[i])[j], da[j]);
  const float v = sum8((da[0] + da[1]) + (da[2] + da[3])) * 1.4426950408889634f;
  const float e = 1.0f + __builtin_amdgcn_exp2f((c & 1) ? v : -v);
  const float e1 = __builtin_fminf(e, 1.0e30f), e0 = 1.0f / e;
  st4(G + (c & 1) * 2048 + row, __float_as_uint((c & 1) ? e1 : e0));
  STAMP(0, 5);
  unsigned d0;
  {
    const char* pc;
    unsigned span;
    asm volatile("s_getpc_b64 s[20:21]\n\ts_mov_b32 %1, kernel1@rel32@lo+4\n\ts_add_u32 s20, s20, %1\n\ts_addc_u32 s21, s21, kernel1@rel32@hi+16"
                 : "={s[20:21]}"(pc), "=s"(span)::"scc");
    span = 0u - span;
    span = span < (1u << 18) ? span : 0u;
    const unsigned o = (unsigned)(((id >> 3) * 8 + wave) * 64 + lane) * 128u;
    gld4(d0, pc, o < span ? o : 0u);
  }
  asm volatile("" ::"v"(d0) : "memory");
}

__global__ __launch_bounds__(512, 2) void kernelQ(const char* __restrict__ X, const char* __restrict__ Wqkv,
                                                   const char* __restrict__ gw, char* __restrict__ T,
                                                   char* __restrict__ XN, float* __restrict__ G,
                                                   char* __restrict__ FL) {
  __shared__ __attribute__((aligned(16))) char lds[NSQ * Q_SLOT];
  const int wg = blockIdx.x;
  const int wave = __builtin_amdgcn_readfirstlane(threadIdx.x >> 6);
  const int lane = threadIdx.x & 63;
  if (wg < 224) {
    const int xcd = wg & 7, t = wg >> 3, rl = t & 3, cl = t >> 2;
    const int joff = (3 * (cl + rl) + (rl == 3 ? 1 : 0)) % 9;
    qkv_role(lds, X, Wqkv, T, (xcd & 3) * 4 + rl, (xcd >> 2) * 7 + cl, joff, wave, lane);
  } else {
    STAMP(0, 0);
    ln_role(X, gw, XN, G, FL, wg - 224, wave, lane);
  }
  STAMP(0, 6);
  STAMP(0, 7);
}

extern "C" void kernel_launch(void* const* d_in, const int* in_sizes, int n_in, void* d_out, int out_size, void* d_ws,
                              size_t ws_size, hipStream_t stream) {
  const char* X = (const char*)d_in[0];
  const char* Wqkv = (const char*)d_in[1];
  const char* Wo = (const char*)d_in[2];
  const char* W1 = (const char*)d_in[4];
  const unsigned short* b1 = (const unsigned short*)d_in[5];
  const char* W2 = (const char*)d_in[6];
  const unsigned short* b2 = (const unsigned short*)d_in[7];
  const char* gw = (const char*)d_in[8];
  char* ws = (char*)d_ws;
  char* T = ws + WS_T;
  char* H = ws + WS_H;
  char* O = ws + WS_O;
  char* P = ws + WS_P;
  char* XN = ws + WS_XN;
  char* FL = ws + WS_FL;
  float* G = (float*)(ws + WS_G);
  kernelQ<<<QGRID, 512, 0, stream>>>(X, Wqkv, gw, T, XN, G, FL);
  kernel1<<<512, 512, 0, stream>>>(T, O, XN, W1, b1, H, W2, b2, Wo, (const char*)G, P, FL, (char*)d_out);
}
```
